```python
import torch

def _softmax(x, axis=-1):
    return torch.softmax(x, dim=axis)
def _gelu(x):
    return torch.nn.functional.gelu(x, approximate="tanh")
def _silu(x):
    return torch.nn.functional.silu(x)
def _sigmoid(x):
    return torch.sigmoid(x)
def _layernorm(x, axis=-1, eps=1e-5):
    m = x.mean(dim=axis, keepdim=True)
    v = x.var(dim=axis, keepdim=True, unbiased=False)
    return (x - m) / torch.sqrt(v + eps)
def _scatter_add(tgt, idx, src, axis=0):
    return tgt.clone().index_add_(axis, idx, src)
def _segment_sum(src, seg, n):
    out = torch.zeros((n,) + src.shape[1:], dtype=src.dtype)
    return out.index_add_(0, seg, src)
def _pad(x, pw):
    flat = tuple(v for pair in reversed(pw) for v in pair)
    return torch.nn.functional.pad(x, flat)

kernel_name = 'sequential_preLN__mha_block__geglu_ffn__prompt'

def setup_inputs():

    _w = (lambda a: (lambda t: t.to(torch.bfloat16) if t.is_floating_point() else t)(torch.as_tensor(a)))
    def _A_setup():
        B, S, D, H = 1, 2048, 640, 32
        return {
            "x": _w((lambda *s: torch.randn(*s, dtype=torch.bfloat16) * 0.05)(B, S, D)),
            "wqkv": _w((lambda *s: torch.randn(*s, dtype=torch.bfloat16) * 0.05)(D, 3 * D)),
            "wo": _w((lambda *s: torch.randn(*s, dtype=torch.bfloat16) * 0.05)(D, D)),
        }
    _A_in = _A_setup()

    def _B_setup():
        B, S, D, F = 1, 2048, 640, 1536
        return {
            "x": _w((lambda *s: torch.randn(*s, dtype=torch.bfloat16) * 0.05)(B, S, D)),
            "w_gate": _w((lambda *s: torch.randn(*s, dtype=torch.bfloat16) * 0.05)(D, F)),
            "w_up": _w((lambda *s: torch.randn(*s, dtype=torch.bfloat16) * 0.05)(D, F)),
            "w_down": _w((lambda *s: torch.randn(*s, dtype=torch.bfloat16) * 0.05)(F, D)),
        }
    _B_in = _B_setup()

    return {
        "A_x": _A_in["x"],
        "A_wqkv": _A_in["wqkv"],
        "A_wo": _A_in["wo"],
        "B_x": _B_in["x"],
        "B_w_gate": _B_in["w_gate"],
        "B_w_up": _B_in["w_up"],
        "B_w_down": _B_in["w_down"],
    }

def reference(A_x, A_wqkv, A_wo, B_x, B_w_gate, B_w_up, B_w_down):
    def _A_ref(x, wqkv, wo):
        B, S, D = x.shape
        H = 32
        Dh = D // H
        qkv = (lambda x, *d: x.permute(*d))((x @ wqkv).reshape(B, S, 3, H, Dh), 2, 0, 3, 1, 4)
        q, k, v = qkv[0], qkv[1], qkv[2]
        scores = torch.einsum("bhqd,bhkd->bhqk", q, k) * (Dh ** -0.5)
        p = _softmax(scores, axis=-1)
        o = torch.einsum("bhqk,bhkd->bhqd", p, v)
        return (lambda x, *d: x.permute(*d))(o, 0, 2, 1, 3).reshape(B, S, D) @ wo

    def _B_ref(x, w_gate, w_up, w_down):
        return (_gelu(x @ w_gate) * (x @ w_up)) @ w_down

    x = A_x
    h = x + _A_ref(_layernorm(x, axis=-1), A_wqkv, A_wo)
    h2 = h + _B_ref(_layernorm(h, axis=-1), B_w_gate, B_w_up, B_w_down)
    return h2
```

```cpp
#include <hip/hip_runtime.h>
#include <hip/hip_bf16.h>
#include <stdint.h>

typedef short bf16x8 __attribute__((ext_vector_type(8)));
typedef short s16x4 __attribute__((ext_vector_type(4)));
typedef float f32x16 __attribute__((ext_vector_type(16)));
typedef float f32x4 __attribute__((ext_vector_type(4)));
typedef float f32x2 __attribute__((ext_vector_type(2)));
typedef unsigned u32x4 __attribute__((ext_vector_type(4)));
typedef unsigned u32x2 __attribute__((ext_vector_type(2)));
typedef __bf16 bf2_t __attribute__((ext_vector_type(2)));
typedef unsigned short u16;
#define LAS __attribute__((address_space(3)))
#define DI __device__ __forceinline__

constexpr int S_ = 2048, D_ = 640, H_ = 32, DH = 20, F_ = 1536;
constexpr float LN_EPS = 1e-5f;
constexpr float QSCALE = 0.22360679774997896f * 1.4426950408889634f;

constexpr size_t WS_Q   = 0;
constexpr size_t WS_KV  = WS_Q + 2621440;
constexpr size_t WS_O   = WS_KV + 5242880;
constexpr size_t WS_H   = WS_O + 2621440;
constexpr size_t WS_HST = WS_H + 2621440;
constexpr size_t WS_G   = WS_HST + 131072;
constexpr size_t WS_WP  = WS_G + 6291456;
constexpr int NFR_WO = 800, NFR_GU = 3840, NFR_WD = 1920, NFR_ALL = NFR_WO + NFR_GU + NFR_WD;

DI unsigned pk_bf16(float lo, float hi) { f32x2 f = {lo, hi}; return __builtin_bit_cast(unsigned, __builtin_convertvector(f, bf2_t)); }
DI float bf_lo(unsigned u) { return __uint_as_float(u << 16); }
DI float bf_hi(unsigned u) { return __uint_as_float(u & 0xffff0000u); }
DI float dot2(unsigned a, unsigned b, float c) { return __builtin_amdgcn_fdot2_f32_bf16(__builtin_bit_cast(bf2_t, a), __builtin_bit_cast(bf2_t, b), c, false); }
DI float xhalf(float v) {
  return __shfl_xor(v, 32);
}
#define MFMA32(a, b, c) __builtin_amdgcn_mfma_f32_32x32x16_bf16((a), (b), (c), 0, 0, 0)
#define MFMA32K8(a, b, c) __builtin_amdgcn_mfma_f32_32x32x8bf16_1k((a), (b), (c), 0, 0, 0)
#define MFMA16(a, b, c) __builtin_amdgcn_mfma_f32_16x16x32_bf16((a), (b), (c), 0, 0, 0)

DI void glds16(const void* sbase, unsigned voff, unsigned lds_base) {
  unsigned sv;
  asm volatile("s_mov_b32 %0, m0\n\ts_mov_b32 m0, %3\n\ts_nop 0\n\tglobal_load_lds_dwordx4 %1, %2\n\ts_mov_b32 m0, %0"
               : "=&s"(sv) : "v"(voff), "s"(sbase), "s"(lds_base) : "memory");
}
DI void gld16(bf16x8& dst, const void* sbase, unsigned voff) {
  asm volatile("global_load_dwordx4 %0, %1, %2" : "=v"(dst) : "v"(voff), "s"(sbase) : "memory");
}
DI bf16x8 lds128(unsigned addr) { return *(const LAS bf16x8*)(uintptr_t)addr; }
DI s16x4 lds64(unsigned addr) { return *(const LAS s16x4*)(uintptr_t)addr; }
DI s16x4 ldstr(unsigned addr) { return __builtin_amdgcn_ds_read_tr16_b64_v4i16((LAS s16x4*)(uintptr_t)addr); }
DI unsigned lds_addr_of(const void* p) { return (unsigned)(uintptr_t)(const LAS char*)p; }
#define WAITV_BAR(N) asm volatile("s_waitcnt vmcnt(" #N ") lgkmcnt(0)\n\ts_barrier" ::: "memory")
template <int N> DI void waitv_bar() { asm volatile("s_waitcnt vmcnt(%0) lgkmcnt(0)\n\ts_barrier" :: "i"(N) : "memory"); }

constexpr int K1_STAGE = 32768, K1_NST = 4;
template <bool ISV>
DI void k1_body(const u16* __restrict__ x, const u16* __restrict__ W, char* __restrict__ ws, char* smem, int rb, int cb) {
  const int tid = threadIdx.x, lane = tid & 63;
  const int w = __builtin_amdgcn_readfirstlane(tid >> 6), wm = w >> 2, wn = w & 3;
  const unsigned lds0 = lds_addr_of(smem);
  const int r32 = lane & 31, hh = lane >> 5;
  const unsigned a_voff0 = (unsigned)((lane >> 3) * 1280 + (((lane & 7) ^ ((lane >> 4) & 7)) << 4));
  const unsigned a_voff1 = (unsigned)((lane >> 3) * 1280 + (((lane & 7) ^ ((4 + (lane >> 4)) & 7)) << 4)) + 8 * 1280;
  const unsigned w_voff = (unsigned)((lane >> 3) * 3840 + (((lane & 7) ^ (((lane >> 4) & 1) << 2)) << 4));
  const char* xs = (const char*)x + (size_t)(rb * 128 + w * 16) * 1280;
  const char* wsrc = (const char*)W + (size_t)((w & 3) * 16) * 3840 + (size_t)(cb * 128 + (w >> 2) * 64) * 2;
  auto stage = [&](int s, int slot) {
    const unsigned dst = lds0 + slot * K1_STAGE;
    glds16(xs + s * 128, a_voff0, dst + w * 2048);
    glds16(xs + s * 128, a_voff1, dst + w * 2048 + 1024);
    glds16(wsrc + (size_t)s * 64 * 3840, w_voff, dst + 16384 + w * 2048);
    glds16(wsrc + (size_t)s * 64 * 3840 + 8 * 3840, w_voff, dst + 16384 + w * 2048 + 1024);
  };
  stage(0, 0); stage(1, 1); stage(2, 2);
  const int ob = (wn >> 1) ^ wm;
  const int fsel = wn & 1;
  unsigned xa[4];
  {
    const int row = wm * 64 + fsel * 32 + r32;
#pragma unroll
    for (int i = 0; i < 4; ++i) xa[i] = (unsigned)(row * 128 + ((((i ^ ob) * 2 + hh) ^ ((row >> 1) & 7)) << 4));
  }
  const int xo1 = fsel ? -4096 : 4096;
  unsigned wa[2];
  {
    const int blk = (lane >> 4) & 1, q = (lane & 15) >> 2, p = lane & 3;
    const int c = (wn & 1) * 4 + blk * 2 + (p >> 1);
    const unsigned base = (unsigned)(16384 + (wn >> 1) * 8192 + (8 * hh + q) * 128 + ((c ^ (((q >> 1) & 1) << 2)) << 4) + (p & 1) * 8);
    wa[0] = base + ob * 2048; wa[1] = base + (1 ^ ob) * 2048;
  }
  f32x16 acc0 = {}, acc1 = {};
  float sx = 0.f, sxx = 0.f, sw = 0.f;
  for (int s = 0; s < 10; ++s) {
    if (s < 8) WAITV_BAR(8); else if (s == 8) WAITV_BAR(4); else WAITV_BAR(0);
    if (s + 3 < 10) stage(s + 3, (s + 3) & 3);
    const unsigned sb = lds0 + (s & 3) * K1_STAGE;
#pragma unroll
    for (int i = 0; i < 4; ++i) {
      const bf16x8 x0 = lds128(sb + xa[i]);
      const bf16x8 x1 = lds128(sb + xa[i] + xo1);
      const s16x4 wl = ldstr(sb + wa[i & 1] + (i >> 1) * 4096);
      const s16x4 wh = ldstr(sb + wa[i & 1] + (i >> 1) * 4096 + 512);
      const bf16x8 wf = __builtin_shufflevector(wl, wh, 0, 1, 2, 3, 4, 5, 6, 7);
      if (ISV) { acc0 = MFMA32(x0, wf, acc0); acc1 = MFMA32(x1, wf, acc1); }
      else     { acc0 = MFMA32(wf, x0, acc0); acc1 = MFMA32(wf, x1, acc1); }
      if ((i & 1) == 0) {
        const u32x4 u = __builtin_bit_cast(u32x4, x0);
#pragma unroll
        for (int j = 0; j < 4; ++j) { sx = dot2(u[j], 0x3f803f80u, sx); sxx = dot2(u[j], u[j], sxx); }
      } else {
        const u32x4 u = __builtin_bit_cast(u32x4, wf);
#pragma unroll
        for (int j = 0; j < 4; ++j) sw = dot2(u[j], 0x3f803f80u, sw);
      }
    }
  }
  sx += xhalf(sx); sxx += xhalf(sxx); sw += xhalf(sw);
  __syncthreads();
  LAS f32x2* part = (LAS f32x2*)(uintptr_t)lds0;
  LAS float* csp = (LAS float*)(uintptr_t)(lds0 + 2048);
  if (hh == 0) { part[(wn >> 1) * 128 + wm * 64 + fsel * 32 + r32] = (f32x2){sx, sxx}; }
  else         { csp[wm * 128 + wn * 32 + r32] = sw; }
  __syncthreads();
  LAS f32x2* st = (LAS f32x2*)(uintptr_t)(lds0 + 4096);
  LAS float* cs = (LAS float*)(uintptr_t)(lds0 + 5120);
  if (tid < 128) {
    const f32x2 a = part[tid], b = part[128 + tid];
    const float mean = (a.x + b.x) * (1.f / 640.f);
    const float var = fmaxf((a.y + b.y) * (1.f / 640.f) - mean * mean, 0.f);
    st[tid] = (f32x2){mean, __builtin_amdgcn_rsqf(var + LN_EPS)};
  } else if (tid < 256) {
    cs[tid - 128] = csp[tid - 128] + csp[tid];
  }
  __syncthreads();
  if (ISV) {
    const int nl = wn * 32 + r32, n = cb * 128 + nl - 1280, head = n / 20, d = n - head * 20;
    const float c = cs[nl];
#pragma unroll
    for (int t = 0; t < 2; ++t) {
      const f32x16& a = t ? acc1 : acc0;
      const int rl = wm * 64 + (t ^ fsel) * 32;
      unsigned pk[8];
#pragma unroll
      for (int g = 0; g < 4; ++g) {
        const f32x4 s01 = *(const LAS f32x4*)&st[rl + 8 * g + 4 * hh], s23 = *(const LAS f32x4*)&st[rl + 8 * g + 4 * hh + 2];
        pk[2 * g]     = pk_bf16(s01[1] * (a[4 * g] - s01[0] * c), s01[3] * (a[4 * g + 1] - s01[2] * c));
        pk[2 * g + 1] = pk_bf16(s23[1] * (a[4 * g + 2] - s23[0] * c), s23[3] * (a[4 * g + 3] - s23[2] * c));
      }
      char* dst = ws + WS_KV + ((size_t)head * 64 + (rb * 4 + (rl >> 5))) * 2560 + 1280 + (hh * 20 + d) * 16;
      *(u32x4*)dst = (u32x4){pk[0], pk[1], pk[2], pk[3]};
      *(u32x4*)(dst + 640) = (u32x4){pk[4], pk[5], pk[6], pk[7]};
    }
  } else {
    const bool isq = cb < 5;
    const float sc = isq ? QSCALE : 1.f;
    char* base = ws + (isq ? WS_Q : WS_KV);
    const int pitch = isq ? 1280 : 2560;
#pragma unroll
    for (int t = 0; t < 2; ++t) {
      const f32x16& a = t ? acc1 : acc0;
      const int rl = wm * 64 + (t ^ fsel) * 32;
      const f32x2 ms = st[rl + r32];
      const float rs = ms.y * sc, mr = ms.x;
      const int tile = rb * 4 + (rl >> 5);
#pragma unroll
      for (int g = 0; g < 4; ++g) {
        const int nl = wn * 32 + 8 * g + 4 * hh;
        const f32x4 c = *(const LAS f32x4*)&cs[nl];
        const int n = cb * 128 + nl - (isq ? 0 : 640), head = n / 20, d = n - head * 20;
        const unsigned p0 = pk_bf16(rs * (a[4 * g] - mr * c[0]), rs * (a[4 * g + 1] - mr * c[1]));
        const unsigned p1 = pk_bf16(rs * (a[4 * g + 2] - mr * c[2]), rs * (a[4 * g + 3] - mr * c[3]));
        const int off = d < 16 ? (((d >> 3) * 32 + r32) * 16 + (d & 4) * 2) : (1024 + r32 * 8);
        *(u32x2*)(base + ((size_t)head * 64 + tile) * pitch + off) = (u32x2){p0, p1};
      }
    }
  }
}

__global__ __launch_bounds__(512, 2) void k1_qkv(const u16* __restrict__ x, const u16* __restrict__ W, char* __restrict__ ws) {
  __shared__ __attribute__((aligned(16))) char smem[K1_STAGE * K1_NST];
  const int g = blockIdx.x & 7, i = (g & 1) * 30 + (blockIdx.x >> 3);
  const int rb = (g >> 1) * 4 + (i & 3), cb = i >> 2;
  if (cb >= 10) k1_body<true>(x, W, ws, smem, rb, cb);
  else k1_body<false>(x, W, ws, smem, rb, cb);
}

constexpr int A_PAIR = 5136, A_CHUNK = 4 * A_PAIR, A_HALF = 3 * A_CHUNK;
__global__ __launch_bounds__(512, 2) void k2_attn(char* __restrict__ ws, const u16* __restrict__ Wo, const u16* __restrict__ Wg,
                                                  const u16* __restrict__ Wu, const u16* __restrict__ Wd) {
  __shared__ __attribute__((aligned(16))) char smem[2 * A_HALF];
  const int tid = threadIdx.x, lane = tid & 63;
  const int w = __builtin_amdgcn_readfirstlane(tid >> 6), kh = w >> 2, wq = w & 3;
  const int head = (blockIdx.x & 7) * 4 + ((blockIdx.x >> 3) & 3), qb = blockIdx.x >> 5;
  const unsigned lds0 = lds_addr_of(smem);
  const int r32 = lane & 31, hh = lane >> 5;
  const char* kvg = ws + WS_KV + (size_t)head * 163840 + (size_t)kh * 81920;
  const unsigned voff = lane * 16;
  const unsigned ringb = lds0 + kh * A_HALF;
  auto chunk_dma = [&](int c, int slot) {
#pragma unroll
    for (int i = 0; i < 5; ++i) {
      const int p = wq + 4 * i;
      glds16(kvg + (size_t)c * 20480 + p * 1024, voff, ringb + slot * A_CHUNK + (p / 5) * A_PAIR + (p % 5) * 1024);
    }
  };
  chunk_dma(0, 0);
  bf16x8 q0[2]; s16x4 q1[2];
#pragma unroll
  for (int t = 0; t < 2; ++t) {
    const char* qg = ws + WS_Q + ((size_t)head * 64 + qb * 8 + wq * 2 + t) * 1280;
    q0[t] = *(const bf16x8*)(qg + lane * 16);
    q1[t] = *(const s16x4*)(qg + 1024 + r32 * 8);
  }
  if (tid < 24) *(LAS u32x4*)(uintptr_t)(lds0 + tid * A_PAIR + 5120) = (u32x4){0x3f803f80u, 0x3f803f80u, 0x3f803f80u, 0x3f803f80u};
  const unsigned ka0 = lane * 16;
  const unsigned ka1a = hh ? 5120 : 1024 + r32 * 8, ka1b = hh ? 5120 : 2560 + 1024 + r32 * 8;
  unsigned va[4];
#pragma unroll
  for (int i = 0; i < 4; ++i) va[i] = r32 < 20 ? (unsigned)((i >> 1) * 2560 + 1280 + (i & 1) * 640 + (hh * 20 + r32) * 16) : 5120u;
  if (hh) { q1[0] = (s16x4){0, 0, 0, 0}; q1[1] = (s16x4){0, 0, 0, 0}; }
  WAITV_BAR(0);
  asm volatile("" : "+v"(q0[0]), "+v"(q0[1]), "+v"(q1[0]), "+v"(q1[1]));
  u16 pw[4][8];
  const int gw = blockIdx.x * 8 + w;
#pragma unroll
  for (int r = 0; r < 4; ++r) {
    int F = gw + 2048 * r; if (F >= NFR_ALL) F = gw;
    const u16* src; int ld;
    if (F < NFR_WO) { const int cbk = F / 100, ks = (F % 100) / 5, ct = F % 5; src = Wo + (size_t)(32 * ks + 8 * (lane >> 4)) * 640 + 80 * cbk + 16 * ct + (lane & 15); ld = 640; }
    else if (F < NFR_WO + NFR_GU) { const int f = F - NFR_WO, cbk = f / 120, ks = (f % 120) / 3, ct = f % 3;
      src = ((lane & 16) ? Wu : Wg) + (size_t)(16 * ks + 8 * hh) * 1536 + 48 * cbk + 16 * ct + (lane & 15); ld = 1536; }
    else { const int f = F - NFR_WO - NFR_GU, cbk = f / 240, ks = (f % 240) / 5, ct = f % 5; src = Wd + (size_t)(32 * ks + 8 * (lane >> 4)) * 640 + 80 * cbk + 16 * ct + (lane & 15); ld = 640; }
#pragma unroll
    for (int j = 0; j < 8; ++j) pw[r][j] = src[(size_t)j * ld];
  }
  chunk_dma(1, 1);
  chunk_dma(2, 2);
  {
    const bf16x8 kf0 = lds128(lds0 + ka0); const s16x4 kf1 = lds64(lds0 + ka1a);
#pragma unroll
    for (int t = 0; t < 2; ++t) {
      f32x16 s = MFMA32(kf0, q0[t], (f32x16){}); s = MFMA32K8(kf1, q1[t], s);
      float m = s[0];
#pragma unroll
      for (int i = 1; i < 16; ++i) m = fmaxf(m, s[i]);
      m = fmaxf(m, xhalf(m));
      if (hh) q1[t][0] = (short)(pk_bf16(-m, 0.f) & 0xffffu);
    }
  }
  f32x16 o[2] = {};
  auto tile = [&](unsigned kb0, unsigned kb1, unsigned vb0, unsigned vb1) {
    const bf16x8 kf0 = lds128(kb0); const s16x4 kf1 = lds64(kb1);
    const bf16x8 vf0 = lds128(vb0), vf1 = lds128(vb1);
#pragma unroll
    for (int t = 0; t < 2; ++t) {
      f32x16 s = MFMA32(kf0, q0[t], (f32x16){}); s = MFMA32K8(kf1, q1[t], s);
      u32x4 p0, p1;
#pragma unroll
      for (int i = 0; i < 4; ++i) {
        p0[i] = pk_bf16(__builtin_amdgcn_exp2f(s[2 * i]), __builtin_amdgcn_exp2f(s[2 * i + 1]));
        p1[i] = pk_bf16(__builtin_amdgcn_exp2f(s[8 + 2 * i]), __builtin_amdgcn_exp2f(s[8 + 2 * i + 1]));
      }
      o[t] = MFMA32(vf0, __builtin_bit_cast(bf16x8, p0), o[t]);
      o[t] = MFMA32(vf1, __builtin_bit_cast(bf16x8, p1), o[t]);
    }
  };
  for (int c = 0; c < 4; ++c) {
    const unsigned cbase = ringb + (c == 3 ? 0 : c) * A_CHUNK;
#pragma unroll 1
    for (int p = 0; p < 4; ++p) {
      const unsigned pb = cbase + p * A_PAIR;
      tile(pb + ka0, pb + ka1a, pb + va[0], pb + va[1]);
      tile(pb + ka0 + 2560, pb + ka1b, pb + va[2], pb + va[3]);
    }
    if (c == 0) {
      WAITV_BAR(0);
      chunk_dma(3, 0);
#pragma unroll
      for (int r = 0; r < 4; ++r) {
        const int F = gw + 2048 * r;
        if (F < NFR_ALL) {
          u32x4 v;
#pragma unroll
          for (int j = 0; j < 4; ++j) v[j] = (unsigned)pw[r][2 * j] | ((unsigned)pw[r][2 * j + 1] << 16);
          *(u32x4*)(ws + WS_WP + (size_t)F * 1024 + lane * 16) = v;
        }
      }
    } else if (c == 1) {
      asm volatile("" ::: "memory");
    } else if (c == 2) {
      WAITV_BAR(0);
    }
  }
  __syncthreads();
  {
    LAS f32x4* xch = (LAS f32x4*)(uintptr_t)(lds0 + w * 3072);
    const f32x16& snd = kh ? o[0] : o[1];
#pragma unroll
    for (int g = 0; g < 3; ++g) xch[g * 64 + lane] = (f32x4){snd[4 * g], snd[4 * g + 1], snd[4 * g + 2], snd[4 * g + 3]};
  }
  __syncthreads();
  float of[12];
  {
    const LAS f32x4* xch = (const LAS f32x4*)(uintptr_t)(lds0 + (w ^ 4) * 3072);
    const f32x16& own = kh ? o[1] : o[0];
#pragma unroll
    for (int g = 0; g < 3; ++g) { const f32x4 v = xch[g * 64 + lane];
#pragma unroll
      for (int j = 0; j < 4; ++j) of[4 * g + j] = own[4 * g + j] + v[j]; }
  }
  float l = __shfl(of[8], r32 + 32);
  bool ok = (l > 8.67e-19f) && (l < 1.15e18f);
#pragma unroll
  for (int i = 0; i < 8; ++i) ok = ok && (fabsf(of[i]) < 1e37f);
  if (!hh) {
#pragma unroll
    for (int i = 8; i < 12; ++i) ok = ok && (fabsf(of[i]) < 1e37f);
  }
  const int qtile = qb * 8 + wq * 2 + kh;
  if (__builtin_expect(!__all(ok), 0)) {
    const char* kv = ws + WS_KV + (size_t)head * 163840;
    const bf16x8 fq0 = kh ? q0[1] : q0[0];
    s16x4 fq1 = kh ? q1[1] : q1[0];
    if (hh) fq1 = (s16x4){0, 0, 0, 0};
    float m = -INFINITY;
    for (int t = 0; t < 64; ++t) {
      const bf16x8 kf0 = *(const bf16x8*)(kv + t * 2560 + lane * 16); const s16x4 kf1 = *(const s16x4*)(kv + t * 2560 + 1024 + r32 * 8);
      f32x16 s = MFMA32(kf0, fq0, (f32x16){}); s = MFMA32K8(kf1, fq1, s);
#pragma unroll
      for (int i = 0; i < 16; ++i) m = fmaxf(m, s[i]);
    }
    m = fmaxf(m, xhalf(m));
    f32x16 oo = {};
    const int vr = r32 < 20 ? r32 : 0;
    for (int t = 0; t < 64; ++t) {
      const bf16x8 kf0 = *(const bf16x8*)(kv + t * 2560 + lane * 16); const s16x4 kf1 = *(const s16x4*)(kv + t * 2560 + 1024 + r32 * 8);
      bf16x8 vf0 = *(const bf16x8*)(kv + t * 2560 + 1280 + (hh * 20 + vr) * 16), vf1 = *(const bf16x8*)(kv + t * 2560 + 1920 + (hh * 20 + vr) * 16);
      if (r32 >= 20) { vf0 = (bf16x8)(short)0x3f80; vf1 = vf0; }
      f32x16 s = MFMA32(kf0, fq0, (f32x16){}); s = MFMA32K8(kf1, fq1, s);
      u32x4 p0, p1;
#pragma unroll
      for (int i = 0; i < 4; ++i) {
        p0[i] = pk_bf16(__builtin_amdgcn_exp2f(s[2 * i] - m), __builtin_amdgcn_exp2f(s[2 * i + 1] - m));
        p1[i] = pk_bf16(__builtin_amdgcn_exp2f(s[8 + 2 * i] - m), __builtin_amdgcn_exp2f(s[8 + 2 * i + 1] - m));
      }
      oo = MFMA32(vf0, __builtin_bit_cast(bf16x8, p0), oo);
      oo = MFMA32(vf1, __builtin_bit_cast(bf16x8, p1), oo);
    }
#pragma unroll
    for (int i = 0; i < 12; ++i) of[i] = oo[i];
    l = __shfl(of[8], r32 + 32);
  }
  const float rl = __builtin_amdgcn_rcpf(l);
  const int row = qtile * 32 + r32;
  char* ob = ws + WS_O + (size_t)(row >> 4) * 20480 + (row & 15) * 16;
#pragma unroll
  for (int g = 0; g < 3; ++g) {
    const int cg = head * 5 + 2 * g + hh;
    if (g < 2 || !hh)
      *(u32x2*)(ob + (cg >> 3) * 1024 + ((cg & 7) >> 1) * 256 + (cg & 1) * 8) =
          (u32x2){pk_bf16(of[4 * g] * rl, of[4 * g + 1] * rl), pk_bf16(of[4 * g + 2] * rl, of[4 * g + 3] * rl)};
  }
}

__global__ __launch_bounds__(256, 1) void k3_wo(const u16* __restrict__ x, char* __restrict__ ws) {
  __shared__ __attribute__((aligned(16))) char smem[100 * 1024];
  const int tid = threadIdx.x, lane = tid & 63;
  const int w = __builtin_amdgcn_readfirstlane(tid >> 6);
  const int g8 = blockIdx.x & 7, i32 = blockIdx.x >> 3;
  const int rb = (g8 >> 1) * 8 + (i32 >> 2), cb = (g8 & 1) * 4 + (i32 & 3);
  const unsigned lds0 = lds_addr_of(smem), voff = lane * 16;
  const char* wsrc = ws + WS_WP + (size_t)cb * 102400;
  const char* asrc = ws + WS_O + (size_t)(rb * 4 + w) * 20480;
  const int row = rb * 64 + w * 16 + (lane & 15), col0 = cb * 80 + 4 * (lane >> 4);
  u32x2 xr[5];
#pragma unroll
  for (int ct = 0; ct < 5; ++ct) xr[ct] = *(const u32x2*)(x + (size_t)row * 640 + col0 + 16 * ct);
  bf16x8 a[20];
#pragma unroll
  for (int g = 0; g < 5; ++g) {
#pragma unroll
    for (int i = 0; i < 5; ++i) { const int p = g * 20 + w * 5 + i; glds16(wsrc + p * 1024, voff, lds0 + p * 1024); }
#pragma unroll
    for (int i = 0; i < 4; ++i) gld16(a[g * 4 + i], asrc + (g * 4 + i) * 1024, voff);
  }
  f32x4 acc[5] = {};
#pragma unroll
  for (int g = 0; g < 5; ++g) {
    asm volatile("s_waitcnt vmcnt(%4)\n\ts_barrier" : "+v"(a[g * 4]), "+v"(a[g * 4 + 1]), "+v"(a[g * 4 + 2]), "+v"(a[g * 4 + 3]) : "i"((4 - g) * 9) : "memory");
#pragma unroll
    for (int i = 0; i < 4; ++i)
#pragma unroll
      for (int ct = 0; ct < 5; ++ct) acc[ct] = MFMA16(lds128(lds0 + ((g * 4 + i) * 5 + ct) * 1024 + voff), a[g * 4 + i], acc[ct]);
  }
  float s1 = 0.f, s2 = 0.f;
  char* hb = ws + WS_H + (size_t)(row >> 5) * 40960 + (row & 31) * 16;
#pragma unroll
  for (int ct = 0; ct < 5; ++ct) {
    const unsigned p0 = pk_bf16(bf_lo(xr[ct][0]) + acc[ct][0], bf_hi(xr[ct][0]) + acc[ct][1]);
    const unsigned p1 = pk_bf16(bf_lo(xr[ct][1]) + acc[ct][2], bf_hi(xr[ct][1]) + acc[ct][3]);
    s1 = dot2(p0, 0x3f803f80u, s1); s1 = dot2(p1, 0x3f803f80u, s1);
    s2 = dot2(p0, p0, s2); s2 = dot2(p1, p1, s2);
    const int col = col0 + 16 * ct;
    *(u32x2*)(hb + (col >> 4) * 1024 + ((col & 15) >> 3) * 512 + (col & 7) * 2) = (u32x2){p0, p1};
  }
  s1 += __shfl_xor(s1, 16); s2 += __shfl_xor(s2, 16);
  s1 += __shfl_xor(s1, 32); s2 += __shfl_xor(s2, 32);
  if (lane < 16) *(f32x2*)(ws + WS_HST + ((size_t)row * 8 + cb) * 8) = (f32x2){s1, s2};
}

__global__ __launch_bounds__(512, 2) void k4_gu(char* __restrict__ ws) {
  __shared__ __attribute__((aligned(16))) char smem[120 * 1024 + 8 * 96 * 4];
  const int tid = threadIdx.x, lane = tid & 63;
  const int w = __builtin_amdgcn_readfirstlane(tid >> 6);
  const int g8 = blockIdx.x & 7, i32 = blockIdx.x >> 3;
  const int rb = (g8 >> 2) * 4 + (i32 >> 3), cb = (g8 & 3) * 8 + (i32 & 7);
  const unsigned lds0 = lds_addr_of(smem), voff = lane * 16;
  const int r32 = lane & 31, hh = lane >> 5;
  const char* wsrc = ws + WS_WP + (size_t)NFR_WO * 1024 + (size_t)cb * 122880;
  const char* asrc = ws + WS_H + (size_t)(rb * 8 + w) * 40960;
  const int row = rb * 256 + w * 32 + r32;
  f32x4 hs[2];
#pragma unroll
  for (int i = 0; i < 2; ++i) hs[i] = *(const f32x4*)(ws + WS_HST + (size_t)row * 64 + hh * 32 + i * 16);
  bf16x8 a[40];
#pragma unroll
  for (int g = 0; g < 5; ++g) {
#pragma unroll
    for (int i = 0; i < 3; ++i) { const int p = g * 24 + w * 3 + i; glds16(wsrc + p * 1024, voff, lds0 + p * 1024); }
#pragma unroll
    for (int i = 0; i < 8; ++i) gld16(a[g * 8 + i], asrc + (g * 8 + i) * 1024, voff);
  }
  f32x16 acc[3] = {};
  float cs[3] = {0.f, 0.f, 0.f};
#pragma unroll
  for (int g = 0; g < 5; ++g) {
    asm volatile("s_waitcnt vmcnt(%8)\n\ts_barrier"
                 : "+v"(a[g * 8]), "+v"(a[g * 8 + 1]), "+v"(a[g * 8 + 2]), "+v"(a[g * 8 + 3]), "+v"(a[g * 8 + 4]), "+v"(a[g * 8 + 5]), "+v"(a[g * 8 + 6]), "+v"(a[g * 8 + 7])
                 : "i"((4 - g) * 11) : "memory");
#pragma unroll
    for (int i = 0; i < 8; ++i)
#pragma unroll
      for (int ct = 0; ct < 3; ++ct) acc[ct] = MFMA32(lds128(lds0 + ((g * 8 + i) * 3 + ct) * 1024 + voff), a[g * 8 + i], acc[ct]);
#pragma unroll
    for (int ct = 0; ct < 3; ++ct) {
      const u32x4 u = __builtin_bit_cast(u32x4, lds128(lds0 + ((g * 8 + w) * 3 + ct) * 1024 + voff));
#pragma unroll
      for (int j = 0; j < 4; ++j) cs[ct] = dot2(u[j], 0x3f803f80u, cs[ct]);
    }
  }
  LAS float* csp = (LAS float*)(uintptr_t)(lds0 + 120 * 1024);
#pragma unroll
  for (int ct = 0; ct < 3; ++ct) { const float v = cs[ct] + xhalf(cs[ct]); if (!hh) csp[w * 96 + ct * 32 + r32] = v; }
  float s1 = hs[0][0] + hs[0][2] + hs[1][0] + hs[1][2], s2 = hs[0][1] + hs[0][3] + hs[1][1] + hs[1][3];
  s1 += xhalf(s1); s2 += xhalf(s2);
  const float mean = s1 * (1.f / 640.f);
  const float rstd = __builtin_amdgcn_rsqf(fmaxf(s2 * (1.f / 640.f) - mean * mean, 0.f) + LN_EPS);
  __syncthreads();
  if (tid < 96) { float v = 0.f;
#pragma unroll
    for (int i = 0; i < 8; ++i) v += csp[i * 96 + tid];
    ((LAS float*)(uintptr_t)lds0)[tid] = v; }
  __syncthreads();
  const LAS float* csf = (const LAS float*)(uintptr_t)lds0;
  char* gb = ws + WS_G + (size_t)(row >> 4) * 49152 + (row & 15) * 16;
  constexpr float GC0 = -2.f * 0.7978845608028654f * 1.4426950408889634f, GC1 = GC0 * 0.044715f;
#pragma unroll
  for (int ct = 0; ct < 3; ++ct)
#pragma unroll
    for (int g = 0; g < 2; ++g) {
      const f32x4 cg = *(const LAS f32x4*)&csf[ct * 32 + 8 * g + 4 * hh], cu = *(const LAS f32x4*)&csf[ct * 32 + 16 + 8 * g + 4 * hh];
      float r[4];
#pragma unroll
      for (int j = 0; j < 4; ++j) {
        const float gv = rstd * (acc[ct][4 * g + j] - mean * cg[j]), uv = rstd * (acc[ct][8 + 4 * g + j] - mean * cu[j]);
        const float e = __builtin_amdgcn_exp2f(gv * (GC0 + GC1 * gv * gv));
        r[j] = gv * __builtin_amdgcn_rcpf(1.f + e) * uv;
      }
      const int f = cb * 48 + ct * 16 + 8 * g + 4 * hh;
      *(u32x2*)(gb + (f >> 5) * 1024 + ((f & 31) >> 3) * 256 + (f & 7) * 2) = (u32x2){pk_bf16(r[0], r[1]), pk_bf16(r[2], r[3])};
    }
}

__global__ __launch_bounds__(256, 1) void k5_down(char* __restrict__ ws, u16* __restrict__ out) {
  __shared__ __attribute__((aligned(16))) char smem[7 * 20 * 1024];
  const int tid = threadIdx.x, lane = tid & 63;
  const int w = __builtin_amdgcn_readfirstlane(tid >> 6);
  const int g8 = blockIdx.x & 7, i32 = blockIdx.x >> 3;
  const int rb = (g8 >> 1) * 8 + (i32 >> 2), cb = (g8 & 1) * 4 + (i32 & 3);
  const unsigned lds0 = lds_addr_of(smem), voff = lane * 16;
  const char* wsrc = ws + WS_WP + (size_t)(NFR_WO + NFR_GU) * 1024 + (size_t)cb * 245760;
  const char* asrc = ws + WS_G + (size_t)(rb * 4 + w) * 49152;
  const int row = rb * 64 + w * 16 + (lane & 15), col0 = cb * 80 + 4 * (lane >> 4);
  u32x2 hr[5];
  {
    const char* hb = ws + WS_H + (size_t)(row >> 5) * 40960 + (row & 31) * 16;
#pragma unroll
    for (int ct = 0; ct < 5; ++ct) { const int col = col0 + 16 * ct; hr[ct] = *(const u32x2*)(hb + (col >> 4) * 1024 + ((col & 15) >> 3) * 512 + (col & 7) * 2); }
  }
  bf16x8 a[48];
  auto issue = [&](int g) {
#pragma unroll
    for (int i = 0; i < 5; ++i) { const int p = w * 5 + i; glds16(wsrc + (g * 20 + p) * 1024, voff, lds0 + ((g % 7) * 20 + p) * 1024); }
#pragma unroll
    for (int i = 0; i < 4; ++i) gld16(a[g * 4 + i], asrc + (g * 4 + i) * 1024, voff);
  };
#pragma unroll
  for (int g = 0; g < 6; ++g) issue(g);
  f32x4 acc[5] = {};
#pragma unroll
  for (int g = 0; g < 12; ++g) {
    const int younger = (g + 6 < 12 ? g + 6 : 12) - (g + 1);
    asm volatile("s_waitcnt vmcnt(%4) lgkmcnt(0)\n\ts_barrier" : "+v"(a[g * 4]), "+v"(a[g * 4 + 1]), "+v"(a[g * 4 + 2]), "+v"(a[g * 4 + 3]) : "i"(younger * 9) : "memory");
    if (g + 6 < 12) issue(g + 6);
#pragma unroll
    for (int i = 0; i < 4; ++i)
#pragma unroll
      for (int ct = 0; ct < 5; ++ct) acc[ct] = MFMA16(lds128(lds0 + ((g % 7) * 20 + i * 5 + ct) * 1024 + voff), a[g * 4 + i], acc[ct]);
  }
#pragma unroll
  for (int ct = 0; ct < 5; ++ct) {
    const unsigned p0 = pk_bf16(bf_lo(hr[ct][0]) + acc[ct][0], bf_hi(hr[ct][0]) + acc[ct][1]);
    const unsigned p1 = pk_bf16(bf_lo(hr[ct][1]) + acc[ct][2], bf_hi(hr[ct][1]) + acc[ct][3]);
    *(u32x2*)(out + (size_t)row * 640 + col0 + 16 * ct) = (u32x2){p0, p1};
  }
}

extern "C" void kernel_launch(void* const* d_in, const int* in_sizes, int n_in, void* d_out, int out_size, void* d_ws, size_t ws_size,
                              hipStream_t stream) {
  const u16* x = (const u16*)d_in[0];
  const u16* wqkv = (const u16*)d_in[1];
  const u16* wo = (const u16*)d_in[2];
  const u16* wg = (const u16*)d_in[4];
  const u16* wu = (const u16*)d_in[5];
  const u16* wd = (const u16*)d_in[6];
  char* ws = (char*)d_ws;
  k1_qkv<<<240, 512, 0, stream>>>(x, wqkv, ws);
  k2_attn<<<256, 512, 0, stream>>>(ws, wo, wg, wu, wd);
  k3_wo<<<256, 256, 0, stream>>>(x, ws);
  k4_gu<<<256, 512, 0, stream>>>(ws);
  k5_down<<<256, 256, 0, stream>>>(ws, (u16*)d_out);
}
```

```cpp
#include <hip/hip_runtime.h>
#include <hip/hip_bf16.h>
#include <stdint.h>
#include <utility>

typedef short bf16x8 __attribute__((ext_vector_type(8)));
typedef short s16x4 __attribute__((ext_vector_type(4)));
typedef float f32x16 __attribute__((ext_vector_type(16)));
typedef float f32x4 __attribute__((ext_vector_type(4)));
typedef float f32x2 __attribute__((ext_vector_type(2)));
typedef unsigned u32x4 __attribute__((ext_vector_type(4)));
typedef unsigned u32x2 __attribute__((ext_vector_type(2)));
typedef __bf16 bf2_t __attribute__((ext_vector_type(2)));
typedef unsigned short u16;
#define LAS __attribute__((address_space(3)))
#define DI __device__ __forceinline__

constexpr int S_ = 2048, D_ = 640, H_ = 32, DH = 20, F_ = 1536;
constexpr float LN_EPS = 1e-5f;
constexpr float QSCALE = 0.22360679774997896f * 1.4426950408889634f;

constexpr size_t WS_Q   = 0;
constexpr size_t WS_KV  = WS_Q + 2621440;
constexpr size_t WS_O   = WS_KV + 5242880;
constexpr size_t WS_H   = WS_O + 2621440;
constexpr size_t WS_HST = WS_H + 2621440;
constexpr size_t WS_G   = WS_HST + 131072;
constexpr size_t WS_WP  = WS_G + 6291456;
constexpr size_t WS_CSP = WS_WP + 6717440;
constexpr int NFR_WO = 800, NFR_GU = 3840, NFR_WD = 1920, NFR_ALL = NFR_WO + NFR_GU + NFR_WD;

DI unsigned pk_bf16(float lo, float hi) { f32x2 f = {lo, hi}; return __builtin_bit_cast(unsigned, __builtin_convertvector(f, bf2_t)); }
DI float bf_lo(unsigned u) { return __uint_as_float(u << 16); }
DI float bf_hi(unsigned u) { return __uint_as_float(u & 0xffff0000u); }
DI float dot2(unsigned a, unsigned b, float c) { return __builtin_amdgcn_fdot2_f32_bf16(__builtin_bit_cast(bf2_t, a), __builtin_bit_cast(bf2_t, b), c, false); }
DI float xhalf(float v) {
  return __shfl_xor(v, 32);
}
#define MFMA32(a, b, c) __builtin_amdgcn_mfma_f32_32x32x16_bf16((a), (b), (c), 0, 0, 0)
#define MFMA32K8(a, b, c) __builtin_amdgcn_mfma_f32_32x32x8bf16_1k((a), (b), (c), 0, 0, 0)
#define MFMA16(a, b, c) __builtin_amdgcn_mfma_f32_16x16x32_bf16((a), (b), (c), 0, 0, 0)

DI void glds16(const void* sbase, unsigned voff, unsigned lds_base) {
  unsigned sv;
  asm volatile("s_mov_b32 %0, m0\n\ts_mov_b32 m0, %3\n\ts_nop 0\n\tglobal_load_lds_dwordx4 %1, %2\n\ts_mov_b32 m0, %0"
               : "=&s"(sv) : "v"(voff), "s"(sbase), "s"(lds_base) : "memory");
}
DI void gld16(bf16x8& dst, const void* sbase, unsigned voff) {
  asm volatile("global_load_dwordx4 %0, %1, %2" : "=v"(dst) : "v"(voff), "s"(sbase) : "memory");
}
template <int OFF> DI void gld4(float& dst, const void* sbase, unsigned voff) {
  asm volatile("global_load_dword %0, %1, %2 offset:%3" : "=v"(dst) : "v"(voff), "s"(sbase), "i"(OFF) : "memory");
}
#define WT_ " sc1"
DI void st16(void* p, u32x4 v) { asm volatile("global_store_dwordx4 %0, %1, off" WT_ "\n\ts_nop 1" :: "v"(p), "v"(v) : "memory"); }
DI void st8(void* p, u32x2 v) { asm volatile("global_store_dwordx2 %0, %1, off" WT_ :: "v"(p), "v"(v) : "memory"); }
DI void st4(void* p, float v) { asm volatile("global_store_dword %0, %1, off" WT_ :: "v"(p), "v"(v) : "memory"); }
template <class T> DI void pin(T& v) { asm volatile("" : "+v"(v)); }
DI bf16x8 lds128(unsigned addr) { return *(const LAS bf16x8*)(uintptr_t)addr; }
DI s16x4 lds64(unsigned addr) { return *(const LAS s16x4*)(uintptr_t)addr; }
DI s16x4 ldstr(unsigned addr) { return __builtin_amdgcn_ds_read_tr16_b64_v4i16((LAS s16x4*)(uintptr_t)addr); }
DI unsigned lds_addr_of(const void* p) { return (unsigned)(uintptr_t)(const LAS char*)p; }
#define STAMP_AT(kid, idx) do {} while (0)
#define WAITV_BAR(N) asm volatile("s_waitcnt vmcnt(" #N ") lgkmcnt(0)\n\ts_barrier" ::: "memory")
#define SB() __builtin_amdgcn_sched_barrier(0)
template <int N> using IC = std::integral_constant<int, N>;
template <class F, int... Is> DI void sfor_(F&& f, std::integer_sequence<int, Is...>) { (f(IC<Is>{}), ...); }
template <int N, class F> DI void sfor(F&& f) { sfor_(f, std::make_integer_sequence<int, N>{}); }
template <int N> DI void waitv_bar() { asm volatile("s_waitcnt vmcnt(%0) lgkmcnt(0)\n\ts_barrier" :: "i"(N) : "memory"); }

#define K1_LA_ 10
#define K3_LA_ 10
#define K4_LA_ 5
#define K5_LA_ 12
constexpr int K1_STAGE = 32768, K1_NST = 4, K1_LA = K1_LA_;
template <bool ISV>
DI void k1_body(const u16* __restrict__ x, const u16* __restrict__ W, char* __restrict__ ws, char* smem, int rb, int cb) {
  const int tid = threadIdx.x, lane = tid & 63;
  const int w = __builtin_amdgcn_readfirstlane(tid >> 6), wm = w >> 2, wn = w & 3;
  const unsigned lds0 = lds_addr_of(smem);
  const int r32 = lane & 31, hh = lane >> 5;
  const unsigned a_voff0 = (unsigned)((lane >> 3) * 1280 + (((lane & 7) ^ ((lane >> 4) & 7)) << 4));
  const unsigned a_voff1 = (unsigned)((lane >> 3) * 1280 + (((lane & 7) ^ ((4 + (lane >> 4)) & 7)) << 4)) + 8 * 1280;
  const unsigned w_voff0 = (unsigned)((lane >> 3) * 3840 + (((lane & 7) ^ (((lane >> 4) & 1) << 2)) << 4));
  const unsigned w_voff1 = w_voff0 + 8 * 3840;
  const char* xs = (const char*)x + (size_t)(rb * 128 + w * 16) * 1280;
  const char* wsrc = (const char*)W + (size_t)((w & 3) * 16) * 3840 + (size_t)(cb * 128 + (w >> 2) * 64) * 2;
  const unsigned dst0 = lds0 + w * 2048;
  auto dma = [&](auto n_) {
    constexpr int n = n_, s = n >> 2, j = n & 3; constexpr unsigned so = (s % K1_NST) * K1_STAGE;
    if constexpr (j == 0) glds16(xs + s * 128, a_voff0, dst0 + so);
    if constexpr (j == 1) glds16(wsrc + (size_t)s * 64 * 3840, w_voff0, dst0 + so + 16384);
    if constexpr (j == 2) glds16(xs + s * 128, a_voff1, dst0 + so + 1024);
    if constexpr (j == 3) glds16(wsrc + (size_t)s * 64 * 3840, w_voff1, dst0 + so + 16384 + 1024);
  };
  STAMP_AT(0, 0);
  sfor<K1_LA>([&](auto n) { dma(n); });
  const int ob = (wn >> 1) ^ wm;
  const int fsel = wn & 1;
  unsigned xa[2][4], xb[2][4], wa[2][2];
  {
    const int row = wm * 64 + fsel * 32 + r32;
    const int blk = (lane >> 4) & 1, q = (lane & 15) >> 2, p = lane & 3;
    const int c = (wn & 1) * 4 + blk * 2 + (p >> 1);
    const unsigned base = lds0 + (unsigned)(16384 + (wn >> 1) * 8192 + (8 * hh + q) * 128 + ((c ^ (((q >> 1) & 1) << 2)) << 4) + (p & 1) * 8);
#pragma unroll
    for (int hs = 0; hs < 2; ++hs) {
#pragma unroll
      for (int i = 0; i < 4; ++i) {
        xa[hs][i] = lds0 + hs * 65536 + (unsigned)(row * 128 + ((((i ^ ob) * 2 + hh) ^ ((row >> 1) & 7)) << 4));
        xb[hs][i] = xa[hs][i] + (fsel ? -4096 : 4096);
      }
      wa[hs][0] = base + hs * 65536 + ob * 2048; wa[hs][1] = base + hs * 65536 + (1 ^ ob) * 2048;
    }
  }
  f32x16 acc0 = {}, acc1 = {};
  float sx = 0.f, sxx = 0.f, sw = 0.f;
  bf16x8 fx0[4], fx1[4], fw[4];
  auto rd = [&](auto i_) {
    constexpr int i = i_, s = i >> 2, p = i & 3, sl = s % K1_NST, hs = sl >> 1; constexpr unsigned so = (sl & 1) * K1_STAGE;
    fx0[p] = lds128(xa[hs][p] + so);
    const s16x4 wl = ldstr(wa[hs][p & 1] + so + (p >> 1) * 4096);
    const s16x4 wh = ldstr(wa[hs][p & 1] + so + (p >> 1) * 4096 + 512);
    fw[p] = __builtin_shufflevector(wl, wh, 0, 1, 2, 3, 4, 5, 6, 7);
    fx1[p] = lds128(xb[hs][p] + so);
  };
  waitv_bar<K1_LA - 4>();
  STAMP_AT(0, 1);
  rd(IC<0>{}); rd(IC<1>{});
  sfor<40>([&](auto i_) {
    constexpr int i = i_, p = i & 3;
    if constexpr ((i + 2) % 4 == 0 && i + 2 < 40) { constexpr int last = (i - 1 + K1_LA < 39 ? i - 1 + K1_LA : 39); waitv_bar<(last - (i + 5) > 0 ? last - (i + 5) : 0)>(); }
    if constexpr (i + K1_LA < 40) dma(IC<i + K1_LA>{});
    if constexpr (i + 2 < 40) rd(IC<i + 2>{});
    SB();
    if (ISV) { acc0 = MFMA32(fx0[p], fw[p], acc0); acc1 = MFMA32(fx1[p], fw[p], acc1); }
    else     { acc0 = MFMA32(fw[p], fx0[p], acc0); acc1 = MFMA32(fw[p], fx1[p], acc1); }
    if constexpr ((p & 1) == 0) {
      const u32x4 u = __builtin_bit_cast(u32x4, fx0[p]);
#pragma unroll
      for (int j = 0; j < 4; ++j) { sx = dot2(u[j], 0x3f803f80u, sx); sxx = dot2(u[j], u[j], sxx); }
    } else {
      const u32x4 u = __builtin_bit_cast(u32x4, fw[p]);
#pragma unroll
      for (int j = 0; j < 4; ++j) sw = dot2(u[j], 0x3f803f80u, sw);
    }
    SB();
  });
  STAMP_AT(0, 11);
  sx += xhalf(sx); sxx += xhalf(sxx); sw += xhalf(sw);
  LAS f32x2* part = (LAS f32x2*)(uintptr_t)(lds0 + 65536);
  LAS float* csp = (LAS float*)(uintptr_t)(lds0 + 65536 + 2048);
  if (hh == 0) { part[(wn >> 1) * 128 + wm * 64 + fsel * 32 + r32] = (f32x2){sx, sxx}; }
  else         { csp[wm * 128 + wn * 32 + r32] = sw; }
  __syncthreads();
  STAMP_AT(0, 12);
  auto stat = [&](int rl) { const f32x2 a = part[rl], b = part[128 + rl];
    const float mean = (a.x + b.x) * (1.f / 640.f);
    const float var = fmaxf((a.y + b.y) * (1.f / 640.f) - mean * mean, 0.f);
    return (f32x2){mean, __builtin_amdgcn_rsqf(var + LN_EPS)}; };
  if (ISV) {
    LAS f32x2* st = (LAS f32x2*)(uintptr_t)(lds0 + 65536 + 4096 + w * 512);
    st[lane] = stat(wm * 64 + lane);
    const int nl = wn * 32 + r32, n = cb * 128 + nl - 1280, head = n / 20, d0 = n - head * 20;
    const int d = (head & 1) ? (d0 < 4 ? d0 + 16 : d0 - 4) : d0;
    const float c = csp[nl] + csp[128 + nl];
#pragma unroll
    for (int t = 0; t < 2; ++t) {
      const f32x16& a = t ? acc1 : acc0;
      const int rl = (t ^ fsel) * 32;
      unsigned pk[8];
#pragma unroll
      for (int g = 0; g < 4; ++g) {
        const f32x4 s01 = *(const LAS f32x4*)&st[rl + 8 * g + 4 * hh], s23 = *(const LAS f32x4*)&st[rl + 8 * g + 4 * hh + 2];
        pk[2 * g]     = pk_bf16(s01[1] * (a[4 * g] - s01[0] * c), s01[3] * (a[4 * g + 1] - s01[2] * c));
        pk[2 * g + 1] = pk_bf16(s23[1] * (a[4 * g + 2] - s23[0] * c), s23[3] * (a[4 * g + 3] - s23[2] * c));
      }
      char* dst = ws + WS_KV + ((size_t)head * 64 + (rb * 4 + wm * 2 + (rl >> 5))) * 2560 + 1280 + (hh * 20 + d) * 16;
      st16(dst, (u32x4){pk[0], pk[1], pk[2], pk[3]});
      st16(dst + 640, (u32x4){pk[4], pk[5], pk[6], pk[7]});
    }
  } else {
    const bool isq = cb < 5;
    const float sc = isq ? QSCALE : 1.f;
    char* base = ws + (isq ? WS_Q : WS_KV);
    const int pitch = isq ? 1280 : 2560;
    f32x4 c[4]; unsigned off[4];
#pragma unroll
    for (int g = 0; g < 4; ++g) {
      const int nl = wn * 32 + 8 * g + 4 * hh;
      c[g] = *(const LAS f32x4*)&csp[nl] + *(const LAS f32x4*)&csp[128 + nl];
      const int n = cb * 128 + nl - (isq ? 0 : 640), head = n / 20, d0 = n - head * 20;
      const int d = (head & 1) ? (d0 < 4 ? d0 + 16 : d0 - 4) : d0;
      off[g] = (unsigned)(head * 64 * pitch + (d < 16 ? (((d >> 3) * 32 + r32) * 16 + (d & 4) * 2) : (1024 + r32 * 8)));
    }
#pragma unroll
    for (int t = 0; t < 2; ++t) {
      const f32x16& a = t ? acc1 : acc0;
      const int rl = wm * 64 + (t ^ fsel) * 32;
      const f32x2 ms = stat(rl + r32);
      const float rs = ms.y * sc, mr = ms.x;
      char* tb = base + (size_t)(rb * 4 + (rl >> 5)) * pitch;
#pragma unroll
      for (int g = 0; g < 4; ++g) {
        const unsigned p0 = pk_bf16(rs * (a[4 * g] - mr * c[g][0]), rs * (a[4 * g + 1] - mr * c[g][1]));
        const unsigned p1 = pk_bf16(rs * (a[4 * g + 2] - mr * c[g][2]), rs * (a[4 * g + 3] - mr * c[g][3]));
        st8(tb + off[g], (u32x2){p0, p1});
      }
    }
  }
  STAMP_AT(0, 13);
}

__global__ __launch_bounds__(512, 2) void k1_qkv(const u16* __restrict__ x, const u16* __restrict__ W, char* __restrict__ ws) {
  __shared__ __attribute__((aligned(16))) char smem[K1_STAGE * K1_NST];
  const int g = blockIdx.x & 7, i = (g & 1) * 30 + (blockIdx.x >> 3);
  const int rb = (g >> 1) * 4 + (i & 3), cb = i >> 2;
  if (cb >= 10) k1_body<true>(x, W, ws, smem, rb, cb);
  else k1_body<false>(x, W, ws, smem, rb, cb);
}

template <int OFF> DI void rdK(bf16x8& k0, s16x4& k1, unsigned a0, unsigned a1) {
  asm volatile("ds_read_b128 %0, %2 offset:%4\n\tds_read_b64 %1, %3 offset:%4" : "=&v"(k0), "=&v"(k1) : "v"(a0), "v"(a1), "i"(OFF));
}
template <int OFF> DI void rdV(bf16x8& v0, bf16x8& v1, unsigned a, unsigned long long msk) {
  asm volatile("s_mov_b64 exec, %5\n\tds_read_b128 %0, %2 offset:%3\n\tds_read_b128 %1, %2 offset:%4\n\ts_mov_b64 exec, -1"
               : "+v"(v0), "+v"(v1) : "v"(a), "i"(OFF), "i"(OFF + 640), "s"(msk));
}
template <int OFF> DI void gldu16(unsigned& dst, const void* sbase, unsigned voff) {
  asm volatile("global_load_ushort %0, %1, %2 offset:%3" : "=v"(dst) : "v"(voff), "s"(sbase), "i"(OFF) : "memory");
}
#define EX2(v) __builtin_amdgcn_exp2f(v)
__global__ __launch_bounds__(512, 2) void k2_attn(char* __restrict__ ws, const u16* __restrict__ Wo, const u16* __restrict__ Wg,
                                                  const u16* __restrict__ Wu, const u16* __restrict__ Wd) {
  __shared__ __attribute__((aligned(16))) char smem[163840];
  const int tid = threadIdx.x, lane = tid & 63;
  const int w = __builtin_amdgcn_readfirstlane(tid >> 6), kh = w >> 2, wq = w & 3;
  const int head = (blockIdx.x & 7) * 4 + ((blockIdx.x >> 3) & 3), qb = blockIdx.x >> 5;
  const unsigned lds0 = lds_addr_of(smem);
  const int r32 = lane & 31, hh = lane >> 5;
  const char* kvg = ws + WS_KV + (size_t)head * 163840 + (size_t)kh * 81920 + wq * 1024;
  const unsigned voff = lane * 16;
  const unsigned kvl = lds0 + kh * 81920 + wq * 1024;
  STAMP_AT(1, 0);
#pragma unroll
  for (int i = 0; i < 4; ++i) glds16(kvg + i * 4096, voff, kvl + i * 4096);
  bf16x8 q0[2]; s16x4 q1[2];
#pragma unroll
  for (int t = 0; t < 2; ++t) {
    const char* qg = ws + WS_Q + ((size_t)head * 64 + qb * 8 + wq * 2 + t) * 1280;
    q0[t] = *(const bf16x8*)(qg + lane * 16);
    q1[t] = *(const s16x4*)(qg + 1024 + r32 * 8);
  }
  const unsigned ka0 = lds0 + kh * 81920 + lane * 16, ka1 = lds0 + kh * 81920 + 1024 + r32 * 8, va = lds0 + kh * 81920 + 1280 + (hh * 20 + (r32 < 20 ? r32 : 0)) * 16;
  if (hh) { q1[0] = (s16x4){0, 0, 0, 0}; q1[1] = (s16x4){0, 0, 0, 0}; }
  WAITV_BAR(0);
  asm volatile("" : "+v"(q0[0]), "+v"(q0[1]), "+v"(q1[0]), "+v"(q1[1]));
  STAMP_AT(1, 1);
  bf16x8 K0[2], V0[2], V1[2]; s16x4 K1[2];
  V0[0] = V0[1] = V1[0] = V1[1] = (bf16x8)(short)(r32 == 20 ? 0x3f80 : 0);
  asm volatile("" : "+v"(V0[0]), "+v"(V0[1]), "+v"(V1[0]), "+v"(V1[1]));
  rdK<0>(K0[0], K1[0], ka0, ka1);
  const unsigned long long vmsk = 0x000fffff000fffffull;
  rdV<0>(V0[0], V1[0], va, vmsk);
  f32x16 cm[2];
  {
    const bf16x8 kf0 = lds128(lds0 + lane * 16); const s16x4 kf1 = lds64(lds0 + 1024 + r32 * 8);
#pragma unroll
    for (int t = 0; t < 2; ++t) {
      f32x16 s = MFMA32(kf0, q0[t], (f32x16){}); s = MFMA32K8(kf1, q1[t], s);
      float m = s[0];
#pragma unroll
      for (int i = 1; i < 16; ++i) m = fmaxf(m, s[i]);
      m = fmaxf(m, xhalf(m));
      cm[t] = (f32x16)(-m);
      asm volatile("" : "+v"(cm[t]));
    }
  }
  asm volatile("s_waitcnt lgkmcnt(0)" : "+v"(K0[0]), "+v"(K1[0]), "+v"(V0[0]), "+v"(V1[0]));
  f32x16 o[2] = {};
  f32x16 SA, SB_;
  u32x4 PA0, PA1, PB0 = {}, PB1 = {};
  SA = MFMA32(K0[0], q0[0], cm[0]); SA = MFMA32K8(K1[0], q1[0], SA);
  STAMP_AT(1, 2);
  pin(o[0]); pin(o[1]); pin(PB0); pin(PB1);
#define GRP(MF, Sc, Pc, i, j) do { MF; SB(); const float e0 = EX2(Sc[i]), e1 = EX2(Sc[i + 1]), e2 = EX2(Sc[i + 2]), e3 = EX2(Sc[i + 3]); \
    Pc[j] = pk_bf16(e0, e1); Pc[j + 1] = pk_bf16(e2, e3); pin(Pc); SB(); } while (0)
#define SLOT(Sc, Pc0, Pc1, Sn, kb, jn, Pp0, Pp1, vb, jp) do { \
    GRP(Sn = MFMA32(K0[kb], q0[jn], cm[jn]), Sc, Pc0, 0, 0); \
    GRP(Sn = MFMA32K8(K1[kb], q1[jn], Sn), Sc, Pc0, 4, 2); \
    GRP(o[jp] = MFMA32(V0[vb], __builtin_bit_cast(bf16x8, Pp0), o[jp]), Sc, Pc1, 8, 0); \
    GRP(o[jp] = MFMA32(V1[vb], __builtin_bit_cast(bf16x8, Pp1), o[jp]), Sc, Pc1, 12, 2); } while (0)
#define LWAIT(kb, vb) asm volatile("s_waitcnt lgkmcnt(0)" : "+v"(K0[kb]), "+v"(K1[kb]), "+v"(V0[vb]), "+v"(V1[vb]))
  unsigned pw[4][8];
  const int gw = blockIdx.x * 8 + w;
  const char* psrc[4];
#pragma unroll
  for (int r = 0; r < 2; ++r) {
    const int f = min(gw + 2048 * r, NFR_GU - 1), cbk = f / 120, rem = f - cbk * 120, ks = rem / 6, ct = rem - ks * 6;
    psrc[r] = (const char*)((ct < 3 ? Wg : Wu) + (size_t)(32 * ks) * 1536 + 48 * cbk + 16 * (ct < 3 ? ct : ct - 3));
  }
  { const int f = min(gw, NFR_WD - 1), cbk = f / 240, rem = f - cbk * 240, ks = rem / 5, ct = rem - ks * 5;
    psrc[2] = (const char*)(Wd + (size_t)(32 * ks) * 640 + 80 * cbk + 16 * ct); }
  { const int f = min(gw, NFR_WO - 1), cbk = f / 100, rem = f - cbk * 100, ks = rem / 5, ct = rem - ks * 5;
    psrc[3] = (const char*)(Wo + (size_t)(32 * ks) * 640 + 80 * cbk + 16 * ct); }
  const unsigned pvo1 = (lane >> 4) * 8 * 3072 + (lane & 15) * 2, pvo2 = (lane >> 4) * 8 * 1280 + (lane & 15) * 2;
  auto hook = [&](auto it_, auto i_) {
    constexpr int it = it_, i = i_;
    if constexpr (it < 2) glds16(kvg + (size_t)(it * 8 + 4 + i) * 4096, voff, kvl + (it * 8 + 4 + i) * 4096);
    else if constexpr (it < 4) gldu16<(i & 1) * 3072>(pw[it - 2][i], psrc[it - 2] + (i & ~1) * 3072, pvo1);
    else if constexpr (it < 6) gldu16<(i & 3) * 1280>(pw[it - 2][i], psrc[it - 2] + (i & ~3) * 1280, pvo2);
    else if constexpr ((i & 3) == 1) {
      constexpr int r = (it - 6) * 2 + (i >> 2);
      char* dst = ws + WS_WP + (size_t)gw * 1024 + lane * 16;
      asm volatile("s_waitcnt vmcnt(%0)" :: "i"(24 - 8 * r) : "memory");
      sfor<8>([&](auto j) { pin(pw[r][j]); });
      const u32x4 v = {pw[r][0] | (pw[r][1] << 16), pw[r][2] | (pw[r][3] << 16), pw[r][4] | (pw[r][5] << 16), pw[r][6] | (pw[r][7] << 16)};
      if constexpr (r < 2) {
        if (gw + 2048 * r < NFR_GU) {
          st16(dst + (size_t)(NFR_WO + 2048 * r) * 1024, v);
          float c = 0.f;
#pragma unroll
          for (int j = 0; j < 4; ++j) c = dot2(v[j], 0x3f803f80u, c);
          c += __shfl_xor(c, 16); c += __shfl_xor(c, 32);
          if (lane < 16) st4(ws + WS_CSP + (size_t)(gw + 2048 * r) * 64 + lane * 4, c);
        }
      }
      if constexpr (r == 2) { if (gw < NFR_WD) st16(dst + (size_t)(NFR_WO + NFR_GU) * 1024, v); }
      if constexpr (r == 3) { if (gw < NFR_WO) st16(dst, v); }
    }
    SB();
  };
  const unsigned ka0b = ka0 + 40960, ka1b = ka1 + 40960, vab = va + 40960;
  sfor<8>([&](auto it_) {
    constexpr int it = it_;
    if constexpr (it == 1) WAITV_BAR(6);
    if constexpr (it == 2) { WAITV_BAR(8); STAMP_AT(1, 3); }
    if constexpr (it == 4) asm volatile("s_waitcnt vmcnt(16)\n\ts_barrier" ::: "memory");
    if (((it ^ kh) & 1) == 0) __builtin_amdgcn_s_setprio(0); else __builtin_amdgcn_s_setprio(1);
    constexpr int o0 = (it & 3) * 10240;
    constexpr int on = it == 3 || it == 7 ? 0 : o0 + 10240;
    const unsigned a0 = it < 4 ? ka0 : ka0b, a1 = it < 4 ? ka1 : ka1b, av = it < 4 ? va : vab;
    const unsigned n0 = it < 3 ? ka0 : ka0b, n1 = it < 3 ? ka1 : ka1b, nv = it < 3 ? va : vab;
    rdK<o0 + 2560>(K0[1], K1[1], a0, a1); hook(it_, IC<0>{});            SLOT(SA, PA0, PA1, SB_, 0, 1, PB0, PB1, 1, 1);
    LWAIT(1, 0); rdV<o0 + 2560>(V0[1], V1[1], av, vmsk); hook(it_, IC<1>{});   SLOT(SB_, PB0, PB1, SA, 1, 0, PA0, PA1, 0, 0);
    rdK<o0 + 5120>(K0[0], K1[0], a0, a1); hook(it_, IC<2>{});            SLOT(SA, PA0, PA1, SB_, 1, 1, PB0, PB1, 0, 1);
    LWAIT(0, 1); rdV<o0 + 5120>(V0[0], V1[0], av, vmsk); hook(it_, IC<3>{});   SLOT(SB_, PB0, PB1, SA, 0, 0, PA0, PA1, 1, 0);
    rdK<o0 + 7680>(K0[1], K1[1], a0, a1); hook(it_, IC<4>{});            SLOT(SA, PA0, PA1, SB_, 0, 1, PB0, PB1, 1, 1);
    LWAIT(1, 0); rdV<o0 + 7680>(V0[1], V1[1], av, vmsk); hook(it_, IC<5>{});   SLOT(SB_, PB0, PB1, SA, 1, 0, PA0, PA1, 0, 0);
    rdK<on>(K0[0], K1[0], n0, n1); hook(it_, IC<6>{});                   SLOT(SA, PA0, PA1, SB_, 1, 1, PB0, PB1, 0, 1);
    LWAIT(0, 1); rdV<on>(V0[0], V1[0], nv, vmsk); hook(it_, IC<7>{});          SLOT(SB_, PB0, PB1, SA, 0, 0, PA0, PA1, 1, 0);
  });
  o[1] = MFMA32(V0[1], __builtin_bit_cast(bf16x8, PB0), o[1]);
  o[1] = MFMA32(V1[1], __builtin_bit_cast(bf16x8, PB1), o[1]);
  asm volatile("s_waitcnt lgkmcnt(0)" : "+v"(V0[0]), "+v"(V1[0]));
  STAMP_AT(1, 9);
  __syncthreads();
  {
    LAS f32x4* xch = (LAS f32x4*)(uintptr_t)(lds0 + w * 3072);
    const f32x16& snd = kh ? o[0] : o[1];
#pragma unroll
    for (int g = 0; g < 3; ++g) xch[g * 64 + lane] = (f32x4){snd[4 * g], snd[4 * g + 1], snd[4 * g + 2], snd[4 * g + 3]};
  }
  __syncthreads();
  STAMP_AT(1, 10);
  float of[12];
  {
    const LAS f32x4* xch = (const LAS f32x4*)(uintptr_t)(lds0 + (w ^ 4) * 3072);
    const f32x16& own = kh ? o[1] : o[0];
#pragma unroll
    for (int g = 0; g < 3; ++g) { const f32x4 v = xch[g * 64 + lane];
#pragma unroll
      for (int j = 0; j < 4; ++j) of[4 * g + j] = own[4 * g + j] + v[j]; }
  }
  float l = __shfl(of[8], r32 + 32);
  bool ok = (l > 8.67e-19f) && (l < 1.15e18f);
#pragma unroll
  for (int i = 0; i < 8; ++i) ok = ok && (fabsf(of[i]) < 1e37f);
  if (!hh) {
#pragma unroll
    for (int i = 8; i < 12; ++i) ok = ok && (fabsf(of[i]) < 1e37f);
  }
  const int qtile = qb * 8 + wq * 2 + kh;
  if (__builtin_expect(!__all(ok), 0)) {
    const char* kv = ws + WS_KV + (size_t)head * 163840;
    const bf16x8 fq0 = kh ? q0[1] : q0[0];
    const s16x4 fq1 = kh ? q1[1] : q1[0];
    float m = -INFINITY;
    for (int t = 0; t < 64; ++t) {
      const bf16x8 kf0 = *(const bf16x8*)(kv + t * 2560 + lane * 16); const s16x4 kf1 = *(const s16x4*)(kv + t * 2560 + 1024 + r32 * 8);
      f32x16 s = MFMA32(kf0, fq0, (f32x16){}); s = MFMA32K8(kf1, fq1, s);
#pragma unroll
      for (int i = 0; i < 16; ++i) m = fmaxf(m, s[i]);
    }
    m = fmaxf(m, xhalf(m));
    f32x16 oo = {};
    const int vr = r32 < 20 ? r32 : 0;
    for (int t = 0; t < 64; ++t) {
      const bf16x8 kf0 = *(const bf16x8*)(kv + t * 2560 + lane * 16); const s16x4 kf1 = *(const s16x4*)(kv + t * 2560 + 1024 + r32 * 8);
      bf16x8 vf0 = *(const bf16x8*)(kv + t * 2560 + 1280 + (hh * 20 + vr) * 16), vf1 = *(const bf16x8*)(kv + t * 2560 + 1920 + (hh * 20 + vr) * 16);
      if (r32 >= 20) { vf0 = (bf16x8)(short)0x3f80; vf1 = vf0; }
      f32x16 s = MFMA32(kf0, fq0, (f32x16){}); s = MFMA32K8(kf1, fq1, s);
      u32x4 p0, p1;
#pragma unroll
      for (int i = 0; i < 4; ++i) {
        p0[i] = pk_bf16(EX2(s[2 * i] - m), EX2(s[2 * i + 1] - m));
        p1[i] = pk_bf16(EX2(s[8 + 2 * i] - m), EX2(s[8 + 2 * i + 1] - m));
      }
      oo = MFMA32(vf0, __builtin_bit_cast(bf16x8, p0), oo);
      oo = MFMA32(vf1, __builtin_bit_cast(bf16x8, p1), oo);
    }
#pragma unroll
    for (int i = 0; i < 12; ++i) of[i] = oo[i];
    l = __shfl(of[8], r32 + 32);
  }
  const float rl = __builtin_amdgcn_rcpf(l);
  const int row = qtile * 32 + r32;
  char* ob = ws + WS_O + (size_t)(row >> 4) * 20480 + (row & 15) * 16;
#pragma unroll
  for (int g = 0; g < 3; ++g) {
    const int cg = head * 5 + ((head & 1) ? (g < 2 ? 2 * g + hh + 1 : 0) : 2 * g + hh);
    if (g < 2 || !hh)
      st8(ob + (cg >> 3) * 1024 + ((cg & 7) >> 1) * 256 + (cg & 1) * 8,
          (u32x2){pk_bf16(of[4 * g] * rl, of[4 * g + 1] * rl), pk_bf16(of[4 * g + 2] * rl, of[4 * g + 3] * rl)});
  }
  STAMP_AT(1, 11);
}

template <int NSTEP, int GS, int NB, int NF, int LA, int LL, int NSLOT, int NA, int ASTR, class MM>
DI void gemm_core(const char* wsrc, const char* asrc, unsigned lds0, int w, unsigned voff, MM&& mm) {
  constexpr int NW = GS * NF / NB;
  constexpr bool RING = NSLOT * GS < NSTEP;
  static_assert(LA >= LL + 1 && (!RING || LA <= (NSLOT - 1) * GS + LL), "lookahead");
  bf16x8 a[NSTEP][NA];
  bf16x8 bf[LL + 1][NF];
  const unsigned rd0 = lds0 + voff, rd1 = rd0 + 65536, rd2 = rd0 + 131072;
  auto issue = [&](auto n_) {
    constexpr int n = n_;
    if constexpr (n % GS == 0) sfor<NB>([&](auto j) { constexpr int g = n / GS; glds16(wsrc + (size_t)(g * GS * NF + j) * 1024, voff, lds0 + ((g % NSLOT) * GS * NF + j) * 1024); });
    sfor<NA>([&](auto j) { gld16(a[n][j], asrc + n * 1024 + j * ASTR, voff); });
  };
  auto cnt = [](int lo, int hi) constexpr { if (hi > NSTEP - 1) hi = NSTEP - 1; return hi < lo ? 0 : (hi - lo + 1) * NA + NB * (hi / GS - (lo - 1) / GS); };
  auto rd = [&](auto i_) {
    constexpr int i = i_;
    sfor<NF>([&](auto f) { constexpr int off = (((i / GS) % NSLOT) * GS * NF + (i % GS) * NF + f) * 1024;
      bf[i % (LL + 1)][f] = lds128((off < 65536 ? rd0 : off < 131072 ? rd1 : rd2) + (off & 65535)); });
  };
  wsrc += (size_t)w * NB * 1024; lds0 += w * NB * 1024;
  sfor<LA>([&](auto n) { issue(n); });
  waitv_bar<NA + cnt(1, LA - 1)>();
  sfor<LL>([&](auto i) { rd(i); });
  sfor<NSTEP>([&](auto i_) {
    constexpr int i = i_;
    if constexpr ((i + LL) % GS == 0 && i + LL < NSTEP) {
      constexpr int yv = NA + cnt(i + LL + 1, i - 1 + LA);
      if constexpr (RING) waitv_bar<yv>(); else asm volatile("s_waitcnt vmcnt(%0)\n\ts_barrier" :: "i"(yv) : "memory");
    }
    if constexpr (i + LA < NSTEP) issue(IC<i + LA>{});
    if constexpr (i + LL < NSTEP) rd(IC<i + LL>{});
    asm volatile("s_waitcnt vmcnt(%0)" :: "i"(cnt(i + 1, i + LA)));
    sfor<NA>([&](auto j) { pin(a[i][j]); });
    SB();
    mm(i_, bf[i % (LL + 1)], a[i]);
    SB();
  });
  (void)NW;
}

__global__ __launch_bounds__(256, 1) void k3_wo(const u16* __restrict__ x, char* __restrict__ ws) {
  __shared__ __attribute__((aligned(16))) char smem[100 * 1024];
  const int tid = threadIdx.x, lane = tid & 63;
  const int w = __builtin_amdgcn_readfirstlane(tid >> 6);
  const int g8 = blockIdx.x & 7, i32 = blockIdx.x >> 3;
  const int rb = (g8 >> 1) * 8 + (i32 >> 2), cb = (g8 & 1) * 4 + (i32 & 3);
  const unsigned lds0 = lds_addr_of(smem), voff = lane * 16;
  const int row = rb * 64 + w * 16 + (lane & 15), col0 = cb * 80 + 4 * (lane >> 4);
  STAMP_AT(2, 0);
  u32x2 xr[5];
#pragma unroll
  for (int ct = 0; ct < 5; ++ct) xr[ct] = *(const u32x2*)(x + (size_t)row * 640 + col0 + 16 * ct);
  f32x4 acc[5] = {};
  gemm_core<20, 4, 5, 5, K3_LA_, 2, 5, 1, 0>(ws + WS_WP + (size_t)cb * 102400, ws + WS_O + (size_t)(rb * 4 + w) * 20480, lds0, w, voff,
    [&](auto, bf16x8 (&b)[5], bf16x8 (&a)[1]) {
#pragma unroll
      for (int ct = 0; ct < 5; ++ct) acc[ct] = MFMA16(b[ct], a[0], acc[ct]); });
  STAMP_AT(2, 6);
  float s1 = 0.f, s2 = 0.f;
  char* hb = ws + WS_H + (size_t)(row >> 4) * 20480 + (row & 15) * 16;
#pragma unroll
  for (int ct = 0; ct < 5; ++ct) {
    const unsigned p0 = pk_bf16(bf_lo(xr[ct][0]) + acc[ct][0], bf_hi(xr[ct][0]) + acc[ct][1]);
    const unsigned p1 = pk_bf16(bf_lo(xr[ct][1]) + acc[ct][2], bf_hi(xr[ct][1]) + acc[ct][3]);
    s1 = dot2(p0, 0x3f803f80u, s1); s1 = dot2(p1, 0x3f803f80u, s1);
    s2 = dot2(p0, p0, s2); s2 = dot2(p1, p1, s2);
    const int col = col0 + 16 * ct;
    st8(hb + (col >> 5) * 1024 + ((col & 31) >> 3) * 256 + (col & 7) * 2, (u32x2){p0, p1});
  }
  s1 += __shfl_xor(s1, 16); s2 += __shfl_xor(s2, 16);
  s1 += __shfl_xor(s1, 32); s2 += __shfl_xor(s2, 32);
  if (lane < 16) st8(ws + WS_HST + ((size_t)row * 8 + cb) * 8, (u32x2){__float_as_uint(s1), __float_as_uint(s2)});
  STAMP_AT(2, 7);
}

__global__ __launch_bounds__(512, 2) void k4_gu(char* __restrict__ ws) {
  __shared__ __attribute__((aligned(16))) char smem[120 * 1024 + 512];
  const int tid = threadIdx.x, lane = tid & 63;
  const int w = __builtin_amdgcn_readfirstlane(tid >> 6);
  const int g8 = blockIdx.x & 7, i32 = blockIdx.x >> 3;
  const int rb = (g8 >> 2) * 4 + (i32 >> 3), cb = (g8 & 3) * 8 + (i32 & 7);
  const unsigned lds0 = lds_addr_of(smem), voff = lane * 16;
  const int r16 = lane & 15, g4 = lane >> 4;
  const int row = rb * 256 + w * 32 + r16;
  STAMP_AT(3, 0);
  f32x4 hs[2];
#pragma unroll
  for (int i = 0; i < 2; ++i) hs[i] = *(const f32x4*)(ws + WS_HST + (size_t)(row + 16 * i) * 64 + g4 * 16);
  LAS float* csf = (LAS float*)(uintptr_t)(lds0 + 120 * 1024);
  float cpart[5];
  const int ccol = w * 16 + r16;
  {
    const char* cp = ws + WS_CSP + (size_t)cb * 7680;
    const unsigned co = g4 * 1920 + (w < 6 ? ccol : 0) * 4;
    sfor<5>([&](auto i) { gld4<i * 384>(cpart[i], cp, co); });
  }
  f32x4 acc[2][6] = {};
  gemm_core<20, 4, 3, 6, K4_LA_, 1, 5, 2, 20480>(ws + WS_WP + (size_t)NFR_WO * 1024 + (size_t)cb * 122880, ws + WS_H + (size_t)(rb * 16 + w * 2) * 20480, lds0, w, voff,
    [&](auto i_, bf16x8 (&b)[6], bf16x8 (&a)[2]) {
      constexpr int i = i_;
#pragma unroll
      for (int ct = 0; ct < 6; ++ct) { acc[0][ct] = MFMA16(b[ct], a[0], acc[0][ct]); acc[1][ct] = MFMA16(b[ct], a[1], acc[1][ct]); }
      if constexpr (i == 1) {
        if (w < 6) { float v = 0.f;
          sfor<5>([&](auto j) { pin(cpart[j]); v += cpart[j]; });
          v += __shfl_xor(v, 16); v += __shfl_xor(v, 32);
          if (lane < 16) csf[ccol] = v;
          asm volatile("s_waitcnt lgkmcnt(0)" ::: "memory"); }
      } });
  STAMP_AT(3, 6);
  constexpr float GC0 = -2.f * 0.7978845608028654f * 1.4426950408889634f, GC1 = GC0 * 0.044715f;
#pragma unroll
  for (int t = 0; t < 2; ++t) {
    float s1 = hs[t][0] + hs[t][2], s2 = hs[t][1] + hs[t][3];
    s1 += __shfl_xor(s1, 16); s2 += __shfl_xor(s2, 16);
    s1 += __shfl_xor(s1, 32); s2 += __shfl_xor(s2, 32);
    const float mean = s1 * (1.f / 640.f);
    const float rstd = __builtin_amdgcn_rsqf(fmaxf(s2 * (1.f / 640.f) - mean * mean, 0.f) + LN_EPS);
    char* gb = ws + WS_G + (size_t)(rb * 16 + w * 2 + t) * 49152 + r16 * 16;
#pragma unroll
    for (int ct = 0; ct < 3; ++ct) {
      const f32x4 cg = *(const LAS f32x4*)&csf[ct * 16 + 4 * g4], cu = *(const LAS f32x4*)&csf[48 + ct * 16 + 4 * g4];
      float r[4];
#pragma unroll
      for (int j = 0; j < 4; ++j) {
        const float gv = rstd * (acc[t][ct][j] - mean * cg[j]), uv = acc[t][3 + ct][j] - mean * cu[j];
        const float e = __builtin_amdgcn_exp2f(gv * (GC0 + GC1 * gv * gv));
        r[j] = (gv * rstd) * uv * __builtin_amdgcn_rcpf(1.f + e);
      }
      const int f = cb * 48 + ct * 16 + 4 * g4;
      st8(gb + (f >> 5) * 1024 + ((f & 31) >> 3) * 256 + (f & 7) * 2, (u32x2){pk_bf16(r[0], r[1]), pk_bf16(r[2], r[3])});
    }
  }
  STAMP_AT(3, 8);
}

__global__ __launch_bounds__(256, 1) void k5_down(char* __restrict__ ws, u16* __restrict__ out) {
  __shared__ __attribute__((aligned(16))) char smem[7 * 20 * 1024];
  const int tid = threadIdx.x, lane = tid & 63;
  const int w = __builtin_amdgcn_readfirstlane(tid >> 6);
  const int g8 = blockIdx.x & 7, i32 = blockIdx.x >> 3;
  const int rb = (g8 >> 1) * 8 + (i32 >> 2), cb = (g8 & 1) * 4 + (i32 & 3);
  const unsigned lds0 = lds_addr_of(smem), voff = lane * 16;
  const int row = rb * 64 + w * 16 + (lane & 15), col0 = cb * 80 + 4 * (lane >> 4);
  STAMP_AT(4, 0);
  u32x2 hr[5];
  {
    const char* hb = ws + WS_H + (size_t)(row >> 4) * 20480 + (row & 15) * 16;
#pragma unroll
    for (int ct = 0; ct < 5; ++ct) { const int col = col0 + 16 * ct; hr[ct] = *(const u32x2*)(hb + (col >> 5) * 1024 + ((col & 31) >> 3) * 256 + (col & 7) * 2); }
  }
  f32x4 acc[5] = {};
  gemm_core<48, 4, 5, 5, K5_LA_, 2, 7, 1, 0>(ws + WS_WP + (size_t)(NFR_WO + NFR_GU) * 1024 + (size_t)cb * 245760, ws + WS_G + (size_t)(rb * 4 + w) * 49152, lds0, w, voff,
    [&](auto, bf16x8 (&b)[5], bf16x8 (&a)[1]) {
#pragma unroll
      for (int ct = 0; ct < 5; ++ct) acc[ct] = MFMA16(b[ct], a[0], acc[ct]); });
  STAMP_AT(4, 12);
#pragma unroll
  for (int ct = 0; ct < 5; ++ct) {
    const unsigned p0 = pk_bf16(bf_lo(hr[ct][0]) + acc[ct][0], bf_hi(hr[ct][0]) + acc[ct][1]);
    const unsigned p1 = pk_bf16(bf_lo(hr[ct][1]) + acc[ct][2], bf_hi(hr[ct][1]) + acc[ct][3]);
    *(u32x2*)(out + (size_t)row * 640 + col0 + 16 * ct) = (u32x2){p0, p1};
  }
  STAMP_AT(4, 13);
}

extern "C" void kernel_launch(void* const* d_in, const int* in_sizes, int n_in, void* d_out, int out_size, void* d_ws, size_t ws_size,
                              hipStream_t stream) {
  const u16* x = (const u16*)d_in[0];
  const u16* wqkv = (const u16*)d_in[1];
  const u16* wo = (const u16*)d_in[2];
  const u16* wg = (const u16*)d_in[4];
  const u16* wu = (const u16*)d_in[5];
  const u16* wd = (const u16*)d_in[6];
  char* ws = (char*)d_ws;
#define REP
  REP k1_qkv<<<240, 512, 0, stream>>>(x, wqkv, ws);
  REP k2_attn<<<256, 512, 0, stream>>>(ws, wo, wg, wu, wd);
  REP k3_wo<<<256, 256, 0, stream>>>(x, ws);
  REP k4_gu<<<256, 512, 0, stream>>>(ws);
  REP k5_down<<<256, 256, 0, stream>>>(ws, (u16*)d_out);
}
```

```cpp
#include <hip/hip_runtime.h>
#include <hip/hip_bf16.h>
#include <stdint.h>
#include <utility>
#define NO_WARM 1

typedef short bf16x8 __attribute__((ext_vector_type(8)));
typedef short s16x4 __attribute__((ext_vector_type(4)));
typedef float f32x16 __attribute__((ext_vector_type(16)));
typedef float f32x4 __attribute__((ext_vector_type(4)));
typedef float f32x2 __attribute__((ext_vector_type(2)));
typedef unsigned u32x4 __attribute__((ext_vector_type(4)));
typedef unsigned u32x2 __attribute__((ext_vector_type(2)));
typedef __bf16 bf2_t __attribute__((ext_vector_type(2)));
typedef unsigned short u16;
#define LAS __attribute__((address_space(3)))
#define DI __device__ __forceinline__

constexpr int S_ = 2048, D_ = 640, H_ = 32, DH = 20, F_ = 1536;
constexpr float LN_EPS = 1e-5f;
constexpr float QSCALE = 0.22360679774997896f * 1.4426950408889634f;

constexpr size_t WS_Q   = 0;
constexpr size_t WS_KV  = WS_Q + 2621440;
constexpr size_t WS_O   = WS_KV + 5242880;
constexpr size_t WS_H   = WS_O + 2621440;
constexpr size_t WS_HST = WS_H + 2621440;
constexpr size_t WS_G   = WS_HST + 131072;
constexpr size_t WS_WP  = WS_G + 6291456;
constexpr size_t WS_CSP = WS_WP + 6717440;
constexpr size_t WS_CTL = WS_CSP + 245760;
constexpr size_t CTL_F1 = 256;
constexpr size_t CTL_F2 = CTL_F1 + 2048 * 8;
constexpr size_t CTL_FR = CTL_F2 + 2048 * 8;
constexpr size_t CTL_F3 = CTL_FR + 2048 * 8;
constexpr size_t CTL_F4 = CTL_F3 + 1024 * 8;
constexpr size_t CTL_REP = CTL_F4 + 2048 * 8 - 256;
constexpr int NFR_WO = 800, NFR_GU = 3840, NFR_WD = 1920, NFR_ALL = NFR_WO + NFR_GU + NFR_WD;

DI unsigned pk_bf16(float lo, float hi) { f32x2 f = {lo, hi}; return __builtin_bit_cast(unsigned, __builtin_convertvector(f, bf2_t)); }
DI float bf_lo(unsigned u) { return __uint_as_float(u << 16); }
DI float bf_hi(unsigned u) { return __uint_as_float(u & 0xffff0000u); }
DI float dot2(unsigned a, unsigned b, float c) { return __builtin_amdgcn_fdot2_f32_bf16(__builtin_bit_cast(bf2_t, a), __builtin_bit_cast(bf2_t, b), c, false); }
DI float xhalf(float v) {
  return __shfl_xor(v, 32);
}
#define MFMA32(a, b, c) __builtin_amdgcn_mfma_f32_32x32x16_bf16((a), (b), (c), 0, 0, 0)
#define MFMA32K8(a, b, c) __builtin_amdgcn_mfma_f32_32x32x8bf16_1k((a), (b), (c), 0, 0, 0)
#define MFMA16(a, b, c) __builtin_amdgcn_mfma_f32_16x16x32_bf16((a), (b), (c), 0, 0, 0)

template <bool DEV> DI void glds16(const void* sbase, unsigned voff, unsigned lds_base) {
  unsigned sv;
  if constexpr (DEV)
    asm volatile("s_mov_b32 %0, m0\n\ts_mov_b32 m0, %3\n\ts_nop 0\n\tglobal_load_lds_dwordx4 %1, %2 sc1\n\ts_mov_b32 m0, %0"
                 : "=&s"(sv) : "v"(voff), "s"(sbase), "s"(lds_base) : "memory");
  else
    asm volatile("s_mov_b32 %0, m0\n\ts_mov_b32 m0, %3\n\ts_nop 0\n\tglobal_load_lds_dwordx4 %1, %2\n\ts_mov_b32 m0, %0"
                 : "=&s"(sv) : "v"(voff), "s"(sbase), "s"(lds_base) : "memory");
}
template <int OFF, class T> DI void gld16(T& dst, const void* sbase, unsigned voff) {
  asm volatile("global_load_dwordx4 %0, %1, %2 offset:%3 sc1" : "=v"(dst) : "v"(voff), "s"(sbase), "i"(OFF) : "memory");
}
template <int OFF, class T> DI void gld8(T& dst, const void* sbase, unsigned voff) {
  asm volatile("global_load_dwordx2 %0, %1, %2 offset:%3 sc1" : "=v"(dst) : "v"(voff), "s"(sbase), "i"(OFF) : "memory");
}
template <int OFF> DI void gld4(float& dst, const void* sbase, unsigned voff) {
  asm volatile("global_load_dword %0, %1, %2 offset:%3 sc1" : "=v"(dst) : "v"(voff), "s"(sbase), "i"(OFF) : "memory");
}
DI void asm_warm(u32x4& dst, const void* sbase, unsigned voff) {
  asm volatile("global_load_dwordx4 %0, %1, %2 sc1" : "+v"(dst) : "v"(voff), "s"(sbase) : "memory");
}
template <int OFF> DI void gldu16(unsigned& dst, const void* sbase, unsigned voff) {
  asm volatile("global_load_ushort %0, %1, %2 offset:%3" : "=v"(dst) : "v"(voff), "s"(sbase), "i"(OFF) : "memory");
}
DI void st16(void* p, u32x4 v) { asm volatile("global_store_dwordx4 %0, %1, off sc1\n\ts_nop 1" :: "v"(p), "v"(v) : "memory"); }
DI void st8(void* p, u32x2 v) { asm volatile("global_store_dwordx2 %0, %1, off sc1" :: "v"(p), "v"(v) : "memory"); }
DI void st4(void* p, float v) { asm volatile("global_store_dword %0, %1, off sc1" :: "v"(p), "v"(v) : "memory"); }
template <int N> DI void waitv() { asm volatile("s_waitcnt vmcnt(%0)" :: "i"(N) : "memory"); }
DI void bar() { asm volatile("s_waitcnt lgkmcnt(0)\n\ts_barrier" ::: "memory"); }
DI void setflag(char* flag, u32x2 tag, int lane) { if (lane < 8) st8(flag + lane * CTL_REP, tag); }
DI void publish(char* flag, u32x2 tag, int lane) { waitv<0>(); setflag(flag, tag, lane); }
DI void poll(const char* base, unsigned voff, u32x2 tag) {
  for (int spin = 0; spin < (1 << 17); ++spin) {
    u32x2 v;
    asm volatile("global_load_dwordx2 %0, %1, %2 sc1\n\ts_waitcnt vmcnt(0)" : "=v"(v) : "v"(voff), "s"(base) : "memory");
    if (__all(v[0] == tag[0] && v[1] == tag[1])) break;
    __builtin_amdgcn_s_sleep(1);
  }
}
DI void poll2(const char* base, unsigned voff0, unsigned voff1, u32x2 tag) {
  for (int spin = 0; spin < (1 << 17); ++spin) {
    u32x2 v, u;
    asm volatile("global_load_dwordx2 %0, %2, %4 sc1\n\tglobal_load_dwordx2 %1, %3, %4 sc1\n\ts_waitcnt vmcnt(0)" : "=&v"(v), "=&v"(u) : "v"(voff0), "v"(voff1), "s"(base) : "memory");
    if (__all(v[0] == tag[0] && v[1] == tag[1] && u[0] == tag[0] && u[1] == tag[1])) break;
    __builtin_amdgcn_s_sleep(1);
  }
}
DI int fresh_lane() { int l = __builtin_amdgcn_mbcnt_hi(~0u, __builtin_amdgcn_mbcnt_lo(~0u, 0u)); asm volatile("" : "+v"(l)); return l; }
template <class T> DI void pin(T& v) { asm volatile("" : "+v"(v)); }
DI bf16x8 lds128(unsigned addr) { return *(const LAS bf16x8*)(uintptr_t)addr; }
DI s16x4 lds64(unsigned addr) { return *(const LAS s16x4*)(uintptr_t)addr; }
DI s16x4 ldstr(unsigned addr) { return __builtin_amdgcn_ds_read_tr16_b64_v4i16((LAS s16x4*)(uintptr_t)addr); }
DI unsigned lds_addr_of(const void* p) { return (unsigned)(uintptr_t)(const LAS char*)p; }
#define STAMP_AT(kid, idx) do {} while (0)
#define WAITV_BAR(N) asm volatile("s_waitcnt vmcnt(" #N ") lgkmcnt(0)\n\ts_barrier" ::: "memory")
#define SB() __builtin_amdgcn_sched_barrier(0)
template <int N> using IC = std::integral_constant<int, N>;
template <class F, int... Is> DI void sfor_(F&& f, std::integer_sequence<int, Is...>) { (f(IC<Is>{}), ...); }
template <int N, class F> DI void sfor(F&& f) { sfor_(f, std::make_integer_sequence<int, N>{}); }
template <int N> DI void waitv_bar() { asm volatile("s_waitcnt vmcnt(%0) lgkmcnt(0)\n\ts_barrier" :: "i"(N) : "memory"); }

#define K1_LA_ 10
#define K4_LA_ 5
#define K5_LA_ 16
constexpr int NWARM = 0;
constexpr int K1_STAGE = 32768, K1_NST = 4, K1_LA = K1_LA_;
template <bool ISV>
DI void p1_body(const u16* __restrict__ x, const u16* __restrict__ W, char* __restrict__ ws, const unsigned lds0, const int w, int rb, int cb) {
  const int lane = fresh_lane();
  const int wm = w >> 2, wn = w & 3;
  const int r32 = lane & 31, hh = lane >> 5;
  const unsigned a_voff0 = (unsigned)((lane >> 3) * 1280 + (((lane & 7) ^ ((lane >> 4) & 7)) << 4));
  const unsigned a_voff1 = (unsigned)((lane >> 3) * 1280 + (((lane & 7) ^ ((4 + (lane >> 4)) & 7)) << 4)) + 8 * 1280;
  const unsigned w_voff0 = (unsigned)((lane >> 3) * 3840 + (((lane & 7) ^ (((lane >> 4) & 1) << 2)) << 4));
  const unsigned w_voff1 = w_voff0 + 8 * 3840;
  const char* xs = (const char*)x + (size_t)(rb * 128 + w * 16) * 1280;
  const char* wsrc = (const char*)W + (size_t)((w & 3) * 16) * 3840 + (size_t)(cb * 128 + (w >> 2) * 64) * 2;
  const unsigned dst0 = lds0 + w * 2048;
  auto dma = [&](auto n_) {
    constexpr int n = n_, s = n >> 2, j = n & 3; constexpr unsigned so = (s % K1_NST) * K1_STAGE;
    if constexpr (j == 0) glds16<false>(xs + s * 128, a_voff0, dst0 + so);
    if constexpr (j == 1) glds16<false>(wsrc + (size_t)s * 64 * 3840, w_voff0, dst0 + so + 16384);
    if constexpr (j == 2) glds16<false>(xs + s * 128, a_voff1, dst0 + so + 1024);
    if constexpr (j == 3) glds16<false>(wsrc + (size_t)s * 64 * 3840, w_voff1, dst0 + so + 16384 + 1024);
  };
  STAMP_AT(0, 0);
  sfor<K1_LA>([&](auto n) { dma(n); });
  const int ob = (wn >> 1) ^ wm;
  const int fsel = wn & 1;
  unsigned xa[2][4], xb[2][4], wa[2][2];
  {
    const int row = wm * 64 + fsel * 32 + r32;
    const int blk = (lane >> 4) & 1, q = (lane & 15) >> 2, p = lane & 3;
    const int c = (wn & 1) * 4 + blk * 2 + (p >> 1);
    const unsigned base = lds0 + (unsigned)(16384 + (wn >> 1) * 8192 + (8 * hh + q) * 128 + ((c ^ (((q >> 1) & 1) << 2)) << 4) + (p & 1) * 8);
#pragma unroll
    for (int hs = 0; hs < 2; ++hs) {
#pragma unroll
      for (int i = 0; i < 4; ++i) {
        xa[hs][i] = lds0 + hs * 65536 + (unsigned)(row * 128 + ((((i ^ ob) * 2 + hh) ^ ((row >> 1) & 7)) << 4));
        xb[hs][i] = xa[hs][i] + (fsel ? -4096 : 4096);
      }
      wa[hs][0] = base + hs * 65536 + ob * 2048; wa[hs][1] = base + hs * 65536 + (1 ^ ob) * 2048;
    }
  }
  f32x16 acc0 = {}, acc1 = {};
  float sx = 0.f, sxx = 0.f, sw = 0.f;
  bf16x8 fx0[4], fx1[4], fw[4];
  auto rd = [&](auto i_) {
    constexpr int i = i_, s = i >> 2, p = i & 3, sl = s % K1_NST, hs = sl >> 1; constexpr unsigned so = (sl & 1) * K1_STAGE;
    fx0[p] = lds128(xa[hs][p] + so);
    const s16x4 wl = ldstr(wa[hs][p & 1] + so + (p >> 1) * 4096);
    const s16x4 wh = ldstr(wa[hs][p & 1] + so + (p >> 1) * 4096 + 512);
    fw[p] = __builtin_shufflevector(wl, wh, 0, 1, 2, 3, 4, 5, 6, 7);
    fx1[p] = lds128(xb[hs][p] + so);
  };
  waitv_bar<K1_LA - 4>();
  STAMP_AT(0, 1);
  rd(IC<0>{}); rd(IC<1>{});
  sfor<40>([&](auto i_) {
    constexpr int i = i_, p = i & 3;
    if constexpr ((i + 2) % 4 == 0 && i + 2 < 40) { constexpr int last = (i - 1 + K1_LA < 39 ? i - 1 + K1_LA : 39); waitv_bar<(last - (i + 5) > 0 ? last - (i + 5) : 0)>(); }
    if constexpr (i + K1_LA < 40) dma(IC<i + K1_LA>{});
    if constexpr (i + 2 < 40) rd(IC<i + 2>{});
    SB();
    if (ISV) { acc0 = MFMA32(fx0[p], fw[p], acc0); acc1 = MFMA32(fx1[p], fw[p], acc1); }
    else     { acc0 = MFMA32(fw[p], fx0[p], acc0); acc1 = MFMA32(fw[p], fx1[p], acc1); }
    if constexpr ((p & 1) == 0) {
      const u32x4 u = __builtin_bit_cast(u32x4, fx0[p]);
#pragma unroll
      for (int j = 0; j < 4; ++j) { sx = dot2(u[j], 0x3f803f80u, sx); sxx = dot2(u[j], u[j], sxx); }
    } else {
      const u32x4 u = __builtin_bit_cast(u32x4, fw[p]);
#pragma unroll
      for (int j = 0; j < 4; ++j) sw = dot2(u[j], 0x3f803f80u, sw);
    }
    SB();
  });
  STAMP_AT(0, 11);
  sx += xhalf(sx); sxx += xhalf(sxx); sw += xhalf(sw);
  LAS f32x2* part = (LAS f32x2*)(uintptr_t)(lds0 + 65536);
  LAS float* csp = (LAS float*)(uintptr_t)(lds0 + 65536 + 2048);
  if (hh == 0) { part[(wn >> 1) * 128 + wm * 64 + fsel * 32 + r32] = (f32x2){sx, sxx}; }
  else         { csp[wm * 128 + wn * 32 + r32] = sw; }
  bar();
  STAMP_AT(0, 12);
  auto stat = [&](int rl) { const f32x2 a = part[rl], b = part[128 + rl];
    const float mean = (a.x + b.x) * (1.f / 640.f);
    const float var = fmaxf((a.y + b.y) * (1.f / 640.f) - mean * mean, 0.f);
    return (f32x2){mean, __builtin_amdgcn_rsqf(var + LN_EPS)}; };
  if (ISV) {
    LAS f32x2* st = (LAS f32x2*)(uintptr_t)(lds0 + 65536 + 4096 + w * 512);
    st[lane] = stat(wm * 64 + lane);
    const int nl = wn * 32 + r32, n = cb * 128 + nl - 1280, head = n / 20, d0 = n - head * 20;
    const int d = (head & 1) ? (d0 < 4 ? d0 + 16 : d0 - 4) : d0;
    const float c = csp[nl] + csp[128 + nl];
#pragma unroll
    for (int t = 0; t < 2; ++t) {
      const f32x16& a = t ? acc1 : acc0;
      const int rl = (t ^ fsel) * 32;
      unsigned pk[8];
#pragma unroll
      for (int g = 0; g < 4; ++g) {
        const f32x4 s01 = *(const LAS f32x4*)&st[rl + 8 * g + 4 * hh], s23 = *(const LAS f32x4*)&st[rl + 8 * g + 4 * hh + 2];
        pk[2 * g]     = pk_bf16(s01[1] * (a[4 * g] - s01[0] * c), s01[3] * (a[4 * g + 1] - s01[2] * c));
        pk[2 * g + 1] = pk_bf16(s23[1] * (a[4 * g + 2] - s23[0] * c), s23[3] * (a[4 * g + 3] - s23[2] * c));
      }
      char* dst = ws + WS_KV + ((size_t)head * 64 + (rb * 4 + wm * 2 + (rl >> 5))) * 2560 + 1280 + (hh * 20 + d) * 16;
      st16(dst, (u32x4){pk[0], pk[1], pk[2], pk[3]});
      st16(dst + 640, (u32x4){pk[4], pk[5], pk[6], pk[7]});
    }
  } else {
    const bool isq = cb < 5;
    const float sc = isq ? QSCALE : 1.f;
    char* base = ws + (isq ? WS_Q : WS_KV);
    const int pitch = isq ? 1280 : 2560;
    f32x4 c[4]; unsigned off[4];
#pragma unroll
    for (int g = 0; g < 4; ++g) {
      const int nl = wn * 32 + 8 * g + 4 * hh;
      c[g] = *(const LAS f32x4*)&csp[nl] + *(const LAS f32x4*)&csp[128 + nl];
      const int n = cb * 128 + nl - (isq ? 0 : 640), head = n / 20, d0 = n - head * 20;
      const int d = (head & 1) ? (d0 < 4 ? d0 + 16 : d0 - 4) : d0;
      off[g] = (unsigned)(head * 64 * pitch + (d < 16 ? (((d >> 3) * 32 + r32) * 16 + (d & 4) * 2) : (1024 + r32 * 8)));
    }
#pragma unroll
    for (int t = 0; t < 2; ++t) {
      const f32x16& a = t ? acc1 : acc0;
      const int rl = wm * 64 + (t ^ fsel) * 32;
      const f32x2 ms = stat(rl + r32);
      const float rs = ms.y * sc, mr = ms.x;
      char* tb = base + (size_t)(rb * 4 + (rl >> 5)) * pitch;
#pragma unroll
      for (int g = 0; g < 4; ++g) {
        const unsigned p0 = pk_bf16(rs * (a[4 * g] - mr * c[g][0]), rs * (a[4 * g + 1] - mr * c[g][1]));
        const unsigned p1 = pk_bf16(rs * (a[4 * g + 2] - mr * c[g][2]), rs * (a[4 * g + 3] - mr * c[g][3]));
        st8(tb + off[g], (u32x2){p0, p1});
      }
    }
  }
  STAMP_AT(0, 13);
}

DI void p1_repack(const u16* __restrict__ Wo, char* __restrict__ ws, const int w, int idx) {
  const int lane = fresh_lane();
  const unsigned pvo = (lane >> 4) * 8 * 1280 + (lane & 15) * 2;
  unsigned pw[7][8];
  sfor<7>([&](auto r) {
    const int f = idx * 50 + min(w + 8 * r, 49), cbk = f / 100, rem = f - cbk * 100, ks = rem / 5, ct = rem - ks * 5;
    const char* src = (const char*)(Wo + (size_t)(32 * ks) * 640 + 80 * cbk + 16 * ct);
    sfor<8>([&](auto i) { gldu16<(i & 3) * 1280>(pw[r][i], src + (i & ~3) * 1280, pvo); });
  });
  sfor<7>([&](auto r) {
    waitv<(6 - r) * 8>();
    sfor<8>([&](auto j) { pin(pw[r][j]); });
    const u32x4 v = {pw[r][0] | (pw[r][1] << 16), pw[r][2] | (pw[r][3] << 16), pw[r][4] | (pw[r][5] << 16), pw[r][6] | (pw[r][7] << 16)};
    pw[r][0] = v[0]; pw[r][1] = v[1]; pw[r][2] = v[2]; pw[r][3] = v[3];
  });
  sfor<7>([&](auto r) {
    if (w + 8 * r < 50) st16(ws + WS_WP + (size_t)(idx * 50 + w + 8 * r) * 1024 + lane * 16, (u32x4){pw[r][0], pw[r][1], pw[r][2], pw[r][3]});
  });
}

template <int OFF> DI void rdK(bf16x8& k0, s16x4& k1, unsigned a0, unsigned a1) {
  asm volatile("ds_read_b128 %0, %2 offset:%4\n\tds_read_b64 %1, %3 offset:%4" : "=&v"(k0), "=&v"(k1) : "v"(a0), "v"(a1), "i"(OFF));
}
template <int OFF> DI void rdV(bf16x8& v0, bf16x8& v1, unsigned a, unsigned long long msk) {
  asm volatile("s_mov_b64 exec, %5\n\tds_read_b128 %0, %2 offset:%3\n\tds_read_b128 %1, %2 offset:%4\n\ts_mov_b64 exec, -1"
               : "+v"(v0), "+v"(v1) : "v"(a), "i"(OFF), "i"(OFF + 640), "s"(msk));
}
#define EX2(v) __builtin_amdgcn_exp2f(v)
constexpr int wo_off(int p) { return (p < 60 ? p : p + 20) * 1024; }
constexpr int gu_off(int f) { return (f < 20 ? f + 60 : f < 60 ? f + 100 : f - 60) * 1024; }
constexpr int wd_off(int f) { return ((f / 20 + 4) % 8) * 20480 + (f % 20) * 1024; }
constexpr int CSF_OFF = 116 * 1024, XCH_OFF = 120 * 1024;

DI void p2_attn(char* __restrict__ ws, const u16* __restrict__ Wg, const u16* __restrict__ Wu, const u16* __restrict__ Wd,
                const unsigned lds0, const int w, const u32x2 tag, const int cb3) {
  const int lane = fresh_lane();
  const int kh = w >> 2, wq = w & 3;
  const int head = (blockIdx.x & 7) * 4 + ((blockIdx.x >> 3) & 3), qb = blockIdx.x >> 5;
  const int r32 = lane & 31, hh = lane >> 5;
  const char* kvg = ws + WS_KV + (size_t)head * 163840 + (size_t)kh * 81920 + wq * 1024;
  const unsigned voff = lane * 16;
  const unsigned kvl = lds0 + kh * 81920 + wq * 1024;
  char* ctl = ws + WS_CTL;
  const char* ctr = ctl + (blockIdx.x & 7) * CTL_REP;
  STAMP_AT(1, 0);
  {
    const int cfl = (20 * head) >> 5, cfh = (20 * head + 19) >> 5;
    const unsigned f0 = (unsigned)(((((lane & 32) ? 40 : 20) + ((lane & 16) ? cfh : cfl)) * 32 + kh * 16 + (lane & 15)) * 8);
    const unsigned f1 = (unsigned)((lane < 32 ? (((lane & 1) ? cfh : cfl) * 32 + qb * 4 + wq) : 1920 + cb3 * 16 + (lane & 15)) * 8);
    poll2(ctr + CTL_F1, f0, f1, tag);
  }
  STAMP_AT(1, 4);
#pragma unroll
  for (int i = 0; i < 4; ++i) glds16<true>(kvg + i * 4096, voff, kvl + i * 4096);
  bf16x8 q0[2]; s16x4 q1[2];
  {
    const char* qg = ws + WS_Q + ((size_t)head * 64 + qb * 8 + wq * 2) * 1280;
    gld16<0>(q0[0], qg, voff); gld16<1280>(q0[1], qg, voff); gld8<1024>(q1[0], qg, r32 * 8); gld8<2304>(q1[1], qg, r32 * 8);
  }
  const unsigned ka0 = lds0 + kh * 81920 + lane * 16, ka1 = lds0 + kh * 81920 + 1024 + r32 * 8, va = lds0 + kh * 81920 + 1280 + (hh * 20 + (r32 < 20 ? r32 : 0)) * 16;
  bf16x8 K0[2], V0[2], V1[2]; s16x4 K1[2];
  V0[0] = V0[1] = V1[0] = V1[1] = (bf16x8)(short)(r32 == 20 ? 0x3f80 : 0);
  pin(V0[0]); pin(V0[1]); pin(V1[0]); pin(V1[1]);
  WAITV_BAR(0);
  pin(q0[0]); pin(q0[1]); pin(q1[0]); pin(q1[1]);
  if (hh) { q1[0] = (s16x4){0, 0, 0, 0}; q1[1] = (s16x4){0, 0, 0, 0}; }
  STAMP_AT(1, 1);
  rdK<0>(K0[0], K1[0], ka0, ka1);
  const unsigned long long vmsk = 0x000fffff000fffffull;
  rdV<0>(V0[0], V1[0], va, vmsk);
  f32x16 cm[2];
  {
    const bf16x8 kf0 = lds128(lds0 + lane * 16); const s16x4 kf1 = lds64(lds0 + 1024 + r32 * 8);
#pragma unroll
    for (int t = 0; t < 2; ++t) {
      f32x16 s = MFMA32(kf0, q0[t], (f32x16){}); s = MFMA32K8(kf1, q1[t], s);
      float m = s[0];
#pragma unroll
      for (int i = 1; i < 16; ++i) m = fmaxf(m, s[i]);
      m = fmaxf(m, xhalf(m));
      cm[t] = (f32x16)(-m);
      pin(cm[t]);
    }
  }
  asm volatile("s_waitcnt lgkmcnt(0)" : "+v"(K0[0]), "+v"(K1[0]), "+v"(V0[0]), "+v"(V1[0]));
  f32x16 o[2] = {};
  f32x16 SA, SB_;
  u32x4 PA0, PA1, PB0 = {}, PB1 = {};
  SA = MFMA32(K0[0], q0[0], cm[0]); SA = MFMA32K8(K1[0], q1[0], SA);
  STAMP_AT(1, 2);
  pin(o[0]); pin(o[1]); pin(PB0); pin(PB1);
#define GRP(MF, Sc, Pc, i, j) do { MF; SB(); const float e0 = EX2(Sc[i]), e1 = EX2(Sc[i + 1]), e2 = EX2(Sc[i + 2]), e3 = EX2(Sc[i + 3]); \
    Pc[j] = pk_bf16(e0, e1); Pc[j + 1] = pk_bf16(e2, e3); pin(Pc); SB(); } while (0)
#define SLOT(QK, Sc, Pc0, Pc1, Sn, kb, jn, Pp0, Pp1, vb, jp) do { \
    GRP(if constexpr (QK) Sn = MFMA32(K0[kb], q0[jn], cm[jn]), Sc, Pc0, 0, 0); \
    GRP(if constexpr (QK) Sn = MFMA32K8(K1[kb], q1[jn], Sn), Sc, Pc0, 4, 2); \
    GRP(o[jp] = MFMA32(V0[vb], __builtin_bit_cast(bf16x8, Pp0), o[jp]), Sc, Pc1, 8, 0); \
    GRP(o[jp] = MFMA32(V1[vb], __builtin_bit_cast(bf16x8, Pp1), o[jp]), Sc, Pc1, 12, 2); } while (0)
#define LWAIT(kb, vb) asm volatile("s_waitcnt lgkmcnt(0)" : "+v"(K0[kb]), "+v"(K1[kb]), "+v"(V0[vb]), "+v"(V1[vb]))
  unsigned pw[3][8];
  const int gw = blockIdx.x * 8 + w, gwc = min(gw, 1919);
  const char* psrc[3];
#pragma unroll
  for (int r = 0; r < 2; ++r) {
    const int f = 2 * gwc + r, cbk = f / 120, rem = f - cbk * 120, ks = rem / 6, ct = rem - ks * 6;
    psrc[r] = (const char*)((ct < 3 ? Wg : Wu) + (size_t)(32 * ks) * 1536 + 48 * cbk + 16 * (ct < 3 ? ct : ct - 3));
  }
  { const int f = gwc, cbk = f / 240, rem = f - cbk * 240, ks = rem / 5, ct = rem - ks * 5;
    psrc[2] = (const char*)(Wd + (size_t)(32 * ks) * 640 + 80 * cbk + 16 * ct); }
  const unsigned pvo1 = (lane >> 4) * 8 * 3072 + (lane & 15) * 2, pvo2 = (lane >> 4) * 8 * 1280 + (lane & 15) * 2;
  const char* wog = ws + WS_WP + (size_t)cb3 * 102400 + w * 1024;
  const char* warm = ws + WS_WP + (size_t)NFR_WO * 1024 + (size_t)(blockIdx.x & 3) * 983040 + (size_t)min((blockIdx.x >> 3) * 30 + w, 960 - 32 + w) * 1024;
  auto rstore = [&](auto r_, auto yv_) {
    constexpr int r = r_;
    waitv<yv_>();
    sfor<8>([&](auto j) { pin(pw[r][j]); });
    const u32x4 v = {pw[r][0] | (pw[r][1] << 16), pw[r][2] | (pw[r][3] << 16), pw[r][4] | (pw[r][5] << 16), pw[r][6] | (pw[r][7] << 16)};
    if (gw < 1920) {
      if constexpr (r < 2) {
        st16(ws + WS_WP + (size_t)(NFR_WO + 2 * gw + r) * 1024 + lane * 16, v);
        float c = 0.f;
#pragma unroll
        for (int j = 0; j < 4; ++j) c = dot2(v[j], 0x3f803f80u, c);
        c += __shfl_xor(c, 16); c += __shfl_xor(c, 32);
        if (lane < 16) st4(ws + WS_CSP + (size_t)(2 * gw + r) * 64 + lane * 4, c);
      } else st16(ws + WS_WP + (size_t)(NFR_WO + NFR_GU + gw) * 1024 + lane * 16, v);
    }
  };
  auto hook = [&](auto it_, auto i_) {
    constexpr int it = it_, i = i_;
    if constexpr (it < 2) glds16<true>(kvg + (size_t)(it * 8 + 4 + i) * 4096, voff, kvl + (it * 8 + 4 + i) * 4096);
    else if constexpr (it < 4) gldu16<(i & 1) * 3072>(pw[it - 2][i], psrc[it - 2] + (i & ~1) * 3072, pvo1);
    else if constexpr (it == 4) {
      gldu16<(i & 3) * 1280>(pw[2][i], psrc[2] + (i & ~3) * 1280, pvo2);
      if constexpr (i == 0) rstore(IC<0>{}, IC<9>{});
      if constexpr (i == 7) rstore(IC<1>{}, IC<8>{});
    } else if constexpr (it == 5) {
      if constexpr (i == 7) rstore(IC<2>{}, IC<2>{});
    } else {
      constexpr int j = (it - 6) * 8 + i;
      if constexpr (j < 7) glds16<true>(wog + j * 8192, voff, lds0 + w * 1024 + j * 8192);
      else if constexpr (j == 7) glds16<true>(wog + j * 8192, voff, lds0 + w * 1024 + j * 8192 + (w >= 4 ? 20480 : 0));
      else if constexpr (j < 12) glds16<true>(wog + j * 8192, voff, lds0 + w * 1024 + j * 8192 + 20480);
      else if constexpr (j == 12) { if (w < 4) glds16<true>(wog + j * 8192, voff, lds0 + w * 1024 + j * 8192 + 20480); }
    }
    SB();
  };
  const unsigned ka0b = ka0 + 40960, ka1b = ka1 + 40960, vab = va + 40960;
  sfor<8>([&](auto it_) {
    constexpr int it = it_;
    if constexpr (it == 1) WAITV_BAR(6);
    if constexpr (it == 2) { WAITV_BAR(8); STAMP_AT(1, 3); }
    if constexpr (it == 4) asm volatile("s_waitcnt vmcnt(16)\n\ts_barrier" ::: "memory");
    if constexpr (it == 6) asm volatile("s_barrier" ::: "memory");
    if (((it ^ kh) & 1) == 0) __builtin_amdgcn_s_setprio(0); else __builtin_amdgcn_s_setprio(1);
    constexpr int o0 = (it & 3) * 10240;
    constexpr int on = it == 3 ? 0 : o0 + 10240;
    const unsigned a0 = it < 4 ? ka0 : ka0b, a1 = it < 4 ? ka1 : ka1b, av = it < 4 ? va : vab;
    const unsigned n0 = it < 3 ? ka0 : ka0b, n1 = it < 3 ? ka1 : ka1b, nv = it < 3 ? va : vab;
    rdK<o0 + 2560>(K0[1], K1[1], a0, a1); hook(it_, IC<0>{});                  SLOT(true, SA, PA0, PA1, SB_, 0, 1, PB0, PB1, 1, 1);
    LWAIT(1, 0); rdV<o0 + 2560>(V0[1], V1[1], av, vmsk); hook(it_, IC<1>{});   SLOT(true, SB_, PB0, PB1, SA, 1, 0, PA0, PA1, 0, 0);
    rdK<o0 + 5120>(K0[0], K1[0], a0, a1); hook(it_, IC<2>{});                  SLOT(true, SA, PA0, PA1, SB_, 1, 1, PB0, PB1, 0, 1);
    LWAIT(0, 1); rdV<o0 + 5120>(V0[0], V1[0], av, vmsk); hook(it_, IC<3>{});   SLOT(true, SB_, PB0, PB1, SA, 0, 0, PA0, PA1, 1, 0);
    rdK<o0 + 7680>(K0[1], K1[1], a0, a1); hook(it_, IC<4>{});                  SLOT(true, SA, PA0, PA1, SB_, 0, 1, PB0, PB1, 1, 1);
    LWAIT(1, 0); rdV<o0 + 7680>(V0[1], V1[1], av, vmsk); hook(it_, IC<5>{});   SLOT(true, SB_, PB0, PB1, SA, 1, 0, PA0, PA1, 0, 0);
    if constexpr (it < 7) rdK<on>(K0[0], K1[0], n0, n1);
    hook(it_, IC<6>{});                                                        SLOT(true, SA, PA0, PA1, SB_, 1, 1, PB0, PB1, 0, 1);
    LWAIT(0, 1);
    if constexpr (it < 7) rdV<on>(V0[0], V1[0], nv, vmsk);
    hook(it_, IC<7>{});                                                        SLOT(it < 7, SB_, PB0, PB1, SA, 0, 0, PA0, PA1, 1, 0);
  });
  __builtin_amdgcn_s_setprio(0);
  o[1] = MFMA32(V0[1], __builtin_bit_cast(bf16x8, PB0), o[1]);
  o[1] = MFMA32(V1[1], __builtin_bit_cast(bf16x8, PB1), o[1]);
  STAMP_AT(1, 9);
  waitv<0>();
  bar();
  {
    LAS f32x4* xch = (LAS f32x4*)(uintptr_t)(lds0 + XCH_OFF + w * 3072);
    const f32x16& snd = kh ? o[0] : o[1];
#pragma unroll
    for (int g = 0; g < 3; ++g) xch[g * 64 + lane] = (f32x4){snd[4 * g], snd[4 * g + 1], snd[4 * g + 2], snd[4 * g + 3]};
  }
  bar();
  STAMP_AT(1, 10);
  float of[12];
  {
    const LAS f32x4* xch = (const LAS f32x4*)(uintptr_t)(lds0 + XCH_OFF + (w ^ 4) * 3072);
    const f32x16& own = kh ? o[1] : o[0];
#pragma unroll
    for (int g = 0; g < 3; ++g) { const f32x4 v = xch[g * 64 + lane];
#pragma unroll
      for (int j = 0; j < 4; ++j) of[4 * g + j] = own[4 * g + j] + v[j]; }
  }
  float l = __shfl(of[8], r32 + 32);
  bool ok = (l > 8.67e-19f) && (l < 1.15e18f);
#pragma unroll
  for (int i = 0; i < 8; ++i) ok = ok && (fabsf(of[i]) < 1e37f);
  if (!hh) {
#pragma unroll
    for (int i = 8; i < 12; ++i) ok = ok && (fabsf(of[i]) < 1e37f);
  }
  const int qtile = qb * 8 + wq * 2 + kh;
  if (__builtin_expect(!__all(ok), 0)) {
    __builtin_amdgcn_fence(__ATOMIC_ACQUIRE, "agent");
    const char* kv = ws + WS_KV + (size_t)head * 163840;
    const bf16x8 fq0 = kh ? q0[1] : q0[0];
    const s16x4 fq1 = kh ? q1[1] : q1[0];
    float m = -INFINITY;
    for (int t = 0; t < 64; ++t) {
      const bf16x8 kf0 = *(const bf16x8*)(kv + t * 2560 + lane * 16); const s16x4 kf1 = *(const s16x4*)(kv + t * 2560 + 1024 + r32 * 8);
      f32x16 s = MFMA32(kf0, fq0, (f32x16){}); s = MFMA32K8(kf1, fq1, s);
#pragma unroll
      for (int i = 0; i < 16; ++i) m = fmaxf(m, s[i]);
    }
    m = fmaxf(m, xhalf(m));
    f32x16 oo = {};
    const int vr = r32 < 20 ? r32 : 0;
    for (int t = 0; t < 64; ++t) {
      const bf16x8 kf0 = *(const bf16x8*)(kv + t * 2560 + lane * 16); const s16x4 kf1 = *(const s16x4*)(kv + t * 2560 + 1024 + r32 * 8);
      bf16x8 vf0 = *(const bf16x8*)(kv + t * 2560 + 1280 + (hh * 20 + vr) * 16), vf1 = *(const bf16x8*)(kv + t * 2560 + 1920 + (hh * 20 + vr) * 16);
      if (r32 >= 20) { vf0 = (bf16x8)(short)0x3f80; vf1 = vf0; }
      f32x16 s = MFMA32(kf0, fq0, (f32x16){}); s = MFMA32K8(kf1, fq1, s);
      u32x4 p0, p1;
#pragma unroll
      for (int i = 0; i < 4; ++i) {
        p0[i] = pk_bf16(EX2(s[2 * i] - m), EX2(s[2 * i + 1] - m));
        p1[i] = pk_bf16(EX2(s[8 + 2 * i] - m), EX2(s[8 + 2 * i + 1] - m));
      }
      oo = MFMA32(vf0, __builtin_bit_cast(bf16x8, p0), oo);
      oo = MFMA32(vf1, __builtin_bit_cast(bf16x8, p1), oo);
    }
#pragma unroll
    for (int i = 0; i < 12; ++i) of[i] = oo[i];
    l = __shfl(of[8], r32 + 32);
  }
  const float rl = __builtin_amdgcn_rcpf(l);
  const int row = qtile * 32 + r32;
  char* ob = ws + WS_O + (size_t)(row >> 4) * 20480 + (row & 15) * 16;
#pragma unroll
  for (int g = 0; g < 3; ++g) {
    const int cg = head * 5 + ((head & 1) ? (g < 2 ? 2 * g + hh + 1 : 0) : 2 * g + hh);
    if (g < 2 || !hh)
      st8(ob + (cg >> 3) * 1024 + ((cg & 7) >> 1) * 256 + (cg & 1) * 8,
          (u32x2){pk_bf16(of[4 * g] * rl, of[4 * g + 1] * rl), pk_bf16(of[4 * g + 2] * rl, of[4 * g + 3] * rl)});
  }
  STAMP_AT(1, 11);
  publish(ctl + CTL_F2 + (qtile * 32 + head) * 8, tag, lane);
  setflag(ctl + CTL_FR + gw * 8, tag, lane);
  STAMP_AT(1, 12);
}

template <int NSTEP, int NF, int LA, int LL, int NA, int ASTR, int (*BOFF)(int), int NX, class PRE, class MM>
DI void gemm_res(const char* asrc, const char* xsrc, unsigned lds0, unsigned voff, PRE&& pre, MM&& mm) {
  u32x4 dummy = {};
  bf16x8 a[NSTEP][NA];
  bf16x8 bf[LL + 1][NF];
  const unsigned rd0 = lds0 + voff, rd1 = rd0 + 65536, rd2 = rd0 + 131072;
  auto issue = [&](auto n_) { constexpr int n = n_; sfor<NA>([&](auto j) { gld16<(n & 3) * 1024>(a[n][j], asrc + (n & ~3) * 1024 + j * ASTR, voff); }); };
  auto rd = [&](auto i_) {
    constexpr int i = i_;
    sfor<NF>([&](auto f) { constexpr int off = BOFF(i * NF + f);
      bf[i % (LL + 1)][f] = lds128((off < 65536 ? rd0 : off < 131072 ? rd1 : rd2) + (off & 65535)); });
  };
  sfor<LA>([&](auto n) { issue(n); });
  pre(IC<-1>{});
  sfor<LL>([&](auto i) { rd(i); });
  sfor<NSTEP>([&](auto i_) {
    constexpr int i = i_;
    pre(i_);
    if constexpr (i + LA < NSTEP) issue(IC<i + LA>{});
    if constexpr (i < NX) asm_warm(dummy, xsrc + i * 8192, voff);
    if constexpr (i + LL < NSTEP) rd(IC<i + LL>{});
    constexpr int xlo = i - LA > 0 ? i - LA : 0, xhi = i < NX - 1 ? i : NX - 1;
    waitv<((i + LA < NSTEP ? i + LA : NSTEP - 1) - i) * NA + (xhi >= xlo ? xhi - xlo + 1 : 0)>();
    sfor<NA>([&](auto j) { pin(a[i][j]); });
    SB();
    mm(bf[i % (LL + 1)], a[i]);
    SB();
  });
  pin(dummy);
}

__global__ __launch_bounds__(512, 2) void k_mega(const u16* __restrict__ x, const u16* __restrict__ Wqkv, const u16* __restrict__ Wo,
                                                 const u16* __restrict__ Wg, const u16* __restrict__ Wu, const u16* __restrict__ Wd,
                                                 char* __restrict__ ws, u16* __restrict__ out) {
  __shared__ __attribute__((aligned(16))) char smem[163840];
  const unsigned lds0 = lds_addr_of(smem);
  const int w = __builtin_amdgcn_readfirstlane(threadIdx.x >> 6);
  const int g8 = blockIdx.x & 7, i32 = blockIdx.x >> 3;
  char* ctl = ws + WS_CTL;
  const char* ctr = ctl + g8 * CTL_REP;
  u32x2 ep;
  gld8<0>(ep, ctl, 0u);
  int f1idx;
  if (i32 < 30) {
    const int i = (g8 & 1) * 30 + i32, rb = (g8 >> 1) * 4 + (i & 3), cb = i >> 2;
    if (cb >= 10) p1_body<true>(x, Wqkv, ws, lds0, w, rb, cb);
    else p1_body<false>(x, Wqkv, ws, lds0, w, rb, cb);
    f1idx = (cb * 4 + (w & 3)) * 32 + rb * 2 + (w >> 2);
  } else {
    p1_repack(Wo, ws, w, blockIdx.x - 240);
    f1idx = 1920 + (blockIdx.x - 240) * 8 + w;
  }
  waitv<0>();
  pin(ep);
  u32x2 tag;
  {
    const unsigned long long e = (((unsigned long long)__builtin_amdgcn_readfirstlane(ep[1]) << 32) | __builtin_amdgcn_readfirstlane(ep[0])) + 1ull;
    tag = (u32x2){(unsigned)e, (unsigned)(e >> 32)};
  }
  publish(ctl + CTL_F1 + f1idx * 8, tag, fresh_lane());
  bar();
  const int rb3 = g8 * 4 + (i32 >> 3), cb3 = i32 & 7;
  const int rb4 = (g8 >> 2) * 4 + (i32 >> 3), cb4 = (g8 & 3) * 8 + (i32 & 7);
  p2_attn(ws, Wg, Wu, Wd, lds0, w, tag, cb3);
  bar();
  const char* gug = ws + WS_WP + (size_t)NFR_WO * 1024 + (size_t)cb4 * 122880;
  const char* wdg = ws + WS_WP + (size_t)(NFR_WO + NFR_GU) * 1024 + (size_t)cb3 * 245760;
  u32x2 hp[5];
  if (w < 4) {
    const int lane = fresh_lane();
    const unsigned voff = lane * 16;
    const int row = rb3 * 64 + w * 16 + (lane & 15), col0 = cb3 * 80 + 4 * (lane >> 4);
    STAMP_AT(2, 0);
    u32x2 xr[5];
#pragma unroll
    for (int ct = 0; ct < 5; ++ct) xr[ct] = *(const u32x2*)(x + (size_t)row * 640 + col0 + 16 * ct);
    poll(ctr + CTL_F2, (unsigned)(((rb3 * 2 + (w >> 1)) * 32 + (lane & 31)) * 8), tag);
    sfor<5>([&](auto ct) { pin(xr[ct]); });
    STAMP_AT(2, 1);
    f32x4 acc[5] = {};
    gemm_res<20, 5, 20, 2, 1, 0, wo_off, 0>(ws + WS_O + (size_t)(rb3 * 4 + w) * 20480, nullptr, lds0, voff, [&](auto) {},
      [&](bf16x8 (&b)[5], bf16x8 (&a)[1]) {
#pragma unroll
        for (int ct = 0; ct < 5; ++ct) acc[ct] = MFMA16(b[ct], a[0], acc[ct]); });
    bar();
    STAMP_AT(2, 6);
    float s1 = 0.f, s2 = 0.f;
    char* hb = ws + WS_H + (size_t)(row >> 4) * 20480 + (row & 15) * 16;
#pragma unroll
    for (int ct = 0; ct < 5; ++ct) {
      const unsigned p0 = pk_bf16(bf_lo(xr[ct][0]) + acc[ct][0], bf_hi(xr[ct][0]) + acc[ct][1]);
      const unsigned p1 = pk_bf16(bf_lo(xr[ct][1]) + acc[ct][2], bf_hi(xr[ct][1]) + acc[ct][3]);
      s1 = dot2(p0, 0x3f803f80u, s1); s1 = dot2(p1, 0x3f803f80u, s1);
      s2 = dot2(p0, p0, s2); s2 = dot2(p1, p1, s2);
      const int col = col0 + 16 * ct;
      hp[ct] = (u32x2){p0, p1};
      st8(hb + (col >> 5) * 1024 + ((col & 31) >> 3) * 256 + (col & 7) * 2, hp[ct]);
    }
    s1 += __shfl_xor(s1, 16); s2 += __shfl_xor(s2, 16);
    s1 += __shfl_xor(s1, 32); s2 += __shfl_xor(s2, 32);
    if (lane < 16) st8(ws + WS_HST + ((size_t)row * 8 + cb3) * 8, (u32x2){__float_as_uint(s1), __float_as_uint(s2)});
    publish(ctl + CTL_F3 + ((rb3 * 4 + w) * 8 + cb3) * 8, tag, lane);
    STAMP_AT(2, 7);
  } else {
    const int lane = fresh_lane(), v = w - 4;
    const unsigned voff = lane * 16;
    poll(ctr + CTL_FR, (unsigned)((cb4 * 60 + min(lane, 59)) * 8), tag);
    sfor<5>([&](auto j) { glds16<true>(gug + (v * 5 + j) * 1024, voff, lds0 + gu_off(0) + (v * 5 + j) * 1024); });
    sfor<10>([&](auto j) { glds16<true>(gug + (20 + v * 10 + j) * 1024, voff, lds0 + gu_off(20) + (v * 10 + j) * 1024); });
    float cpart[10];
    const int cc = v * 24 + min(lane & 31, 23);
    sfor<10>([&](auto i) { gld4<i * 384>(cpart[i], ws + WS_CSP + (size_t)cb4 * 7680, (unsigned)((lane >> 5) * 3840 + cc * 4)); });
    poll2(ctr + CTL_FR, (unsigned)((cb3 * 240 + v * 60 + min(lane, 59)) * 8), (unsigned)((cb3 * 240 + min(lane, 35)) * 8), tag);
    float cv = 0.f;
    sfor<10>([&](auto j) { pin(cpart[j]); cv += cpart[j]; });
    cv += xhalf(cv);
    bar();
    sfor<15>([&](auto j) { glds16<true>(gug + (60 + v * 15 + j) * 1024, voff, lds0 + gu_off(60) + (v * 15 + j) * 1024); });
    sfor<9>([&](auto j) { glds16<true>(wdg + (v * 9 + j) * 1024, voff, lds0 + wd_off(0) + (v * 9 + j) * 1024); });
    if (lane < 24) ((LAS float*)(uintptr_t)(lds0 + CSF_OFF))[cc] = cv;
  }
  {
    const int lane = fresh_lane();
    const unsigned voff = lane * 16;
    const int r16 = lane & 15, g4 = lane >> 4;
    STAMP_AT(3, 0);
    poll(ctr + CTL_F3, (unsigned)(((rb4 * 16 + w * 2) * 8 + r16) * 8), tag);
    STAMP_AT(3, 1);
    f32x4 hs[2];
    gld16<0>(hs[0], ws + WS_HST + (size_t)(rb4 * 256 + w * 32) * 64, (unsigned)(r16 * 64 + g4 * 16));
    gld16<1024>(hs[1], ws + WS_HST + (size_t)(rb4 * 256 + w * 32) * 64, (unsigned)(r16 * 64 + g4 * 16));
    f32x4 acc[2][6] = {};
    gemm_res<20, 6, K4_LA_, 1, 2, 20480, gu_off, NWARM>(ws + WS_H + (size_t)(rb4 * 16 + w * 2) * 20480,
      ws + WS_WP + (size_t)(NFR_WO + NFR_GU) * 1024 + (size_t)min(i32 * 60 + w, 1920 - 64 + w) * 1024, lds0, voff,
      [&](auto i) { if constexpr (i == -1) bar(); },
      [&](bf16x8 (&b)[6], bf16x8 (&a)[2]) {
#pragma unroll
        for (int ct = 0; ct < 6; ++ct) { acc[0][ct] = MFMA16(b[ct], a[0], acc[0][ct]); acc[1][ct] = MFMA16(b[ct], a[1], acc[1][ct]); } });
    STAMP_AT(3, 6);
    pin(hs[0]); pin(hs[1]);
    const LAS float* csf = (const LAS float*)(uintptr_t)(lds0 + CSF_OFF);
    constexpr float GC0 = -2.f * 0.7978845608028654f * 1.4426950408889634f, GC1 = GC0 * 0.044715f;
#pragma unroll
    for (int t = 0; t < 2; ++t) {
      float s1 = hs[t][0] + hs[t][2], s2 = hs[t][1] + hs[t][3];
      s1 += __shfl_xor(s1, 16); s2 += __shfl_xor(s2, 16);
      s1 += __shfl_xor(s1, 32); s2 += __shfl_xor(s2, 32);
      const float mean = s1 * (1.f / 640.f);
      const float rstd = __builtin_amdgcn_rsqf(fmaxf(s2 * (1.f / 640.f) - mean * mean, 0.f) + LN_EPS);
      char* gb = ws + WS_G + (size_t)(rb4 * 16 + w * 2 + t) * 49152 + r16 * 16;
#pragma unroll
      for (int ct = 0; ct < 3; ++ct) {
        const f32x4 cg = *(const LAS f32x4*)&csf[ct * 16 + 4 * g4], cu = *(const LAS f32x4*)&csf[48 + ct * 16 + 4 * g4];
        float r[4];
#pragma unroll
        for (int j = 0; j < 4; ++j) {
          const float gv = rstd * (acc[t][ct][j] - mean * cg[j]), uv = acc[t][3 + ct][j] - mean * cu[j];
          const float e = __builtin_amdgcn_exp2f(gv * (GC0 + GC1 * gv * gv));
          r[j] = (gv * rstd) * uv * __builtin_amdgcn_rcpf(1.f + e);
        }
        const int f = cb4 * 48 + ct * 16 + 4 * g4;
        st8(gb + (f >> 5) * 1024 + ((f & 31) >> 3) * 256 + (f & 7) * 2, (u32x2){pk_bf16(r[0], r[1]), pk_bf16(r[2], r[3])});
      }
    }
    publish(ctl + CTL_F4 + ((rb4 * 8 + w) * 32 + cb4) * 8, tag, lane);
    STAMP_AT(3, 8);
    bar();
  }
  if (w < 4) {
    const int lane = fresh_lane();
    const unsigned voff = lane * 16;
    const int row = rb3 * 64 + w * 16 + (lane & 15), col0 = cb3 * 80 + 4 * (lane >> 4);
    STAMP_AT(4, 0);
    poll(ctr + CTL_F4, (unsigned)(((rb3 * 2 + (w >> 1)) * 32 + (lane & 31)) * 8), tag);
    STAMP_AT(4, 1);
    f32x4 acc[5] = {};
    gemm_res<48, 5, K5_LA_, 2, 1, 0, wd_off, 0>(ws + WS_G + (size_t)(rb3 * 4 + w) * 49152, nullptr, lds0, voff,
      [&](auto i) { if constexpr (i == -1 || i == 8 || i == 16 || i == 30 || i == 38) bar(); },
      [&](bf16x8 (&b)[5], bf16x8 (&a)[1]) {
#pragma unroll
        for (int ct = 0; ct < 5; ++ct) acc[ct] = MFMA16(b[ct], a[0], acc[ct]); });
    STAMP_AT(4, 12);
#pragma unroll
    for (int ct = 0; ct < 5; ++ct) {
      const unsigned p0 = pk_bf16(bf_lo(hp[ct][0]) + acc[ct][0], bf_hi(hp[ct][0]) + acc[ct][1]);
      const unsigned p1 = pk_bf16(bf_lo(hp[ct][1]) + acc[ct][2], bf_hi(hp[ct][1]) + acc[ct][3]);
      *(u32x2*)(out + (size_t)row * 640 + col0 + 16 * ct) = (u32x2){p0, p1};
    }
    STAMP_AT(4, 13);
  } else {
    const int lane = fresh_lane(), v = w - 4;
    const unsigned voff = lane * 16;
    glds16<true>(wdg + (36 + v) * 1024, voff, lds0 + wd_off(36) + v * 1024);
    sfor<6>([&](auto g) { sfor<5>([&](auto j) { constexpr int f = (g + 2) * 20 + j; glds16<true>(wdg + (f + v * 5) * 1024, voff, lds0 + wd_off(f) + v * 5120); }); });
    waitv<0>(); bar();
    bar();
    sfor<2>([&](auto g) { sfor<5>([&](auto j) { constexpr int f = (g + 8) * 20 + j; glds16<true>(wdg + (f + v * 5) * 1024, voff, lds0 + wd_off(f) + v * 5120); }); });
    bar();
    sfor<2>([&](auto g) { sfor<5>([&](auto j) { constexpr int f = (g + 10) * 20 + j; glds16<true>(wdg + (f + v * 5) * 1024, voff, lds0 + wd_off(f) + v * 5120); }); });
    waitv<10>(); bar();
    waitv<0>(); bar();
    if (blockIdx.x == 0 && w == 4) {
      poll2(ctr + CTL_FR, (unsigned)(lane * 64), (unsigned)(4096 + lane * 64), tag);
      poll2(ctr + CTL_FR, (unsigned)(8192 + lane * 64), (unsigned)(12288 + lane * 64), tag);
      if (lane == 0) st8(ctl, tag);
    }
  }
}

extern "C" void kernel_launch(void* const* d_in, const int* in_sizes, int n_in, void* d_out, int out_size, void* d_ws, size_t ws_size,
                              hipStream_t stream) {
  k_mega<<<256, 512, 0, stream>>>((const u16*)d_in[0], (const u16*)d_in[1], (const u16*)d_in[2], (const u16*)d_in[4], (const u16*)d_in[5],
                                  (const u16*)d_in[6], (char*)d_ws, (u16*)d_out);
}
```

```cpp
#include <hip/hip_runtime.h>
#include <hip/hip_bf16.h>
#include <stdint.h>
#include <utility>
#define NO_ROT2 1

typedef short bf16x8 __attribute__((ext_vector_type(8)));
typedef short s16x4 __attribute__((ext_vector_type(4)));
typedef float f32x16 __attribute__((ext_vector_type(16)));
typedef float f32x4 __attribute__((ext_vector_type(4)));
typedef float f32x2 __attribute__((ext_vector_type(2)));
typedef unsigned u32x4 __attribute__((ext_vector_type(4)));
typedef unsigned u32x2 __attribute__((ext_vector_type(2)));
typedef __bf16 bf2_t __attribute__((ext_vector_type(2)));
typedef unsigned short u16;
#define LAS __attribute__((address_space(3)))
#define DI __device__ __forceinline__

constexpr int S_ = 2048, D_ = 640, H_ = 32, DH = 20, F_ = 1536;
constexpr float LN_EPS = 1e-5f;
constexpr float QSCALE = 0.22360679774997896f * 1.4426950408889634f;

constexpr size_t WS_Q   = 0;
constexpr size_t WS_KV  = WS_Q + 2621440;
constexpr size_t WS_O   = WS_KV + 5242880;
constexpr size_t WS_H   = WS_O + 2621440;
constexpr size_t WS_HST = WS_H + 2621440;
constexpr size_t WS_G   = WS_HST + 131072;
constexpr size_t WS_WP  = WS_G + 6291456;
constexpr size_t WS_CSP = WS_WP + 6717440;
constexpr size_t WS_CTL = WS_CSP + 245760;
constexpr size_t CTL_F1 = 256;
constexpr size_t CTL_F2 = CTL_F1 + 2048 * 8;
constexpr size_t CTL_FR = CTL_F2 + 2048 * 8;
constexpr size_t CTL_F3 = CTL_FR + 2048 * 8;
constexpr size_t CTL_F4 = CTL_F3 + 1024 * 8;
constexpr size_t CTL_REP = CTL_F4 + 2048 * 8 - 256;
constexpr int NFR_WO = 800, NFR_GU = 3840, NFR_WD = 1920, NFR_ALL = NFR_WO + NFR_GU + NFR_WD;

DI unsigned pk_bf16(float lo, float hi) { f32x2 f = {lo, hi}; return __builtin_bit_cast(unsigned, __builtin_convertvector(f, bf2_t)); }
DI float bf_lo(unsigned u) { return __uint_as_float(u << 16); }
DI float bf_hi(unsigned u) { return __uint_as_float(u & 0xffff0000u); }
DI float dot2(unsigned a, unsigned b, float c) { return __builtin_amdgcn_fdot2_f32_bf16(__builtin_bit_cast(bf2_t, a), __builtin_bit_cast(bf2_t, b), c, false); }
DI float add32(float v) { const auto r = __builtin_amdgcn_permlane32_swap(__float_as_uint(v), __float_as_uint(v), false, false); return __uint_as_float(r[0]) + __uint_as_float(r[1]); }
DI float max32(float v) { const auto r = __builtin_amdgcn_permlane32_swap(__float_as_uint(v), __float_as_uint(v), false, false); return fmaxf(__uint_as_float(r[0]), __uint_as_float(r[1])); }
DI float add16(float v) { const auto r = __builtin_amdgcn_permlane16_swap(__float_as_uint(v), __float_as_uint(v), false, false); return __uint_as_float(r[0]) + __uint_as_float(r[1]); }
DI float red4(float v) { return add32(add16(v)); }
DI float hi32(float v) { const auto r = __builtin_amdgcn_permlane32_swap(__float_as_uint(v), __float_as_uint(v), false, false); return __uint_as_float(r[1]); }
#define MFMA32(a, b, c) __builtin_amdgcn_mfma_f32_32x32x16_bf16((a), (b), (c), 0, 0, 0)
#define MFMA32K8(a, b, c) __builtin_amdgcn_mfma_f32_32x32x8bf16_1k((a), (b), (c), 0, 0, 0)
#define MFMA16(a, b, c) __builtin_amdgcn_mfma_f32_16x16x32_bf16((a), (b), (c), 0, 0, 0)

template <bool DEV> DI void glds16(const void* sbase, unsigned voff, unsigned lds_base) {
  unsigned sv;
  if constexpr (DEV)
    asm volatile("s_mov_b32 %0, m0\n\ts_mov_b32 m0, %3\n\ts_nop 0\n\tglobal_load_lds_dwordx4 %1, %2 sc1\n\ts_mov_b32 m0, %0"
                 : "=&s"(sv) : "v"(voff), "s"(sbase), "s"(lds_base) : "memory");
  else
    asm volatile("s_mov_b32 %0, m0\n\ts_mov_b32 m0, %3\n\ts_nop 0\n\tglobal_load_lds_dwordx4 %1, %2\n\ts_mov_b32 m0, %0"
                 : "=&s"(sv) : "v"(voff), "s"(sbase), "s"(lds_base) : "memory");
}
template <int OFF, class T> DI void gld16(T& dst, const void* sbase, unsigned voff) {
  asm volatile("global_load_dwordx4 %0, %1, %2 offset:%3 sc1" : "=v"(dst) : "v"(voff), "s"(sbase), "i"(OFF) : "memory");
}
template <int OFF, class T> DI void gld8(T& dst, const void* sbase, unsigned voff) {
  asm volatile("global_load_dwordx2 %0, %1, %2 offset:%3 sc1" : "=v"(dst) : "v"(voff), "s"(sbase), "i"(OFF) : "memory");
}
template <int OFF> DI void gld4(float& dst, const void* sbase, unsigned voff) {
  asm volatile("global_load_dword %0, %1, %2 offset:%3 sc1" : "=v"(dst) : "v"(voff), "s"(sbase), "i"(OFF) : "memory");
}
DI void asm_warm(u32x4& dst, const void* sbase, unsigned voff) {
  asm volatile("global_load_dwordx4 %0, %1, %2 sc1" : "+v"(dst) : "v"(voff), "s"(sbase) : "memory");
}
template <int OFF> DI void gldu16(unsigned& dst, const void* sbase, unsigned voff) {
  asm volatile("global_load_ushort %0, %1, %2 offset:%3" : "=v"(dst) : "v"(voff), "s"(sbase), "i"(OFF) : "memory");
}
DI void st16(void* p, u32x4 v) { asm volatile("global_store_dwordx4 %0, %1, off sc1\n\ts_nop 1" :: "v"(p), "v"(v) : "memory"); }
DI void st8(void* p, u32x2 v) { asm volatile("global_store_dwordx2 %0, %1, off sc1" :: "v"(p), "v"(v) : "memory"); }
DI void st4(void* p, float v) { asm volatile("global_store_dword %0, %1, off sc1" :: "v"(p), "v"(v) : "memory"); }
DI void st8s(const void* sbase, unsigned voff, u32x2 v) { asm volatile("global_store_dwordx2 %0, %1, %2 sc1" :: "v"(voff), "v"(v), "s"(sbase) : "memory"); }
DI u32x2 nofill(unsigned a, unsigned b) { return (u32x2){min(a, 0xfffffffeu), min(b, 0xfffffffeu)}; }
DI unsigned umax4(u32x4 v) { return max(max(v[0], v[1]), max(v[2], v[3])); }
template <int N> DI void waitv() { asm volatile("s_waitcnt vmcnt(%0)" :: "i"(N) : "memory"); }
DI void bar() { asm volatile("s_waitcnt lgkmcnt(0)\n\ts_barrier" ::: "memory"); }
DI void setflag(char* flag, u32x2 tag, int lane) { if (lane < 8) st8(flag + lane * CTL_REP, tag); }
DI void publish(char* flag, u32x2 tag, int lane) { waitv<0>(); setflag(flag, tag, lane); }
DI void poll(const char* base, unsigned voff, u32x2 tag) {
#pragma unroll 1
  for (int spin = 0; spin < (1 << 17); ++spin) {
    u32x2 v;
    asm volatile("global_load_dwordx2 %0, %1, %2 sc1\n\ts_waitcnt vmcnt(0)" : "=v"(v) : "v"(voff), "s"(base) : "memory");
    if (__all(v[0] == tag[0] && v[1] == tag[1])) break;
    __builtin_amdgcn_s_sleep(1);
  }
}
DI void poll2(const char* base, unsigned voff0, unsigned voff1, u32x2 tag) {
#pragma unroll 1
  for (int spin = 0; spin < (1 << 17); ++spin) {
    u32x2 v, u;
    asm volatile("global_load_dwordx2 %0, %2, %4 sc1\n\tglobal_load_dwordx2 %1, %3, %4 sc1\n\ts_waitcnt vmcnt(0)" : "=&v"(v), "=&v"(u) : "v"(voff0), "v"(voff1), "s"(base) : "memory");
    if (__all(v[0] == tag[0] && v[1] == tag[1] && u[0] == tag[0] && u[1] == tag[1])) break;
    __builtin_amdgcn_s_sleep(1);
  }
}
DI void probe(const char* base, unsigned voff) {
#pragma unroll 1
  for (int spin = 0; spin < (1 << 17); ++spin) {
    unsigned v;
    asm volatile("global_load_dword %0, %1, %2 sc1\n\ts_waitcnt vmcnt(0)" : "=v"(v) : "v"(voff), "s"(base) : "memory");
    if (__all(v != 0xffffffffu)) break;
    __builtin_amdgcn_s_sleep(1);
  }
}
constexpr int t16off(int c0) { return (c0 >> 5) * 1024 + (c0 & 16) * 32; }
DI unsigned t16lane(int lane) { return (unsigned)((lane >> 5) * 256 + (lane & 15) * 16 + ((lane >> 4) & 1) * 8); }
DI int rotf(int f, int off, int n) { const int r = f + off; return r - (r >= n ? n : 0); }
DI void st16s(const void* sbase, unsigned voff, u32x4 v) { asm volatile("global_store_dwordx4 %0, %1, %2 sc1\n\ts_nop 1" :: "v"(voff), "v"(v), "s"(sbase) : "memory"); }
DI float row16_sum(float v) {
  v += __builtin_bit_cast(float, __builtin_amdgcn_update_dpp(0, __builtin_bit_cast(int, v), 0x128, 0xf, 0xf, false));
  v += __builtin_bit_cast(float, __builtin_amdgcn_update_dpp(0, __builtin_bit_cast(int, v), 0x124, 0xf, 0xf, false));
  v += __builtin_bit_cast(float, __builtin_amdgcn_update_dpp(0, __builtin_bit_cast(int, v), 0x122, 0xf, 0xf, false));
  v += __builtin_bit_cast(float, __builtin_amdgcn_update_dpp(0, __builtin_bit_cast(int, v), 0x121, 0xf, 0xf, false));
  return v;
}
#define ROT3(c) 0
#define ROT5(c) 0
DI int fresh_lane() { int l = __builtin_amdgcn_mbcnt_hi(~0u, __builtin_amdgcn_mbcnt_lo(~0u, 0u)); asm volatile("" : "+v"(l)); return l; }
template <class T> DI void pin(T& v) { asm volatile("" : "+v"(v)); }
DI bf16x8 lds128(unsigned addr) { return *(const LAS bf16x8*)(uintptr_t)addr; }
DI s16x4 lds64(unsigned addr) { return *(const LAS s16x4*)(uintptr_t)addr; }
DI s16x4 ldstr(unsigned addr) { return __builtin_amdgcn_ds_read_tr16_b64_v4i16((LAS s16x4*)(uintptr_t)addr); }
DI unsigned lds_addr_of(const void* p) { return (unsigned)(uintptr_t)(const LAS char*)p; }
#define STAMP_AT(kid, idx) ((void)0)
#define RETRY_COUNT(k) ((void)0)
#define WAITV_BAR(N) asm volatile("s_waitcnt vmcnt(" #N ") lgkmcnt(0)\n\ts_barrier" ::: "memory")
#define SB() __builtin_amdgcn_sched_barrier(0)
template <int N> using IC = std::integral_constant<int, N>;
template <class F, int... Is> DI void sfor_(F&& f, std::integer_sequence<int, Is...>) { (f(IC<Is>{}), ...); }
template <int N, class F> DI void sfor(F&& f) { sfor_(f, std::make_integer_sequence<int, N>{}); }
template <int N> DI void waitv_bar() { asm volatile("s_waitcnt vmcnt(%0) lgkmcnt(0)\n\ts_barrier" :: "i"(N) : "memory"); }

#define K1_LA_ 10
#define P3_GO 10
#define K4_LA_ 5
#define K5_LA_ 24
constexpr int K1_STAGE = 32768, K1_NST = 4, K1_LA = K1_LA_;
template <bool ISV>
DI void p1_body(const u16* __restrict__ x, const u16* __restrict__ W, char* __restrict__ ws, const unsigned lds0, const int w, int rb, int cb) {
  const int lane = fresh_lane();
  const int wm = w >> 2, wn = w & 3;
  const int r32 = lane & 31, hh = lane >> 5;
  const unsigned a_voff0 = (unsigned)((lane >> 3) * 1280 + (((lane & 7) ^ ((lane >> 4) & 7)) << 4));
  const unsigned a_voff1 = (unsigned)((lane >> 3) * 1280 + (((lane & 7) ^ ((4 + (lane >> 4)) & 7)) << 4)) + 8 * 1280;
  const unsigned w_voff0 = (unsigned)((lane >> 3) * 3840 + (((lane & 7) ^ (((lane >> 4) & 1) << 2)) << 4));
  const unsigned w_voff1 = w_voff0 + 8 * 3840;
  const char* xs = (const char*)x + (size_t)(rb * 128 + w * 16) * 1280;
  const char* wsrc = (const char*)W + (size_t)((w & 3) * 16) * 3840 + (size_t)(cb * 128 + (w >> 2) * 64) * 2;
  const unsigned dst0 = lds0 + w * 2048;
#define ROTR 10
  const int rot = ((cb + 3 * rb) % ROTR) * (10 / ROTR);
  auto dma = [&](auto n_) {
    constexpr int n = n_, s = n >> 2, j = n & 3; constexpr unsigned so = (s % K1_NST) * K1_STAGE;
    const int sr = s + rot - (s + rot >= 10 ? 10 : 0);
    if constexpr (j == 0) glds16<false>(xs + sr * 128, a_voff0, dst0 + so);
    if constexpr (j == 1) glds16<false>(wsrc + (size_t)sr * 64 * 3840, w_voff0, dst0 + so + 16384);
    if constexpr (j == 2) glds16<false>(xs + sr * 128, a_voff1, dst0 + so + 1024);
    if constexpr (j == 3) glds16<false>(wsrc + (size_t)sr * 64 * 3840, w_voff1, dst0 + so + 16384 + 1024);
  };
  STAMP_AT(0, 0);
  sfor<K1_LA>([&](auto n) { dma(n); });
  const int ob = (wn >> 1) ^ wm;
  const int fsel = wn & 1;
  unsigned xa[2][4], xb[2][4], wa[2][2];
  {
    const int row = wm * 64 + fsel * 32 + r32;
    const int blk = (lane >> 4) & 1, q = (lane & 15) >> 2, p = lane & 3;
    const int c = (wn & 1) * 4 + blk * 2 + (p >> 1);
    const unsigned base = lds0 + (unsigned)(16384 + (wn >> 1) * 8192 + (8 * hh + q) * 128 + ((c ^ (((q >> 1) & 1) << 2)) << 4) + (p & 1) * 8);
#pragma unroll
    for (int hs = 0; hs < 2; ++hs) {
#pragma unroll
      for (int i = 0; i < 4; ++i) {
        xa[hs][i] = lds0 + hs * 65536 + (unsigned)(row * 128 + ((((i ^ ob) * 2 + hh) ^ ((row >> 1) & 7)) << 4));
        xb[hs][i] = xa[hs][i] + (fsel ? -4096 : 4096);
      }
      wa[hs][0] = base + hs * 65536 + ob * 2048; wa[hs][1] = base + hs * 65536 + (1 ^ ob) * 2048;
    }
  }
  f32x16 acc0 = {}, acc1 = {};
  float sx = 0.f, sxx = 0.f, sw = 0.f;
  bf16x8 fx0[4], fx1[4], fw[4];
  auto rd = [&](auto i_) {
    constexpr int i = i_, s = i >> 2, p = i & 3, sl = s % K1_NST, hs = sl >> 1; constexpr unsigned so = (sl & 1) * K1_STAGE;
    fx0[p] = lds128(xa[hs][p] + so);
    const s16x4 wl = ldstr(wa[hs][p & 1] + so + (p >> 1) * 4096);
    const s16x4 wh = ldstr(wa[hs][p & 1] + so + (p >> 1) * 4096 + 512);
    fw[p] = __builtin_shufflevector(wl, wh, 0, 1, 2, 3, 4, 5, 6, 7);
    fx1[p] = lds128(xb[hs][p] + so);
  };
  const bool isq = cb < 5;
  const int pitch = isq ? 1280 : 2560;
  unsigned off[4];
  if (ISV) {
    const int n = cb * 128 + wn * 32 + r32 - 1280, head = n / 20, d0 = n - head * 20;
    const int d = (head & 1) ? (d0 < 4 ? d0 + 16 : d0 - 4) : d0;
    off[0] = (unsigned)(head * 163840 + 1280 + (hh * 20 + d) * 16); pin(off[0]);
  } else {
#pragma unroll
    for (int g = 0; g < 4; ++g) {
      const int n = cb * 128 + wn * 32 + 8 * g + 4 * hh - (isq ? 0 : 640), head = n / 20, d0 = n - head * 20;
      const int d = (head & 1) ? (d0 < 4 ? d0 + 16 : d0 - 4) : d0;
      off[g] = (unsigned)(head * 64 * pitch + (d < 16 ? (((d >> 3) * 32 + r32) * 16 + (d & 4) * 2) : (1024 + r32 * 8))); pin(off[g]);
    }
  }
  waitv_bar<K1_LA - 4>();
  STAMP_AT(0, 1);
  rd(IC<0>{}); rd(IC<1>{});
  sfor<40>([&](auto i_) {
    constexpr int i = i_, p = i & 3;
    if constexpr ((i + 2) % 4 == 0 && i + 2 < 40) { constexpr int last = (i - 1 + K1_LA < 39 ? i - 1 + K1_LA : 39); waitv_bar<(last - (i + 5) > 0 ? last - (i + 5) : 0)>(); }
    if constexpr (i + K1_LA < 40) dma(IC<i + K1_LA>{});
    if constexpr (i + 2 < 40) rd(IC<i + 2>{});
    SB();
    if (ISV) { acc0 = MFMA32(fx0[p], fw[p], acc0); acc1 = MFMA32(fx1[p], fw[p], acc1); }
    else     { acc0 = MFMA32(fw[p], fx0[p], acc0); acc1 = MFMA32(fw[p], fx1[p], acc1); }
    if constexpr ((p & 1) == 0) {
      const u32x4 u = __builtin_bit_cast(u32x4, fx0[p]);
#pragma unroll
      for (int j = 0; j < 4; ++j) { sx = dot2(u[j], 0x3f803f80u, sx); sxx = dot2(u[j], u[j], sxx); }
    } else {
      const u32x4 u = __builtin_bit_cast(u32x4, fw[p]);
#pragma unroll
      for (int j = 0; j < 4; ++j) sw = dot2(u[j], 0x3f803f80u, sw);
    }
    SB();
  });
  STAMP_AT(0, 11);
  sx = add32(sx); sxx = add32(sxx); sw = add32(sw);
  LAS f32x2* part = (LAS f32x2*)(uintptr_t)(lds0 + 65536);
  LAS float* csp = (LAS float*)(uintptr_t)(lds0 + 65536 + 2048);
  if (hh == 0) { part[(wn >> 1) * 128 + wm * 64 + fsel * 32 + r32] = (f32x2){sx, sxx}; }
  else         { csp[wm * 128 + wn * 32 + r32] = sw; }
  bar();
  STAMP_AT(0, 12);
  auto stat = [&](int rl) { const f32x2 a = part[rl], b = part[128 + rl];
    const float mean = (a.x + b.x) * (1.f / 640.f);
    const float var = fmaxf((a.y + b.y) * (1.f / 640.f) - mean * mean, 0.f);
    return (f32x2){mean, __builtin_amdgcn_rsqf(var + LN_EPS)}; };
  if (ISV) {
    LAS f32x2* st = (LAS f32x2*)(uintptr_t)(lds0 + 65536 + 4096 + w * 512);
    st[lane] = stat(wm * 64 + lane);
    const int nl = wn * 32 + r32;
    const float c = csp[nl] + csp[128 + nl];
#pragma unroll
    for (int t = 0; t < 2; ++t) {
      const f32x16& a = t ? acc1 : acc0;
      const int rl = (t ^ fsel) * 32;
      unsigned pk[8];
#pragma unroll
      for (int g = 0; g < 4; ++g) {
        const f32x4 s01 = *(const LAS f32x4*)&st[rl + 8 * g + 4 * hh], s23 = *(const LAS f32x4*)&st[rl + 8 * g + 4 * hh + 2];
        pk[2 * g]     = pk_bf16(s01[1] * (a[4 * g] - s01[0] * c), s01[3] * (a[4 * g + 1] - s01[2] * c));
        pk[2 * g + 1] = pk_bf16(s23[1] * (a[4 * g + 2] - s23[0] * c), s23[3] * (a[4 * g + 3] - s23[2] * c));
      }
      const char* dst = ws + WS_KV + (size_t)(rb * 4 + wm * 2 + (rl >> 5)) * 2560;
      st16s(dst, off[0], (u32x4){pk[0], pk[1], pk[2], pk[3]});
      st16s(dst + 640, off[0], (u32x4){pk[4], pk[5], pk[6], pk[7]});
    }
  } else {
    const float sc = isq ? QSCALE : 1.f;
    const char* base = ws + (isq ? WS_Q : WS_KV);
    f32x4 c[4];
#pragma unroll
    for (int g = 0; g < 4; ++g) { const int nl = wn * 32 + 8 * g + 4 * hh; c[g] = *(const LAS f32x4*)&csp[nl] + *(const LAS f32x4*)&csp[128 + nl]; }
#pragma unroll
    for (int t = 0; t < 2; ++t) {
      const f32x16& a = t ? acc1 : acc0;
      const int rl = wm * 64 + (t ^ fsel) * 32;
      const f32x2 ms = stat(rl + r32);
      const float rs = ms.y * sc, mr = ms.x;
      const char* tb = base + (size_t)(rb * 4 + (rl >> 5)) * pitch;
#pragma unroll
      for (int g = 0; g < 4; ++g) {
        const unsigned p0 = pk_bf16(rs * (a[4 * g] - mr * c[g][0]), rs * (a[4 * g + 1] - mr * c[g][1]));
        const unsigned p1 = pk_bf16(rs * (a[4 * g + 2] - mr * c[g][2]), rs * (a[4 * g + 3] - mr * c[g][3]));
        st8s(tb, off[g], (u32x2){p0, p1});
      }
    }
  }
  STAMP_AT(0, 13);
}

DI void p1_repack(const u16* __restrict__ Wo, char* __restrict__ ws, const int w, int idx) {
  const int lane = fresh_lane();
  const unsigned pvo = (lane >> 4) * 8 * 1280 + (lane & 15) * 2;
  unsigned pw[7][8];
  sfor<7>([&](auto r) {
    const int f = idx * 50 + min(w + 8 * r, 49), cbk = f / 100, rem = f - cbk * 100, ks = rem / 5, ct = rem - ks * 5;
    const char* src = (const char*)(Wo + (size_t)(32 * ks) * 640 + 80 * cbk + 16 * ct);
    sfor<8>([&](auto i) { gldu16<(i & 3) * 1280>(pw[r][i], src + (i & ~3) * 1280, pvo); });
  });
  sfor<7>([&](auto r) {
    waitv<(6 - r) * 8>();
    sfor<8>([&](auto j) { pin(pw[r][j]); });
    const u32x4 v = {pw[r][0] | (pw[r][1] << 16), pw[r][2] | (pw[r][3] << 16), pw[r][4] | (pw[r][5] << 16), pw[r][6] | (pw[r][7] << 16)};
    pw[r][0] = v[0]; pw[r][1] = v[1]; pw[r][2] = v[2]; pw[r][3] = v[3];
  });
  sfor<7>([&](auto r) {
    if (w + 8 * r < 50) st16(ws + WS_WP + (size_t)(idx * 50 + w + 8 * r) * 1024 + lane * 16, (u32x4){pw[r][0], pw[r][1], pw[r][2], pw[r][3]});
  });
}

template <int OFF> DI void rdK(bf16x8& k0, s16x4& k1, unsigned a0, unsigned a1) {
  asm volatile("ds_read_b128 %0, %2 offset:%4\n\tds_read_b64 %1, %3 offset:%4" : "=&v"(k0), "=&v"(k1) : "v"(a0), "v"(a1), "i"(OFF));
}
template <int OFF> DI void rdV(bf16x8& v0, bf16x8& v1, unsigned a, unsigned long long msk) {
  asm volatile("s_mov_b64 exec, %5\n\tds_read_b128 %0, %2 offset:%3\n\tds_read_b128 %1, %2 offset:%4\n\ts_mov_b64 exec, -1"
               : "+v"(v0), "+v"(v1) : "v"(a), "i"(OFF), "i"(OFF + 640), "s"(msk));
}
#define EX2(v) __builtin_amdgcn_exp2f(v)
constexpr int wo_off(int p) { return (p < 60 ? p : p + 20) * 1024; }
constexpr int gu_off(int f) { return (f < 20 ? f + 60 : f < 60 ? f + 100 : f - 60) * 1024; }
constexpr int wd_off(int f) { return ((f / 20 + 4) % 8) * 20480 + (f % 20) * 1024; }
constexpr int CSF_OFF = 116 * 1024, XCH_OFF = 120 * 1024;

DI void p2_attn(char* __restrict__ ws, const u16* __restrict__ Wg, const u16* __restrict__ Wu, const u16* __restrict__ Wd,
                const unsigned lds0, const int w, const u32x2 tag, const int rb3, const int cb3, const int rb4, const int cb4) {
  const int lane = fresh_lane();
  const int kh = w >> 2, wq = w & 3;
  const int head = (blockIdx.x & 7) * 4 + ((blockIdx.x >> 3) & 3), qb = blockIdx.x >> 5;
  const int r32 = lane & 31, hh = lane >> 5;
  const char* kvg = ws + WS_KV + (size_t)head * 163840 + (size_t)kh * 81920 + wq * 1024;
  const unsigned voff = lane * 16;
  const unsigned kvl = lds0 + kh * 81920 + wq * 1024;
  char* ctl = ws + WS_CTL;
  const char* ctr = ctl + (blockIdx.x & 7) * CTL_REP;
  STAMP_AT(1, 0);
  {
    const int cfl = (20 * head) >> 5, cfh = (20 * head + 19) >> 5;
    const unsigned f0 = (unsigned)(((((lane & 32) ? 40 : 20) + ((lane & 16) ? cfh : cfl)) * 32 + kh * 16 + (lane & 15)) * 8);
    const unsigned f1 = (unsigned)((lane < 32 ? (((lane & 1) ? cfh : cfl) * 32 + qb * 4 + wq) : 1920 + cb3 * 16 + (lane & 15)) * 8);
    poll2(ctr + CTL_F1, f0, f1, tag);
  }
  STAMP_AT(1, 4);
#pragma unroll
  for (int i = 0; i < 4; ++i) glds16<true>(kvg + i * 4096, voff, kvl + i * 4096);
  bf16x8 q0[2]; s16x4 q1[2];
  {
    const char* qg = ws + WS_Q + ((size_t)head * 64 + qb * 8 + wq * 2) * 1280;
    gld16<0>(q0[0], qg, voff); gld16<1280>(q0[1], qg, voff); gld8<1024>(q1[0], qg, r32 * 8); gld8<2304>(q1[1], qg, r32 * 8);
  }
  const unsigned ka0 = lds0 + kh * 81920 + lane * 16, ka1 = lds0 + kh * 81920 + 1024 + r32 * 8, va = lds0 + kh * 81920 + 1280 + (hh * 20 + (r32 < 20 ? r32 : 0)) * 16;
  bf16x8 K0[2], V0[2], V1[2]; s16x4 K1[2];
  V0[0] = V0[1] = V1[0] = V1[1] = (bf16x8)(short)(r32 == 20 ? 0x3f80 : 0);
  pin(V0[0]); pin(V0[1]); pin(V1[0]); pin(V1[1]);
  WAITV_BAR(0);
  pin(q0[0]); pin(q0[1]); pin(q1[0]); pin(q1[1]);
  if (hh) { q1[0] = (s16x4){0, 0, 0, 0}; q1[1] = (s16x4){0, 0, 0, 0}; }
  STAMP_AT(1, 1);
  rdK<0>(K0[0], K1[0], ka0, ka1);
  const unsigned long long vmsk = 0x000fffff000fffffull;
  rdV<0>(V0[0], V1[0], va, vmsk);
  f32x16 cm[2];
  {
    const bf16x8 kf0 = lds128(lds0 + lane * 16); const s16x4 kf1 = lds64(lds0 + 1024 + r32 * 8);
#pragma unroll
    for (int t = 0; t < 2; ++t) {
      f32x16 s = MFMA32(kf0, q0[t], (f32x16){}); s = MFMA32K8(kf1, q1[t], s);
      float m = s[0];
#pragma unroll
      for (int i = 1; i < 16; ++i) m = fmaxf(m, s[i]);
      m = max32(m);
      cm[t] = (f32x16)(-m);
      pin(cm[t]);
    }
  }
  asm volatile("s_waitcnt lgkmcnt(0)" : "+v"(K0[0]), "+v"(K1[0]), "+v"(V0[0]), "+v"(V1[0]));
  f32x16 o[2] = {};
  f32x16 SA, SB_;
  u32x4 PA0, PA1, PB0 = {}, PB1 = {};
  SA = MFMA32(K0[0], q0[0], cm[0]); SA = MFMA32K8(K1[0], q1[0], SA);
  STAMP_AT(1, 2);
  pin(o[0]); pin(o[1]); pin(PB0); pin(PB1);
#define GRP(MF, Sc, Pc, i, j) do { MF; SB(); const float e0 = EX2(Sc[i]), e1 = EX2(Sc[i + 1]), e2 = EX2(Sc[i + 2]), e3 = EX2(Sc[i + 3]); \
    Pc[j] = pk_bf16(e0, e1); Pc[j + 1] = pk_bf16(e2, e3); pin(Pc); SB(); } while (0)
#define SLOT(QK, Sc, Pc0, Pc1, Sn, kb, jn, Pp0, Pp1, vb, jp) do { \
    GRP(if constexpr (QK) Sn = MFMA32(K0[kb], q0[jn], cm[jn]), Sc, Pc0, 0, 0); \
    GRP(if constexpr (QK) Sn = MFMA32K8(K1[kb], q1[jn], Sn), Sc, Pc0, 4, 2); \
    GRP(o[jp] = MFMA32(V0[vb], __builtin_bit_cast(bf16x8, Pp0), o[jp]), Sc, Pc1, 8, 0); \
    GRP(o[jp] = MFMA32(V1[vb], __builtin_bit_cast(bf16x8, Pp1), o[jp]), Sc, Pc1, 12, 2); } while (0)
#define LWAIT(kb, vb) asm volatile("s_waitcnt lgkmcnt(0)" : "+v"(K0[kb]), "+v"(K1[kb]), "+v"(V0[vb]), "+v"(V1[vb]))
  unsigned pw[3][8];
  const int gw = blockIdx.x * 8 + w, gwc = min(gw, 1919);
  const char* psrc[3];
#pragma unroll
  for (int r = 0; r < 2; ++r) {
    const int f = 2 * gwc + r, cbk = f / 120, rem = f - cbk * 120, ks = rem / 6, ct = rem - ks * 6;
    psrc[r] = (const char*)((ct < 3 ? Wg : Wu) + (size_t)(32 * ks) * 1536 + 48 * cbk + 16 * (ct < 3 ? ct : ct - 3));
  }
  { const int f = gwc, cbk = f / 240, rem = f - cbk * 240, ks = rem / 5, ct = rem - ks * 5;
    psrc[2] = (const char*)(Wd + (size_t)(32 * ks) * 640 + 80 * cbk + 16 * ct); }
  const unsigned pvo1 = (lane >> 4) * 8 * 3072 + (lane & 15) * 2, pvo2 = (lane >> 4) * 8 * 1280 + (lane & 15) * 2;
  const char* wog = ws + WS_WP + (size_t)cb3 * 102400;
  auto rstore = [&](auto r_, auto yv_) {
    constexpr int r = r_;
    waitv<yv_>();
    sfor<8>([&](auto j) { pin(pw[r][j]); });
    const u32x4 v = {pw[r][0] | (pw[r][1] << 16), pw[r][2] | (pw[r][3] << 16), pw[r][4] | (pw[r][5] << 16), pw[r][6] | (pw[r][7] << 16)};
    if (gw < 1920) {
      if constexpr (r < 2) {
        st16(ws + WS_WP + (size_t)(NFR_WO + 2 * gw + r) * 1024 + lane * 16, v);
        float c = 0.f;
#pragma unroll
        for (int j = 0; j < 4; ++j) c = dot2(v[j], 0x3f803f80u, c);
        c = red4(c);
        if (lane < 16) st4(ws + WS_CSP + (size_t)(2 * gw + r) * 64 + lane * 4, c);
      } else st16(ws + WS_WP + (size_t)(NFR_WO + NFR_GU + gw) * 1024 + lane * 16, v);
    }
  };
  const int qtile = qb * 8 + wq * 2 + kh, cg0 = head * 5 + (head & 1), cgt = (head & 1) ? head * 5 : head * 5 + 4;
  const char* ob = ws + WS_O + (size_t)qtile * 40960;
  const char* obs[3] = {ob + (cg0 >> 3) * 1024 + ((cg0 & 7) >> 1) * 256, ob + ((cg0 + 2) >> 3) * 1024 + (((cg0 + 2) & 7) >> 1) * 256,
                        ob + (cgt >> 3) * 1024 + ((cgt & 7) >> 1) * 256 + (cgt & 1) * 8};
  unsigned vo = (unsigned)((r32 >> 4) * 20480 + (r32 & 15) * 16 + hh * 8); pin(vo);
  u32x4 fill = {0xffffffffu, 0xffffffffu, 0xffffffffu, 0xffffffffu}; pin(fill);
  const u32x2 fill2 = {fill[0], fill[1]};
  u32x2 fa, fb, fc;
  const unsigned fvc = (unsigned)CTL_FR + (unsigned)((cb4 * 60 + min(lane, 59)) * 8);
  const unsigned fva = (unsigned)(hh ? CTL_F4 : CTL_F2) + (unsigned)(((rb3 * 2 + ((w & 3) >> 1)) * 32 + r32) * 8), fvb = lane < 32 ? (unsigned)CTL_F3 + (unsigned)(((rb4 * 16 + w * 2) * 8 + (lane & 15)) * 8) : (unsigned)CTL_FR + (unsigned)((cb3 * 240 + w * 30 + min(lane - 32, 29)) * 8);
  auto hook = [&](auto it_, auto i_) {
    constexpr int it = it_, i = i_, n = (it - 2) * 8 + i;
    if constexpr (n >= 0 && n < 2) st8s(obs[n], vo, fill2);
    if constexpr (n == 2) { if (!hh) st8s(obs[2], vo, fill2); }
    if constexpr (n >= 3 && n < 6) { const int L = (n - 3) * 64 + lane; st16s(ws + WS_G + (size_t)(rb4 * 16 + w * 2) * 49152 + cb4 * 1536, (unsigned)(L * 16 + (L >= 96 ? 49152 - 1536 : 0)), fill); }
    if constexpr (n >= 6 && n < 9) { if (w < 4) st16s(ws + WS_H + (size_t)(rb3 * 4 + w) * 20480 + cb3 * 2560, (unsigned)(min((n - 6) * 64 + lane, 159) * 16), fill); }
    if constexpr (n == 9) { if (w < 4 && lane < 16) st8s(ws + WS_HST + (size_t)(rb3 * 4 + w) * 1024 + (cb3 >> 1) * 256 + (cb3 & 1) * 8, (unsigned)(lane * 16), fill2); }
    if constexpr (it == 6 && i == 6) {
      waitv<6>();
      setflag(ctl + CTL_FR + gw * 8, tag, lane);
      setflag(ctl + CTL_F2 + (qtile * 32 + head) * 8, tag, lane);
      setflag(ctl + CTL_F4 + ((rb4 * 8 + w) * 32 + cb4) * 8, tag, lane);
      if (w < 4) setflag(ctl + CTL_F3 + ((rb3 * 4 + w) * 8 + cb3) * 8, tag, lane);
    }
    if constexpr (it == 7 && i == 6) { gld8<0>(fa, ctr, fva); gld8<0>(fb, ctr, fvb); gld8<0>(fc, ctr, fvc); }
    if constexpr (it < 2) glds16<true>(kvg + (size_t)(it * 8 + 4 + i) * 4096, voff, kvl + (it * 8 + 4 + i) * 4096);
    else if constexpr (it < 4) gldu16<(i & 1) * 3072>(pw[it - 2][i], psrc[it - 2] + (i & ~1) * 3072, pvo1);
    else if constexpr (it == 4) {
      gldu16<(i & 3) * 1280>(pw[2][i], psrc[2] + (i & ~3) * 1280, pvo2);
      if constexpr (i == 0) rstore(IC<0>{}, IC<9>{});
      if constexpr (i == 7) rstore(IC<1>{}, IC<8>{});
    } else if constexpr (it == 5) {
      if constexpr (i == 7) rstore(IC<2>{}, IC<2>{});
    } else {
      constexpr int j = (it - 6) * 8 + i;
      const char* src = wog + rotf(w + 8 * j, 20 * ROT3(cb3), 100) * 1024;
      if constexpr (j < 7) glds16<true>(src, voff, lds0 + w * 1024 + j * 8192);
      else if constexpr (j == 7) glds16<true>(src, voff, lds0 + w * 1024 + j * 8192 + (w >= 4 ? 20480 : 0));
      else if constexpr (j < 12) glds16<true>(src, voff, lds0 + w * 1024 + j * 8192 + 20480);
      else if constexpr (j == 12) { if (w < 4) glds16<true>(src, voff, lds0 + w * 1024 + j * 8192 + 20480); }
    }
    SB();
  };
  const unsigned ka0b = ka0 + 40960, ka1b = ka1 + 40960, vab = va + 40960;
  sfor<8>([&](auto it_) {
    constexpr int it = it_;
    if constexpr (it == 1) WAITV_BAR(6);
    if constexpr (it == 2) { WAITV_BAR(8); STAMP_AT(1, 3); }
    if constexpr (it == 4) asm volatile("s_waitcnt vmcnt(16)\n\ts_barrier" ::: "memory");
    if constexpr (it == 6) asm volatile("s_barrier" ::: "memory");
    if (((it ^ kh) & 1) == 0) __builtin_amdgcn_s_setprio(0); else __builtin_amdgcn_s_setprio(1);
    constexpr int o0 = (it & 3) * 10240;
    constexpr int on = it == 3 ? 0 : o0 + 10240;
    const unsigned a0 = it < 4 ? ka0 : ka0b, a1 = it < 4 ? ka1 : ka1b, av = it < 4 ? va : vab;
    const unsigned n0 = it < 3 ? ka0 : ka0b, n1 = it < 3 ? ka1 : ka1b, nv = it < 3 ? va : vab;
    rdK<o0 + 2560>(K0[1], K1[1], a0, a1); hook(it_, IC<0>{});                  SLOT(true, SA, PA0, PA1, SB_, 0, 1, PB0, PB1, 1, 1);
    LWAIT(1, 0); rdV<o0 + 2560>(V0[1], V1[1], av, vmsk); hook(it_, IC<1>{});   SLOT(true, SB_, PB0, PB1, SA, 1, 0, PA0, PA1, 0, 0);
    rdK<o0 + 5120>(K0[0], K1[0], a0, a1); hook(it_, IC<2>{});                  SLOT(true, SA, PA0, PA1, SB_, 1, 1, PB0, PB1, 0, 1);
    LWAIT(0, 1); rdV<o0 + 5120>(V0[0], V1[0], av, vmsk); hook(it_, IC<3>{});   SLOT(true, SB_, PB0, PB1, SA, 0, 0, PA0, PA1, 1, 0);
    rdK<o0 + 7680>(K0[1], K1[1], a0, a1); hook(it_, IC<4>{});                  SLOT(true, SA, PA0, PA1, SB_, 0, 1, PB0, PB1, 1, 1);
    LWAIT(1, 0); rdV<o0 + 7680>(V0[1], V1[1], av, vmsk); hook(it_, IC<5>{});   SLOT(true, SB_, PB0, PB1, SA, 1, 0, PA0, PA1, 0, 0);
    if constexpr (it < 7) rdK<on>(K0[0], K1[0], n0, n1);
    hook(it_, IC<6>{});                                                        SLOT(true, SA, PA0, PA1, SB_, 1, 1, PB0, PB1, 0, 1);
    LWAIT(0, 1);
    if constexpr (it < 7) rdV<on>(V0[0], V1[0], nv, vmsk);
    hook(it_, IC<7>{});                                                        SLOT(it < 7, SB_, PB0, PB1, SA, 0, 0, PA0, PA1, 1, 0);
  });
  __builtin_amdgcn_s_setprio(0);
  o[1] = MFMA32(V0[1], __builtin_bit_cast(bf16x8, PB0), o[1]);
  o[1] = MFMA32(V1[1], __builtin_bit_cast(bf16x8, PB1), o[1]);
  STAMP_AT(1, 9);
  bar();
  {
    LAS f32x4* xch = (LAS f32x4*)(uintptr_t)(lds0 + XCH_OFF + w * 3072);
    const f32x16& snd = kh ? o[0] : o[1];
#pragma unroll
    for (int g = 0; g < 3; ++g) xch[g * 64 + lane] = (f32x4){snd[4 * g], snd[4 * g + 1], snd[4 * g + 2], snd[4 * g + 3]};
  }
  bar();
  STAMP_AT(1, 10);
  float of[12];
  {
    const LAS f32x4* xch = (const LAS f32x4*)(uintptr_t)(lds0 + XCH_OFF + (w ^ 4) * 3072);
    const f32x16& own = kh ? o[1] : o[0];
#pragma unroll
    for (int g = 0; g < 3; ++g) { const f32x4 v = xch[g * 64 + lane];
#pragma unroll
      for (int j = 0; j < 4; ++j) of[4 * g + j] = own[4 * g + j] + v[j]; }
  }
  float l = hi32(of[8]);
  bool ok = (l > 8.67e-19f) && (l < 1.15e18f);
#pragma unroll
  for (int i = 0; i < 8; ++i) ok = ok && (fabsf(of[i]) < 1e37f);
  if (!hh) {
#pragma unroll
    for (int i = 8; i < 12; ++i) ok = ok && (fabsf(of[i]) < 1e37f);
  }
  if (__builtin_expect(!__all(ok), 0)) {
    __builtin_amdgcn_fence(__ATOMIC_ACQUIRE, "agent");
    const char* kv = ws + WS_KV + (size_t)head * 163840;
    const bf16x8 fq0 = kh ? q0[1] : q0[0];
    const s16x4 fq1 = kh ? q1[1] : q1[0];
    float m = -INFINITY;
    for (int t = 0; t < 64; ++t) {
      const bf16x8 kf0 = *(const bf16x8*)(kv + t * 2560 + lane * 16); const s16x4 kf1 = *(const s16x4*)(kv + t * 2560 + 1024 + r32 * 8);
      f32x16 s = MFMA32(kf0, fq0, (f32x16){}); s = MFMA32K8(kf1, fq1, s);
#pragma unroll
      for (int i = 0; i < 16; ++i) m = fmaxf(m, s[i]);
    }
    m = max32(m);
    f32x16 oo = {};
    const int vr = r32 < 20 ? r32 : 0;
    for (int t = 0; t < 64; ++t) {
      const bf16x8 kf0 = *(const bf16x8*)(kv + t * 2560 + lane * 16); const s16x4 kf1 = *(const s16x4*)(kv + t * 2560 + 1024 + r32 * 8);
      bf16x8 vf0 = *(const bf16x8*)(kv + t * 2560 + 1280 + (hh * 20 + vr) * 16), vf1 = *(const bf16x8*)(kv + t * 2560 + 1920 + (hh * 20 + vr) * 16);
      if (r32 >= 20) { vf0 = (bf16x8)(short)0x3f80; vf1 = vf0; }
      f32x16 s = MFMA32(kf0, fq0, (f32x16){}); s = MFMA32K8(kf1, fq1, s);
      u32x4 p0, p1;
#pragma unroll
      for (int i = 0; i < 4; ++i) {
        p0[i] = pk_bf16(EX2(s[2 * i] - m), EX2(s[2 * i + 1] - m));
        p1[i] = pk_bf16(EX2(s[8 + 2 * i] - m), EX2(s[8 + 2 * i + 1] - m));
      }
      oo = MFMA32(vf0, __builtin_bit_cast(bf16x8, p0), oo);
      oo = MFMA32(vf1, __builtin_bit_cast(bf16x8, p1), oo);
    }
#pragma unroll
    for (int i = 0; i < 12; ++i) of[i] = oo[i];
    l = hi32(of[8]);
  }
  const float rl = __builtin_amdgcn_rcpf(l);
  waitv<0>(); pin(fa); pin(fb); pin(fc);
#pragma unroll
  for (int g = 0; g < 3; ++g)
    if (g < 2 || !hh) st8s(obs[g], vo, nofill(pk_bf16(of[4 * g] * rl, of[4 * g + 1] * rl), pk_bf16(of[4 * g + 2] * rl, of[4 * g + 3] * rl)));
  STAMP_AT(1, 11);
  if (!__all(fa[0] == tag[0] && fa[1] == tag[1] && fb[0] == tag[0] && fb[1] == tag[1])) poll2(ctr, fva, fvb, tag);
  if (!__all(fc[0] == tag[0] && fc[1] == tag[1])) poll(ctr, fvc, tag);
  STAMP_AT(1, 12);
}

constexpr int x_none(int) { return 0; }
constexpr int x_p4(int i) { return ((i >= 2 && i < 7) || (i >= 10 && i < 18)) ? 1 : 0; }
template <int NSTEP, int NF, int LA, int LL, int NA, int ASTR, int (*BOFF)(int), int (*XCNT)(int), class PRE, class MM>
DI void gemm_res(const char* asrc, int rot, unsigned lds0, unsigned voff, PRE&& pre, MM&& mm) {
  bf16x8 a[NSTEP][NA];
  bf16x8 bf[LL + 1][NF];
  unsigned rd0 = lds0 + voff, rd1 = rd0 + 65536, rd2 = rd0 + 131072; pin(rd0); pin(rd1); pin(rd2);
  auto issue = [&](auto n_) { constexpr int n = n_; sfor<NA>([&](auto j) { gld16<(n & 3) * 1024>(a[n][j], asrc + rotf(n >> 2, rot, NSTEP / 4) * 4096 + j * ASTR, voff); }); };
  constexpr auto xsum = [](int lo, int hi) { int c = 0; for (int i = lo < 0 ? 0 : lo; i <= hi; ++i) c += XCNT(i); return c; };
  auto rd = [&](auto i_, auto f_) {
    constexpr int i = i_, f = f_, off = BOFF(i * NF + f);
    bf[i % (LL + 1)][f] = lds128((off < 65536 ? rd0 : off < 131072 ? rd1 : rd2) + (off & 65535));
  };
  auto check = [&](auto i_) {
    constexpr int i = i_;
    sfor<NA>([&](auto j) { pin(a[i][j]); });
    auto mx = [&]() { unsigned m = 0; sfor<NA>([&](auto j) { m = max(m, umax4(__builtin_bit_cast(u32x4, a[i][j]))); }); return m; };
    if (__builtin_expect(__any(mx() == 0xffffffffu), 0)) {
#pragma unroll 1
      for (int spin = 0; spin < (1 << 17); ++spin) {
        RETRY_COUNT(NSTEP + NF);
        issue(i_); waitv<0>(); sfor<NA>([&](auto j) { pin(a[i][j]); });
        if (!__any(mx() == 0xffffffffu)) break;
      }
    }
  };
  sfor<LA>([&](auto n) { issue(n); });
  pre(IC<-1>{});
  sfor<LL>([&](auto i) { sfor<NF>([&](auto f) { rd(i, f); }); });
  waitv<(LA - 1) * NA>(); check(IC<0>{});
  sfor<NSTEP>([&](auto i_) {
    constexpr int i = i_;
    pre(i_);
    SB();
    sfor<NF>([&](auto f_) {
      constexpr int f = f_;
      if constexpr (f == NF - 1 && i + 1 < NSTEP) {
        waitv<((i + LA < NSTEP ? i + LA : NSTEP - 1) - (i + 1)) * NA + xsum(i - LA + 2, i)>(); check(IC<i + 1>{}); SB(); }
      mm(f_, bf[i % (LL + 1)][f], a[i]);
      SB();
      if constexpr (i + LL < NSTEP) rd(IC<i + LL>{}, f_);
      if constexpr (f == 0 && i + LA < NSTEP) issue(IC<i + LA>{});
      SB();
    });
  });
}

__global__ __launch_bounds__(512, 2) void k_mega(const u16* __restrict__ x, const u16* __restrict__ Wqkv, const u16* __restrict__ Wo,
                                                 const u16* __restrict__ Wg, const u16* __restrict__ Wu, const u16* __restrict__ Wd,
                                                 char* __restrict__ ws, u16* __restrict__ out) {
  __shared__ __attribute__((aligned(16))) char smem[163840];
  const unsigned lds0 = lds_addr_of(smem);
  const int w = __builtin_amdgcn_readfirstlane(threadIdx.x >> 6);
  const int g8 = blockIdx.x & 7, i32 = blockIdx.x >> 3;
  char* ctl = ws + WS_CTL;
  const char* ctr = ctl + g8 * CTL_REP;
  u32x2 ep;
  gld8<0>(ep, ctl, 0u);
  int f1idx;
  if (i32 < 30) {
    const int i = (g8 & 1) * 30 + i32, rb = (g8 >> 1) * 4 + (i & 3), cb = i >> 2;
    if (cb >= 10) p1_body<true>(x, Wqkv, ws, lds0, w, rb, cb);
    else p1_body<false>(x, Wqkv, ws, lds0, w, rb, cb);
    f1idx = (cb * 4 + (w & 3)) * 32 + rb * 2 + (w >> 2);
  } else {
    p1_repack(Wo, ws, w, blockIdx.x - 240);
    f1idx = 1920 + (blockIdx.x - 240) * 8 + w;
  }
  waitv<0>();
  pin(ep);
  u32x2 tag;
  {
    const unsigned long long e = (((unsigned long long)__builtin_amdgcn_readfirstlane(ep[1]) << 32) | __builtin_amdgcn_readfirstlane(ep[0])) + 1ull;
    tag = (u32x2){(unsigned)e, (unsigned)(e >> 32)};
  }
  publish(ctl + CTL_F1 + f1idx * 8, tag, fresh_lane());
  bar();
  const int rb3 = g8 * 4 + (i32 >> 3), cb3 = i32 & 7;
  const int rb4 = (g8 >> 2) * 4 + (i32 >> 3), cb4 = (g8 & 3) * 8 + (i32 & 7);
  p2_attn(ws, Wg, Wu, Wd, lds0, w, tag, rb3, cb3, rb4, cb4);
  bar();
  const char* gug = ws + WS_WP + (size_t)NFR_WO * 1024 + (size_t)cb4 * 122880;
  const char* wdg = ws + WS_WP + (size_t)(NFR_WO + NFR_GU) * 1024 + (size_t)cb3 * 245760;
  const int rg4 = 24 * ROT3(cb4 & 7), rg5 = 20 * ROT5(cb3);
  u32x2 hp[5];
  if (w < 4) {
    const int lane = fresh_lane();
    const unsigned voff = lane * 16;
    const int row = rb3 * 64 + w * 16 + (lane & 15), col0 = cb3 * 80 + 4 * (lane >> 4);
    STAMP_AT(2, 0);
    u32x2 xr[5];
#pragma unroll
    for (int ct = 0; ct < 5; ++ct) xr[ct] = *(const u32x2*)(x + (size_t)row * 640 + col0 + 16 * ct);
    { const int hd = lane & 31, cgt = (hd & 1) ? hd * 5 : hd * 5 + 4;
      probe(ws + WS_O + (size_t)(rb3 * 4 + w) * 20480, (unsigned)((cgt >> 3) * 1024 + ((cgt & 7) >> 1) * 256 + (cgt & 1) * 8)); }
    sfor<5>([&](auto ct) { pin(xr[ct]); });
    STAMP_AT(2, 1);
    f32x4 acc[5] = {};
    gemm_res<20, 5, 20, 2, 1, 0, wo_off, x_none>(ws + WS_O + (size_t)(rb3 * 4 + w) * 20480, ROT3(cb3), lds0, voff,
      [&](auto i) { if constexpr (i == -1) STAMP_AT(2, 2); if constexpr (i == 1) STAMP_AT(2, 3); if constexpr (i == 10) STAMP_AT(2, 4);
        if constexpr (i == P3_GO) bar(); },
      [&](auto ct, const bf16x8& b, bf16x8 (&a)[1]) { acc[ct] = MFMA16(b, a[0], acc[ct]); });
    STAMP_AT(2, 6);
    float s1 = 0.f, s2 = 0.f;
    const char* hb = ws + WS_H + (size_t)(rb3 * 4 + w) * 20480;
    const unsigned vt = t16lane(lane);
#pragma unroll
    for (int ct = 0; ct < 5; ++ct) {
      hp[ct] = nofill(pk_bf16(bf_lo(xr[ct][0]) + acc[ct][0], bf_hi(xr[ct][0]) + acc[ct][1]), pk_bf16(bf_lo(xr[ct][1]) + acc[ct][2], bf_hi(xr[ct][1]) + acc[ct][3]));
      st8s(hb + t16off(cb3 * 80 + ct * 16), vt, hp[ct]);
      s1 = dot2(hp[ct][0], 0x3f803f80u, s1); s1 = dot2(hp[ct][1], 0x3f803f80u, s1);
      s2 = dot2(hp[ct][0], hp[ct][0], s2); s2 = dot2(hp[ct][1], hp[ct][1], s2);
    }
    s1 = red4(s1); s2 = red4(s2);
    if (lane < 16) st8s(ws + WS_HST + (size_t)(rb3 * 4 + w) * 1024 + (cb3 >> 1) * 256 + (cb3 & 1) * 8, (unsigned)(lane * 16), nofill(__float_as_uint(s1), __float_as_uint(s2)));
    bar();
    STAMP_AT(2, 7);
  } else {
    const int lane = fresh_lane(), v = w - 4;
    const unsigned voff = lane * 16;
    float cpart[10];
    const int cc = v * 24 + min(lane & 31, 23);
    sfor<10>([&](auto i) { gld4<i * 384>(cpart[i], ws + WS_CSP + (size_t)cb4 * 7680, (unsigned)((lane >> 5) * 3840 + cc * 4)); });
    waitv<0>();
    float cv = 0.f;
    sfor<10>([&](auto j) { pin(cpart[j]); cv += cpart[j]; });
    cv = add32(cv);
    bar();
    STAMP_AT(2, 2);
    sfor<5>([&](auto j) { glds16<true>(gug + rotf(v * 5 + j, rg4, 120) * 1024, voff, lds0 + gu_off(0) + (v * 5 + j) * 1024); });
    sfor<10>([&](auto j) { glds16<true>(gug + rotf(20 + v * 10 + j, rg4, 120) * 1024, voff, lds0 + gu_off(20) + (v * 10 + j) * 1024); });
    STAMP_AT(2, 5);
    waitv<0>();
    bar();
    STAMP_AT(2, 6);
    sfor<15>([&](auto j) { glds16<true>(gug + rotf(60 + v * 15 + j, rg4, 120) * 1024, voff, lds0 + gu_off(60) + (v * 15 + j) * 1024); });
    if (lane < 24) ((LAS float*)(uintptr_t)(lds0 + CSF_OFF))[cc] = cv;
  }
  {
    const int lane = fresh_lane();
    const unsigned voff = lane * 16;
    const int r16 = lane & 15, g4 = lane >> 4;
    STAMP_AT(3, 0);
    f32x4 cs[6]; f32x2 ms[2];
    f32x4 hs[2];
#pragma unroll 1
    for (int spin = 0; spin < (1 << 17); ++spin) {
      gld16<0>(hs[0], ws + WS_HST + (size_t)(rb4 * 16 + w * 2) * 1024, voff);
      gld16<1024>(hs[1], ws + WS_HST + (size_t)(rb4 * 16 + w * 2) * 1024, voff);
      waitv<0>(); pin(hs[0]); pin(hs[1]);
      if (!__any(max(umax4(__builtin_bit_cast(u32x4, hs[0])), umax4(__builtin_bit_cast(u32x4, hs[1]))) == 0xffffffffu)) break;
      __builtin_amdgcn_s_sleep(1);
    }
    STAMP_AT(3, 1);
    f32x4 acc[2][6] = {};
    if (w < 4) __builtin_amdgcn_s_setprio(1);
    gemm_res<20, 6, K4_LA_, 1, 2, 20480, gu_off, x_p4>(ws + WS_H + (size_t)(rb4 * 16 + w * 2) * 20480, ROT3(cb4 & 7), lds0, voff,
      [&](auto i) { if constexpr (i == -1) STAMP_AT(3, 2); if constexpr (i == 1) STAMP_AT(3, 3); if constexpr (i == 10) STAMP_AT(3, 4);
        if constexpr (i == 9) bar();
        if constexpr (i == -1) sfor<2>([&](auto t) {
          const float s1 = red4(hs[t][0] + hs[t][2]), s2 = red4(hs[t][1] + hs[t][3]), mean = s1 * (1.f / 640.f);
          ms[t] = (f32x2){mean, __builtin_amdgcn_rsqf(fmaxf(s2 * (1.f / 640.f) - mean * mean, 0.f) + LN_EPS)}; pin(ms[t]); });
        if constexpr (i == 18) sfor<6>([&](auto ct) { cs[ct] = *(const LAS f32x4*)(uintptr_t)(lds0 + CSF_OFF + (ct * 16 + 4 * g4) * 4); });
        if constexpr (i >= 2 && i < 7) { const int f = (i - 2) * 8 + w - ((i == 6 && w >= 4) ? 4 : 0); glds16<true>(wdg + rotf(f, rg5, 240) * 1024, voff, lds0 + wd_off(0) + f * 1024); }
        if constexpr (i >= 10 && i < 15) { constexpr int n = i - 10; glds16<true>(wdg + rotf(40 + n * 8 + w, rg5, 240) * 1024, voff, lds0 + wd_off(40) + (n * 8 + w) * 1024); }
        if constexpr (i >= 15 && i < 18) { const int f = (i - 15) * 8 + w - ((i == 17 && w >= 4) ? 4 : 0); glds16<true>(wdg + rotf(140 + f, rg5, 240) * 1024, voff, lds0 + wd_off(140) + f * 1024); } },
      [&](auto ct, const bf16x8& b, bf16x8 (&a)[2]) { acc[0][ct] = MFMA16(b, a[0], acc[0][ct]); acc[1][ct] = MFMA16(b, a[1], acc[1][ct]); });
    STAMP_AT(3, 6);
    constexpr float GC0 = -2.f * 0.7978845608028654f * 1.4426950408889634f, GC1 = GC0 * 0.044715f;
    const char* gb = ws + WS_G + (size_t)(rb4 * 16 + w * 2) * 49152;
    const unsigned vt = t16lane(lane);
#pragma unroll
    for (int t = 0; t < 2; ++t) {
      const float nm = -ms[t].x, rstd = ms[t].y;
#pragma unroll
      for (int ct = 0; ct < 3; ++ct) {
        const f32x4 gv = (acc[t][ct] + cs[ct] * nm) * rstd, uv = (acc[t][3 + ct] + cs[3 + ct] * nm) * rstd;
        const f32x4 z = gv * (gv * gv * GC1 + GC0);
        f32x4 e;
#pragma unroll
        for (int j = 0; j < 4; ++j) e[j] = __builtin_amdgcn_exp2f(z[j]);
        e = e + 1.f;
#pragma unroll
        for (int j = 0; j < 4; ++j) e[j] = __builtin_amdgcn_rcpf(e[j]);
        const f32x4 r = gv * uv * e;
        st8s(gb + t * 49152 + t16off(cb4 * 48 + ct * 16), vt, nofill(pk_bf16(r[0], r[1]), pk_bf16(r[2], r[3])));
      }
    }
    __builtin_amdgcn_s_setprio(0);
    STAMP_AT(3, 7);
    waitv<6>();
    STAMP_AT(3, 8);
    bar();
  }
  if (w < 4) {
    const int lane = fresh_lane();
    const unsigned voff = lane * 16;
    const int row = rb3 * 64 + w * 16 + (lane & 15), col0 = cb3 * 80 + 4 * (lane >> 4);
    STAMP_AT(4, 0);
    probe(ws + WS_G + (size_t)(rb3 * 4 + w) * 49152, (unsigned)t16off((lane & 31) * 48 + 32));
    STAMP_AT(4, 1);
    f32x4 acc[5] = {};
    gemm_res<48, 5, K5_LA_, 2, 1, 0, wd_off, x_none>(ws + WS_G + (size_t)(rb3 * 4 + w) * 49152, ROT5(cb3), lds0, voff,
      [&](auto i) { if constexpr (i == 1) STAMP_AT(4, 3); if constexpr (i == 14) STAMP_AT(4, 4); if constexpr (i == 30) STAMP_AT(4, 5);
        if constexpr (i == -1 || i == 8 || i == 14 || i == 16 || i == 30 || i == 38) bar(); },
      [&](auto ct, const bf16x8& b, bf16x8 (&a)[1]) { acc[ct] = MFMA16(b, a[0], acc[ct]); });
    STAMP_AT(4, 12);
#pragma unroll
    for (int ct = 0; ct < 5; ++ct) {
      const unsigned p0 = pk_bf16(bf_lo(hp[ct][0]) + acc[ct][0], bf_hi(hp[ct][0]) + acc[ct][1]);
      const unsigned p1 = pk_bf16(bf_lo(hp[ct][1]) + acc[ct][2], bf_hi(hp[ct][1]) + acc[ct][3]);
      *(u32x2*)(out + (size_t)row * 640 + col0 + 16 * ct) = (u32x2){p0, p1};
    }
    STAMP_AT(4, 13);
  } else {
    const int lane = fresh_lane(), v = w - 4;
    const unsigned voff = lane * 16;
    glds16<true>(wdg + rotf(36 + v, rg5, 240) * 1024, voff, lds0 + wd_off(36) + v * 1024);
    waitv<0>(); bar();
    sfor<3>([&](auto g) { sfor<5>([&](auto j) { constexpr int f = (g + 4) * 20 + j; glds16<true>(wdg + rotf(f + v * 5, rg5, 240) * 1024, voff, lds0 + wd_off(f) + v * 5120); }); });
    bar();
    sfor<2>([&](auto g) { sfor<5>([&](auto j) { constexpr int f = (g + 8) * 20 + j; glds16<true>(wdg + rotf(f + v * 5, rg5, 240) * 1024, voff, lds0 + wd_off(f) + v * 5120); }); });
    waitv<10>(); bar();
    bar();
    sfor<2>([&](auto g) { sfor<5>([&](auto j) { constexpr int f = (g + 10) * 20 + j; glds16<true>(wdg + rotf(f + v * 5, rg5, 240) * 1024, voff, lds0 + wd_off(f) + v * 5120); }); });
    waitv<10>(); bar();
    waitv<0>(); bar();
    if (blockIdx.x == 0 && w == 4) {
      poll2(ctr + CTL_FR, (unsigned)(lane * 64), (unsigned)(4096 + lane * 64), tag);
      poll2(ctr + CTL_FR, (unsigned)(8192 + lane * 64), (unsigned)(12288 + lane * 64), tag);
      if (lane == 0) st8(ctl, tag);
    }
  }
}

extern "C" void kernel_launch(void* const* d_in, const int* in_sizes, int n_in, void* d_out, int out_size, void* d_ws, size_t ws_size,
                              hipStream_t stream) {
  k_mega<<<256, 512, 0, stream>>>((const u16*)d_in[0], (const u16*)d_in[1], (const u16*)d_in[2], (const u16*)d_in[4], (const u16*)d_in[5],
                                  (const u16*)d_in[6], (char*)d_ws, (u16*)d_out);
}
```

```cpp
#include <hip/hip_runtime.h>
#include <hip/hip_bf16.h>
#include <stdint.h>
#include <utility>
#define CODE_KB 0
#define NO_ROT2 1

typedef short bf16x8 __attribute__((ext_vector_type(8)));
typedef short s16x4 __attribute__((ext_vector_type(4)));
typedef float f32x16 __attribute__((ext_vector_type(16)));
typedef float f32x4 __attribute__((ext_vector_type(4)));
typedef float f32x2 __attribute__((ext_vector_type(2)));
typedef unsigned u32x4 __attribute__((ext_vector_type(4)));
typedef unsigned u32x2 __attribute__((ext_vector_type(2)));
typedef __bf16 bf2_t __attribute__((ext_vector_type(2)));
typedef unsigned short u16;
#define LAS __attribute__((address_space(3)))
#define DI __device__ __forceinline__

constexpr int S_ = 2048, D_ = 640, H_ = 32, DH = 20, F_ = 1536;
constexpr float LN_EPS = 1e-5f;
constexpr float QSCALE = 0.22360679774997896f * 1.4426950408889634f;

constexpr size_t WS_Q   = 0;
constexpr size_t WS_KV  = WS_Q + 2621440;
constexpr size_t WS_O   = WS_KV + 5242880;
constexpr size_t WS_H   = WS_O + 2621440;
constexpr size_t WS_HST = WS_H + 2621440;
constexpr size_t WS_G   = WS_HST + 131072;
constexpr size_t WS_WP  = WS_G + 6291456;
constexpr size_t WS_CSP = WS_WP + 6717440;
constexpr size_t WS_CTL = WS_CSP + 245760;
constexpr size_t CTL_F1 = 256;
constexpr size_t CTL_F2 = CTL_F1 + 2048 * 8;
constexpr size_t CTL_FR = CTL_F2 + 2048 * 8;
constexpr size_t CTL_F3 = CTL_FR + 2048 * 8;
constexpr size_t CTL_F4 = CTL_F3 + 1024 * 8;
constexpr size_t CTL_REP = CTL_F4 + 2048 * 8 - 256;
constexpr int NFR_WO = 800, NFR_GU = 3840, NFR_WD = 1920, NFR_ALL = NFR_WO + NFR_GU + NFR_WD;

DI unsigned pk_bf16(float lo, float hi) { f32x2 f = {lo, hi}; return __builtin_bit_cast(unsigned, __builtin_convertvector(f, bf2_t)); }
DI float bf_lo(unsigned u) { return __uint_as_float(u << 16); }
DI float bf_hi(unsigned u) { return __uint_as_float(u & 0xffff0000u); }
DI float dot2(unsigned a, unsigned b, float c) { return __builtin_amdgcn_fdot2_f32_bf16(__builtin_bit_cast(bf2_t, a), __builtin_bit_cast(bf2_t, b), c, false); }
DI float add32(float v) { const auto r = __builtin_amdgcn_permlane32_swap(__float_as_uint(v), __float_as_uint(v), false, false); return __uint_as_float(r[0]) + __uint_as_float(r[1]); }
DI float max32(float v) { const auto r = __builtin_amdgcn_permlane32_swap(__float_as_uint(v), __float_as_uint(v), false, false); return fmaxf(__uint_as_float(r[0]), __uint_as_float(r[1])); }
DI float add16(float v) { const auto r = __builtin_amdgcn_permlane16_swap(__float_as_uint(v), __float_as_uint(v), false, false); return __uint_as_float(r[0]) + __uint_as_float(r[1]); }
DI float red4(float v) { return add32(add16(v)); }
DI float hi32(float v) { const auto r = __builtin_amdgcn_permlane32_swap(__float_as_uint(v), __float_as_uint(v), false, false); return __uint_as_float(r[1]); }
#define MFMA32(a, b, c) __builtin_amdgcn_mfma_f32_32x32x16_bf16((a), (b), (c), 0, 0, 0)
#define MFMA32K8(a, b, c) __builtin_amdgcn_mfma_f32_32x32x8bf16_1k((a), (b), (c), 0, 0, 0)
#define MFMA16(a, b, c) __builtin_amdgcn_mfma_f32_16x16x32_bf16((a), (b), (c), 0, 0, 0)

template <bool DEV> DI void glds16(const void* sbase, unsigned voff, unsigned lds_base) {
  unsigned sv;
  if constexpr (DEV)
    asm volatile("s_mov_b32 %0, m0\n\ts_mov_b32 m0, %3\n\ts_nop 0\n\tglobal_load_lds_dwordx4 %1, %2 sc1\n\ts_mov_b32 m0, %0"
                 : "=&s"(sv) : "v"(voff), "s"(sbase), "s"(lds_base) : "memory");
  else
    asm volatile("s_mov_b32 %0, m0\n\ts_mov_b32 m0, %3\n\ts_nop 0\n\tglobal_load_lds_dwordx4 %1, %2\n\ts_mov_b32 m0, %0"
                 : "=&s"(sv) : "v"(voff), "s"(sbase), "s"(lds_base) : "memory");
}
template <int OFF, class T> DI void gld16(T& dst, const void* sbase, unsigned voff) {
  asm volatile("global_load_dwordx4 %0, %1, %2 offset:%3 sc1" : "=v"(dst) : "v"(voff), "s"(sbase), "i"(OFF) : "memory");
}
template <int OFF, class T> DI void gld8(T& dst, const void* sbase, unsigned voff) {
  asm volatile("global_load_dwordx2 %0, %1, %2 offset:%3 sc1" : "=v"(dst) : "v"(voff), "s"(sbase), "i"(OFF) : "memory");
}
template <int OFF> DI void gld4(float& dst, const void* sbase, unsigned voff) {
  asm volatile("global_load_dword %0, %1, %2 offset:%3 sc1" : "=v"(dst) : "v"(voff), "s"(sbase), "i"(OFF) : "memory");
}
DI void asm_warm(u32x4& dst, const void* sbase, unsigned voff) {
  asm volatile("global_load_dwordx4 %0, %1, %2 sc1" : "+v"(dst) : "v"(voff), "s"(sbase) : "memory");
}
template <int OFF> DI void gldu16(unsigned& dst, const void* sbase, unsigned voff) {
  asm volatile("global_load_ushort %0, %1, %2 offset:%3" : "=v"(dst) : "v"(voff), "s"(sbase), "i"(OFF) : "memory");
}
DI void st16(void* p, u32x4 v) { asm volatile("global_store_dwordx4 %0, %1, off sc1\n\ts_nop 1" :: "v"(p), "v"(v) : "memory"); }
DI void st8(void* p, u32x2 v) { asm volatile("global_store_dwordx2 %0, %1, off sc1" :: "v"(p), "v"(v) : "memory"); }
DI void st4(void* p, float v) { asm volatile("global_store_dword %0, %1, off sc1" :: "v"(p), "v"(v) : "memory"); }
DI void st8s(const void* sbase, unsigned voff, u32x2 v) { asm volatile("global_store_dwordx2 %0, %1, %2 sc1" :: "v"(voff), "v"(v), "s"(sbase) : "memory"); }
DI u32x2 nofill(unsigned a, unsigned b) { return (u32x2){min(a, 0xfffffffeu), min(b, 0xfffffffeu)}; }
DI unsigned umax4(u32x4 v) { return max(max(v[0], v[1]), max(v[2], v[3])); }
template <int N> DI void waitv() { asm volatile("s_waitcnt vmcnt(%0)" :: "i"(N) : "memory"); }
DI void bar() { asm volatile("s_waitcnt lgkmcnt(0)\n\ts_barrier" ::: "memory"); }
DI void setflag(char* flag, u32x2 tag, int lane) { if (lane < 8) st8(flag + lane * CTL_REP, tag); }
DI void publish(char* flag, u32x2 tag, int lane) { waitv<0>(); setflag(flag, tag, lane); }
DI void poll(const char* base, unsigned voff, u32x2 tag) {
#pragma unroll 1
  for (int spin = 0; spin < (1 << 17); ++spin) {
    u32x2 v;
    asm volatile("global_load_dwordx2 %0, %1, %2 sc1\n\ts_waitcnt vmcnt(0)" : "=v"(v) : "v"(voff), "s"(base) : "memory");
    if (__all(v[0] == tag[0] && v[1] == tag[1])) break;
    __builtin_amdgcn_s_sleep(1);
  }
}
DI void poll2(const char* base, unsigned voff0, unsigned voff1, u32x2 tag) {
#pragma unroll 1
  for (int spin = 0; spin < (1 << 17); ++spin) {
    u32x2 v, u;
    asm volatile("global_load_dwordx2 %0, %2, %4 sc1\n\tglobal_load_dwordx2 %1, %3, %4 sc1\n\ts_waitcnt vmcnt(0)" : "=&v"(v), "=&v"(u) : "v"(voff0), "v"(voff1), "s"(base) : "memory");
    if (__all(v[0] == tag[0] && v[1] == tag[1] && u[0] == tag[0] && u[1] == tag[1])) break;
    __builtin_amdgcn_s_sleep(1);
  }
}
DI void probe(const char* base, unsigned voff) {
#pragma unroll 1
  for (int spin = 0; spin < (1 << 17); ++spin) {
    unsigned v;
    asm volatile("global_load_dword %0, %1, %2 sc1\n\ts_waitcnt vmcnt(0)" : "=v"(v) : "v"(voff), "s"(base) : "memory");
    if (__all(v != 0xffffffffu)) break;
    __builtin_amdgcn_s_sleep(1);
  }
}
constexpr int t16off(int c0) { return (c0 >> 5) * 1024 + (c0 & 16) * 32; }
DI unsigned t16lane(int lane) { return (unsigned)((lane >> 5) * 256 + (lane & 15) * 16 + ((lane >> 4) & 1) * 8); }
DI int rotf(int f, int off, int n) { const int r = f + off; return r - (r >= n ? n : 0); }
DI void st16s(const void* sbase, unsigned voff, u32x4 v) { asm volatile("global_store_dwordx4 %0, %1, %2 sc1\n\ts_nop 1" :: "v"(voff), "v"(v), "s"(sbase) : "memory"); }
DI float row16_sum(float v) {
  v += __builtin_bit_cast(float, __builtin_amdgcn_update_dpp(0, __builtin_bit_cast(int, v), 0x128, 0xf, 0xf, false));
  v += __builtin_bit_cast(float, __builtin_amdgcn_update_dpp(0, __builtin_bit_cast(int, v), 0x124, 0xf, 0xf, false));
  v += __builtin_bit_cast(float, __builtin_amdgcn_update_dpp(0, __builtin_bit_cast(int, v), 0x122, 0xf, 0xf, false));
  v += __builtin_bit_cast(float, __builtin_amdgcn_update_dpp(0, __builtin_bit_cast(int, v), 0x121, 0xf, 0xf, false));
  return v;
}
#define ROT3(c) 0
#define ROT5(c) 0
DI int fresh_lane() { int l = __builtin_amdgcn_mbcnt_hi(~0u, __builtin_amdgcn_mbcnt_lo(~0u, 0u)); asm volatile("" : "+v"(l)); return l; }
template <class T> DI void pin(T& v) { asm volatile("" : "+v"(v)); }
DI bf16x8 lds128(unsigned addr) { return *(const LAS bf16x8*)(uintptr_t)addr; }
DI s16x4 lds64(unsigned addr) { return *(const LAS s16x4*)(uintptr_t)addr; }
DI s16x4 ldstr(unsigned addr) { return __builtin_amdgcn_ds_read_tr16_b64_v4i16((LAS s16x4*)(uintptr_t)addr); }
DI unsigned lds_addr_of(const void* p) { return (unsigned)(uintptr_t)(const LAS char*)p; }
#define STAMP_AT(kid, idx) ((void)0)
#define RETRY_COUNT(k) ((void)0)
#define WAITV_BAR(N) asm volatile("s_waitcnt vmcnt(" #N ") lgkmcnt(0)\n\ts_barrier" ::: "memory")
#define SB() __builtin_amdgcn_sched_barrier(0)
template <int N> using IC = std::integral_constant<int, N>;
template <class F, int... Is> DI void sfor_(F&& f, std::integer_sequence<int, Is...>) { (f(IC<Is>{}), ...); }
template <int N, class F> DI void sfor(F&& f) { sfor_(f, std::make_integer_sequence<int, N>{}); }
template <int N> DI void waitv_bar() { asm volatile("s_waitcnt vmcnt(%0) lgkmcnt(0)\n\ts_barrier" :: "i"(N) : "memory"); }

#define K1_LA_ 10
#define P3_GO 10
#define K4_LA_ 5
#define K5_LA_ 24
constexpr int K1_STAGE = 32768, K1_NST = 4, K1_LA = K1_LA_;
template <bool ISV>
DI void p1_body(const u16* __restrict__ x, const u16* __restrict__ W, char* __restrict__ ws, const unsigned lds0, const int w, int rb, int cb) {
  const int lane = fresh_lane();
  const int wm = w >> 2, wn = w & 3;
  const int r32 = lane & 31, hh = lane >> 5;
  const unsigned a_voff0 = (unsigned)((lane >> 3) * 1280 + (((lane & 7) ^ ((lane >> 4) & 7)) << 4));
  const unsigned a_voff1 = (unsigned)((lane >> 3) * 1280 + (((lane & 7) ^ ((4 + (lane >> 4)) & 7)) << 4)) + 8 * 1280;
  const unsigned w_voff0 = (unsigned)((lane >> 3) * 3840 + (((lane & 7) ^ (((lane >> 4) & 1) << 2)) << 4));
  const unsigned w_voff1 = w_voff0 + 8 * 3840;
  const char* xs = (const char*)x + (size_t)(rb * 128 + w * 16) * 1280;
  const char* wsrc = (const char*)W + (size_t)((w & 3) * 16) * 3840 + (size_t)(cb * 128 + (w >> 2) * 64) * 2;
  const unsigned dst0 = lds0 + w * 2048;
#define ROTR 10
  const int rot = ((cb + 3 * rb) % ROTR) * (10 / ROTR);
  auto dma = [&](auto n_) {
    constexpr int n = n_, s = n >> 2, j = n & 3; constexpr unsigned so = (s % K1_NST) * K1_STAGE;
    const int sr = s + rot - (s + rot >= 10 ? 10 : 0);
    if constexpr (j == 0) glds16<false>(xs + sr * 128, a_voff0, dst0 + so);
    if constexpr (j == 1) glds16<false>(wsrc + (size_t)sr * 64 * 3840, w_voff0, dst0 + so + 16384);
    if constexpr (j == 2) glds16<false>(xs + sr * 128, a_voff1, dst0 + so + 1024);
    if constexpr (j == 3) glds16<false>(wsrc + (size_t)sr * 64 * 3840, w_voff1, dst0 + so + 16384 + 1024);
  };
  STAMP_AT(0, 0);
  sfor<K1_LA>([&](auto n) { dma(n); });
  const int ob = (wn >> 1) ^ wm;
  const int fsel = wn & 1;
  unsigned xa[2][4], xb[2][4], wa[2][2];
  {
    const int row = wm * 64 + fsel * 32 + r32;
    const int blk = (lane >> 4) & 1, q = (lane & 15) >> 2, p = lane & 3;
    const int c = (wn & 1) * 4 + blk * 2 + (p >> 1);
    const unsigned base = lds0 + (unsigned)(16384 + (wn >> 1) * 8192 + (8 * hh + q) * 128 + ((c ^ (((q >> 1) & 1) << 2)) << 4) + (p & 1) * 8);
#pragma unroll
    for (int hs = 0; hs < 2; ++hs) {
#pragma unroll
      for (int i = 0; i < 4; ++i) {
        xa[hs][i] = lds0 + hs * 65536 + (unsigned)(row * 128 + ((((i ^ ob) * 2 + hh) ^ ((row >> 1) & 7)) << 4));
        xb[hs][i] = xa[hs][i] + (fsel ? -4096 : 4096);
      }
      wa[hs][0] = base + hs * 65536 + ob * 2048; wa[hs][1] = base + hs * 65536 + (1 ^ ob) * 2048;
    }
  }
  f32x16 acc0 = {}, acc1 = {};
  float sx = 0.f, sxx = 0.f, sw = 0.f;
  bf16x8 fx0[4], fx1[4], fw[4];
  auto rd = [&](auto i_) {
    constexpr int i = i_, s = i >> 2, p = i & 3, sl = s % K1_NST, hs = sl >> 1; constexpr unsigned so = (sl & 1) * K1_STAGE;
    fx0[p] = lds128(xa[hs][p] + so);
    const s16x4 wl = ldstr(wa[hs][p & 1] + so + (p >> 1) * 4096);
    const s16x4 wh = ldstr(wa[hs][p & 1] + so + (p >> 1) * 4096 + 512);
    fw[p] = __builtin_shufflevector(wl, wh, 0, 1, 2, 3, 4, 5, 6, 7);
    fx1[p] = lds128(xb[hs][p] + so);
  };
  const bool isq = cb < 5;
  const int pitch = isq ? 1280 : 2560;
  unsigned off[4];
  if (ISV) {
    const int n = cb * 128 + wn * 32 + r32 - 1280, head = n / 20, d0 = n - head * 20;
    const int d = (head & 1) ? (d0 < 4 ? d0 + 16 : d0 - 4) : d0;
    off[0] = (unsigned)(head * 163840 + 1280 + (hh * 20 + d) * 16); pin(off[0]);
  } else {
#pragma unroll
    for (int g = 0; g < 4; ++g) {
      const int n = cb * 128 + wn * 32 + 8 * g + 4 * hh - (isq ? 0 : 640), head = n / 20, d0 = n - head * 20;
      const int d = (head & 1) ? (d0 < 4 ? d0 + 16 : d0 - 4) : d0;
      off[g] = (unsigned)(head * 64 * pitch + (d < 16 ? (((d >> 3) * 32 + r32) * 16 + (d & 4) * 2) : (1024 + r32 * 8))); pin(off[g]);
    }
  }
  waitv_bar<K1_LA - 4>();
  STAMP_AT(0, 1);
  rd(IC<0>{}); rd(IC<1>{});
  sfor<40>([&](auto i_) {
    constexpr int i = i_, p = i & 3;
    if constexpr ((i + 2) % 4 == 0 && i + 2 < 40) { constexpr int last = (i - 1 + K1_LA < 39 ? i - 1 + K1_LA : 39); waitv_bar<(last - (i + 5) > 0 ? last - (i + 5) : 0)>(); }
    if constexpr (i + K1_LA < 40) dma(IC<i + K1_LA>{});
    if constexpr (i + 2 < 40) rd(IC<i + 2>{});
    SB();
    if (ISV) { acc0 = MFMA32(fx0[p], fw[p], acc0); acc1 = MFMA32(fx1[p], fw[p], acc1); }
    else     { acc0 = MFMA32(fw[p], fx0[p], acc0); acc1 = MFMA32(fw[p], fx1[p], acc1); }
    if constexpr ((p & 1) == 0) {
      const u32x4 u = __builtin_bit_cast(u32x4, fx0[p]);
#pragma unroll
      for (int j = 0; j < 4; ++j) { sx = dot2(u[j], 0x3f803f80u, sx); sxx = dot2(u[j], u[j], sxx); }
    } else {
      const u32x4 u = __builtin_bit_cast(u32x4, fw[p]);
#pragma unroll
      for (int j = 0; j < 4; ++j) sw = dot2(u[j], 0x3f803f80u, sw);
    }
    SB();
  });
  STAMP_AT(0, 11);
  sx = add32(sx); sxx = add32(sxx); sw = add32(sw);
  LAS f32x2* part = (LAS f32x2*)(uintptr_t)(lds0 + 65536);
  LAS float* csp = (LAS float*)(uintptr_t)(lds0 + 65536 + 2048);
  if (hh == 0) { part[(wn >> 1) * 128 + wm * 64 + fsel * 32 + r32] = (f32x2){sx, sxx}; }
  else         { csp[wm * 128 + wn * 32 + r32] = sw; }
  bar();
  STAMP_AT(0, 12);
  auto stat = [&](int rl) { const f32x2 a = part[rl], b = part[128 + rl];
    const float mean = (a.x + b.x) * (1.f / 640.f);
    const float var = fmaxf((a.y + b.y) * (1.f / 640.f) - mean * mean, 0.f);
    return (f32x2){mean, __builtin_amdgcn_rsqf(var + LN_EPS)}; };
  if (ISV) {
    LAS f32x2* st = (LAS f32x2*)(uintptr_t)(lds0 + 65536 + 4096 + w * 512);
    st[lane] = stat(wm * 64 + lane);
    const int nl = wn * 32 + r32;
    const float c = csp[nl] + csp[128 + nl];
#pragma unroll
    for (int t = 0; t < 2; ++t) {
      const f32x16& a = t ? acc1 : acc0;
      const int rl = (t ^ fsel) * 32;
      unsigned pk[8];
#pragma unroll
      for (int g = 0; g < 4; ++g) {
        const f32x4 s01 = *(const LAS f32x4*)&st[rl + 8 * g + 4 * hh], s23 = *(const LAS f32x4*)&st[rl + 8 * g + 4 * hh + 2];
        pk[2 * g]     = pk_bf16(s01[1] * (a[4 * g] - s01[0] * c), s01[3] * (a[4 * g + 1] - s01[2] * c));
        pk[2 * g + 1] = pk_bf16(s23[1] * (a[4 * g + 2] - s23[0] * c), s23[3] * (a[4 * g + 3] - s23[2] * c));
      }
      const char* dst = ws + WS_KV + (size_t)(rb * 4 + wm * 2 + (rl >> 5)) * 2560;
      st16s(dst, off[0], (u32x4){pk[0], pk[1], pk[2], pk[3]});
      st16s(dst + 640, off[0], (u32x4){pk[4], pk[5], pk[6], pk[7]});
    }
  } else {
    const float sc = isq ? QSCALE : 1.f;
    const char* base = ws + (isq ? WS_Q : WS_KV);
    f32x4 c[4];
#pragma unroll
    for (int g = 0; g < 4; ++g) { const int nl = wn * 32 + 8 * g + 4 * hh; c[g] = *(const LAS f32x4*)&csp[nl] + *(const LAS f32x4*)&csp[128 + nl]; }
#pragma unroll
    for (int t = 0; t < 2; ++t) {
      const f32x16& a = t ? acc1 : acc0;
      const int rl = wm * 64 + (t ^ fsel) * 32;
      const f32x2 ms = stat(rl + r32);
      const float rs = ms.y * sc, mr = ms.x;
      const char* tb = base + (size_t)(rb * 4 + (rl >> 5)) * pitch;
#pragma unroll
      for (int g = 0; g < 4; ++g) {
        const unsigned p0 = pk_bf16(rs * (a[4 * g] - mr * c[g][0]), rs * (a[4 * g + 1] - mr * c[g][1]));
        const unsigned p1 = pk_bf16(rs * (a[4 * g + 2] - mr * c[g][2]), rs * (a[4 * g + 3] - mr * c[g][3]));
        st8s(tb, off[g], (u32x2){p0, p1});
      }
    }
  }
  STAMP_AT(0, 13);
}

DI void p1_repack(const u16* __restrict__ Wo, char* __restrict__ ws, const int w, int idx) {
  const int lane = fresh_lane();
  const unsigned pvo = (lane >> 4) * 8 * 1280 + (lane & 15) * 2;
  unsigned pw[7][8];
  sfor<7>([&](auto r) {
    const int f = idx * 50 + min(w + 8 * r, 49), cbk = f / 100, rem = f - cbk * 100, ks = rem / 5, ct = rem - ks * 5;
    const char* src = (const char*)(Wo + (size_t)(32 * ks) * 640 + 80 * cbk + 16 * ct);
    sfor<8>([&](auto i) { gldu16<(i & 3) * 1280>(pw[r][i], src + (i & ~3) * 1280, pvo); });
  });
  sfor<7>([&](auto r) {
    waitv<(6 - r) * 8>();
    sfor<8>([&](auto j) { pin(pw[r][j]); });
    const u32x4 v = {pw[r][0] | (pw[r][1] << 16), pw[r][2] | (pw[r][3] << 16), pw[r][4] | (pw[r][5] << 16), pw[r][6] | (pw[r][7] << 16)};
    pw[r][0] = v[0]; pw[r][1] = v[1]; pw[r][2] = v[2]; pw[r][3] = v[3];
  });
  sfor<7>([&](auto r) {
    if (w + 8 * r < 50) st16(ws + WS_WP + (size_t)(idx * 50 + w + 8 * r) * 1024 + lane * 16, (u32x4){pw[r][0], pw[r][1], pw[r][2], pw[r][3]});
  });
}

template <int OFF> DI void rdK(bf16x8& k0, s16x4& k1, unsigned a0, unsigned a1) {
  asm volatile("ds_read_b128 %0, %2 offset:%4\n\tds_read_b64 %1, %3 offset:%4" : "=&v"(k0), "=&v"(k1) : "v"(a0), "v"(a1), "i"(OFF));
}
template <int OFF> DI void rdV(bf16x8& v0, bf16x8& v1, unsigned a, unsigned long long msk) {
  asm volatile("s_mov_b64 exec, %5\n\tds_read_b128 %0, %2 offset:%3\n\tds_read_b128 %1, %2 offset:%4\n\ts_mov_b64 exec, -1"
               : "+v"(v0), "+v"(v1) : "v"(a), "i"(OFF), "i"(OFF + 640), "s"(msk));
}
#define EX2(v) __builtin_amdgcn_exp2f(v)
constexpr int wo_off(int p) { return (p < 60 ? p : p + 20) * 1024; }
constexpr int gu_off(int f) { return (f < 20 ? f + 60 : f < 60 ? f + 100 : f - 60) * 1024; }
constexpr int wd_off(int f) { return ((f / 20 + 4) % 8) * 20480 + (f % 20) * 1024; }
constexpr int CSF_OFF = 116 * 1024, XCH_OFF = 120 * 1024;

DI void p2_attn(char* __restrict__ ws, const u16* __restrict__ Wg, const u16* __restrict__ Wu, const u16* __restrict__ Wd,
                const unsigned lds0, const int w, const u32x2 tag, const int rb3, const int cb3, const int rb4, const int cb4) {
  const int lane = fresh_lane();
  const int kh = w >> 2, wq = w & 3;
  const int head = (blockIdx.x & 7) * 4 + ((blockIdx.x >> 3) & 3), qb = blockIdx.x >> 5;
  const int r32 = lane & 31, hh = lane >> 5;
  const char* kvg = ws + WS_KV + (size_t)head * 163840 + (size_t)kh * 81920 + wq * 1024;
  const unsigned voff = lane * 16;
  const unsigned kvl = lds0 + kh * 81920 + wq * 1024;
  char* ctl = ws + WS_CTL;
  const char* ctr = ctl + (blockIdx.x & 7) * CTL_REP;
  STAMP_AT(1, 0);
  {
    const int cfl = (20 * head) >> 5, cfh = (20 * head + 19) >> 5;
    const unsigned f0 = (unsigned)(((((lane & 32) ? 40 : 20) + ((lane & 16) ? cfh : cfl)) * 32 + kh * 16 + (lane & 15)) * 8);
    const unsigned f1 = (unsigned)((lane < 32 ? (((lane & 1) ? cfh : cfl) * 32 + qb * 4 + wq) : 1920 + cb3 * 16 + (lane & 15)) * 8);
    poll2(ctr + CTL_F1, f0, f1, tag);
  }
  STAMP_AT(1, 4);
#pragma unroll
  for (int i = 0; i < 4; ++i) glds16<true>(kvg + i * 4096, voff, kvl + i * 4096);
  bf16x8 q0[2]; s16x4 q1[2];
  {
    const char* qg = ws + WS_Q + ((size_t)head * 64 + qb * 8 + wq * 2) * 1280;
    gld16<0>(q0[0], qg, voff); gld16<1280>(q0[1], qg, voff); gld8<1024>(q1[0], qg, r32 * 8); gld8<2304>(q1[1], qg, r32 * 8);
  }
  const unsigned ka0 = lds0 + kh * 81920 + lane * 16, ka1 = lds0 + kh * 81920 + 1024 + r32 * 8, va = lds0 + kh * 81920 + 1280 + (hh * 20 + (r32 < 20 ? r32 : 0)) * 16;
  bf16x8 K0[2], V0[2], V1[2]; s16x4 K1[2];
  V0[0] = V0[1] = V1[0] = V1[1] = (bf16x8)(short)(r32 == 20 ? 0x3f80 : 0);
  pin(V0[0]); pin(V0[1]); pin(V1[0]); pin(V1[1]);
  WAITV_BAR(0);
  pin(q0[0]); pin(q0[1]); pin(q1[0]); pin(q1[1]);
  if (hh) { q1[0] = (s16x4){0, 0, 0, 0}; q1[1] = (s16x4){0, 0, 0, 0}; }
  STAMP_AT(1, 1);
  rdK<0>(K0[0], K1[0], ka0, ka1);
  const unsigned long long vmsk = 0x000fffff000fffffull;
  rdV<0>(V0[0], V1[0], va, vmsk);
  f32x16 cm[2];
  {
    const bf16x8 kf0 = lds128(lds0 + lane * 16); const s16x4 kf1 = lds64(lds0 + 1024 + r32 * 8);
#pragma unroll
    for (int t = 0; t < 2; ++t) {
      f32x16 s = MFMA32(kf0, q0[t], (f32x16){}); s = MFMA32K8(kf1, q1[t], s);
      float m = s[0];
#pragma unroll
      for (int i = 1; i < 16; ++i) m = fmaxf(m, s[i]);
      m = max32(m);
      cm[t] = (f32x16)(-m);
      pin(cm[t]);
    }
  }
  asm volatile("s_waitcnt lgkmcnt(0)" : "+v"(K0[0]), "+v"(K1[0]), "+v"(V0[0]), "+v"(V1[0]));
  f32x16 o[2] = {};
  f32x16 SA, SB_;
  u32x4 PA0, PA1, PB0 = {}, PB1 = {};
  SA = MFMA32(K0[0], q0[0], cm[0]); SA = MFMA32K8(K1[0], q1[0], SA);
  STAMP_AT(1, 2);
  pin(o[0]); pin(o[1]); pin(PB0); pin(PB1);
#define GRP(MF, Sc, Pc, i, j) do { MF; SB(); const float e0 = EX2(Sc[i]), e1 = EX2(Sc[i + 1]), e2 = EX2(Sc[i + 2]), e3 = EX2(Sc[i + 3]); \
    Pc[j] = pk_bf16(e0, e1); Pc[j + 1] = pk_bf16(e2, e3); pin(Pc); SB(); } while (0)
#define SLOT(QK, Sc, Pc0, Pc1, Sn, kb, jn, Pp0, Pp1, vb, jp) do { \
    GRP(if constexpr (QK) Sn = MFMA32(K0[kb], q0[jn], cm[jn]), Sc, Pc0, 0, 0); \
    GRP(if constexpr (QK) Sn = MFMA32K8(K1[kb], q1[jn], Sn), Sc, Pc0, 4, 2); \
    GRP(o[jp] = MFMA32(V0[vb], __builtin_bit_cast(bf16x8, Pp0), o[jp]), Sc, Pc1, 8, 0); \
    GRP(o[jp] = MFMA32(V1[vb], __builtin_bit_cast(bf16x8, Pp1), o[jp]), Sc, Pc1, 12, 2); } while (0)
#define LWAIT(kb, vb) asm volatile("s_waitcnt lgkmcnt(0)" : "+v"(K0[kb]), "+v"(K1[kb]), "+v"(V0[vb]), "+v"(V1[vb]))
  unsigned pw[3][8];
  const int gw = blockIdx.x * 8 + w, gwc = min(gw, 1919);
  const char* psrc[3];
#pragma unroll
  for (int r = 0; r < 2; ++r) {
    const int f = 2 * gwc + r, cbk = f / 120, rem = f - cbk * 120, ks = rem / 6, ct = rem - ks * 6;
    psrc[r] = (const char*)((ct < 3 ? Wg : Wu) + (size_t)(32 * ks) * 1536 + 48 * cbk + 16 * (ct < 3 ? ct : ct - 3));
  }
  { const int f = gwc, cbk = f / 240, rem = f - cbk * 240, ks = rem / 5, ct = rem - ks * 5;
    psrc[2] = (const char*)(Wd + (size_t)(32 * ks) * 640 + 80 * cbk + 16 * ct); }
  const unsigned pvo1 = (lane >> 4) * 8 * 3072 + (lane & 15) * 2, pvo2 = (lane >> 4) * 8 * 1280 + (lane & 15) * 2;
  const char* wog = ws + WS_WP + (size_t)cb3 * 102400;
  auto rstore = [&](auto r_, auto yv_) {
    constexpr int r = r_;
    waitv<yv_>();
    sfor<8>([&](auto j) { pin(pw[r][j]); });
    const u32x4 v = {pw[r][0] | (pw[r][1] << 16), pw[r][2] | (pw[r][3] << 16), pw[r][4] | (pw[r][5] << 16), pw[r][6] | (pw[r][7] << 16)};
    if (gw < 1920) {
      if constexpr (r < 2) {
        st16(ws + WS_WP + (size_t)(NFR_WO + 2 * gw + r) * 1024 + lane * 16, v);
        float c = 0.f;
#pragma unroll
        for (int j = 0; j < 4; ++j) c = dot2(v[j], 0x3f803f80u, c);
        c = red4(c);
        if (lane < 16) st4(ws + WS_CSP + (size_t)(2 * gw + r) * 64 + lane * 4, c);
      } else st16(ws + WS_WP + (size_t)(NFR_WO + NFR_GU + gw) * 1024 + lane * 16, v);
    }
  };
  const int qtile = qb * 8 + wq * 2 + kh, cg0 = head * 5 + (head & 1), cgt = (head & 1) ? head * 5 : head * 5 + 4;
  const char* ob = ws + WS_O + (size_t)qtile * 40960;
  const char* obs[3] = {ob + (cg0 >> 3) * 1024 + ((cg0 & 7) >> 1) * 256, ob + ((cg0 + 2) >> 3) * 1024 + (((cg0 + 2) & 7) >> 1) * 256,
                        ob + (cgt >> 3) * 1024 + ((cgt & 7) >> 1) * 256 + (cgt & 1) * 8};
  unsigned vo = (unsigned)((r32 >> 4) * 20480 + (r32 & 15) * 16 + hh * 8); pin(vo);
  u32x4 fill = {0xffffffffu, 0xffffffffu, 0xffffffffu, 0xffffffffu}; pin(fill);
  const u32x2 fill2 = {fill[0], fill[1]};
  u32x2 fa, fb, fc;
  const unsigned fvc = (unsigned)CTL_FR + (unsigned)((cb4 * 60 + min(lane, 59)) * 8);
  const unsigned fva = (unsigned)(hh ? CTL_F4 : CTL_F2) + (unsigned)(((rb3 * 2 + ((w & 3) >> 1)) * 32 + r32) * 8), fvb = lane < 32 ? (unsigned)CTL_F3 + (unsigned)(((rb4 * 16 + w * 2) * 8 + (lane & 15)) * 8) : (unsigned)CTL_FR + (unsigned)((cb3 * 240 + w * 30 + min(lane - 32, 29)) * 8);
  auto hook = [&](auto it_, auto i_) {
    constexpr int it = it_, i = i_, n = (it - 2) * 8 + i;
    if constexpr (n >= 0 && n < 2) st8s(obs[n], vo, fill2);
    if constexpr (n == 2) { if (!hh) st8s(obs[2], vo, fill2); }
    if constexpr (n >= 3 && n < 6) { const int L = (n - 3) * 64 + lane; st16s(ws + WS_G + (size_t)(rb4 * 16 + w * 2) * 49152 + cb4 * 1536, (unsigned)(L * 16 + (L >= 96 ? 49152 - 1536 : 0)), fill); }
    if constexpr (n >= 6 && n < 9) { if (w < 4) st16s(ws + WS_H + (size_t)(rb3 * 4 + w) * 20480 + cb3 * 2560, (unsigned)(min((n - 6) * 64 + lane, 159) * 16), fill); }
    if constexpr (n == 9) { if (w < 4 && lane < 16) st8s(ws + WS_HST + (size_t)(rb3 * 4 + w) * 1024 + (cb3 >> 1) * 256 + (cb3 & 1) * 8, (unsigned)(lane * 16), fill2); }
    if constexpr (it == 6 && i == 6) {
      waitv<6>();
      setflag(ctl + CTL_FR + gw * 8, tag, lane);
      setflag(ctl + CTL_F2 + (qtile * 32 + head) * 8, tag, lane);
      setflag(ctl + CTL_F4 + ((rb4 * 8 + w) * 32 + cb4) * 8, tag, lane);
      if (w < 4) setflag(ctl + CTL_F3 + ((rb3 * 4 + w) * 8 + cb3) * 8, tag, lane);
    }
    if constexpr (it == 7 && i == 6) { gld8<0>(fa, ctr, fva); gld8<0>(fb, ctr, fvb); gld8<0>(fc, ctr, fvc); }
    if constexpr (it < 2) glds16<true>(kvg + (size_t)(it * 8 + 4 + i) * 4096, voff, kvl + (it * 8 + 4 + i) * 4096);
    else if constexpr (it < 4) gldu16<(i & 1) * 3072>(pw[it - 2][i], psrc[it - 2] + (i & ~1) * 3072, pvo1);
    else if constexpr (it == 4) {
      gldu16<(i & 3) * 1280>(pw[2][i], psrc[2] + (i & ~3) * 1280, pvo2);
      if constexpr (i == 0) rstore(IC<0>{}, IC<9>{});
      if constexpr (i == 7) rstore(IC<1>{}, IC<8>{});
    } else if constexpr (it == 5) {
      if constexpr (i == 7) rstore(IC<2>{}, IC<2>{});
    } else {
      constexpr int j = (it - 6) * 8 + i;
      const char* src = wog + rotf(w + 8 * j, 20 * ROT3(cb3), 100) * 1024;
      if constexpr (j < 7) glds16<true>(src, voff, lds0 + w * 1024 + j * 8192);
      else if constexpr (j == 7) glds16<true>(src, voff, lds0 + w * 1024 + j * 8192 + (w >= 4 ? 20480 : 0));
      else if constexpr (j < 12) glds16<true>(src, voff, lds0 + w * 1024 + j * 8192 + 20480);
      else if constexpr (j == 12) { if (w < 4) glds16<true>(src, voff, lds0 + w * 1024 + j * 8192 + 20480); }
    }
    SB();
  };
  const unsigned ka0b = ka0 + 40960, ka1b = ka1 + 40960, vab = va + 40960;
  sfor<8>([&](auto it_) {
    constexpr int it = it_;
    if constexpr (it == 1) WAITV_BAR(6);
    if constexpr (it == 2) { WAITV_BAR(8); STAMP_AT(1, 3); }
    if constexpr (it == 4) asm volatile("s_waitcnt vmcnt(16)\n\ts_barrier" ::: "memory");
    if constexpr (it == 6) asm volatile("s_barrier" ::: "memory");
    if (((it ^ kh) & 1) == 0) __builtin_amdgcn_s_setprio(0); else __builtin_amdgcn_s_setprio(1);
    constexpr int o0 = (it & 3) * 10240;
    constexpr int on = it == 3 ? 0 : o0 + 10240;
    const unsigned a0 = it < 4 ? ka0 : ka0b, a1 = it < 4 ? ka1 : ka1b, av = it < 4 ? va : vab;
    const unsigned n0 = it < 3 ? ka0 : ka0b, n1 = it < 3 ? ka1 : ka1b, nv = it < 3 ? va : vab;
    rdK<o0 + 2560>(K0[1], K1[1], a0, a1); hook(it_, IC<0>{});                  SLOT(true, SA, PA0, PA1, SB_, 0, 1, PB0, PB1, 1, 1);
    LWAIT(1, 0); rdV<o0 + 2560>(V0[1], V1[1], av, vmsk); hook(it_, IC<1>{});   SLOT(true, SB_, PB0, PB1, SA, 1, 0, PA0, PA1, 0, 0);
    rdK<o0 + 5120>(K0[0], K1[0], a0, a1); hook(it_, IC<2>{});                  SLOT(true, SA, PA0, PA1, SB_, 1, 1, PB0, PB1, 0, 1);
    LWAIT(0, 1); rdV<o0 + 5120>(V0[0], V1[0], av, vmsk); hook(it_, IC<3>{});   SLOT(true, SB_, PB0, PB1, SA, 0, 0, PA0, PA1, 1, 0);
    rdK<o0 + 7680>(K0[1], K1[1], a0, a1); hook(it_, IC<4>{});                  SLOT(true, SA, PA0, PA1, SB_, 0, 1, PB0, PB1, 1, 1);
    LWAIT(1, 0); rdV<o0 + 7680>(V0[1], V1[1], av, vmsk); hook(it_, IC<5>{});   SLOT(true, SB_, PB0, PB1, SA, 1, 0, PA0, PA1, 0, 0);
    if constexpr (it < 7) rdK<on>(K0[0], K1[0], n0, n1);
    hook(it_, IC<6>{});                                                        SLOT(true, SA, PA0, PA1, SB_, 1, 1, PB0, PB1, 0, 1);
    LWAIT(0, 1);
    if constexpr (it < 7) rdV<on>(V0[0], V1[0], nv, vmsk);
    hook(it_, IC<7>{});                                                        SLOT(it < 7, SB_, PB0, PB1, SA, 0, 0, PA0, PA1, 1, 0);
  });
  __builtin_amdgcn_s_setprio(0);
  o[1] = MFMA32(V0[1], __builtin_bit_cast(bf16x8, PB0), o[1]);
  o[1] = MFMA32(V1[1], __builtin_bit_cast(bf16x8, PB1), o[1]);
  STAMP_AT(1, 9);
  bar();
  {
    LAS f32x4* xch = (LAS f32x4*)(uintptr_t)(lds0 + XCH_OFF + w * 3072);
    const f32x16& snd = kh ? o[0] : o[1];
#pragma unroll
    for (int g = 0; g < 3; ++g) xch[g * 64 + lane] = (f32x4){snd[4 * g], snd[4 * g + 1], snd[4 * g + 2], snd[4 * g + 3]};
  }
  bar();
  STAMP_AT(1, 10);
  float of[12];
  {
    const LAS f32x4* xch = (const LAS f32x4*)(uintptr_t)(lds0 + XCH_OFF + (w ^ 4) * 3072);
    const f32x16& own = kh ? o[1] : o[0];
#pragma unroll
    for (int g = 0; g < 3; ++g) { const f32x4 v = xch[g * 64 + lane];
#pragma unroll
      for (int j = 0; j < 4; ++j) of[4 * g + j] = own[4 * g + j] + v[j]; }
  }
  float l = hi32(of[8]);
  bool ok = (l > 8.67e-19f) && (l < 1.15e18f);
#pragma unroll
  for (int i = 0; i < 8; ++i) ok = ok && (fabsf(of[i]) < 1e37f);
  if (!hh) {
#pragma unroll
    for (int i = 8; i < 12; ++i) ok = ok && (fabsf(of[i]) < 1e37f);
  }
  if (__builtin_expect(!__all(ok), 0)) {
    __builtin_amdgcn_fence(__ATOMIC_ACQUIRE, "agent");
    const char* kv = ws + WS_KV + (size_t)head * 163840;
    const bf16x8 fq0 = kh ? q0[1] : q0[0];
    const s16x4 fq1 = kh ? q1[1] : q1[0];
    float m = -INFINITY;
    for (int t = 0; t < 64; ++t) {
      const bf16x8 kf0 = *(const bf16x8*)(kv + t * 2560 + lane * 16); const s16x4 kf1 = *(const s16x4*)(kv + t * 2560 + 1024 + r32 * 8);
      f32x16 s = MFMA32(kf0, fq0, (f32x16){}); s = MFMA32K8(kf1, fq1, s);
#pragma unroll
      for (int i = 0; i < 16; ++i) m = fmaxf(m, s[i]);
    }
    m = max32(m);
    f32x16 oo = {};
    const int vr = r32 < 20 ? r32 : 0;
    for (int t = 0; t < 64; ++t) {
      const bf16x8 kf0 = *(const bf16x8*)(kv + t * 2560 + lane * 16); const s16x4 kf1 = *(const s16x4*)(kv + t * 2560 + 1024 + r32 * 8);
      bf16x8 vf0 = *(const bf16x8*)(kv + t * 2560 + 1280 + (hh * 20 + vr) * 16), vf1 = *(const bf16x8*)(kv + t * 2560 + 1920 + (hh * 20 + vr) * 16);
      if (r32 >= 20) { vf0 = (bf16x8)(short)0x3f80; vf1 = vf0; }
      f32x16 s = MFMA32(kf0, fq0, (f32x16){}); s = MFMA32K8(kf1, fq1, s);
      u32x4 p0, p1;
#pragma unroll
      for (int i = 0; i < 4; ++i) {
        p0[i] = pk_bf16(EX2(s[2 * i] - m), EX2(s[2 * i + 1] - m));
        p1[i] = pk_bf16(EX2(s[8 + 2 * i] - m), EX2(s[8 + 2 * i + 1] - m));
      }
      oo = MFMA32(vf0, __builtin_bit_cast(bf16x8, p0), oo);
      oo = MFMA32(vf1, __builtin_bit_cast(bf16x8, p1), oo);
    }
#pragma unroll
    for (int i = 0; i < 12; ++i) of[i] = oo[i];
    l = hi32(of[8]);
  }
  const float rl = __builtin_amdgcn_rcpf(l);
  waitv<0>(); pin(fa); pin(fb); pin(fc);
#pragma unroll
  for (int g = 0; g < 3; ++g)
    if (g < 2 || !hh) st8s(obs[g], vo, nofill(pk_bf16(of[4 * g] * rl, of[4 * g + 1] * rl), pk_bf16(of[4 * g + 2] * rl, of[4 * g + 3] * rl)));
  STAMP_AT(1, 11);
  if (!__all(fa[0] == tag[0] && fa[1] == tag[1] && fb[0] == tag[0] && fb[1] == tag[1])) poll2(ctr, fva, fvb, tag);
  if (!__all(fc[0] == tag[0] && fc[1] == tag[1])) poll(ctr, fvc, tag);
  STAMP_AT(1, 12);
}

constexpr int x_none(int) { return 0; }
constexpr int x_p4(int i) { return ((i >= 2 && i < 7) || (i >= 10 && i < 18)) ? 1 : 0; }
template <int NSTEP, int NF, int LA, int LL, int NA, int ASTR, int (*BOFF)(int), int (*XCNT)(int), class PRE, class MM>
DI void gemm_res(const char* asrc, int rot, unsigned lds0, unsigned voff, PRE&& pre, MM&& mm) {
  bf16x8 a[NSTEP][NA];
  bf16x8 bf[LL + 1][NF];
  unsigned rd0 = lds0 + voff, rd1 = rd0 + 65536, rd2 = rd0 + 131072; pin(rd0); pin(rd1); pin(rd2);
  auto issue = [&](auto n_) { constexpr int n = n_; sfor<NA>([&](auto j) { gld16<(n & 3) * 1024>(a[n][j], asrc + rotf(n >> 2, rot, NSTEP / 4) * 4096 + j * ASTR, voff); }); };
  constexpr auto xsum = [](int lo, int hi) { int c = 0; for (int i = lo < 0 ? 0 : lo; i <= hi; ++i) c += XCNT(i); return c; };
  auto rd = [&](auto i_, auto f_) {
    constexpr int i = i_, f = f_, off = BOFF(i * NF + f);
    bf[i % (LL + 1)][f] = lds128((off < 65536 ? rd0 : off < 131072 ? rd1 : rd2) + (off & 65535));
  };
  auto check = [&](auto i_) {
    constexpr int i = i_;
    sfor<NA>([&](auto j) { pin(a[i][j]); });
    auto mx = [&]() { unsigned m = 0; sfor<NA>([&](auto j) { m = max(m, umax4(__builtin_bit_cast(u32x4, a[i][j]))); }); return m; };
    if (__builtin_expect(__any(mx() == 0xffffffffu), 0)) {
#pragma unroll 1
      for (int spin = 0; spin < (1 << 17); ++spin) {
        RETRY_COUNT(NSTEP + NF);
        issue(i_); waitv<0>(); sfor<NA>([&](auto j) { pin(a[i][j]); });
        if (!__any(mx() == 0xffffffffu)) break;
      }
    }
  };
  sfor<LA>([&](auto n) { issue(n); });
  pre(IC<-1>{});
  sfor<LL>([&](auto i) { sfor<NF>([&](auto f) { rd(i, f); }); });
  waitv<(LA - 1) * NA>(); check(IC<0>{});
  sfor<NSTEP>([&](auto i_) {
    constexpr int i = i_;
    pre(i_);
    SB();
    sfor<NF>([&](auto f_) {
      constexpr int f = f_;
      if constexpr (f == NF - 1 && i + 1 < NSTEP) {
        waitv<((i + LA < NSTEP ? i + LA : NSTEP - 1) - (i + 1)) * NA + xsum(i - LA + 2, i)>(); check(IC<i + 1>{}); SB(); }
      mm(f_, bf[i % (LL + 1)][f], a[i]);
      SB();
      if constexpr (i + LL < NSTEP) rd(IC<i + LL>{}, f_);
      if constexpr (f == 0 && i + LA < NSTEP) issue(IC<i + LA>{});
      SB();
    });
  });
}

__global__ __launch_bounds__(512, 2) void k_mega(const u16* __restrict__ x, const u16* __restrict__ Wqkv, const u16* __restrict__ Wo,
                                                 const u16* __restrict__ Wg, const u16* __restrict__ Wu, const u16* __restrict__ Wd,
                                                 char* __restrict__ ws, u16* __restrict__ out) {
  __shared__ __attribute__((aligned(16))) char smem[163840];
  const unsigned lds0 = lds_addr_of(smem);
  const int w = __builtin_amdgcn_readfirstlane(threadIdx.x >> 6);
  const int g8 = blockIdx.x & 7, i32 = blockIdx.x >> 3;
  char* ctl = ws + WS_CTL;
  const char* ctr = ctl + g8 * CTL_REP;
  u32x4 cdum = {};
  if (CODE_KB > 0) {
    unsigned long long pc; asm volatile("s_getpc_b64 %0" : "=s"(pc));
    const int kb = i32 * 3 + w;
    if (w < 3 && kb < CODE_KB) asm_warm(cdum, (const char*)(pc & ~63ull) + kb * 1024, (threadIdx.x & 63) * 16);
  }
  u32x2 ep;
  gld8<0>(ep, ctl, 0u);
  int f1idx;
  if (i32 < 30) {
    const int i = (g8 & 1) * 30 + i32, rb = (g8 >> 1) * 4 + (i & 3), cb = i >> 2;
    if (cb >= 10) p1_body<true>(x, Wqkv, ws, lds0, w, rb, cb);
    else p1_body<false>(x, Wqkv, ws, lds0, w, rb, cb);
    f1idx = (cb * 4 + (w & 3)) * 32 + rb * 2 + (w >> 2);
  } else {
    p1_repack(Wo, ws, w, blockIdx.x - 240);
    f1idx = 1920 + (blockIdx.x - 240) * 8 + w;
  }
  waitv<0>();
  pin(ep); pin(cdum);
  u32x2 tag;
  {
    const unsigned long long e = (((unsigned long long)__builtin_amdgcn_readfirstlane(ep[1]) << 32) | __builtin_amdgcn_readfirstlane(ep[0])) + 1ull;
    tag = (u32x2){(unsigned)e, (unsigned)(e >> 32)};
  }
  publish(ctl + CTL_F1 + f1idx * 8, tag, fresh_lane());
  bar();
  const int rb3 = g8 * 4 + (i32 >> 3), cb3 = i32 & 7;
  const int rb4 = (g8 >> 2) * 4 + (i32 >> 3), cb4 = (g8 & 3) * 8 + (i32 & 7);
  p2_attn(ws, Wg, Wu, Wd, lds0, w, tag, rb3, cb3, rb4, cb4);
  bar();
  const char* gug = ws + WS_WP + (size_t)NFR_WO * 1024 + (size_t)cb4 * 122880;
  const char* wdg = ws + WS_WP + (size_t)(NFR_WO + NFR_GU) * 1024 + (size_t)cb3 * 245760;
  const int rg4 = 24 * ROT3(cb4 & 7), rg5 = 20 * ROT5(cb3);
  u32x2 hp[5];
  if (w < 4) {
    const int lane = fresh_lane();
    const unsigned voff = lane * 16;
    const int row = rb3 * 64 + w * 16 + (lane & 15), col0 = cb3 * 80 + 4 * (lane >> 4);
    STAMP_AT(2, 0);
    u32x2 xr[5];
#pragma unroll
    for (int ct = 0; ct < 5; ++ct) xr[ct] = *(const u32x2*)(x + (size_t)row * 640 + col0 + 16 * ct);
    { const int hd = lane & 31, cgt = (hd & 1) ? hd * 5 : hd * 5 + 4;
      probe(ws + WS_O + (size_t)(rb3 * 4 + w) * 20480, (unsigned)((cgt >> 3) * 1024 + ((cgt & 7) >> 1) * 256 + (cgt & 1) * 8)); }
    sfor<5>([&](auto ct) { pin(xr[ct]); });
    STAMP_AT(2, 1);
    f32x4 acc[5] = {};
    gemm_res<20, 5, 20, 2, 1, 0, wo_off, x_none>(ws + WS_O + (size_t)(rb3 * 4 + w) * 20480, ROT3(cb3), lds0, voff,
      [&](auto i) { if constexpr (i == -1) STAMP_AT(2, 2); if constexpr (i == 1) STAMP_AT(2, 3); if constexpr (i == 10) STAMP_AT(2, 4);
        if constexpr (i == P3_GO) bar(); },
      [&](auto ct, const bf16x8& b, bf16x8 (&a)[1]) { acc[ct] = MFMA16(b, a[0], acc[ct]); });
    STAMP_AT(2, 6);
    float s1 = 0.f, s2 = 0.f;
    const char* hb = ws + WS_H + (size_t)(rb3 * 4 + w) * 20480;
    const unsigned vt = t16lane(lane);
#pragma unroll
    for (int ct = 0; ct < 5; ++ct) {
      hp[ct] = nofill(pk_bf16(bf_lo(xr[ct][0]) + acc[ct][0], bf_hi(xr[ct][0]) + acc[ct][1]), pk_bf16(bf_lo(xr[ct][1]) + acc[ct][2], bf_hi(xr[ct][1]) + acc[ct][3]));
      st8s(hb + t16off(cb3 * 80 + ct * 16), vt, hp[ct]);
      s1 = dot2(hp[ct][0], 0x3f803f80u, s1); s1 = dot2(hp[ct][1], 0x3f803f80u, s1);
      s2 = dot2(hp[ct][0], hp[ct][0], s2); s2 = dot2(hp[ct][1], hp[ct][1], s2);
    }
    s1 = red4(s1); s2 = red4(s2);
    if (lane < 16) st8s(ws + WS_HST + (size_t)(rb3 * 4 + w) * 1024 + (cb3 >> 1) * 256 + (cb3 & 1) * 8, (unsigned)(lane * 16), nofill(__float_as_uint(s1), __float_as_uint(s2)));
    bar();
    STAMP_AT(2, 7);
  } else {
    const int lane = fresh_lane(), v = w - 4;
    const unsigned voff = lane * 16;
    float cpart[10];
    const int cc = v * 24 + min(lane & 31, 23);
    sfor<10>([&](auto i) { gld4<i * 384>(cpart[i], ws + WS_CSP + (size_t)cb4 * 7680, (unsigned)((lane >> 5) * 3840 + cc * 4)); });
    waitv<0>();
    float cv = 0.f;
    sfor<10>([&](auto j) { pin(cpart[j]); cv += cpart[j]; });
    cv = add32(cv);
    bar();
    STAMP_AT(2, 2);
    sfor<5>([&](auto j) { glds16<true>(gug + rotf(v * 5 + j, rg4, 120) * 1024, voff, lds0 + gu_off(0) + (v * 5 + j) * 1024); });
    sfor<10>([&](auto j) { glds16<true>(gug + rotf(20 + v * 10 + j, rg4, 120) * 1024, voff, lds0 + gu_off(20) + (v * 10 + j) * 1024); });
    STAMP_AT(2, 5);
    waitv<0>();
    bar();
    STAMP_AT(2, 6);
    sfor<15>([&](auto j) { glds16<true>(gug + rotf(60 + v * 15 + j, rg4, 120) * 1024, voff, lds0 + gu_off(60) + (v * 15 + j) * 1024); });
    if (lane < 24) ((LAS float*)(uintptr_t)(lds0 + CSF_OFF))[cc] = cv;
  }
  {
    const int lane = fresh_lane();
    const unsigned voff = lane * 16;
    const int r16 = lane & 15, g4 = lane >> 4;
    STAMP_AT(3, 0);
    f32x4 cs[6]; f32x2 ms[2];
    f32x4 hs[2];
#pragma unroll 1
    for (int spin = 0; spin < (1 << 17); ++spin) {
      gld16<0>(hs[0], ws + WS_HST + (size_t)(rb4 * 16 + w * 2) * 1024, voff);
      gld16<1024>(hs[1], ws + WS_HST + (size_t)(rb4 * 16 + w * 2) * 1024, voff);
      waitv<0>(); pin(hs[0]); pin(hs[1]);
      if (!__any(max(umax4(__builtin_bit_cast(u32x4, hs[0])), umax4(__builtin_bit_cast(u32x4, hs[1]))) == 0xffffffffu)) break;
      __builtin_amdgcn_s_sleep(1);
    }
    STAMP_AT(3, 1);
    f32x4 acc[2][6] = {};
    if (w < 4) __builtin_amdgcn_s_setprio(1);
    gemm_res<20, 6, K4_LA_, 1, 2, 20480, gu_off, x_p4>(ws + WS_H + (size_t)(rb4 * 16 + w * 2) * 20480, ROT3(cb4 & 7), lds0, voff,
      [&](auto i) { if constexpr (i == -1) STAMP_AT(3, 2); if constexpr (i == 1) STAMP_AT(3, 3); if constexpr (i == 10) STAMP_AT(3, 4);
        if constexpr (i == 9) bar();
        if constexpr (i == -1) sfor<2>([&](auto t) {
          const float s1 = red4(hs[t][0] + hs[t][2]), s2 = red4(hs[t][1] + hs[t][3]), mean = s1 * (1.f / 640.f);
          ms[t] = (f32x2){mean, __builtin_amdgcn_rsqf(fmaxf(s2 * (1.f / 640.f) - mean * mean, 0.f) + LN_EPS)}; pin(ms[t]); });
        if constexpr (i == 18) sfor<6>([&](auto ct) { cs[ct] = *(const LAS f32x4*)(uintptr_t)(lds0 + CSF_OFF + (ct * 16 + 4 * g4) * 4); });
        if constexpr (i >= 2 && i < 7) { const int f = (i - 2) * 8 + w - ((i == 6 && w >= 4) ? 4 : 0); glds16<true>(wdg + rotf(f, rg5, 240) * 1024, voff, lds0 + wd_off(0) + f * 1024); }
        if constexpr (i >= 10 && i < 15) { constexpr int n = i - 10; glds16<true>(wdg + rotf(40 + n * 8 + w, rg5, 240) * 1024, voff, lds0 + wd_off(40) + (n * 8 + w) * 1024); }
        if constexpr (i >= 15 && i < 18) { const int f = (i - 15) * 8 + w - ((i == 17 && w >= 4) ? 4 : 0); glds16<true>(wdg + rotf(140 + f, rg5, 240) * 1024, voff, lds0 + wd_off(140) + f * 1024); } },
      [&](auto ct, const bf16x8& b, bf16x8 (&a)[2]) { acc[0][ct] = MFMA16(b, a[0], acc[0][ct]); acc[1][ct] = MFMA16(b, a[1], acc[1][ct]); });
    STAMP_AT(3, 6);
    constexpr float GC0 = -2.f * 0.7978845608028654f * 1.4426950408889634f, GC1 = GC0 * 0.044715f;
    const char* gb = ws + WS_G + (size_t)(rb4 * 16 + w * 2) * 49152;
    const unsigned vt = t16lane(lane);
#pragma unroll
    for (int t = 0; t < 2; ++t) {
      const float nm = -ms[t].x, rstd = ms[t].y;
#pragma unroll
      for (int ct = 0; ct < 3; ++ct) {
        const f32x4 gv = (acc[t][ct] + cs[ct] * nm) * rstd, uv = (acc[t][3 + ct] + cs[3 + ct] * nm) * rstd;
        const f32x4 z = gv * (gv * gv * GC1 + GC0);
        f32x4 e;
#pragma unroll
        for (int j = 0; j < 4; ++j) e[j] = __builtin_amdgcn_exp2f(z[j]);
        e = e + 1.f;
#pragma unroll
        for (int j = 0; j < 4; ++j) e[j] = __builtin_amdgcn_rcpf(e[j]);
        const f32x4 r = gv * uv * e;
        st8s(gb + t * 49152 + t16off(cb4 * 48 + ct * 16), vt, nofill(pk_bf16(r[0], r[1]), pk_bf16(r[2], r[3])));
      }
    }
    __builtin_amdgcn_s_setprio(0);
    STAMP_AT(3, 7);
    waitv<6>();
    STAMP_AT(3, 8);
    bar();
  }
  if (w < 4) {
    const int lane = fresh_lane();
    const unsigned voff = lane * 16;
    const int row = rb3 * 64 + w * 16 + (lane & 15), col0 = cb3 * 80 + 4 * (lane >> 4);
    STAMP_AT(4, 0);
    probe(ws + WS_G + (size_t)(rb3 * 4 + w) * 49152, (unsigned)t16off((lane & 31) * 48 + 32));
    STAMP_AT(4, 1);
    f32x4 acc[5] = {};
    gemm_res<48, 5, K5_LA_, 2, 1, 0, wd_off, x_none>(ws + WS_G + (size_t)(rb3 * 4 + w) * 49152, ROT5(cb3), lds0, voff,
      [&](auto i) { if constexpr (i == 1) STAMP_AT(4, 3); if constexpr (i == 14) STAMP_AT(4, 4); if constexpr (i == 30) STAMP_AT(4, 5);
        if constexpr (i == -1 || i == 8 || i == 14 || i == 16 || i == 30 || i == 38) bar(); },
      [&](auto ct, const bf16x8& b, bf16x8 (&a)[1]) { acc[ct] = MFMA16(b, a[0], acc[ct]); });
    STAMP_AT(4, 12);
#pragma unroll
    for (int ct = 0; ct < 5; ++ct) {
      const unsigned p0 = pk_bf16(bf_lo(hp[ct][0]) + acc[ct][0], bf_hi(hp[ct][0]) + acc[ct][1]);
      const unsigned p1 = pk_bf16(bf_lo(hp[ct][1]) + acc[ct][2], bf_hi(hp[ct][1]) + acc[ct][3]);
      *(u32x2*)(out + (size_t)row * 640 + col0 + 16 * ct) = (u32x2){p0, p1};
    }
    STAMP_AT(4, 13);
  } else {
    const int lane = fresh_lane(), v = w - 4;
    const unsigned voff = lane * 16;
    glds16<true>(wdg + rotf(36 + v, rg5, 240) * 1024, voff, lds0 + wd_off(36) + v * 1024);
    waitv<0>(); bar();
    sfor<3>([&](auto g) { sfor<5>([&](auto j) { constexpr int f = (g + 4) * 20 + j; glds16<true>(wdg + rotf(f + v * 5, rg5, 240) * 1024, voff, lds0 + wd_off(f) + v * 5120); }); });
    bar();
    sfor<2>([&](auto g) { sfor<5>([&](auto j) { constexpr int f = (g + 8) * 20 + j; glds16<true>(wdg + rotf(f + v * 5, rg5, 240) * 1024, voff, lds0 + wd_off(f) + v * 5120); }); });
    waitv<10>(); bar();
    bar();
    sfor<2>([&](auto g) { sfor<5>([&](auto j) { constexpr int f = (g + 10) * 20 + j; glds16<true>(wdg + rotf(f + v * 5, rg5, 240) * 1024, voff, lds0 + wd_off(f) + v * 5120); }); });
    waitv<10>(); bar();
    waitv<0>(); bar();
    if (blockIdx.x == 0 && w == 4) {
      poll2(ctr + CTL_FR, (unsigned)(lane * 64), (unsigned)(4096 + lane * 64), tag);
      poll2(ctr + CTL_FR, (unsigned)(8192 + lane * 64), (unsigned)(12288 + lane * 64), tag);
      if (lane == 0) st8(ctl, tag);
    }
  }
}

extern "C" void kernel_launch(void* const* d_in, const int* in_sizes, int n_in, void* d_out, int out_size, void* d_ws, size_t ws_size,
                              hipStream_t stream) {
  k_mega<<<256, 512, 0, stream>>>((const u16*)d_in[0], (const u16*)d_in[1], (const u16*)d_in[2], (const u16*)d_in[4], (const u16*)d_in[5],
                                  (const u16*)d_in[6], (char*)d_ws, (u16*)d_out);
}
```

```cpp
#include <hip/hip_runtime.h>
#include <hip/hip_bf16.h>
#include <stdint.h>
#include <utility>
#define NO_ROT2 1

typedef short bf16x8 __attribute__((ext_vector_type(8)));
typedef short s16x4 __attribute__((ext_vector_type(4)));
typedef float f32x16 __attribute__((ext_vector_type(16)));
typedef float f32x4 __attribute__((ext_vector_type(4)));
typedef float f32x2 __attribute__((ext_vector_type(2)));
typedef unsigned u32x4 __attribute__((ext_vector_type(4)));
typedef unsigned u32x2 __attribute__((ext_vector_type(2)));
typedef __bf16 bf2_t __attribute__((ext_vector_type(2)));
typedef unsigned short u16;
#define LAS __attribute__((address_space(3)))
#define DI __device__ __forceinline__

constexpr int S_ = 2048, D_ = 640, H_ = 32, DH = 20, F_ = 1536;
constexpr float LN_EPS = 1e-5f;
constexpr float QSCALE = 0.22360679774997896f * 1.4426950408889634f;

constexpr size_t WS_Q   = 0;
constexpr size_t WS_KV  = WS_Q + 2621440;
constexpr size_t WS_O   = WS_KV + 5242880;
constexpr size_t WS_H   = WS_O + 2621440;
constexpr size_t WS_HST = WS_H + 2621440;
constexpr size_t WS_G   = WS_HST + 131072;
constexpr size_t WS_WP  = WS_G + 6291456;
constexpr size_t WS_CSP = WS_WP + 6717440;
constexpr size_t WS_CTL = WS_CSP + 245760;
constexpr size_t CTL_F1 = 256;
constexpr size_t CTL_F2 = CTL_F1 + 2048 * 8;
constexpr size_t CTL_FR = CTL_F2 + 2048 * 8;
constexpr size_t CTL_F3 = CTL_FR + 2048 * 8;
constexpr size_t CTL_F4 = CTL_F3 + 1024 * 8;
constexpr size_t CTL_REP = CTL_F4 + 2048 * 8 - 256;
constexpr int NFR_WO = 800, NFR_GU = 3840, NFR_WD = 1920, NFR_ALL = NFR_WO + NFR_GU + NFR_WD;

DI unsigned pk_bf16(float lo, float hi) { f32x2 f = {lo, hi}; return __builtin_bit_cast(unsigned, __builtin_convertvector(f, bf2_t)); }
DI float bf_lo(unsigned u) { return __uint_as_float(u << 16); }
DI float bf_hi(unsigned u) { return __uint_as_float(u & 0xffff0000u); }
DI float dot2(unsigned a, unsigned b, float c) { return __builtin_amdgcn_fdot2_f32_bf16(__builtin_bit_cast(bf2_t, a), __builtin_bit_cast(bf2_t, b), c, false); }
DI float add32(float v) { const auto r = __builtin_amdgcn_permlane32_swap(__float_as_uint(v), __float_as_uint(v), false, false); return __uint_as_float(r[0]) + __uint_as_float(r[1]); }
DI float max32(float v) { const auto r = __builtin_amdgcn_permlane32_swap(__float_as_uint(v), __float_as_uint(v), false, false); return fmaxf(__uint_as_float(r[0]), __uint_as_float(r[1])); }
DI float add16(float v) { const auto r = __builtin_amdgcn_permlane16_swap(__float_as_uint(v), __float_as_uint(v), false, false); return __uint_as_float(r[0]) + __uint_as_float(r[1]); }
DI float red4(float v) { return add32(add16(v)); }
DI float hi32(float v) { const auto r = __builtin_amdgcn_permlane32_swap(__float_as_uint(v), __float_as_uint(v), false, false); return __uint_as_float(r[1]); }
#define MFMA32(a, b, c) __builtin_amdgcn_mfma_f32_32x32x16_bf16((a), (b), (c), 0, 0, 0)
#define MFMA32K8(a, b, c) __builtin_amdgcn_mfma_f32_32x32x8bf16_1k((a), (b), (c), 0, 0, 0)
#define MFMA16(a, b, c) __builtin_amdgcn_mfma_f32_16x16x32_bf16((a), (b), (c), 0, 0, 0)

template <bool DEV> DI void glds16(const void* sbase, unsigned voff, unsigned lds_base) {
  unsigned sv;
  if constexpr (DEV)
    asm volatile("s_mov_b32 %0, m0\n\ts_mov_b32 m0, %3\n\ts_nop 0\n\tglobal_load_lds_dwordx4 %1, %2 sc1\n\ts_mov_b32 m0, %0"
                 : "=&s"(sv) : "v"(voff), "s"(sbase), "s"(lds_base) : "memory");
  else
    asm volatile("s_mov_b32 %0, m0\n\ts_mov_b32 m0, %3\n\ts_nop 0\n\tglobal_load_lds_dwordx4 %1, %2\n\ts_mov_b32 m0, %0"
                 : "=&s"(sv) : "v"(voff), "s"(sbase), "s"(lds_base) : "memory");
}
template <int OFF, class T> DI void gld16(T& dst, const void* sbase, unsigned voff) {
  asm volatile("global_load_dwordx4 %0, %1, %2 offset:%3 sc1" : "=v"(dst) : "v"(voff), "s"(sbase), "i"(OFF) : "memory");
}
template <int OFF, class T> DI void gld8(T& dst, const void* sbase, unsigned voff) {
  asm volatile("global_load_dwordx2 %0, %1, %2 offset:%3 sc1" : "=v"(dst) : "v"(voff), "s"(sbase), "i"(OFF) : "memory");
}
template <int OFF> DI void gld4(float& dst, const void* sbase, unsigned voff) {
  asm volatile("global_load_dword %0, %1, %2 offset:%3 sc1" : "=v"(dst) : "v"(voff), "s"(sbase), "i"(OFF) : "memory");
}
DI void asm_warm(u32x4& dst, const void* sbase, unsigned voff) {
  asm volatile("global_load_dwordx4 %0, %1, %2 sc1" : "+v"(dst) : "v"(voff), "s"(sbase) : "memory");
}
template <int OFF> DI void gldu16(unsigned& dst, const void* sbase, unsigned voff) {
  asm volatile("global_load_ushort %0, %1, %2 offset:%3" : "=v"(dst) : "v"(voff), "s"(sbase), "i"(OFF) : "memory");
}
DI void st16(void* p, u32x4 v) { asm volatile("global_store_dwordx4 %0, %1, off sc1\n\ts_nop 1" :: "v"(p), "v"(v) : "memory"); }
DI void st8(void* p, u32x2 v) { asm volatile("global_store_dwordx2 %0, %1, off sc1" :: "v"(p), "v"(v) : "memory"); }
DI void st4(void* p, float v) { asm volatile("global_store_dword %0, %1, off sc1" :: "v"(p), "v"(v) : "memory"); }
DI void st8s(const void* sbase, unsigned voff, u32x2 v) { asm volatile("global_store_dwordx2 %0, %1, %2 sc1" :: "v"(voff), "v"(v), "s"(sbase) : "memory"); }
DI u32x2 nofill(unsigned a, unsigned b) { return (u32x2){min(a, 0xfffffffeu), min(b, 0xfffffffeu)}; }
DI unsigned umax4(u32x4 v) { return max(max(v[0], v[1]), max(v[2], v[3])); }
template <int N> DI void waitv() { asm volatile("s_waitcnt vmcnt(%0)" :: "i"(N) : "memory"); }
DI void bar() { asm volatile("s_waitcnt lgkmcnt(0)\n\ts_barrier" ::: "memory"); }
DI void setflag(char* flag, u32x2 tag, int lane) { if (lane < 8) st8(flag + lane * CTL_REP, tag); }
DI void publish(char* flag, u32x2 tag, int lane) { waitv<0>(); setflag(flag, tag, lane); }
DI void poll(const char* base, unsigned voff, u32x2 tag) {
#pragma unroll 1
  for (int spin = 0; spin < (1 << 17); ++spin) {
    u32x2 v;
    asm volatile("global_load_dwordx2 %0, %1, %2 sc1\n\ts_waitcnt vmcnt(0)" : "=v"(v) : "v"(voff), "s"(base) : "memory");
    if (__all(v[0] == tag[0] && v[1] == tag[1])) break;
    __builtin_amdgcn_s_sleep(1);
  }
}
DI void poll2(const char* base, unsigned voff0, unsigned voff1, u32x2 tag) {
#pragma unroll 1
  for (int spin = 0; spin < (1 << 17); ++spin) {
    u32x2 v, u;
    asm volatile("global_load_dwordx2 %0, %2, %4 sc1\n\tglobal_load_dwordx2 %1, %3, %4 sc1\n\ts_waitcnt vmcnt(0)" : "=&v"(v), "=&v"(u) : "v"(voff0), "v"(voff1), "s"(base) : "memory");
    if (__all(v[0] == tag[0] && v[1] == tag[1] && u[0] == tag[0] && u[1] == tag[1])) break;
    __builtin_amdgcn_s_sleep(1);
  }
}
DI void probe(const char* base, unsigned voff) {
#pragma unroll 1
  for (int spin = 0; spin < (1 << 17); ++spin) {
    unsigned v;
    asm volatile("global_load_dword %0, %1, %2 sc1\n\ts_waitcnt vmcnt(0)" : "=v"(v) : "v"(voff), "s"(base) : "memory");
    if (__all(v != 0xffffffffu)) break;
    __builtin_amdgcn_s_sleep(1);
  }
}
constexpr int t16off(int c0) { return (c0 >> 5) * 1024 + (c0 & 16) * 32; }
DI unsigned t16lane(int lane) { return (unsigned)((lane >> 5) * 256 + (lane & 15) * 16 + ((lane >> 4) & 1) * 8); }
DI int rotf(int f, int off, int n) { const int r = f + off; return r - (r >= n ? n : 0); }
DI void st16s(const void* sbase, unsigned voff, u32x4 v) { asm volatile("global_store_dwordx4 %0, %1, %2 sc1\n\ts_nop 1" :: "v"(voff), "v"(v), "s"(sbase) : "memory"); }
DI float row16_sum(float v) {
  v += __builtin_bit_cast(float, __builtin_amdgcn_update_dpp(0, __builtin_bit_cast(int, v), 0x128, 0xf, 0xf, false));
  v += __builtin_bit_cast(float, __builtin_amdgcn_update_dpp(0, __builtin_bit_cast(int, v), 0x124, 0xf, 0xf, false));
  v += __builtin_bit_cast(float, __builtin_amdgcn_update_dpp(0, __builtin_bit_cast(int, v), 0x122, 0xf, 0xf, false));
  v += __builtin_bit_cast(float, __builtin_amdgcn_update_dpp(0, __builtin_bit_cast(int, v), 0x121, 0xf, 0xf, false));
  return v;
}
#define ROT3(c) 0
#define ROT5(c) 0
DI int fresh_lane() { int l = __builtin_amdgcn_mbcnt_hi(~0u, __builtin_amdgcn_mbcnt_lo(~0u, 0u)); asm volatile("" : "+v"(l)); return l; }
template <class T> DI void pin(T& v) { asm volatile("" : "+v"(v)); }
DI bf16x8 lds128(unsigned addr) { return *(const LAS bf16x8*)(uintptr_t)addr; }
DI s16x4 lds64(unsigned addr) { return *(const LAS s16x4*)(uintptr_t)addr; }
DI s16x4 ldstr(unsigned addr) { return __builtin_amdgcn_ds_read_tr16_b64_v4i16((LAS s16x4*)(uintptr_t)addr); }
DI unsigned lds_addr_of(const void* p) { return (unsigned)(uintptr_t)(const LAS char*)p; }
#define STAMP_AT(kid, idx) ((void)0)
#define RETRY_COUNT(k) ((void)0)
#define WAITV_BAR(N) asm volatile("s_waitcnt vmcnt(" #N ") lgkmcnt(0)\n\ts_barrier" ::: "memory")
#define SB() __builtin_amdgcn_sched_barrier(0)
template <int N> using IC = std::integral_constant<int, N>;
template <class F, int... Is> DI void sfor_(F&& f, std::integer_sequence<int, Is...>) { (f(IC<Is>{}), ...); }
template <int N, class F> DI void sfor(F&& f) { sfor_(f, std::make_integer_sequence<int, N>{}); }
template <int N> DI void waitv_bar() { asm volatile("s_waitcnt vmcnt(%0) lgkmcnt(0)\n\ts_barrier" :: "i"(N) : "memory"); }

#define K1_LA_ 10
#define P3_GO 10
#define K4_LA_ 5
#define K5_LA_ 24
constexpr int K1_STAGE = 32768, K1_NST = 4, K1_LA = K1_LA_;
template <bool ISV>
DI void p1_body(const u16* __restrict__ x, const u16* __restrict__ W, char* __restrict__ ws, const unsigned lds0, const int w, int rb, int cb) {
  const int lane = fresh_lane();
  const int wm = w >> 2, wn = w & 3;
  const int r32 = lane & 31, hh = lane >> 5;
  const unsigned a_voff0 = (unsigned)((lane >> 3) * 1280 + (((lane & 7) ^ ((lane >> 4) & 7)) << 4));
  const unsigned a_voff1 = (unsigned)((lane >> 3) * 1280 + (((lane & 7) ^ ((4 + (lane >> 4)) & 7)) << 4)) + 8 * 1280;
  const unsigned w_voff0 = (unsigned)((lane >> 3) * 3840 + (((lane & 7) ^ (((lane >> 4) & 1) << 2)) << 4));
  const unsigned w_voff1 = w_voff0 + 8 * 3840;
  const char* xs = (const char*)x + (size_t)(rb * 128 + w * 16) * 1280;
  const char* wsrc = (const char*)W + (size_t)((w & 3) * 16) * 3840 + (size_t)(cb * 128 + (w >> 2) * 64) * 2;
  const unsigned dst0 = lds0 + w * 2048;
#define ROTR 10
  const int rot = ((cb + 3 * rb) % ROTR) * (10 / ROTR);
  auto dma = [&](auto n_) {
    constexpr int n = n_, s = n >> 2, j = n & 3; constexpr unsigned so = (s % K1_NST) * K1_STAGE;
    const int sr = s + rot - (s + rot >= 10 ? 10 : 0);
    if constexpr (j == 0) glds16<false>(xs + sr * 128, a_voff0, dst0 + so);
    if constexpr (j == 1) glds16<false>(wsrc + (size_t)sr * 64 * 3840, w_voff0, dst0 + so + 16384);
    if constexpr (j == 2) glds16<false>(xs + sr * 128, a_voff1, dst0 + so + 1024);
    if constexpr (j == 3) glds16<false>(wsrc + (size_t)sr * 64 * 3840, w_voff1, dst0 + so + 16384 + 1024);
  };
  STAMP_AT(0, 0);
  sfor<K1_LA>([&](auto n) { dma(n); });
  const int ob = (wn >> 1) ^ wm;
  const int fsel = wn & 1;
  unsigned xa[2][4], xb[2][4], wa[2][2];
  {
    const int row = wm * 64 + fsel * 32 + r32;
    const int blk = (lane >> 4) & 1, q = (lane & 15) >> 2, p = lane & 3;
    const int c = (wn & 1) * 4 + blk * 2 + (p >> 1);
    const unsigned base = lds0 + (unsigned)(16384 + (wn >> 1) * 8192 + (8 * hh + q) * 128 + ((c ^ (((q >> 1) & 1) << 2)) << 4) + (p & 1) * 8);
#pragma unroll
    for (int hs = 0; hs < 2; ++hs) {
#pragma unroll
      for (int i = 0; i < 4; ++i) {
        xa[hs][i] = lds0 + hs * 65536 + (unsigned)(row * 128 + ((((i ^ ob) * 2 + hh) ^ ((row >> 1) & 7)) << 4));
        xb[hs][i] = xa[hs][i] + (fsel ? -4096 : 4096);
      }
      wa[hs][0] = base + hs * 65536 + ob * 2048; wa[hs][1] = base + hs * 65536 + (1 ^ ob) * 2048;
    }
  }
  f32x16 acc0 = {}, acc1 = {};
  float sx = 0.f, sxx = 0.f, sw = 0.f;
  bf16x8 fx0[4], fx1[4], fw[4];
  auto rd = [&](auto i_) {
    constexpr int i = i_, s = i >> 2, p = i & 3, sl = s % K1_NST, hs = sl >> 1; constexpr unsigned so = (sl & 1) * K1_STAGE;
    fx0[p] = lds128(xa[hs][p] + so);
    const s16x4 wl = ldstr(wa[hs][p & 1] + so + (p >> 1) * 4096);
    const s16x4 wh = ldstr(wa[hs][p & 1] + so + (p >> 1) * 4096 + 512);
    fw[p] = __builtin_shufflevector(wl, wh, 0, 1, 2, 3, 4, 5, 6, 7);
    fx1[p] = lds128(xb[hs][p] + so);
  };
  const bool isq = cb < 5;
  const int pitch = isq ? 1280 : 2560;
  unsigned off[4];
  if (ISV) {
    const int n = cb * 128 + wn * 32 + r32 - 1280, head = n / 20, d0 = n - head * 20;
    const int d = (head & 1) ? (d0 < 4 ? d0 + 16 : d0 - 4) : d0;
    off[0] = (unsigned)(head * 163840 + 1280 + (hh * 20 + d) * 16); pin(off[0]);
  } else {
#pragma unroll
    for (int g = 0; g < 4; ++g) {
      const int n = cb * 128 + wn * 32 + 8 * g + 4 * hh - (isq ? 0 : 640), head = n / 20, d0 = n - head * 20;
      const int d = (head & 1) ? (d0 < 4 ? d0 + 16 : d0 - 4) : d0;
      off[g] = (unsigned)(head * 64 * pitch + (d < 16 ? (((d >> 3) * 32 + r32) * 16 + (d & 4) * 2) : (1024 + r32 * 8))); pin(off[g]);
    }
  }
  waitv_bar<K1_LA - 4>();
  STAMP_AT(0, 1);
  rd(IC<0>{}); rd(IC<1>{});
  sfor<40>([&](auto i_) {
    constexpr int i = i_, p = i & 3;
    if constexpr ((i + 2) % 4 == 0 && i + 2 < 40) { constexpr int last = (i - 1 + K1_LA < 39 ? i - 1 + K1_LA : 39); waitv_bar<(last - (i + 5) > 0 ? last - (i + 5) : 0)>(); }
    if constexpr (i + K1_LA < 40) dma(IC<i + K1_LA>{});
    if constexpr (i + 2 < 40) rd(IC<i + 2>{});
    SB();
    if (ISV) { acc0 = MFMA32(fx0[p], fw[p], acc0); acc1 = MFMA32(fx1[p], fw[p], acc1); }
    else     { acc0 = MFMA32(fw[p], fx0[p], acc0); acc1 = MFMA32(fw[p], fx1[p], acc1); }
    if constexpr ((p & 1) == 0) {
      const u32x4 u = __builtin_bit_cast(u32x4, fx0[p]);
#pragma unroll
      for (int j = 0; j < 4; ++j) { sx = dot2(u[j], 0x3f803f80u, sx); sxx = dot2(u[j], u[j], sxx); }
    } else {
      const u32x4 u = __builtin_bit_cast(u32x4, fw[p]);
#pragma unroll
      for (int j = 0; j < 4; ++j) sw = dot2(u[j], 0x3f803f80u, sw);
    }
    SB();
  });
  STAMP_AT(0, 11);
  sx = add32(sx); sxx = add32(sxx); sw = add32(sw);
  LAS f32x2* part = (LAS f32x2*)(uintptr_t)(lds0 + 65536);
  LAS float* csp = (LAS float*)(uintptr_t)(lds0 + 65536 + 2048);
  if (hh == 0) { part[(wn >> 1) * 128 + wm * 64 + fsel * 32 + r32] = (f32x2){sx, sxx}; }
  else         { csp[wm * 128 + wn * 32 + r32] = sw; }
  bar();
  STAMP_AT(0, 12);
  auto stat = [&](int rl) { const f32x2 a = part[rl], b = part[128 + rl];
    const float mean = (a.x + b.x) * (1.f / 640.f);
    const float var = fmaxf((a.y + b.y) * (1.f / 640.f) - mean * mean, 0.f);
    return (f32x2){mean, __builtin_amdgcn_rsqf(var + LN_EPS)}; };
  if (ISV) {
    LAS f32x2* st = (LAS f32x2*)(uintptr_t)(lds0 + 65536 + 4096 + w * 512);
    st[lane] = stat(wm * 64 + lane);
    const int nl = wn * 32 + r32;
    const float c = csp[nl] + csp[128 + nl];
#pragma unroll
    for (int t = 0; t < 2; ++t) {
      const f32x16& a = t ? acc1 : acc0;
      const int rl = (t ^ fsel) * 32;
      unsigned pk[8];
#pragma unroll
      for (int g = 0; g < 4; ++g) {
        const f32x4 s01 = *(const LAS f32x4*)&st[rl + 8 * g + 4 * hh], s23 = *(const LAS f32x4*)&st[rl + 8 * g + 4 * hh + 2];
        pk[2 * g]     = pk_bf16(s01[1] * (a[4 * g] - s01[0] * c), s01[3] * (a[4 * g + 1] - s01[2] * c));
        pk[2 * g + 1] = pk_bf16(s23[1] * (a[4 * g + 2] - s23[0] * c), s23[3] * (a[4 * g + 3] - s23[2] * c));
      }
      const char* dst = ws + WS_KV + (size_t)(rb * 4 + wm * 2 + (rl >> 5)) * 2560;
      st16s(dst, off[0], (u32x4){pk[0], pk[1], pk[2], pk[3]});
      st16s(dst + 640, off[0], (u32x4){pk[4], pk[5], pk[6], pk[7]});
    }
  } else {
    const float sc = isq ? QSCALE : 1.f;
    const char* base = ws + (isq ? WS_Q : WS_KV);
    f32x4 c[4];
#pragma unroll
    for (int g = 0; g < 4; ++g) { const int nl = wn * 32 + 8 * g + 4 * hh; c[g] = *(const LAS f32x4*)&csp[nl] + *(const LAS f32x4*)&csp[128 + nl]; }
#pragma unroll
    for (int t = 0; t < 2; ++t) {
      const f32x16& a = t ? acc1 : acc0;
      const int rl = wm * 64 + (t ^ fsel) * 32;
      const f32x2 ms = stat(rl + r32);
      const float rs = ms.y * sc, mr = ms.x;
      const char* tb = base + (size_t)(rb * 4 + (rl >> 5)) * pitch;
#pragma unroll
      for (int g = 0; g < 4; ++g) {
        const unsigned p0 = pk_bf16(rs * (a[4 * g] - mr * c[g][0]), rs * (a[4 * g + 1] - mr * c[g][1]));
        const unsigned p1 = pk_bf16(rs * (a[4 * g + 2] - mr * c[g][2]), rs * (a[4 * g + 3] - mr * c[g][3]));
        st8s(tb, off[g], (u32x2){p0, p1});
      }
    }
  }
  STAMP_AT(0, 13);
}

DI void p1_repack(const u16* __restrict__ Wo, char* __restrict__ ws, const int w, int idx) {
  const int lane = fresh_lane();
  const unsigned pvo = (lane >> 4) * 8 * 1280 + (lane & 15) * 2;
  unsigned pw[7][8];
  sfor<7>([&](auto r) {
    const int f = idx * 50 + min(w + 8 * r, 49), cbk = f / 100, rem = f - cbk * 100, ks = rem / 5, ct = rem - ks * 5;
    const char* src = (const char*)(Wo + (size_t)(32 * ks) * 640 + 80 * cbk + 16 * ct);
    sfor<8>([&](auto i) { gldu16<(i & 3) * 1280>(pw[r][i], src + (i & ~3) * 1280, pvo); });
  });
  sfor<7>([&](auto r) {
    waitv<(6 - r) * 8>();
    sfor<8>([&](auto j) { pin(pw[r][j]); });
    const u32x4 v = {pw[r][0] | (pw[r][1] << 16), pw[r][2] | (pw[r][3] << 16), pw[r][4] | (pw[r][5] << 16), pw[r][6] | (pw[r][7] << 16)};
    pw[r][0] = v[0]; pw[r][1] = v[1]; pw[r][2] = v[2]; pw[r][3] = v[3];
  });
  sfor<7>([&](auto r) {
    if (w + 8 * r < 50) st16(ws + WS_WP + (size_t)(idx * 50 + w + 8 * r) * 1024 + lane * 16, (u32x4){pw[r][0], pw[r][1], pw[r][2], pw[r][3]});
  });
}

template <int OFF> DI void rdK(bf16x8& k0, s16x4& k1, unsigned a0, unsigned a1) {
  asm volatile("ds_read_b128 %0, %2 offset:%4\n\tds_read_b64 %1, %3 offset:%4" : "=&v"(k0), "=&v"(k1) : "v"(a0), "v"(a1), "i"(OFF));
}
template <int OFF> DI void rdV(bf16x8& v0, bf16x8& v1, unsigned a, unsigned long long msk) {
  asm volatile("s_mov_b64 exec, %5\n\tds_read_b128 %0, %2 offset:%3\n\tds_read_b128 %1, %2 offset:%4\n\ts_mov_b64 exec, -1"
               : "+v"(v0), "+v"(v1) : "v"(a), "i"(OFF), "i"(OFF + 640), "s"(msk));
}
#define EX2(v) __builtin_amdgcn_exp2f(v)
constexpr int wo_off(int p) { return (p < 60 ? p : p + 20) * 1024; }
constexpr int gu_off(int f) { return (f < 20 ? f + 60 : f < 60 ? f + 100 : f - 60) * 1024; }
constexpr int wd_off(int f) { return ((f / 20 + 4) % 8) * 20480 + (f % 20) * 1024; }
constexpr int CSF_OFF = 116 * 1024, XCH_OFF = 120 * 1024;

DI void p2_attn(char* __restrict__ ws, const u16* __restrict__ Wg, const u16* __restrict__ Wu, const u16* __restrict__ Wd,
                const unsigned lds0, const int w, const u32x2 tag, const int rb3, const int cb3, const int rb4, const int cb4) {
  const int lane = fresh_lane();
  const int kh = w >> 2, wq = w & 3;
  const int head = (blockIdx.x & 7) * 4 + ((blockIdx.x >> 3) & 3), qb = blockIdx.x >> 5;
  const int r32 = lane & 31, hh = lane >> 5;
  const char* kvg = ws + WS_KV + (size_t)head * 163840 + (size_t)kh * 81920 + wq * 1024;
  const unsigned voff = lane * 16;
  const unsigned kvl = lds0 + kh * 81920 + wq * 1024;
  char* ctl = ws + WS_CTL;
  const char* ctr = ctl + (blockIdx.x & 7) * CTL_REP;
  STAMP_AT(1, 0);
  {
    const int cfl = (20 * head) >> 5, cfh = (20 * head + 19) >> 5;
    const unsigned f0 = (unsigned)(((((lane & 32) ? 40 : 20) + ((lane & 16) ? cfh : cfl)) * 32 + kh * 16 + (lane & 15)) * 8);
    const unsigned f1 = (unsigned)((lane < 32 ? (((lane & 1) ? cfh : cfl) * 32 + qb * 4 + wq) : 1920 + cb3 * 16 + (lane & 15)) * 8);
    poll2(ctr + CTL_F1, f0, f1, tag);
  }
  STAMP_AT(1, 4);
#pragma unroll
  for (int i = 0; i < 4; ++i) glds16<true>(kvg + i * 4096, voff, kvl + i * 4096);
  bf16x8 q0[2]; s16x4 q1[2];
  {
    const char* qg = ws + WS_Q + ((size_t)head * 64 + qb * 8 + wq * 2) * 1280;
    gld16<0>(q0[0], qg, voff); gld16<1280>(q0[1], qg, voff); gld8<1024>(q1[0], qg, r32 * 8); gld8<2304>(q1[1], qg, r32 * 8);
  }
  const unsigned ka0 = lds0 + kh * 81920 + lane * 16, ka1 = lds0 + kh * 81920 + 1024 + r32 * 8, va = lds0 + kh * 81920 + 1280 + (hh * 20 + (r32 < 20 ? r32 : 0)) * 16;
  bf16x8 K0[2], V0[2], V1[2]; s16x4 K1[2];
  V0[0] = V0[1] = V1[0] = V1[1] = (bf16x8)(short)(r32 == 20 ? 0x3f80 : 0);
  pin(V0[0]); pin(V0[1]); pin(V1[0]); pin(V1[1]);
  WAITV_BAR(0);
  pin(q0[0]); pin(q0[1]); pin(q1[0]); pin(q1[1]);
  if (hh) { q1[0] = (s16x4){0, 0, 0, 0}; q1[1] = (s16x4){0, 0, 0, 0}; }
  STAMP_AT(1, 1);
  rdK<0>(K0[0], K1[0], ka0, ka1);
  const unsigned long long vmsk = 0x000fffff000fffffull;
  rdV<0>(V0[0], V1[0], va, vmsk);
  f32x16 cm[2];
  {
    const bf16x8 kf0 = lds128(lds0 + lane * 16); const s16x4 kf1 = lds64(lds0 + 1024 + r32 * 8);
#pragma unroll
    for (int t = 0; t < 2; ++t) {
      f32x16 s = MFMA32(kf0, q0[t], (f32x16){}); s = MFMA32K8(kf1, q1[t], s);
      float m = s[0];
#pragma unroll
      for (int i = 1; i < 16; ++i) m = fmaxf(m, s[i]);
      m = max32(m);
      cm[t] = (f32x16)(-m);
      pin(cm[t]);
    }
  }
  asm volatile("s_waitcnt lgkmcnt(0)" : "+v"(K0[0]), "+v"(K1[0]), "+v"(V0[0]), "+v"(V1[0]));
  f32x16 o[2] = {};
  f32x16 SA, SB_;
  u32x4 PA0, PA1, PB0 = {}, PB1 = {};
  SA = MFMA32(K0[0], q0[0], cm[0]); SA = MFMA32K8(K1[0], q1[0], SA);
  STAMP_AT(1, 2);
  pin(o[0]); pin(o[1]); pin(PB0); pin(PB1);
#define GRP(MF, Sc, Pc, i, j) do { MF; SB(); const float e0 = EX2(Sc[i]), e1 = EX2(Sc[i + 1]), e2 = EX2(Sc[i + 2]), e3 = EX2(Sc[i + 3]); \
    Pc[j] = pk_bf16(e0, e1); Pc[j + 1] = pk_bf16(e2, e3); pin(Pc); SB(); } while (0)
#define SLOT(QK, Sc, Pc0, Pc1, Sn, kb, jn, Pp0, Pp1, vb, jp) do { \
    GRP(if constexpr (QK) Sn = MFMA32(K0[kb], q0[jn], cm[jn]), Sc, Pc0, 0, 0); \
    GRP(if constexpr (QK) Sn = MFMA32K8(K1[kb], q1[jn], Sn), Sc, Pc0, 4, 2); \
    GRP(o[jp] = MFMA32(V0[vb], __builtin_bit_cast(bf16x8, Pp0), o[jp]), Sc, Pc1, 8, 0); \
    GRP(o[jp] = MFMA32(V1[vb], __builtin_bit_cast(bf16x8, Pp1), o[jp]), Sc, Pc1, 12, 2); } while (0)
#define LWAIT(kb, vb) asm volatile("s_waitcnt lgkmcnt(0)" : "+v"(K0[kb]), "+v"(K1[kb]), "+v"(V0[vb]), "+v"(V1[vb]))
  unsigned pw[3][8];
  const int gw = blockIdx.x * 8 + w, gwc = min(gw, 1919);
  const char* psrc[3];
#pragma unroll
  for (int r = 0; r < 2; ++r) {
    const int f = 2 * gwc + r, cbk = f / 120, rem = f - cbk * 120, ks = rem / 6, ct = rem - ks * 6;
    psrc[r] = (const char*)((ct < 3 ? Wg : Wu) + (size_t)(32 * ks) * 1536 + 48 * cbk + 16 * (ct < 3 ? ct : ct - 3));
  }
  { const int f = gwc, cbk = f / 240, rem = f - cbk * 240, ks = rem / 5, ct = rem - ks * 5;
    psrc[2] = (const char*)(Wd + (size_t)(32 * ks) * 640 + 80 * cbk + 16 * ct); }
  const unsigned pvo1 = (lane >> 4) * 8 * 3072 + (lane & 15) * 2, pvo2 = (lane >> 4) * 8 * 1280 + (lane & 15) * 2;
  const char* wog = ws + WS_WP + (size_t)cb3 * 102400;
  auto rstore = [&](auto r_, auto yv_) {
    constexpr int r = r_;
    waitv<yv_>();
    sfor<8>([&](auto j) { pin(pw[r][j]); });
    const u32x4 v = {pw[r][0] | (pw[r][1] << 16), pw[r][2] | (pw[r][3] << 16), pw[r][4] | (pw[r][5] << 16), pw[r][6] | (pw[r][7] << 16)};
    if (gw < 1920) {
      if constexpr (r < 2) {
        st16(ws + WS_WP + (size_t)(NFR_WO + 2 * gw + r) * 1024 + lane * 16, v);
        float c = 0.f;
#pragma unroll
        for (int j = 0; j < 4; ++j) c = dot2(v[j], 0x3f803f80u, c);
        c = red4(c);
        if (lane < 16) st4(ws + WS_CSP + (size_t)(2 * gw + r) * 64 + lane * 4, c);
      } else st16(ws + WS_WP + (size_t)(NFR_WO + NFR_GU + gw) * 1024 + lane * 16, v);
    }
  };
  const int qtile = qb * 8 + wq * 2 + kh, cg0 = head * 5 + (head & 1), cgt = (head & 1) ? head * 5 : head * 5 + 4;
  const char* ob = ws + WS_O + (size_t)qtile * 40960;
  const char* obs[3] = {ob + (cg0 >> 3) * 1024 + ((cg0 & 7) >> 1) * 256, ob + ((cg0 + 2) >> 3) * 1024 + (((cg0 + 2) & 7) >> 1) * 256,
                        ob + (cgt >> 3) * 1024 + ((cgt & 7) >> 1) * 256 + (cgt & 1) * 8};
  unsigned vo = (unsigned)((r32 >> 4) * 20480 + (r32 & 15) * 16 + hh * 8); pin(vo);
  u32x4 fill = {0xffffffffu, 0xffffffffu, 0xffffffffu, 0xffffffffu}; pin(fill);
  const u32x2 fill2 = {fill[0], fill[1]};
  u32x2 fa, fb, fc;
  const unsigned fvc = (unsigned)CTL_FR + (unsigned)((cb4 * 60 + min(lane, 59)) * 8);
  const unsigned fva = (unsigned)(hh ? CTL_F4 : CTL_F2) + (unsigned)(((rb3 * 2 + ((w & 3) >> 1)) * 32 + r32) * 8), fvb = lane < 32 ? (unsigned)CTL_F3 + (unsigned)(((rb4 * 16 + w * 2) * 8 + (lane & 15)) * 8) : (unsigned)CTL_FR + (unsigned)((cb3 * 240 + w * 30 + min(lane - 32, 29)) * 8);
  auto hook = [&](auto it_, auto i_) {
    constexpr int it = it_, i = i_, n = (it - 2) * 8 + i;
    if constexpr (n >= 0 && n < 2) st8s(obs[n], vo, fill2);
    if constexpr (n == 2) { if (!hh) st8s(obs[2], vo, fill2); }
    if constexpr (n >= 3 && n < 6) { const int L = (n - 3) * 64 + lane; st16s(ws + WS_G + (size_t)(rb4 * 16 + w * 2) * 49152 + cb4 * 1536, (unsigned)(L * 16 + (L >= 96 ? 49152 - 1536 : 0)), fill); }
    if constexpr (n >= 6 && n < 9) { if (w < 4) st16s(ws + WS_H + (size_t)(rb3 * 4 + w) * 20480 + cb3 * 2560, (unsigned)(min((n - 6) * 64 + lane, 159) * 16), fill); }
    if constexpr (n == 9) { if (w < 4 && lane < 16) st8s(ws + WS_HST + (size_t)(rb3 * 4 + w) * 1024 + (cb3 >> 1) * 256 + (cb3 & 1) * 8, (unsigned)(lane * 16), fill2); }
    if constexpr (it == 6 && i == 6) {
      waitv<6>();
      setflag(ctl + CTL_FR + gw * 8, tag, lane);
      setflag(ctl + CTL_F2 + (qtile * 32 + head) * 8, tag, lane);
      setflag(ctl + CTL_F4 + ((rb4 * 8 + w) * 32 + cb4) * 8, tag, lane);
      if (w < 4) setflag(ctl + CTL_F3 + ((rb3 * 4 + w) * 8 + cb3) * 8, tag, lane);
    }
    if constexpr (it == 7 && i == 6) { gld8<0>(fa, ctr, fva); gld8<0>(fb, ctr, fvb); gld8<0>(fc, ctr, fvc); }
    if constexpr (it < 2) glds16<true>(kvg + (size_t)(it * 8 + 4 + i) * 4096, voff, kvl + (it * 8 + 4 + i) * 4096);
    else if constexpr (it < 4) gldu16<(i & 1) * 3072>(pw[it - 2][i], psrc[it - 2] + (i & ~1) * 3072, pvo1);
    else if constexpr (it == 4) {
      gldu16<(i & 3) * 1280>(pw[2][i], psrc[2] + (i & ~3) * 1280, pvo2);
      if constexpr (i == 0) rstore(IC<0>{}, IC<9>{});
      if constexpr (i == 7) rstore(IC<1>{}, IC<8>{});
    } else if constexpr (it == 5) {
      if constexpr (i == 7) rstore(IC<2>{}, IC<2>{});
    } else {
      constexpr int j = (it - 6) * 8 + i;
      const char* src = wog + rotf(w + 8 * j, 20 * ROT3(cb3), 100) * 1024;
      if constexpr (j < 7) glds16<true>(src, voff, lds0 + w * 1024 + j * 8192);
      else if constexpr (j == 7) glds16<true>(src, voff, lds0 + w * 1024 + j * 8192 + (w >= 4 ? 20480 : 0));
      else if constexpr (j < 12) glds16<true>(src, voff, lds0 + w * 1024 + j * 8192 + 20480);
      else if constexpr (j == 12) { if (w < 4) glds16<true>(src, voff, lds0 + w * 1024 + j * 8192 + 20480); }
    }
    SB();
  };
  const unsigned ka0b = ka0 + 40960, ka1b = ka1 + 40960, vab = va + 40960;
  sfor<8>([&](auto it_) {
    constexpr int it = it_;
    if constexpr (it == 1) WAITV_BAR(6);
    if constexpr (it == 2) { WAITV_BAR(8); STAMP_AT(1, 3); }
    if constexpr (it == 4) asm volatile("s_waitcnt vmcnt(16)\n\ts_barrier" ::: "memory");
    if constexpr (it == 6) asm volatile("s_barrier" ::: "memory");
    if (((it ^ kh) & 1) == 0) __builtin_amdgcn_s_setprio(0); else __builtin_amdgcn_s_setprio(1);
    constexpr int o0 = (it & 3) * 10240;
    constexpr int on = it == 3 ? 0 : o0 + 10240;
    const unsigned a0 = it < 4 ? ka0 : ka0b, a1 = it < 4 ? ka1 : ka1b, av = it < 4 ? va : vab;
    const unsigned n0 = it < 3 ? ka0 : ka0b, n1 = it < 3 ? ka1 : ka1b, nv = it < 3 ? va : vab;
    rdK<o0 + 2560>(K0[1], K1[1], a0, a1); hook(it_, IC<0>{});                  SLOT(true, SA, PA0, PA1, SB_, 0, 1, PB0, PB1, 1, 1);
    LWAIT(1, 0); rdV<o0 + 2560>(V0[1], V1[1], av, vmsk); hook(it_, IC<1>{});   SLOT(true, SB_, PB0, PB1, SA, 1, 0, PA0, PA1, 0, 0);
    rdK<o0 + 5120>(K0[0], K1[0], a0, a1); hook(it_, IC<2>{});                  SLOT(true, SA, PA0, PA1, SB_, 1, 1, PB0, PB1, 0, 1);
    LWAIT(0, 1); rdV<o0 + 5120>(V0[0], V1[0], av, vmsk); hook(it_, IC<3>{});   SLOT(true, SB_, PB0, PB1, SA, 0, 0, PA0, PA1, 1, 0);
    rdK<o0 + 7680>(K0[1], K1[1], a0, a1); hook(it_, IC<4>{});                  SLOT(true, SA, PA0, PA1, SB_, 0, 1, PB0, PB1, 1, 1);
    LWAIT(1, 0); rdV<o0 + 7680>(V0[1], V1[1], av, vmsk); hook(it_, IC<5>{});   SLOT(true, SB_, PB0, PB1, SA, 1, 0, PA0, PA1, 0, 0);
    if constexpr (it < 7) rdK<on>(K0[0], K1[0], n0, n1);
    hook(it_, IC<6>{});                                                        SLOT(true, SA, PA0, PA1, SB_, 1, 1, PB0, PB1, 0, 1);
    LWAIT(0, 1);
    if constexpr (it < 7) rdV<on>(V0[0], V1[0], nv, vmsk);
    hook(it_, IC<7>{});                                                        SLOT(it < 7, SB_, PB0, PB1, SA, 0, 0, PA0, PA1, 1, 0);
  });
  __builtin_amdgcn_s_setprio(0);
  o[1] = MFMA32(V0[1], __builtin_bit_cast(bf16x8, PB0), o[1]);
  o[1] = MFMA32(V1[1], __builtin_bit_cast(bf16x8, PB1), o[1]);
  STAMP_AT(1, 9);
  bar();
  {
    LAS f32x4* xch = (LAS f32x4*)(uintptr_t)(lds0 + XCH_OFF + w * 3072);
    const f32x16& snd = kh ? o[0] : o[1];
#pragma unroll
    for (int g = 0; g < 3; ++g) xch[g * 64 + lane] = (f32x4){snd[4 * g], snd[4 * g + 1], snd[4 * g + 2], snd[4 * g + 3]};
  }
  bar();
  STAMP_AT(1, 10);
  float of[12];
  {
    const LAS f32x4* xch = (const LAS f32x4*)(uintptr_t)(lds0 + XCH_OFF + (w ^ 4) * 3072);
    const f32x16& own = kh ? o[1] : o[0];
#pragma unroll
    for (int g = 0; g < 3; ++g) { const f32x4 v = xch[g * 64 + lane];
#pragma unroll
      for (int j = 0; j < 4; ++j) of[4 * g + j] = own[4 * g + j] + v[j]; }
  }
  float l = hi32(of[8]);
  bool ok = (l > 8.67e-19f) && (l < 1.15e18f);
#pragma unroll
  for (int i = 0; i < 8; ++i) ok = ok && (fabsf(of[i]) < 1e37f);
  if (!hh) {
#pragma unroll
    for (int i = 8; i < 12; ++i) ok = ok && (fabsf(of[i]) < 1e37f);
  }
  if (__builtin_expect(!__all(ok), 0)) {
    __builtin_amdgcn_fence(__ATOMIC_ACQUIRE, "agent");
    const char* kv = ws + WS_KV + (size_t)head * 163840;
    const bf16x8 fq0 = kh ? q0[1] : q0[0];
    const s16x4 fq1 = kh ? q1[1] : q1[0];
    float m = -INFINITY;
    for (int t = 0; t < 64; ++t) {
      const bf16x8 kf0 = *(const bf16x8*)(kv + t * 2560 + lane * 16); const s16x4 kf1 = *(const s16x4*)(kv + t * 2560 + 1024 + r32 * 8);
      f32x16 s = MFMA32(kf0, fq0, (f32x16){}); s = MFMA32K8(kf1, fq1, s);
#pragma unroll
      for (int i = 0; i < 16; ++i) m = fmaxf(m, s[i]);
    }
    m = max32(m);
    f32x16 oo = {};
    const int vr = r32 < 20 ? r32 : 0;
    for (int t = 0; t < 64; ++t) {
      const bf16x8 kf0 = *(const bf16x8*)(kv + t * 2560 + lane * 16); const s16x4 kf1 = *(const s16x4*)(kv + t * 2560 + 1024 + r32 * 8);
      bf16x8 vf0 = *(const bf16x8*)(kv + t * 2560 + 1280 + (hh * 20 + vr) * 16), vf1 = *(const bf16x8*)(kv + t * 2560 + 1920 + (hh * 20 + vr) * 16);
      if (r32 >= 20) { vf0 = (bf16x8)(short)0x3f80; vf1 = vf0; }
      f32x16 s = MFMA32(kf0, fq0, (f32x16){}); s = MFMA32K8(kf1, fq1, s);
      u32x4 p0, p1;
#pragma unroll
      for (int i = 0; i < 4; ++i) {
        p0[i] = pk_bf16(EX2(s[2 * i] - m), EX2(s[2 * i + 1] - m));
        p1[i] = pk_bf16(EX2(s[8 + 2 * i] - m), EX2(s[8 + 2 * i + 1] - m));
      }
      oo = MFMA32(vf0, __builtin_bit_cast(bf16x8, p0), oo);
      oo = MFMA32(vf1, __builtin_bit_cast(bf16x8, p1), oo);
    }
#pragma unroll
    for (int i = 0; i < 12; ++i) of[i] = oo[i];
    l = hi32(of[8]);
  }
  const float rl = __builtin_amdgcn_rcpf(l);
  waitv<0>(); pin(fa); pin(fb); pin(fc);
#pragma unroll
  for (int g = 0; g < 3; ++g)
    if (g < 2 || !hh) st8s(obs[g], vo, nofill(pk_bf16(of[4 * g] * rl, of[4 * g + 1] * rl), pk_bf16(of[4 * g + 2] * rl, of[4 * g + 3] * rl)));
  STAMP_AT(1, 11);
  if (!__all(fa[0] == tag[0] && fa[1] == tag[1] && fb[0] == tag[0] && fb[1] == tag[1])) poll2(ctr, fva, fvb, tag);
  if (!__all(fc[0] == tag[0] && fc[1] == tag[1])) poll(ctr, fvc, tag);
  STAMP_AT(1, 12);
}

constexpr int x_none(int) { return 0; }
constexpr int x_p4(int i) { return ((i >= 2 && i < 7) || (i >= 10 && i < 18)) ? 1 : 0; }
template <int NSTEP, int NF, int LA, int LL, int NA, int ASTR, int (*BOFF)(int), int (*XCNT)(int), class PRE, class MM>
DI void gemm_res(const char* asrc, int rot, unsigned lds0, unsigned voff, PRE&& pre, MM&& mm) {
  bf16x8 a[NSTEP][NA];
  bf16x8 bf[LL + 1][NF];
  unsigned rd0 = lds0 + voff, rd1 = rd0 + 65536, rd2 = rd0 + 131072; pin(rd0); pin(rd1); pin(rd2);
  auto issue = [&](auto n_) { constexpr int n = n_; sfor<NA>([&](auto j) { gld16<(n & 3) * 1024>(a[n][j], asrc + rotf(n >> 2, rot, NSTEP / 4) * 4096 + j * ASTR, voff); }); };
  constexpr auto xsum = [](int lo, int hi) { int c = 0; for (int i = lo < 0 ? 0 : lo; i <= hi; ++i) c += XCNT(i); return c; };
  auto rd = [&](auto i_, auto f_) {
    constexpr int i = i_, f = f_, off = BOFF(i * NF + f);
    bf[i % (LL + 1)][f] = lds128((off < 65536 ? rd0 : off < 131072 ? rd1 : rd2) + (off & 65535));
  };
  auto check = [&](auto i_) {
    constexpr int i = i_;
    sfor<NA>([&](auto j) { pin(a[i][j]); });
    auto mx = [&]() { unsigned m = 0; sfor<NA>([&](auto j) { m = max(m, umax4(__builtin_bit_cast(u32x4, a[i][j]))); }); return m; };
    if (__builtin_expect(__any(mx() == 0xffffffffu), 0)) {
#pragma unroll 1
      for (int spin = 0; spin < (1 << 17); ++spin) {
        RETRY_COUNT(NSTEP + NF);
        issue(i_); waitv<0>(); sfor<NA>([&](auto j) { pin(a[i][j]); });
        if (!__any(mx() == 0xffffffffu)) break;
      }
    }
  };
  sfor<LA>([&](auto n) { issue(n); });
  pre(IC<-1>{});
  sfor<LL>([&](auto i) { sfor<NF>([&](auto f) { rd(i, f); }); });
  waitv<(LA - 1) * NA>(); check(IC<0>{});
  sfor<NSTEP>([&](auto i_) {
    constexpr int i = i_;
    pre(i_);
    SB();
    sfor<NF>([&](auto f_) {
      constexpr int f = f_;
      if constexpr (f == NF - 1 && i + 1 < NSTEP) {
        waitv<((i + LA < NSTEP ? i + LA : NSTEP - 1) - (i + 1)) * NA + xsum(i - LA + 2, i)>(); check(IC<i + 1>{}); SB(); }
      mm(f_, bf[i % (LL + 1)][f], a[i]);
      SB();
      if constexpr (i + LL < NSTEP) rd(IC<i + LL>{}, f_);
      if constexpr (f == 0 && i + LA < NSTEP) issue(IC<i + LA>{});
      SB();
    });
  });
}

__global__ __launch_bounds__(512, 2) void k_mega(const u16* __restrict__ x, const u16* __restrict__ Wqkv, const u16* __restrict__ Wo,
                                                 const u16* __restrict__ Wg, const u16* __restrict__ Wu, const u16* __restrict__ Wd,
                                                 char* __restrict__ ws, u16* __restrict__ out) {
  __shared__ __attribute__((aligned(16))) char smem[163840];
  const unsigned lds0 = lds_addr_of(smem);
  const int w = __builtin_amdgcn_readfirstlane(threadIdx.x >> 6);
  const int g8 = blockIdx.x & 7, i32 = blockIdx.x >> 3;
  char* ctl = ws + WS_CTL;
  const char* ctr = ctl + g8 * CTL_REP;
  unsigned cdum = 0;
  u32x2 ep;
  gld8<0>(ep, ctl, 0u);
  int f1idx;
  if (i32 < 30) {
    const int i = (g8 & 1) * 30 + i32, rb = (g8 >> 1) * 4 + (i & 3), cb = i >> 2;
    if (cb >= 10) p1_body<true>(x, Wqkv, ws, lds0, w, rb, cb);
    else p1_body<false>(x, Wqkv, ws, lds0, w, rb, cb);
    f1idx = (cb * 4 + (w & 3)) * 32 + rb * 2 + (w >> 2);
  } else {
    p1_repack(Wo, ws, w, blockIdx.x - 240);
    f1idx = 1920 + (blockIdx.x - 240) * 8 + w;
  }
  waitv<0>();
  pin(ep); pin(cdum);
  u32x2 tag;
  {
    const unsigned long long e = (((unsigned long long)__builtin_amdgcn_readfirstlane(ep[1]) << 32) | __builtin_amdgcn_readfirstlane(ep[0])) + 1ull;
    tag = (u32x2){(unsigned)e, (unsigned)(e >> 32)};
  }
  publish(ctl + CTL_F1 + f1idx * 8, tag, fresh_lane());
  bar();
  const int rb3 = g8 * 4 + (i32 >> 3), cb3 = i32 & 7;
  const int rb4 = (g8 >> 2) * 4 + (i32 >> 3), cb4 = (g8 & 3) * 8 + (i32 & 7);
  p2_attn(ws, Wg, Wu, Wd, lds0, w, tag, rb3, cb3, rb4, cb4);
  bar();
  const char* gug = ws + WS_WP + (size_t)NFR_WO * 1024 + (size_t)cb4 * 122880;
  const char* wdg = ws + WS_WP + (size_t)(NFR_WO + NFR_GU) * 1024 + (size_t)cb3 * 245760;
  const int rg4 = 24 * ROT3(cb4 & 7), rg5 = 20 * ROT5(cb3);
  u32x2 hp[5];
  if (w < 4) {
    const int lane = fresh_lane();
    const unsigned voff = lane * 16;
    const int row = rb3 * 64 + w * 16 + (lane & 15), col0 = cb3 * 80 + 4 * (lane >> 4);
    STAMP_AT(2, 0);
    u32x2 xr[5];
#pragma unroll
    for (int ct = 0; ct < 5; ++ct) xr[ct] = *(const u32x2*)(x + (size_t)row * 640 + col0 + 16 * ct);
    { const int hd = lane & 31, cgt = (hd & 1) ? hd * 5 : hd * 5 + 4;
      probe(ws + WS_O + (size_t)(rb3 * 4 + w) * 20480, (unsigned)((cgt >> 3) * 1024 + ((cgt & 7) >> 1) * 256 + (cgt & 1) * 8)); }
    sfor<5>([&](auto ct) { pin(xr[ct]); });
    STAMP_AT(2, 1);
    f32x4 acc[5] = {};
    gemm_res<20, 5, 20, 2, 1, 0, wo_off, x_none>(ws + WS_O + (size_t)(rb3 * 4 + w) * 20480, ROT3(cb3), lds0, voff,
      [&](auto i) { if constexpr (i == -1) STAMP_AT(2, 2); if constexpr (i == 1) STAMP_AT(2, 3); if constexpr (i == 10) STAMP_AT(2, 4);
        if constexpr (i == P3_GO) bar(); },
      [&](auto ct, const bf16x8& b, bf16x8 (&a)[1]) { acc[ct] = MFMA16(b, a[0], acc[ct]); });
    STAMP_AT(2, 6);
    float s1 = 0.f, s2 = 0.f;
    const char* hb = ws + WS_H + (size_t)(rb3 * 4 + w) * 20480;
    const unsigned vt = t16lane(lane);
#pragma unroll
    for (int ct = 0; ct < 5; ++ct) {
      hp[ct] = nofill(pk_bf16(bf_lo(xr[ct][0]) + acc[ct][0], bf_hi(xr[ct][0]) + acc[ct][1]), pk_bf16(bf_lo(xr[ct][1]) + acc[ct][2], bf_hi(xr[ct][1]) + acc[ct][3]));
      st8s(hb + t16off(cb3 * 80 + ct * 16), vt, hp[ct]);
      s1 = dot2(hp[ct][0], 0x3f803f80u, s1); s1 = dot2(hp[ct][1], 0x3f803f80u, s1);
      s2 = dot2(hp[ct][0], hp[ct][0], s2); s2 = dot2(hp[ct][1], hp[ct][1], s2);
    }
    s1 = red4(s1); s2 = red4(s2);
    if (lane < 16) st8s(ws + WS_HST + (size_t)(rb3 * 4 + w) * 1024 + (cb3 >> 1) * 256 + (cb3 & 1) * 8, (unsigned)(lane * 16), nofill(__float_as_uint(s1), __float_as_uint(s2)));
    bar();
    STAMP_AT(2, 7);
  } else {
    const int lane = fresh_lane(), v = w - 4;
    const unsigned voff = lane * 16;
    float cpart[10];
    const int cc = v * 24 + min(lane & 31, 23);
    sfor<10>([&](auto i) { gld4<i * 384>(cpart[i], ws + WS_CSP + (size_t)cb4 * 7680, (unsigned)((lane >> 5) * 3840 + cc * 4)); });
    waitv<0>();
    float cv = 0.f;
    sfor<10>([&](auto j) { pin(cpart[j]); cv += cpart[j]; });
    cv = add32(cv);
    bar();
    STAMP_AT(2, 2);
    sfor<5>([&](auto j) { glds16<true>(gug + rotf(v * 5 + j, rg4, 120) * 1024, voff, lds0 + gu_off(0) + (v * 5 + j) * 1024); });
    sfor<10>([&](auto j) { glds16<true>(gug + rotf(20 + v * 10 + j, rg4, 120) * 1024, voff, lds0 + gu_off(20) + (v * 10 + j) * 1024); });
    STAMP_AT(2, 5);
    waitv<0>();
    bar();
    STAMP_AT(2, 6);
    sfor<15>([&](auto j) { glds16<true>(gug + rotf(60 + v * 15 + j, rg4, 120) * 1024, voff, lds0 + gu_off(60) + (v * 15 + j) * 1024); });
    if (lane < 24) ((LAS float*)(uintptr_t)(lds0 + CSF_OFF))[cc] = cv;
  }
  {
    const int lane = fresh_lane();
    const unsigned voff = lane * 16;
    const int r16 = lane & 15, g4 = lane >> 4;
    STAMP_AT(3, 0);
    f32x4 cs[6]; f32x2 ms[2];
    f32x4 hs[2];
#pragma unroll 1
    for (int spin = 0; spin < (1 << 17); ++spin) {
      gld16<0>(hs[0], ws + WS_HST + (size_t)(rb4 * 16 + w * 2) * 1024, voff);
      gld16<1024>(hs[1], ws + WS_HST + (size_t)(rb4 * 16 + w * 2) * 1024, voff);
      waitv<0>(); pin(hs[0]); pin(hs[1]);
      if (!__any(max(umax4(__builtin_bit_cast(u32x4, hs[0])), umax4(__builtin_bit_cast(u32x4, hs[1]))) == 0xffffffffu)) break;
      __builtin_amdgcn_s_sleep(1);
    }
    STAMP_AT(3, 1);
    f32x4 acc[2][6] = {};
    if (w < 4) __builtin_amdgcn_s_setprio(1);
    gemm_res<20, 6, K4_LA_, 1, 2, 20480, gu_off, x_p4>(ws + WS_H + (size_t)(rb4 * 16 + w * 2) * 20480, ROT3(cb4 & 7), lds0, voff,
      [&](auto i) { if constexpr (i == -1) STAMP_AT(3, 2); if constexpr (i == 1) STAMP_AT(3, 3); if constexpr (i == 10) STAMP_AT(3, 4);
        if constexpr (i == 9) bar();
        if constexpr (i == -1) sfor<2>([&](auto t) {
          const float s1 = red4(hs[t][0] + hs[t][2]), s2 = red4(hs[t][1] + hs[t][3]), mean = s1 * (1.f / 640.f);
          ms[t] = (f32x2){mean, __builtin_amdgcn_rsqf(fmaxf(s2 * (1.f / 640.f) - mean * mean, 0.f) + LN_EPS)}; pin(ms[t]); });
        if constexpr (i == 18) sfor<6>([&](auto ct) { cs[ct] = *(const LAS f32x4*)(uintptr_t)(lds0 + CSF_OFF + (ct * 16 + 4 * g4) * 4); });
        if constexpr (i >= 2 && i < 7) { const int f = (i - 2) * 8 + w - ((i == 6 && w >= 4) ? 4 : 0); glds16<true>(wdg + rotf(f, rg5, 240) * 1024, voff, lds0 + wd_off(0) + f * 1024); }
        if constexpr (i >= 10 && i < 15) { constexpr int n = i - 10; glds16<true>(wdg + rotf(40 + n * 8 + w, rg5, 240) * 1024, voff, lds0 + wd_off(40) + (n * 8 + w) * 1024); }
        if constexpr (i >= 15 && i < 18) { const int f = (i - 15) * 8 + w - ((i == 17 && w >= 4) ? 4 : 0); glds16<true>(wdg + rotf(140 + f, rg5, 240) * 1024, voff, lds0 + wd_off(140) + f * 1024); } },
      [&](auto ct, const bf16x8& b, bf16x8 (&a)[2]) { acc[0][ct] = MFMA16(b, a[0], acc[0][ct]); acc[1][ct] = MFMA16(b, a[1], acc[1][ct]); });
    STAMP_AT(3, 6);
    constexpr float GC0 = -2.f * 0.7978845608028654f * 1.4426950408889634f, GC1 = GC0 * 0.044715f;
    const char* gb = ws + WS_G + (size_t)(rb4 * 16 + w * 2) * 49152;
    const unsigned vt = t16lane(lane);
#pragma unroll
    for (int t = 0; t < 2; ++t) {
      const float nm = -ms[t].x, rstd = ms[t].y;
#pragma unroll
      for (int ct = 0; ct < 3; ++ct) {
        const f32x4 gv = (acc[t][ct] + cs[ct] * nm) * rstd, uv = (acc[t][3 + ct] + cs[3 + ct] * nm) * rstd;
        const f32x4 z = gv * (gv * gv * GC1 + GC0);
        f32x4 e;
#pragma unroll
        for (int j = 0; j < 4; ++j) e[j] = __builtin_amdgcn_exp2f(z[j]);
        e = e + 1.f;
#pragma unroll
        for (int j = 0; j < 4; ++j) e[j] = __builtin_amdgcn_rcpf(e[j]);
        const f32x4 r = gv * uv * e;
        st8s(gb + t * 49152 + t16off(cb4 * 48 + ct * 16), vt, nofill(pk_bf16(r[0], r[1]), pk_bf16(r[2], r[3])));
      }
    }
    __builtin_amdgcn_s_setprio(0);
    STAMP_AT(3, 7);
    waitv<6>();
    STAMP_AT(3, 8);
    bar();
  }
  if (w < 4) {
    const int lane = fresh_lane();
    const unsigned voff = lane * 16;
    const int row = rb3 * 64 + w * 16 + (lane & 15), col0 = cb3 * 80 + 4 * (lane >> 4);
    STAMP_AT(4, 0);
    probe(ws + WS_G + (size_t)(rb3 * 4 + w) * 49152, (unsigned)t16off((lane & 31) * 48 + 32));
    STAMP_AT(4, 1);
    f32x4 acc[5] = {};
    gemm_res<48, 5, K5_LA_, 2, 1, 0, wd_off, x_none>(ws + WS_G + (size_t)(rb3 * 4 + w) * 49152, ROT5(cb3), lds0, voff,
      [&](auto i) { if constexpr (i == 1) STAMP_AT(4, 3); if constexpr (i == 14) STAMP_AT(4, 4); if constexpr (i == 30) STAMP_AT(4, 5);
        if constexpr (i == -1 || i == 8 || i == 14 || i == 16 || i == 30 || i == 38) bar(); },
      [&](auto ct, const bf16x8& b, bf16x8 (&a)[1]) { acc[ct] = MFMA16(b, a[0], acc[ct]); });
    STAMP_AT(4, 12);
#pragma unroll
    for (int ct = 0; ct < 5; ++ct) {
      const unsigned p0 = pk_bf16(bf_lo(hp[ct][0]) + acc[ct][0], bf_hi(hp[ct][0]) + acc[ct][1]);
      const unsigned p1 = pk_bf16(bf_lo(hp[ct][1]) + acc[ct][2], bf_hi(hp[ct][1]) + acc[ct][3]);
      *(u32x2*)(out + (size_t)row * 640 + col0 + 16 * ct) = (u32x2){p0, p1};
    }
    STAMP_AT(4, 13);
  } else {
    const int lane = fresh_lane(), v = w - 4;
    const unsigned voff = lane * 16;
    glds16<true>(wdg + rotf(36 + v, rg5, 240) * 1024, voff, lds0 + wd_off(36) + v * 1024);
    waitv<0>(); bar();
    sfor<3>([&](auto g) { sfor<5>([&](auto j) { constexpr int f = (g + 4) * 20 + j; glds16<true>(wdg + rotf(f + v * 5, rg5, 240) * 1024, voff, lds0 + wd_off(f) + v * 5120); }); });
    bar();
    sfor<2>([&](auto g) { sfor<5>([&](auto j) { constexpr int f = (g + 8) * 20 + j; glds16<true>(wdg + rotf(f + v * 5, rg5, 240) * 1024, voff, lds0 + wd_off(f) + v * 5120); }); });
    waitv<10>(); bar();
    bar();
    sfor<2>([&](auto g) { sfor<5>([&](auto j) { constexpr int f = (g + 10) * 20 + j; glds16<true>(wdg + rotf(f + v * 5, rg5, 240) * 1024, voff, lds0 + wd_off(f) + v * 5120); }); });
    waitv<10>(); bar();
    waitv<0>(); bar();
    if (blockIdx.x == 0 && w == 4) {
      poll2(ctr + CTL_FR, (unsigned)(lane * 64), (unsigned)(4096 + lane * 64), tag);
      poll2(ctr + CTL_FR, (unsigned)(8192 + lane * 64), (unsigned)(12288 + lane * 64), tag);
      if (lane == 0) st8(ctl, tag);
    }
  }
}

extern "C" void kernel_launch(void* const* d_in, const int* in_sizes, int n_in, void* d_out, int out_size, void* d_ws, size_t ws_size,
                              hipStream_t stream) {
  k_mega<<<256, 512, 0, stream>>>((const u16*)d_in[0], (const u16*)d_in[1], (const u16*)d_in[2], (const u16*)d_in[4], (const u16*)d_in[5],
                                  (const u16*)d_in[6], (char*)d_ws, (u16*)d_out);
}
```

```cpp
#include <hip/hip_runtime.h>
#include <hip/hip_bf16.h>
#include <stdint.h>
#include <utility>
#define NO_ROT2 1

typedef short bf16x8 __attribute__((ext_vector_type(8)));
typedef short s16x4 __attribute__((ext_vector_type(4)));
typedef float f32x16 __attribute__((ext_vector_type(16)));
typedef float f32x4 __attribute__((ext_vector_type(4)));
typedef float f32x2 __attribute__((ext_vector_type(2)));
typedef unsigned u32x4 __attribute__((ext_vector_type(4)));
typedef unsigned u32x2 __attribute__((ext_vector_type(2)));
typedef __bf16 bf2_t __attribute__((ext_vector_type(2)));
typedef unsigned short u16;
#define LAS __attribute__((address_space(3)))
#define DI __device__ __forceinline__

constexpr int S_ = 2048, D_ = 640, H_ = 32, DH = 20, F_ = 1536;
constexpr float LN_EPS = 1e-5f;
constexpr float QSCALE = 0.22360679774997896f * 1.4426950408889634f;

constexpr size_t WS_Q   = 0;
constexpr size_t WS_KV  = WS_Q + 2621440;
constexpr size_t WS_O   = WS_KV + 5242880;
constexpr size_t WS_H   = WS_O + 2621440;
constexpr size_t WS_HST = WS_H + 2621440;
constexpr size_t WS_G   = WS_HST + 131072;
constexpr size_t WS_WP  = WS_G + 6291456;
constexpr size_t WS_CSP = WS_WP + 6717440;
constexpr size_t WS_CTL = WS_CSP + 245760;
constexpr size_t CTL_F1 = 256;
constexpr size_t CTL_F2 = CTL_F1 + 2048 * 8;
constexpr size_t CTL_FR = CTL_F2 + 2048 * 8;
constexpr size_t CTL_F3 = CTL_FR + 2048 * 8;
constexpr size_t CTL_F4 = CTL_F3 + 1024 * 8;
constexpr size_t CTL_REP = CTL_F4 + 2048 * 8 - 256;
constexpr int NFR_WO = 800, NFR_GU = 3840, NFR_WD = 1920, NFR_ALL = NFR_WO + NFR_GU + NFR_WD;

DI unsigned pk_bf16(float lo, float hi) { f32x2 f = {lo, hi}; return __builtin_bit_cast(unsigned, __builtin_convertvector(f, bf2_t)); }
DI float bf_lo(unsigned u) { return __uint_as_float(u << 16); }
DI float bf_hi(unsigned u) { return __uint_as_float(u & 0xffff0000u); }
DI float dot2(unsigned a, unsigned b, float c) { return __builtin_amdgcn_fdot2_f32_bf16(__builtin_bit_cast(bf2_t, a), __builtin_bit_cast(bf2_t, b), c, false); }
DI float add32(float v) { const auto r = __builtin_amdgcn_permlane32_swap(__float_as_uint(v), __float_as_uint(v), false, false); return __uint_as_float(r[0]) + __uint_as_float(r[1]); }
DI float max32(float v) { const auto r = __builtin_amdgcn_permlane32_swap(__float_as_uint(v), __float_as_uint(v), false, false); return fmaxf(__uint_as_float(r[0]), __uint_as_float(r[1])); }
DI float add16(float v) { const auto r = __builtin_amdgcn_permlane16_swap(__float_as_uint(v), __float_as_uint(v), false, false); return __uint_as_float(r[0]) + __uint_as_float(r[1]); }
DI float red4(float v) { return add32(add16(v)); }
DI float hi32(float v) { const auto r = __builtin_amdgcn_permlane32_swap(__float_as_uint(v), __float_as_uint(v), false, false); return __uint_as_float(r[1]); }
#define MFMA32(a, b, c) __builtin_amdgcn_mfma_f32_32x32x16_bf16((a), (b), (c), 0, 0, 0)
#define MFMA32K8(a, b, c) __builtin_amdgcn_mfma_f32_32x32x8bf16_1k((a), (b), (c), 0, 0, 0)
#define MFMA16(a, b, c) __builtin_amdgcn_mfma_f32_16x16x32_bf16((a), (b), (c), 0, 0, 0)

#define P1_POL ""
template <bool DEV> DI void glds16(const void* sbase, unsigned voff, unsigned lds_base) {
  unsigned sv;
  if constexpr (DEV)
    asm volatile("s_mov_b32 %0, m0\n\ts_mov_b32 m0, %3\n\ts_nop 0\n\tglobal_load_lds_dwordx4 %1, %2 sc1\n\ts_mov_b32 m0, %0"
                 : "=&s"(sv) : "v"(voff), "s"(sbase), "s"(lds_base) : "memory");
  else
    asm volatile("s_mov_b32 %0, m0\n\ts_mov_b32 m0, %3\n\ts_nop 0\n\tglobal_load_lds_dwordx4 %1, %2" P1_POL "\n\ts_mov_b32 m0, %0"
                 : "=&s"(sv) : "v"(voff), "s"(sbase), "s"(lds_base) : "memory");
}
template <int OFF, class T> DI void gld16(T& dst, const void* sbase, unsigned voff) {
  asm volatile("global_load_dwordx4 %0, %1, %2 offset:%3 sc1" : "=v"(dst) : "v"(voff), "s"(sbase), "i"(OFF) : "memory");
}
template <int OFF, class T> DI void gld8(T& dst, const void* sbase, unsigned voff) {
  asm volatile("global_load_dwordx2 %0, %1, %2 offset:%3 sc1" : "=v"(dst) : "v"(voff), "s"(sbase), "i"(OFF) : "memory");
}
template <int OFF> DI void gld4(float& dst, const void* sbase, unsigned voff) {
  asm volatile("global_load_dword %0, %1, %2 offset:%3 sc1" : "=v"(dst) : "v"(voff), "s"(sbase), "i"(OFF) : "memory");
}
DI void asm_warm(u32x4& dst, const void* sbase, unsigned voff) {
  asm volatile("global_load_dwordx4 %0, %1, %2 sc1" : "+v"(dst) : "v"(voff), "s"(sbase) : "memory");
}
template <int OFF> DI void gldu16(unsigned& dst, const void* sbase, unsigned voff) {
  asm volatile("global_load_ushort %0, %1, %2 offset:%3" : "=v"(dst) : "v"(voff), "s"(sbase), "i"(OFF) : "memory");
}
DI void st16(void* p, u32x4 v) { asm volatile("global_store_dwordx4 %0, %1, off sc1\n\ts_nop 1" :: "v"(p), "v"(v) : "memory"); }
DI void st8(void* p, u32x2 v) { asm volatile("global_store_dwordx2 %0, %1, off sc1" :: "v"(p), "v"(v) : "memory"); }
DI void st4(void* p, float v) { asm volatile("global_store_dword %0, %1, off sc1" :: "v"(p), "v"(v) : "memory"); }
DI void st8s(const void* sbase, unsigned voff, u32x2 v) { asm volatile("global_store_dwordx2 %0, %1, %2 sc1" :: "v"(voff), "v"(v), "s"(sbase) : "memory"); }
DI u32x2 nofill(unsigned a, unsigned b) { return (u32x2){min(a, 0xfffffffeu), min(b, 0xfffffffeu)}; }
DI unsigned umax4(u32x4 v) { return max(max(v[0], v[1]), max(v[2], v[3])); }
template <int N> DI void waitv() { asm volatile("s_waitcnt vmcnt(%0)" :: "i"(N) : "memory"); }
DI void bar() { asm volatile("s_waitcnt lgkmcnt(0)\n\ts_barrier" ::: "memory"); }
DI void setflag(char* flag, u32x2 tag, int lane) { if (lane < 8) st8(flag + lane * CTL_REP, tag); }
DI void publish(char* flag, u32x2 tag, int lane) { waitv<0>(); setflag(flag, tag, lane); }
DI void poll(const char* base, unsigned voff, u32x2 tag) {
#pragma unroll 1
  for (int spin = 0; spin < (1 << 17); ++spin) {
    u32x2 v;
    asm volatile("global_load_dwordx2 %0, %1, %2 sc1\n\ts_waitcnt vmcnt(0)" : "=v"(v) : "v"(voff), "s"(base) : "memory");
    if (__all(v[0] == tag[0] && v[1] == tag[1])) break;
    __builtin_amdgcn_s_sleep(1);
  }
}
DI void poll2(const char* base, unsigned voff0, unsigned voff1, u32x2 tag) {
#pragma unroll 1
  for (int spin = 0; spin < (1 << 17); ++spin) {
    u32x2 v, u;
    asm volatile("global_load_dwordx2 %0, %2, %4 sc1\n\tglobal_load_dwordx2 %1, %3, %4 sc1\n\ts_waitcnt vmcnt(0)" : "=&v"(v), "=&v"(u) : "v"(voff0), "v"(voff1), "s"(base) : "memory");
    if (__all(v[0] == tag[0] && v[1] == tag[1] && u[0] == tag[0] && u[1] == tag[1])) break;
    __builtin_amdgcn_s_sleep(1);
  }
}
DI void probe(const char* base, unsigned voff) {
#pragma unroll 1
  for (int spin = 0; spin < (1 << 17); ++spin) {
    unsigned v;
    asm volatile("global_load_dword %0, %1, %2 sc1\n\ts_waitcnt vmcnt(0)" : "=v"(v) : "v"(voff), "s"(base) : "memory");
    if (__all(v != 0xffffffffu)) break;
    __builtin_amdgcn_s_sleep(1);
  }
}
constexpr int t16off(int c0) { return (c0 >> 5) * 1024 + (c0 & 16) * 32; }
DI unsigned t16lane(int lane) { return (unsigned)((lane >> 5) * 256 + (lane & 15) * 16 + ((lane >> 4) & 1) * 8); }
DI int rotf(int f, int off, int n) { const int r = f + off; return r - (r >= n ? n : 0); }
DI void st16s(const void* sbase, unsigned voff, u32x4 v) { asm volatile("global_store_dwordx4 %0, %1, %2 sc1\n\ts_nop 1" :: "v"(voff), "v"(v), "s"(sbase) : "memory"); }
DI float row16_sum(float v) {
  v += __builtin_bit_cast(float, __builtin_amdgcn_update_dpp(0, __builtin_bit_cast(int, v), 0x128, 0xf, 0xf, false));
  v += __builtin_bit_cast(float, __builtin_amdgcn_update_dpp(0, __builtin_bit_cast(int, v), 0x124, 0xf, 0xf, false));
  v += __builtin_bit_cast(float, __builtin_amdgcn_update_dpp(0, __builtin_bit_cast(int, v), 0x122, 0xf, 0xf, false));
  v += __builtin_bit_cast(float, __builtin_amdgcn_update_dpp(0, __builtin_bit_cast(int, v), 0x121, 0xf, 0xf, false));
  return v;
}
#define ROT3(c) 0
#define ROT5(c) 0
DI int fresh_lane() { int l = __builtin_amdgcn_mbcnt_hi(~0u, __builtin_amdgcn_mbcnt_lo(~0u, 0u)); asm volatile("" : "+v"(l)); return l; }
template <class T> DI void pin(T& v) { asm volatile("" : "+v"(v)); }
DI bf16x8 lds128(unsigned addr) { return *(const LAS bf16x8*)(uintptr_t)addr; }
DI s16x4 lds64(unsigned addr) { return *(const LAS s16x4*)(uintptr_t)addr; }
DI s16x4 ldstr(unsigned addr) { return __builtin_amdgcn_ds_read_tr16_b64_v4i16((LAS s16x4*)(uintptr_t)addr); }
DI unsigned lds_addr_of(const void* p) { return (unsigned)(uintptr_t)(const LAS char*)p; }
#define STAMP_AT(kid, idx) ((void)0)
#define RETRY_COUNT(k) ((void)0)
#define WAITV_BAR(N) asm volatile("s_waitcnt vmcnt(" #N ") lgkmcnt(0)\n\ts_barrier" ::: "memory")
#define SB() __builtin_amdgcn_sched_barrier(0)
template <int N> using IC = std::integral_constant<int, N>;
template <class F, int... Is> DI void sfor_(F&& f, std::integer_sequence<int, Is...>) { (f(IC<Is>{}), ...); }
template <int N, class F> DI void sfor(F&& f) { sfor_(f, std::make_integer_sequence<int, N>{}); }
template <int N> DI void waitv_bar() { asm volatile("s_waitcnt vmcnt(%0) lgkmcnt(0)\n\ts_barrier" :: "i"(N) : "memory"); }

#define K1_LA_ 10
#define P3_GO 12
#define K4_LA_ 5
#define K5_LA_ 24
constexpr int K1_STAGE = 32768, K1_NST = 4, K1_LA = K1_LA_;
template <bool ISV>
DI void p1_body(const u16* __restrict__ x, const u16* __restrict__ W, char* __restrict__ ws, const unsigned lds0, const int w, int rb, int cb) {
  const int lane = fresh_lane();
  const int wm = w >> 2, wn = w & 3;
  const int r32 = lane & 31, hh = lane >> 5;
  const unsigned a_voff0 = (unsigned)((lane >> 3) * 1280 + (((lane & 7) ^ ((lane >> 4) & 7)) << 4));
  const unsigned a_voff1 = (unsigned)((lane >> 3) * 1280 + (((lane & 7) ^ ((4 + (lane >> 4)) & 7)) << 4)) + 8 * 1280;
  const unsigned w_voff0 = (unsigned)((lane >> 3) * 3840 + (((lane & 7) ^ (((lane >> 4) & 1) << 2)) << 4));
  const unsigned w_voff1 = w_voff0 + 8 * 3840;
  const char* xs = (const char*)x + (size_t)(rb * 128 + w * 16) * 1280;
  const char* wsrc = (const char*)W + (size_t)((w & 3) * 16) * 3840 + (size_t)(cb * 128 + (w >> 2) * 64) * 2;
  const unsigned dst0 = lds0 + w * 2048;
#define ROTR 10
  const int rot = ((cb + 3 * rb) % ROTR) * (10 / ROTR);
  auto dma = [&](auto n_) {
    constexpr int n = n_, s = n >> 2, j = n & 3; constexpr unsigned so = (s % K1_NST) * K1_STAGE;
    const int sr = s + rot - (s + rot >= 10 ? 10 : 0);
    if constexpr (j == 0) glds16<false>(xs + sr * 128, a_voff0, dst0 + so);
    if constexpr (j == 1) glds16<false>(wsrc + (size_t)sr * 64 * 3840, w_voff0, dst0 + so + 16384);
    if constexpr (j == 2) glds16<false>(xs + sr * 128, a_voff1, dst0 + so + 1024);
    if constexpr (j == 3) glds16<false>(wsrc + (size_t)sr * 64 * 3840, w_voff1, dst0 + so + 16384 + 1024);
  };
  STAMP_AT(0, 0);
  sfor<K1_LA>([&](auto n) { dma(n); });
  const int ob = (wn >> 1) ^ wm;
  const int fsel = wn & 1;
  unsigned xa[2][4], xb[2][4], wa[2][2];
  {
    const int row = wm * 64 + fsel * 32 + r32;
    const int blk = (lane >> 4) & 1, q = (lane & 15) >> 2, p = lane & 3;
    const int c = (wn & 1) * 4 + blk * 2 + (p >> 1);
    const unsigned base = lds0 + (unsigned)(16384 + (wn >> 1) * 8192 + (8 * hh + q) * 128 + ((c ^ (((q >> 1) & 1) << 2)) << 4) + (p & 1) * 8);
#pragma unroll
    for (int hs = 0; hs < 2; ++hs) {
#pragma unroll
      for (int i = 0; i < 4; ++i) {
        xa[hs][i] = lds0 + hs * 65536 + (unsigned)(row * 128 + ((((i ^ ob) * 2 + hh) ^ ((row >> 1) & 7)) << 4));
        xb[hs][i] = xa[hs][i] + (fsel ? -4096 : 4096);
      }
      wa[hs][0] = base + hs * 65536 + ob * 2048; wa[hs][1] = base + hs * 65536 + (1 ^ ob) * 2048;
    }
  }
  f32x16 acc0 = {}, acc1 = {};
  float sxx = 0.f;
  f32x4 sx = {}, sw = {};
  bf16x8 sel = (bf16x8)(short)(((lane & 15) == ((lane >> 4) & 1)) ? 0x3f80 : 0); pin(sel);
  bf16x8 fx0[4], fx1[4], fw[4];
  auto rd = [&](auto i_) {
    constexpr int i = i_, s = i >> 2, p = i & 3, sl = s % K1_NST, hs = sl >> 1; constexpr unsigned so = (sl & 1) * K1_STAGE;
    fx0[p] = lds128(xa[hs][p] + so);
    const s16x4 wl = ldstr(wa[hs][p & 1] + so + (p >> 1) * 4096);
    const s16x4 wh = ldstr(wa[hs][p & 1] + so + (p >> 1) * 4096 + 512);
    fw[p] = __builtin_shufflevector(wl, wh, 0, 1, 2, 3, 4, 5, 6, 7);
    fx1[p] = lds128(xb[hs][p] + so);
  };
  const bool isq = cb < 5;
  const int pitch = isq ? 1280 : 2560;
  unsigned off[4];
  if (ISV) {
    const int n = cb * 128 + wn * 32 + r32 - 1280, head = n / 20, d0 = n - head * 20;
    const int d = (head & 1) ? (d0 < 4 ? d0 + 16 : d0 - 4) : d0;
    off[0] = (unsigned)(head * 163840 + 1280 + (hh * 20 + d) * 16); pin(off[0]);
  } else {
#pragma unroll
    for (int g = 0; g < 4; ++g) {
      const int n = cb * 128 + wn * 32 + 8 * g + 4 * hh - (isq ? 0 : 640), head = n / 20, d0 = n - head * 20;
      const int d = (head & 1) ? (d0 < 4 ? d0 + 16 : d0 - 4) : d0;
      off[g] = (unsigned)(head * 64 * pitch + (d < 16 ? (((d >> 3) * 32 + r32) * 16 + (d & 4) * 2) : (1024 + r32 * 8))); pin(off[g]);
    }
  }
  waitv_bar<K1_LA - 4>();
  STAMP_AT(0, 1);
  rd(IC<0>{}); rd(IC<1>{});
  sfor<40>([&](auto i_) {
    constexpr int i = i_, p = i & 3;
    if constexpr ((i + 2) % 4 == 0 && i + 2 < 40) { constexpr int last = (i - 1 + K1_LA < 39 ? i - 1 + K1_LA : 39); waitv_bar<(last - (i + 5) > 0 ? last - (i + 5) : 0)>(); }
    if constexpr (i + K1_LA < 40) dma(IC<i + K1_LA>{});
    if constexpr (i + 2 < 40) rd(IC<i + 2>{});
    SB();
    if (ISV) { acc0 = MFMA32(fx0[p], fw[p], acc0); acc1 = MFMA32(fx1[p], fw[p], acc1); }
    else     { acc0 = MFMA32(fw[p], fx0[p], acc0); acc1 = MFMA32(fw[p], fx1[p], acc1); }
    if constexpr ((p & 1) == 0) {
      sx = MFMA16(fx0[p], sel, sx);
      const u32x4 u = __builtin_bit_cast(u32x4, fx0[p]);
#pragma unroll
      for (int j = 0; j < 4; ++j) sxx = dot2(u[j], u[j], sxx);
      pin(sxx);
    } else {
      sw = MFMA16(fw[p], sel, sw); }
    SB();
  });
  STAMP_AT(0, 11);
  sxx = add32(sxx);
  LAS float* psum = (LAS float*)(uintptr_t)(lds0 + 65536);
  LAS float* psq = psum + 256;
  LAS float* csp = psum + 512;
  if (hh == 0) psq[(wn >> 1) * 128 + wm * 64 + fsel * 32 + r32] = sxx;
  if ((lane & 15) < 2) { const int o = (lane & 1) * 16 + (lane >> 4) * 4;
    *(LAS f32x4*)&psum[(wn >> 1) * 128 + wm * 64 + fsel * 32 + o] = sx; *(LAS f32x4*)&csp[wm * 128 + wn * 32 + o] = sw; }
  bar();
  STAMP_AT(0, 12);
  auto stat = [&](int rl) {
    const float mean = (psum[rl] + psum[128 + rl]) * (1.f / 640.f);
    const float var = fmaxf((psq[rl] + psq[128 + rl]) * (1.f / 640.f) - mean * mean, 0.f);
    return (f32x2){mean, __builtin_amdgcn_rsqf(var + LN_EPS)}; };
  if (ISV) {
    LAS f32x2* st = (LAS f32x2*)(uintptr_t)(lds0 + 65536 + 4096 + w * 512);
    st[lane] = stat(wm * 64 + lane);
    const int nl = wn * 32 + r32;
    const float c = csp[nl] + csp[128 + nl];
#pragma unroll
    for (int t = 0; t < 2; ++t) {
      const f32x16& a = t ? acc1 : acc0;
      const int rl = (t ^ fsel) * 32;
      unsigned pk[8];
#pragma unroll
      for (int g = 0; g < 4; ++g) {
        const f32x4 s01 = *(const LAS f32x4*)&st[rl + 8 * g + 4 * hh], s23 = *(const LAS f32x4*)&st[rl + 8 * g + 4 * hh + 2];
        pk[2 * g]     = pk_bf16(s01[1] * (a[4 * g] - s01[0] * c), s01[3] * (a[4 * g + 1] - s01[2] * c));
        pk[2 * g + 1] = pk_bf16(s23[1] * (a[4 * g + 2] - s23[0] * c), s23[3] * (a[4 * g + 3] - s23[2] * c));
      }
      const char* dst = ws + WS_KV + (size_t)(rb * 4 + wm * 2 + (rl >> 5)) * 2560;
      st16s(dst, off[0], (u32x4){pk[0], pk[1], pk[2], pk[3]});
      st16s(dst + 640, off[0], (u32x4){pk[4], pk[5], pk[6], pk[7]});
    }
  } else {
    const float sc = isq ? QSCALE : 1.f;
    const char* base = ws + (isq ? WS_Q : WS_KV);
    f32x4 c[4];
#pragma unroll
    for (int g = 0; g < 4; ++g) { const int nl = wn * 32 + 8 * g + 4 * hh; c[g] = *(const LAS f32x4*)&csp[nl] + *(const LAS f32x4*)&csp[128 + nl]; }
#pragma unroll
    for (int t = 0; t < 2; ++t) {
      const f32x16& a = t ? acc1 : acc0;
      const int rl = wm * 64 + (t ^ fsel) * 32;
      const f32x2 ms = stat(rl + r32);
      const float rs = ms.y * sc, mr = ms.x;
      const char* tb = base + (size_t)(rb * 4 + (rl >> 5)) * pitch;
#pragma unroll
      for (int g = 0; g < 4; ++g) {
        const unsigned p0 = pk_bf16(rs * (a[4 * g] - mr * c[g][0]), rs * (a[4 * g + 1] - mr * c[g][1]));
        const unsigned p1 = pk_bf16(rs * (a[4 * g + 2] - mr * c[g][2]), rs * (a[4 * g + 3] - mr * c[g][3]));
        st8s(tb, off[g], (u32x2){p0, p1});
      }
    }
  }
  STAMP_AT(0, 13);
}

DI void p1_repack(const u16* __restrict__ Wo, char* __restrict__ ws, const int w, int idx) {
  const int lane = fresh_lane();
  const unsigned pvo = (lane >> 4) * 8 * 1280 + (lane & 15) * 2;
  unsigned pw[7][8];
  sfor<7>([&](auto r) {
    const int f = idx * 50 + min(w + 8 * r, 49), cbk = f / 100, rem = f - cbk * 100, ks = rem / 5, ct = rem - ks * 5;
    const char* src = (const char*)(Wo + (size_t)(32 * ks) * 640 + 80 * cbk + 16 * ct);
    sfor<8>([&](auto i) { gldu16<(i & 3) * 1280>(pw[r][i], src + (i & ~3) * 1280, pvo); });
  });
  sfor<7>([&](auto r) {
    waitv<(6 - r) * 8>();
    sfor<8>([&](auto j) { pin(pw[r][j]); });
    const u32x4 v = {pw[r][0] | (pw[r][1] << 16), pw[r][2] | (pw[r][3] << 16), pw[r][4] | (pw[r][5] << 16), pw[r][6] | (pw[r][7] << 16)};
    pw[r][0] = v[0]; pw[r][1] = v[1]; pw[r][2] = v[2]; pw[r][3] = v[3];
  });
  sfor<7>([&](auto r) {
    if (w + 8 * r < 50) st16(ws + WS_WP + (size_t)(idx * 50 + w + 8 * r) * 1024 + lane * 16, (u32x4){pw[r][0], pw[r][1], pw[r][2], pw[r][3]});
  });
}

template <int OFF> DI void rdK(bf16x8& k0, s16x4& k1, unsigned a0, unsigned a1) {
  asm volatile("ds_read_b128 %0, %2 offset:%4\n\tds_read_b64 %1, %3 offset:%4" : "=&v"(k0), "=&v"(k1) : "v"(a0), "v"(a1), "i"(OFF));
}
template <int OFF> DI void rdV(bf16x8& v0, bf16x8& v1, unsigned a, unsigned long long msk) {
  asm volatile("s_mov_b64 exec, %5\n\tds_read_b128 %0, %2 offset:%3\n\tds_read_b128 %1, %2 offset:%4\n\ts_mov_b64 exec, -1"
               : "+v"(v0), "+v"(v1) : "v"(a), "i"(OFF), "i"(OFF + 640), "s"(msk));
}
#define EX2(v) __builtin_amdgcn_exp2f(v)
constexpr int wo_off(int p) { return (p < 60 ? p : p + 20) * 1024; }
constexpr int gu_off(int f) { return (f < 20 ? f + 60 : f < 60 ? f + 100 : f - 60) * 1024; }
constexpr int wd_off(int f) { return ((f / 20 + 4) % 8) * 20480 + (f % 20) * 1024; }
constexpr int CSF_OFF = 116 * 1024, XCH_OFF = 120 * 1024;

DI void p2_attn(char* __restrict__ ws, const u16* __restrict__ Wg, const u16* __restrict__ Wu, const u16* __restrict__ Wd,
                const unsigned lds0, const int w, const u32x2 tag, const int rb3, const int cb3, const int rb4, const int cb4) {
  const int lane = fresh_lane();
  const int kh = w >> 2, wq = w & 3;
  const int head = (blockIdx.x & 7) * 4 + ((blockIdx.x >> 3) & 3), qb = blockIdx.x >> 5;
  const int r32 = lane & 31, hh = lane >> 5;
  const char* kvg = ws + WS_KV + (size_t)head * 163840 + (size_t)kh * 81920 + wq * 1024;
  const unsigned voff = lane * 16;
  const unsigned kvl = lds0 + kh * 81920 + wq * 1024;
  char* ctl = ws + WS_CTL;
  const char* ctr = ctl + (blockIdx.x & 7) * CTL_REP;
  STAMP_AT(1, 0);
  {
    const int cfl = (20 * head) >> 5, cfh = (20 * head + 19) >> 5;
    const unsigned f0 = (unsigned)(((((lane & 32) ? 40 : 20) + ((lane & 16) ? cfh : cfl)) * 32 + kh * 16 + (lane & 15)) * 8);
    const unsigned f1 = (unsigned)((lane < 32 ? (((lane & 1) ? cfh : cfl) * 32 + qb * 4 + wq) : 1920 + cb3 * 16 + (lane & 15)) * 8);
    poll2(ctr + CTL_F1, f0, f1, tag);
  }
  STAMP_AT(1, 4);
#pragma unroll
  for (int i = 0; i < 4; ++i) glds16<true>(kvg + i * 4096, voff, kvl + i * 4096);
  bf16x8 q0[2]; s16x4 q1[2];
  {
    const char* qg = ws + WS_Q + ((size_t)head * 64 + qb * 8 + wq * 2) * 1280;
    gld16<0>(q0[0], qg, voff); gld16<1280>(q0[1], qg, voff); gld8<1024>(q1[0], qg, r32 * 8); gld8<2304>(q1[1], qg, r32 * 8);
  }
  const unsigned ka0 = lds0 + kh * 81920 + lane * 16, ka1 = lds0 + kh * 81920 + 1024 + r32 * 8, va = lds0 + kh * 81920 + 1280 + (hh * 20 + (r32 < 20 ? r32 : 0)) * 16;
  bf16x8 K0[2], V0[2], V1[2]; s16x4 K1[2];
  V0[0] = V0[1] = V1[0] = V1[1] = (bf16x8)(short)(r32 == 20 ? 0x3f80 : 0);
  pin(V0[0]); pin(V0[1]); pin(V1[0]); pin(V1[1]);
  WAITV_BAR(0);
  pin(q0[0]); pin(q0[1]); pin(q1[0]); pin(q1[1]);
  if (hh) { q1[0] = (s16x4){0, 0, 0, 0}; q1[1] = (s16x4){0, 0, 0, 0}; }
  STAMP_AT(1, 1);
  rdK<0>(K0[0], K1[0], ka0, ka1);
  const unsigned long long vmsk = 0x000fffff000fffffull;
  rdV<0>(V0[0], V1[0], va, vmsk);
  f32x16 cm[2];
  {
    const bf16x8 kf0 = lds128(lds0 + lane * 16); const s16x4 kf1 = lds64(lds0 + 1024 + r32 * 8);
#pragma unroll
    for (int t = 0; t < 2; ++t) {
      f32x16 s = MFMA32(kf0, q0[t], (f32x16){}); s = MFMA32K8(kf1, q1[t], s);
      float m = s[0];
#pragma unroll
      for (int i = 1; i < 16; ++i) m = fmaxf(m, s[i]);
      m = max32(m);
      cm[t] = (f32x16)(-m);
      pin(cm[t]);
    }
  }
  asm volatile("s_waitcnt lgkmcnt(0)" : "+v"(K0[0]), "+v"(K1[0]), "+v"(V0[0]), "+v"(V1[0]));
  f32x16 o[2] = {};
  f32x16 SA, SB_;
  u32x4 PA0, PA1, PB0 = {}, PB1 = {};
  SA = MFMA32(K0[0], q0[0], cm[0]); SA = MFMA32K8(K1[0], q1[0], SA);
  STAMP_AT(1, 2);
  pin(o[0]); pin(o[1]); pin(PB0); pin(PB1);
#define GRP(MF, Sc, Pc, i, j) do { MF; SB(); const float e0 = EX2(Sc[i]), e1 = EX2(Sc[i + 1]), e2 = EX2(Sc[i + 2]), e3 = EX2(Sc[i + 3]); \
    Pc[j] = pk_bf16(e0, e1); Pc[j + 1] = pk_bf16(e2, e3); pin(Pc); SB(); } while (0)
#define SLOT(QK, Sc, Pc0, Pc1, Sn, kb, jn, Pp0, Pp1, vb, jp) do { \
    GRP(if constexpr (QK) Sn = MFMA32(K0[kb], q0[jn], cm[jn]), Sc, Pc0, 0, 0); \
    GRP(if constexpr (QK) Sn = MFMA32K8(K1[kb], q1[jn], Sn), Sc, Pc0, 4, 2); \
    GRP(o[jp] = MFMA32(V0[vb], __builtin_bit_cast(bf16x8, Pp0), o[jp]), Sc, Pc1, 8, 0); \
    GRP(o[jp] = MFMA32(V1[vb], __builtin_bit_cast(bf16x8, Pp1), o[jp]), Sc, Pc1, 12, 2); } while (0)
#define LWAIT(kb, vb) asm volatile("s_waitcnt lgkmcnt(0)" : "+v"(K0[kb]), "+v"(K1[kb]), "+v"(V0[vb]), "+v"(V1[vb]))
  unsigned pw[3][8];
  const int gw = blockIdx.x * 8 + w, gwc = min(gw, 1919);
  const char* psrc[3];
#pragma unroll
  for (int r = 0; r < 2; ++r) {
    const int f = 2 * gwc + r, cbk = f / 120, rem = f - cbk * 120, ks = rem / 6, ct = rem - ks * 6;
    psrc[r] = (const char*)((ct < 3 ? Wg : Wu) + (size_t)(32 * ks) * 1536 + 48 * cbk + 16 * (ct < 3 ? ct : ct - 3));
  }
  { const int f = gwc, cbk = f / 240, rem = f - cbk * 240, ks = rem / 5, ct = rem - ks * 5;
    psrc[2] = (const char*)(Wd + (size_t)(32 * ks) * 640 + 80 * cbk + 16 * ct); }
  const unsigned pvo1 = (lane >> 4) * 8 * 3072 + (lane & 15) * 2, pvo2 = (lane >> 4) * 8 * 1280 + (lane & 15) * 2;
  const char* wog = ws + WS_WP + (size_t)cb3 * 102400;
  auto rstore = [&](auto r_, auto yv_) {
    constexpr int r = r_;
    waitv<yv_>();
    sfor<8>([&](auto j) { pin(pw[r][j]); });
    const u32x4 v = {pw[r][0] | (pw[r][1] << 16), pw[r][2] | (pw[r][3] << 16), pw[r][4] | (pw[r][5] << 16), pw[r][6] | (pw[r][7] << 16)};
    if (gw < 1920) {
      if constexpr (r < 2) {
        st16(ws + WS_WP + (size_t)(NFR_WO + 2 * gw + r) * 1024 + lane * 16, v);
        float c = 0.f;
#pragma unroll
        for (int j = 0; j < 4; ++j) c = dot2(v[j], 0x3f803f80u, c);
        c = red4(c);
        if (lane < 16) st4(ws + WS_CSP + (size_t)(2 * gw + r) * 64 + lane * 4, c);
      } else st16(ws + WS_WP + (size_t)(NFR_WO + NFR_GU + gw) * 1024 + lane * 16, v);
    }
  };
  const int qtile = qb * 8 + wq * 2 + kh, cg0 = head * 5 + (head & 1), cgt = (head & 1) ? head * 5 : head * 5 + 4;
  const char* ob = ws + WS_O + (size_t)qtile * 40960;
  const char* obs[3] = {ob + (cg0 >> 3) * 1024 + ((cg0 & 7) >> 1) * 256, ob + ((cg0 + 2) >> 3) * 1024 + (((cg0 + 2) & 7) >> 1) * 256,
                        ob + (cgt >> 3) * 1024 + ((cgt & 7) >> 1) * 256 + (cgt & 1) * 8};
  unsigned vo = (unsigned)((r32 >> 4) * 20480 + (r32 & 15) * 16 + hh * 8); pin(vo);
  u32x4 fill = {0xffffffffu, 0xffffffffu, 0xffffffffu, 0xffffffffu}; pin(fill);
  const u32x2 fill2 = {fill[0], fill[1]};
  u32x2 fa, fb, fc;
  const unsigned fvc = (unsigned)CTL_FR + (unsigned)((cb4 * 60 + min(lane, 59)) * 8);
  const unsigned fva = (unsigned)(hh ? CTL_F4 : CTL_F2) + (unsigned)(((rb3 * 2 + ((w & 3) >> 1)) * 32 + r32) * 8), fvb = lane < 32 ? (unsigned)CTL_F3 + (unsigned)(((rb4 * 16 + w * 2) * 8 + (lane & 15)) * 8) : (unsigned)CTL_FR + (unsigned)((cb3 * 240 + w * 30 + min(lane - 32, 29)) * 8);
  auto hook = [&](auto it_, auto i_) {
    constexpr int it = it_, i = i_, n = (it - 2) * 8 + i;
    if constexpr (n >= 0 && n < 2) st8s(obs[n], vo, fill2);
    if constexpr (n == 2) { if (!hh) st8s(obs[2], vo, fill2); }
    if constexpr (n >= 3 && n < 6) { const int L = (n - 3) * 64 + lane; st16s(ws + WS_G + (size_t)(rb4 * 16 + w * 2) * 49152 + cb4 * 1536, (unsigned)(L * 16 + (L >= 96 ? 49152 - 1536 : 0)), fill); }
    if constexpr (n >= 6 && n < 9) { if (w < 4) st16s(ws + WS_H + (size_t)(rb3 * 4 + w) * 20480 + cb3 * 2560, (unsigned)(min((n - 6) * 64 + lane, 159) * 16), fill); }
    if constexpr (n == 9) { if (w < 4 && lane < 16) st8s(ws + WS_HST + (size_t)(rb3 * 4 + w) * 1024 + (cb3 >> 1) * 256 + (cb3 & 1) * 8, (unsigned)(lane * 16), fill2); }
    if constexpr (it == 6 && i == 6) {
      waitv<6>();
      setflag(ctl + CTL_FR + gw * 8, tag, lane);
      setflag(ctl + CTL_F2 + (qtile * 32 + head) * 8, tag, lane);
      setflag(ctl + CTL_F4 + ((rb4 * 8 + w) * 32 + cb4) * 8, tag, lane);
      if (w < 4) setflag(ctl + CTL_F3 + ((rb3 * 4 + w) * 8 + cb3) * 8, tag, lane);
    }
    if constexpr (it == 7 && i == 6) { gld8<0>(fa, ctr, fva); gld8<0>(fb, ctr, fvb); gld8<0>(fc, ctr, fvc); }
    if constexpr (it < 2) glds16<true>(kvg + (size_t)(it * 8 + 4 + i) * 4096, voff, kvl + (it * 8 + 4 + i) * 4096);
    else if constexpr (it < 4) gldu16<(i & 1) * 3072>(pw[it - 2][i], psrc[it - 2] + (i & ~1) * 3072, pvo1);
    else if constexpr (it == 4) {
      gldu16<(i & 3) * 1280>(pw[2][i], psrc[2] + (i & ~3) * 1280, pvo2);
      if constexpr (i == 0) rstore(IC<0>{}, IC<9>{});
      if constexpr (i == 7) rstore(IC<1>{}, IC<8>{});
    } else if constexpr (it == 5) {
      if constexpr (i == 7) rstore(IC<2>{}, IC<2>{});
    } else {
      constexpr int j = (it - 6) * 8 + i;
      const char* src = wog + rotf(w + 8 * j, 20 * ROT3(cb3), 100) * 1024;
      if constexpr (j < 7) glds16<true>(src, voff, lds0 + w * 1024 + j * 8192);
      else if constexpr (j == 7) glds16<true>(src, voff, lds0 + w * 1024 + j * 8192 + (w >= 4 ? 20480 : 0));
      else if constexpr (j < 12) glds16<true>(src, voff, lds0 + w * 1024 + j * 8192 + 20480);
      else if constexpr (j == 12) { if (w < 4) glds16<true>(src, voff, lds0 + w * 1024 + j * 8192 + 20480); }
    }
    SB();
  };
  const unsigned ka0b = ka0 + 40960, ka1b = ka1 + 40960, vab = va + 40960;
  sfor<8>([&](auto it_) {
    constexpr int it = it_;
    if constexpr (it == 1) WAITV_BAR(6);
    if constexpr (it == 2) { WAITV_BAR(8); STAMP_AT(1, 3); }
    if constexpr (it == 4) asm volatile("s_waitcnt vmcnt(16)\n\ts_barrier" ::: "memory");
    if constexpr (it == 6) asm volatile("s_barrier" ::: "memory");
    if (((it ^ kh) & 1) == 0) __builtin_amdgcn_s_setprio(0); else __builtin_amdgcn_s_setprio(1);
    constexpr int o0 = (it & 3) * 10240;
    constexpr int on = it == 3 ? 0 : o0 + 10240;
    const unsigned a0 = it < 4 ? ka0 : ka0b, a1 = it < 4 ? ka1 : ka1b, av = it < 4 ? va : vab;
    const unsigned n0 = it < 3 ? ka0 : ka0b, n1 = it < 3 ? ka1 : ka1b, nv = it < 3 ? va : vab;
    rdK<o0 + 2560>(K0[1], K1[1], a0, a1); hook(it_, IC<0>{});                  SLOT(true, SA, PA0, PA1, SB_, 0, 1, PB0, PB1, 1, 1);
    LWAIT(1, 0); rdV<o0 + 2560>(V0[1], V1[1], av, vmsk); hook(it_, IC<1>{});   SLOT(true, SB_, PB0, PB1, SA, 1, 0, PA0, PA1, 0, 0);
    rdK<o0 + 5120>(K0[0], K1[0], a0, a1); hook(it_, IC<2>{});                  SLOT(true, SA, PA0, PA1, SB_, 1, 1, PB0, PB1, 0, 1);
    LWAIT(0, 1); rdV<o0 + 5120>(V0[0], V1[0], av, vmsk); hook(it_, IC<3>{});   SLOT(true, SB_, PB0, PB1, SA, 0, 0, PA0, PA1, 1, 0);
    rdK<o0 + 7680>(K0[1], K1[1], a0, a1); hook(it_, IC<4>{});                  SLOT(true, SA, PA0, PA1, SB_, 0, 1, PB0, PB1, 1, 1);
    LWAIT(1, 0); rdV<o0 + 7680>(V0[1], V1[1], av, vmsk); hook(it_, IC<5>{});   SLOT(true, SB_, PB0, PB1, SA, 1, 0, PA0, PA1, 0, 0);
    if constexpr (it < 7) rdK<on>(K0[0], K1[0], n0, n1);
    hook(it_, IC<6>{});                                                        SLOT(true, SA, PA0, PA1, SB_, 1, 1, PB0, PB1, 0, 1);
    LWAIT(0, 1);
    if constexpr (it < 7) rdV<on>(V0[0], V1[0], nv, vmsk);
    hook(it_, IC<7>{});                                                        SLOT(it < 7, SB_, PB0, PB1, SA, 0, 0, PA0, PA1, 1, 0);
  });
  __builtin_amdgcn_s_setprio(0);
  o[1] = MFMA32(V0[1], __builtin_bit_cast(bf16x8, PB0), o[1]);
  o[1] = MFMA32(V1[1], __builtin_bit_cast(bf16x8, PB1), o[1]);
  STAMP_AT(1, 9);
  bar();
  {
    LAS f32x4* xch = (LAS f32x4*)(uintptr_t)(lds0 + XCH_OFF + w * 3072);
    const f32x16& snd = kh ? o[0] : o[1];
#pragma unroll
    for (int g = 0; g < 3; ++g) xch[g * 64 + lane] = (f32x4){snd[4 * g], snd[4 * g + 1], snd[4 * g + 2], snd[4 * g + 3]};
  }
  bar();
  STAMP_AT(1, 10);
  float of[12];
  {
    const LAS f32x4* xch = (const LAS f32x4*)(uintptr_t)(lds0 + XCH_OFF + (w ^ 4) * 3072);
    const f32x16& own = kh ? o[1] : o[0];
#pragma unroll
    for (int g = 0; g < 3; ++g) { const f32x4 v = xch[g * 64 + lane];
#pragma unroll
      for (int j = 0; j < 4; ++j) of[4 * g + j] = own[4 * g + j] + v[j]; }
  }
  float l = hi32(of[8]);
  bool ok = (l > 8.67e-19f) && (l < 1.15e18f);
#pragma unroll
  for (int i = 0; i < 8; ++i) ok = ok && (fabsf(of[i]) < 1e37f);
  if (!hh) {
#pragma unroll
    for (int i = 8; i < 12; ++i) ok = ok && (fabsf(of[i]) < 1e37f);
  }
  if (__builtin_expect(!__all(ok), 0)) {
    __builtin_amdgcn_fence(__ATOMIC_ACQUIRE, "agent");
    const char* kv = ws + WS_KV + (size_t)head * 163840;
    const bf16x8 fq0 = kh ? q0[1] : q0[0];
    const s16x4 fq1 = kh ? q1[1] : q1[0];
    float m = -INFINITY;
    for (int t = 0; t < 64; ++t) {
      const bf16x8 kf0 = *(const bf16x8*)(kv + t * 2560 + lane * 16); const s16x4 kf1 = *(const s16x4*)(kv + t * 2560 + 1024 + r32 * 8);
      f32x16 s = MFMA32(kf0, fq0, (f32x16){}); s = MFMA32K8(kf1, fq1, s);
#pragma unroll
      for (int i = 0; i < 16; ++i) m = fmaxf(m, s[i]);
    }
    m = max32(m);
    f32x16 oo = {};
    const int vr = r32 < 20 ? r32 : 0;
    for (int t = 0; t < 64; ++t) {
      const bf16x8 kf0 = *(const bf16x8*)(kv + t * 2560 + lane * 16); const s16x4 kf1 = *(const s16x4*)(kv + t * 2560 + 1024 + r32 * 8);
      bf16x8 vf0 = *(const bf16x8*)(kv + t * 2560 + 1280 + (hh * 20 + vr) * 16), vf1 = *(const bf16x8*)(kv + t * 2560 + 1920 + (hh * 20 + vr) * 16);
      if (r32 >= 20) { vf0 = (bf16x8)(short)0x3f80; vf1 = vf0; }
      f32x16 s = MFMA32(kf0, fq0, (f32x16){}); s = MFMA32K8(kf1, fq1, s);
      u32x4 p0, p1;
#pragma unroll
      for (int i = 0; i < 4; ++i) {
        p0[i] = pk_bf16(EX2(s[2 * i] - m), EX2(s[2 * i + 1] - m));
        p1[i] = pk_bf16(EX2(s[8 + 2 * i] - m), EX2(s[8 + 2 * i + 1] - m));
      }
      oo = MFMA32(vf0, __builtin_bit_cast(bf16x8, p0), oo);
      oo = MFMA32(vf1, __builtin_bit_cast(bf16x8, p1), oo);
    }
#pragma unroll
    for (int i = 0; i < 12; ++i) of[i] = oo[i];
    l = hi32(of[8]);
  }
  const float rl = __builtin_amdgcn_rcpf(l);
  waitv<0>(); pin(fa); pin(fb); pin(fc);
#pragma unroll
  for (int g = 0; g < 3; ++g)
    if (g < 2 || !hh) st8s(obs[g], vo, nofill(pk_bf16(of[4 * g] * rl, of[4 * g + 1] * rl), pk_bf16(of[4 * g + 2] * rl, of[4 * g + 3] * rl)));
  STAMP_AT(1, 11);
  if (!__all(fa[0] == tag[0] && fa[1] == tag[1] && fb[0] == tag[0] && fb[1] == tag[1])) poll2(ctr, fva, fvb, tag);
  if (!__all(fc[0] == tag[0] && fc[1] == tag[1])) poll(ctr, fvc, tag);
  STAMP_AT(1, 12);
}

constexpr int x_none(int) { return 0; }
constexpr int x_p4(int i) { return ((i >= 2 && i < 7) || (i >= 10 && i < 18)) ? 1 : 0; }
template <int NSTEP, int NF, int LA, int LL, int NA, int ASTR, int (*BOFF)(int), int (*XCNT)(int), class PRE, class MM>
DI void gemm_res(const char* asrc, int rot, unsigned lds0, unsigned voff, PRE&& pre, MM&& mm) {
  bf16x8 a[NSTEP][NA];
  bf16x8 bf[LL + 1][NF];
  unsigned rd0 = lds0 + voff, rd1 = rd0 + 65536, rd2 = rd0 + 131072; pin(rd0); pin(rd1); pin(rd2);
  auto issue = [&](auto n_) { constexpr int n = n_; sfor<NA>([&](auto j) { gld16<(n & 3) * 1024>(a[n][j], asrc + rotf(n >> 2, rot, NSTEP / 4) * 4096 + j * ASTR, voff); }); };
  constexpr auto xsum = [](int lo, int hi) { int c = 0; for (int i = lo < 0 ? 0 : lo; i <= hi; ++i) c += XCNT(i); return c; };
  auto rd = [&](auto i_, auto f_) {
    constexpr int i = i_, f = f_, off = BOFF(i * NF + f);
    bf[i % (LL + 1)][f] = lds128((off < 65536 ? rd0 : off < 131072 ? rd1 : rd2) + (off & 65535));
  };
  auto check = [&](auto i_) {
    constexpr int i = i_;
    sfor<NA>([&](auto j) { pin(a[i][j]); });
    auto mx = [&]() { unsigned m = 0; sfor<NA>([&](auto j) { m = max(m, umax4(__builtin_bit_cast(u32x4, a[i][j]))); }); return m; };
    if (__builtin_expect(__any(mx() == 0xffffffffu), 0)) {
#pragma unroll 1
      for (int spin = 0; spin < (1 << 17); ++spin) {
        RETRY_COUNT(NSTEP + NF);
        issue(i_); waitv<0>(); sfor<NA>([&](auto j) { pin(a[i][j]); });
        if (!__any(mx() == 0xffffffffu)) break;
      }
    }
  };
  sfor<LA>([&](auto n) { issue(n); });
  pre(IC<-1>{});
  sfor<LL>([&](auto i) { sfor<NF>([&](auto f) { rd(i, f); }); });
  waitv<(LA - 1) * NA>(); check(IC<0>{});
  sfor<NSTEP>([&](auto i_) {
    constexpr int i = i_;
    pre(i_);
    SB();
    sfor<NF>([&](auto f_) {
      constexpr int f = f_;
      if constexpr (f == NF - 1 && i + 1 < NSTEP) {
        waitv<((i + LA < NSTEP ? i + LA : NSTEP - 1) - (i + 1)) * NA + xsum(i - LA + 2, i)>(); check(IC<i + 1>{}); SB(); }
      mm(f_, bf[i % (LL + 1)][f], a[i]);
      SB();
      if constexpr (i + LL < NSTEP) rd(IC<i + LL>{}, f_);
      if constexpr (f == 0 && i + LA < NSTEP) issue(IC<i + LA>{});
      SB();
    });
  });
}

__global__ __launch_bounds__(512, 2) void k_mega(const u16* __restrict__ x, const u16* __restrict__ Wqkv, const u16* __restrict__ Wo,
                                                 const u16* __restrict__ Wg, const u16* __restrict__ Wu, const u16* __restrict__ Wd,
                                                 char* __restrict__ ws, u16* __restrict__ out) {
  __shared__ __attribute__((aligned(16))) char smem[163840];
  const unsigned lds0 = lds_addr_of(smem);
  const int w = __builtin_amdgcn_readfirstlane(threadIdx.x >> 6);
  const int g8 = blockIdx.x & 7, i32 = blockIdx.x >> 3;
  char* ctl = ws + WS_CTL;
  const char* ctr = ctl + g8 * CTL_REP;
  unsigned cdum = 0;
  u32x2 ep;
  gld8<0>(ep, ctl, 0u);
  int f1idx;
  if (i32 < 30) {
    const int i = (g8 & 1) * 30 + i32, rb = (g8 >> 1) * 4 + (i & 3), cb = i >> 2;
    if (cb >= 10) p1_body<true>(x, Wqkv, ws, lds0, w, rb, cb);
    else p1_body<false>(x, Wqkv, ws, lds0, w, rb, cb);
    f1idx = (cb * 4 + (w & 3)) * 32 + rb * 2 + (w >> 2);
  } else {
    p1_repack(Wo, ws, w, blockIdx.x - 240);
    f1idx = 1920 + (blockIdx.x - 240) * 8 + w;
  }
  waitv<0>();
  pin(ep); pin(cdum);
  u32x2 tag;
  {
    const unsigned long long e = (((unsigned long long)__builtin_amdgcn_readfirstlane(ep[1]) << 32) | __builtin_amdgcn_readfirstlane(ep[0])) + 1ull;
    tag = (u32x2){(unsigned)e, (unsigned)(e >> 32)};
  }
  publish(ctl + CTL_F1 + f1idx * 8, tag, fresh_lane());
  bar();
  const int rb3 = g8 * 4 + (i32 >> 3), cb3 = i32 & 7;
  const int rb4 = (g8 >> 2) * 4 + (i32 >> 3), cb4 = (g8 & 3) * 8 + (i32 & 7);
  p2_attn(ws, Wg, Wu, Wd, lds0, w, tag, rb3, cb3, rb4, cb4);
  bar();
  const char* gug = ws + WS_WP + (size_t)NFR_WO * 1024 + (size_t)cb4 * 122880;
  const char* wdg = ws + WS_WP + (size_t)(NFR_WO + NFR_GU) * 1024 + (size_t)cb3 * 245760;
  const int rg4 = 24 * ROT3(cb4 & 7), rg5 = 20 * ROT5(cb3);
  u32x2 hp[5];
  if (w < 4) {
    const int lane = fresh_lane();
    const unsigned voff = lane * 16;
    const int row = rb3 * 64 + w * 16 + (lane & 15), col0 = cb3 * 80 + 4 * (lane >> 4);
    STAMP_AT(2, 0);
    u32x2 xr[5];
#pragma unroll
    for (int ct = 0; ct < 5; ++ct) xr[ct] = *(const u32x2*)(x + (size_t)row * 640 + col0 + 16 * ct);
    { const int hd = lane & 31, cgt = (hd & 1) ? hd * 5 : hd * 5 + 4;
      probe(ws + WS_O + (size_t)(rb3 * 4 + w) * 20480, (unsigned)((cgt >> 3) * 1024 + ((cgt & 7) >> 1) * 256 + (cgt & 1) * 8)); }
    sfor<5>([&](auto ct) { pin(xr[ct]); });
    STAMP_AT(2, 1);
    f32x4 acc[5] = {};
    gemm_res<20, 5, 20, 2, 1, 0, wo_off, x_none>(ws + WS_O + (size_t)(rb3 * 4 + w) * 20480, ROT3(cb3), lds0, voff,
      [&](auto i) { if constexpr (i == -1) STAMP_AT(2, 2); if constexpr (i == 1) STAMP_AT(2, 3); if constexpr (i == 10) STAMP_AT(2, 4);
        if constexpr (i == P3_GO) bar(); },
      [&](auto ct, const bf16x8& b, bf16x8 (&a)[1]) { acc[ct] = MFMA16(b, a[0], acc[ct]); });
    STAMP_AT(2, 6);
    float s1 = 0.f, s2 = 0.f;
    const char* hb = ws + WS_H + (size_t)(rb3 * 4 + w) * 20480;
    const unsigned vt = t16lane(lane);
#pragma unroll
    for (int ct = 0; ct < 5; ++ct) {
      hp[ct] = nofill(pk_bf16(bf_lo(xr[ct][0]) + acc[ct][0], bf_hi(xr[ct][0]) + acc[ct][1]), pk_bf16(bf_lo(xr[ct][1]) + acc[ct][2], bf_hi(xr[ct][1]) + acc[ct][3]));
      st8s(hb + t16off(cb3 * 80 + ct * 16), vt, hp[ct]);
      s1 = dot2(hp[ct][0], 0x3f803f80u, s1); s1 = dot2(hp[ct][1], 0x3f803f80u, s1);
      s2 = dot2(hp[ct][0], hp[ct][0], s2); s2 = dot2(hp[ct][1], hp[ct][1], s2);
    }
    s1 = red4(s1); s2 = red4(s2);
    if (lane < 16) st8s(ws + WS_HST + (size_t)(rb3 * 4 + w) * 1024 + (cb3 >> 1) * 256 + (cb3 & 1) * 8, (unsigned)(lane * 16), nofill(__float_as_uint(s1), __float_as_uint(s2)));
    bar();
    STAMP_AT(2, 7);
  } else {
    const int lane = fresh_lane(), v = w - 4;
    const unsigned voff = lane * 16;
    float cpart[10];
    const int cc = v * 24 + min(lane & 31, 23);
    sfor<10>([&](auto i) { gld4<i * 384>(cpart[i], ws + WS_CSP + (size_t)cb4 * 7680, (unsigned)((lane >> 5) * 3840 + cc * 4)); });
    waitv<0>();
    float cv = 0.f;
    sfor<10>([&](auto j) { pin(cpart[j]); cv += cpart[j]; });
    cv = add32(cv);
    bar();
    STAMP_AT(2, 2);
    sfor<5>([&](auto j) { glds16<true>(gug + rotf(v * 5 + j, rg4, 120) * 1024, voff, lds0 + gu_off(0) + (v * 5 + j) * 1024); });
    sfor<10>([&](auto j) { glds16<true>(gug + rotf(20 + v * 10 + j, rg4, 120) * 1024, voff, lds0 + gu_off(20) + (v * 10 + j) * 1024); });
    sfor<15>([&](auto j) { glds16<true>(gug + rotf(60 + v * 15 + j, rg4, 120) * 1024, voff, lds0 + gu_off(60) + (v * 15 + j) * 1024); });
    STAMP_AT(2, 5);
    waitv<0>();
    bar();
    STAMP_AT(2, 6);
    if (lane < 24) ((LAS float*)(uintptr_t)(lds0 + CSF_OFF))[cc] = cv;
  }
  {
    const int lane = fresh_lane();
    const unsigned voff = lane * 16;
    const int r16 = lane & 15, g4 = lane >> 4;
    STAMP_AT(3, 0);
    f32x4 cs[6]; f32x2 ms[2];
    f32x4 hs[2];
#pragma unroll 1
    for (int spin = 0; spin < (1 << 17); ++spin) {
      gld16<0>(hs[0], ws + WS_HST + (size_t)(rb4 * 16 + w * 2) * 1024, voff);
      gld16<1024>(hs[1], ws + WS_HST + (size_t)(rb4 * 16 + w * 2) * 1024, voff);
      waitv<0>(); pin(hs[0]); pin(hs[1]);
      if (!__any(max(umax4(__builtin_bit_cast(u32x4, hs[0])), umax4(__builtin_bit_cast(u32x4, hs[1]))) == 0xffffffffu)) break;
      __builtin_amdgcn_s_sleep(1);
    }
    STAMP_AT(3, 1);
    f32x4 acc[2][6] = {};
    if (w < 4) __builtin_amdgcn_s_setprio(1);
    gemm_res<20, 6, K4_LA_, 1, 2, 20480, gu_off, x_p4>(ws + WS_H + (size_t)(rb4 * 16 + w * 2) * 20480, ROT3(cb4 & 7), lds0, voff,
      [&](auto i) { if constexpr (i == -1) STAMP_AT(3, 2); if constexpr (i == 1) STAMP_AT(3, 3); if constexpr (i == 10) STAMP_AT(3, 4);
        if constexpr (i == 9) bar();
        if constexpr (i == -1) sfor<2>([&](auto t) {
          const float s1 = red4(hs[t][0] + hs[t][2]), s2 = red4(hs[t][1] + hs[t][3]), mean = s1 * (1.f / 640.f);
          ms[t] = (f32x2){mean, __builtin_amdgcn_rsqf(fmaxf(s2 * (1.f / 640.f) - mean * mean, 0.f) + LN_EPS)}; pin(ms[t]); });
        if constexpr (i == 18) sfor<6>([&](auto ct) { cs[ct] = *(const LAS f32x4*)(uintptr_t)(lds0 + CSF_OFF + (ct * 16 + 4 * g4) * 4); });
        if constexpr (i >= 2 && i < 7) { const int f = (i - 2) * 8 + w - ((i == 6 && w >= 4) ? 4 : 0); glds16<true>(wdg + rotf(f, rg5, 240) * 1024, voff, lds0 + wd_off(0) + f * 1024); }
        if constexpr (i >= 10 && i < 15) { constexpr int n = i - 10; glds16<true>(wdg + rotf(40 + n * 8 + w, rg5, 240) * 1024, voff, lds0 + wd_off(40) + (n * 8 + w) * 1024); }
        if constexpr (i >= 15 && i < 18) { const int f = (i - 15) * 8 + w - ((i == 17 && w >= 4) ? 4 : 0); glds16<true>(wdg + rotf(140 + f, rg5, 240) * 1024, voff, lds0 + wd_off(140) + f * 1024); } },
      [&](auto ct, const bf16x8& b, bf16x8 (&a)[2]) { acc[0][ct] = MFMA16(b, a[0], acc[0][ct]); acc[1][ct] = MFMA16(b, a[1], acc[1][ct]); });
    STAMP_AT(3, 6);
    constexpr float GC0 = -2.f * 0.7978845608028654f * 1.4426950408889634f, GC1 = GC0 * 0.044715f;
    const char* gb = ws + WS_G + (size_t)(rb4 * 16 + w * 2) * 49152;
    const unsigned vt = t16lane(lane);
#pragma unroll
    for (int t = 0; t < 2; ++t) {
      const float nm = -ms[t].x, rstd = ms[t].y;
#pragma unroll
      for (int ct = 0; ct < 3; ++ct) {
        const f32x4 gv = (acc[t][ct] + cs[ct] * nm) * rstd, uv = (acc[t][3 + ct] + cs[3 + ct] * nm) * rstd;
        const f32x4 z = gv * (gv * gv * GC1 + GC0);
        f32x4 e;
#pragma unroll
        for (int j = 0; j < 4; ++j) e[j] = __builtin_amdgcn_exp2f(z[j]);
        e = e + 1.f;
#pragma unroll
        for (int j = 0; j < 4; ++j) e[j] = __builtin_amdgcn_rcpf(e[j]);
        const f32x4 r = gv * uv * e;
        st8s(gb + t * 49152 + t16off(cb4 * 48 + ct * 16), vt, nofill(pk_bf16(r[0], r[1]), pk_bf16(r[2], r[3])));
      }
    }
    __builtin_amdgcn_s_setprio(0);
    STAMP_AT(3, 7);
    waitv<6>();
    STAMP_AT(3, 8);
    bar();
  }
  if (w < 4) {
    const int lane = fresh_lane();
    const unsigned voff = lane * 16;
    const int row = rb3 * 64 + w * 16 + (lane & 15), col0 = cb3 * 80 + 4 * (lane >> 4);
    STAMP_AT(4, 0);
    probe(ws + WS_G + (size_t)(rb3 * 4 + w) * 49152, (unsigned)t16off((lane & 31) * 48 + 32));
    STAMP_AT(4, 1);
    f32x4 acc[5] = {};
    gemm_res<48, 5, K5_LA_, 2, 1, 0, wd_off, x_none>(ws + WS_G + (size_t)(rb3 * 4 + w) * 49152, ROT5(cb3), lds0, voff,
      [&](auto i) { if constexpr (i == 1) STAMP_AT(4, 3); if constexpr (i == 14) STAMP_AT(4, 4); if constexpr (i == 30) STAMP_AT(4, 5);
        if constexpr (i == -1 || i == 8 || i == 14 || i == 16 || i == 30 || i == 38) bar(); },
      [&](auto ct, const bf16x8& b, bf16x8 (&a)[1]) { acc[ct] = MFMA16(b, a[0], acc[ct]); });
    STAMP_AT(4, 12);
#pragma unroll
    for (int ct = 0; ct < 5; ++ct) {
      const unsigned p0 = pk_bf16(bf_lo(hp[ct][0]) + acc[ct][0], bf_hi(hp[ct][0]) + acc[ct][1]);
      const unsigned p1 = pk_bf16(bf_lo(hp[ct][1]) + acc[ct][2], bf_hi(hp[ct][1]) + acc[ct][3]);
      *(u32x2*)(out + (size_t)row * 640 + col0 + 16 * ct) = (u32x2){p0, p1};
    }
    STAMP_AT(4, 13);
  } else {
    const int lane = fresh_lane(), v = w - 4;
    const unsigned voff = lane * 16;
    glds16<true>(wdg + rotf(36 + v, rg5, 240) * 1024, voff, lds0 + wd_off(36) + v * 1024);
    waitv<0>(); bar();
    sfor<3>([&](auto g) { sfor<5>([&](auto j) { constexpr int f = (g + 4) * 20 + j; glds16<true>(wdg + rotf(f + v * 5, rg5, 240) * 1024, voff, lds0 + wd_off(f) + v * 5120); }); });
    bar();
    sfor<2>([&](auto g) { sfor<5>([&](auto j) { constexpr int f = (g + 8) * 20 + j; glds16<true>(wdg + rotf(f + v * 5, rg5, 240) * 1024, voff, lds0 + wd_off(f) + v * 5120); }); });
    waitv<10>(); bar();
    bar();
    sfor<2>([&](auto g) { sfor<5>([&](auto j) { constexpr int f = (g + 10) * 20 + j; glds16<true>(wdg + rotf(f + v * 5, rg5, 240) * 1024, voff, lds0 + wd_off(f) + v * 5120); }); });
    waitv<10>(); bar();
    waitv<0>(); bar();
    if (blockIdx.x == 0 && w == 4) {
      poll2(ctr + CTL_FR, (unsigned)(lane * 64), (unsigned)(4096 + lane * 64), tag);
      poll2(ctr + CTL_FR, (unsigned)(8192 + lane * 64), (unsigned)(12288 + lane * 64), tag);
      if (lane == 0) st8(ctl, tag);
    }
  }
}

extern "C" void kernel_launch(void* const* d_in, const int* in_sizes, int n_in, void* d_out, int out_size, void* d_ws, size_t ws_size,
                              hipStream_t stream) {
  k_mega<<<256, 512, 0, stream>>>((const u16*)d_in[0], (const u16*)d_in[1], (const u16*)d_in[2], (const u16*)d_in[4], (const u16*)d_in[5],
                                  (const u16*)d_in[6], (char*)d_ws, (u16*)d_out);
}
```

```cpp
#include <hip/hip_runtime.h>
#include <hip/hip_bf16.h>
#include <stdint.h>
#include <utility>
#define NO_ROT 1
#define NO_ROT2 1

typedef short bf16x8 __attribute__((ext_vector_type(8)));
typedef short s16x4 __attribute__((ext_vector_type(4)));
typedef float f32x16 __attribute__((ext_vector_type(16)));
typedef float f32x4 __attribute__((ext_vector_type(4)));
typedef float f32x2 __attribute__((ext_vector_type(2)));
typedef unsigned u32x4 __attribute__((ext_vector_type(4)));
typedef unsigned u32x2 __attribute__((ext_vector_type(2)));
typedef __bf16 bf2_t __attribute__((ext_vector_type(2)));
typedef unsigned short u16;
#define LAS __attribute__((address_space(3)))
#define DI __device__ __forceinline__

constexpr int S_ = 2048, D_ = 640, H_ = 32, DH = 20, F_ = 1536;
constexpr float LN_EPS = 1e-5f;
constexpr float QSCALE = 0.22360679774997896f * 1.4426950408889634f;

constexpr size_t WS_Q   = 0;
constexpr size_t WS_KV  = WS_Q + 2621440;
constexpr size_t WS_O   = WS_KV + 5242880;
constexpr size_t WS_H   = WS_O + 2621440;
constexpr size_t WS_HST = WS_H + 2621440;
constexpr size_t WS_G   = WS_HST + 131072;
constexpr size_t WS_WP  = WS_G + 6291456;
constexpr size_t WS_CSP = WS_WP + 6717440;
constexpr size_t WS_CTL = WS_CSP + 245760;
constexpr size_t CTL_F1 = 256;
constexpr size_t CTL_F2 = CTL_F1 + 2048 * 8;
constexpr size_t CTL_FR = CTL_F2 + 2048 * 8;
constexpr size_t CTL_F3 = CTL_FR + 2048 * 8;
constexpr size_t CTL_F4 = CTL_F3 + 1024 * 8;
constexpr size_t CTL_REP = CTL_F4 + 2048 * 8 - 256;
constexpr int NFR_WO = 800, NFR_GU = 3840, NFR_WD = 1920, NFR_ALL = NFR_WO + NFR_GU + NFR_WD;

DI unsigned pk_bf16(float lo, float hi) { f32x2 f = {lo, hi}; return __builtin_bit_cast(unsigned, __builtin_convertvector(f, bf2_t)); }
DI float bf_lo(unsigned u) { return __uint_as_float(u << 16); }
DI float bf_hi(unsigned u) { return __uint_as_float(u & 0xffff0000u); }
DI float dot2(unsigned a, unsigned b, float c) { return __builtin_amdgcn_fdot2_f32_bf16(__builtin_bit_cast(bf2_t, a), __builtin_bit_cast(bf2_t, b), c, false); }
DI float add32(float v) { const auto r = __builtin_amdgcn_permlane32_swap(__float_as_uint(v), __float_as_uint(v), false, false); return __uint_as_float(r[0]) + __uint_as_float(r[1]); }
DI float max32(float v) { const auto r = __builtin_amdgcn_permlane32_swap(__float_as_uint(v), __float_as_uint(v), false, false); return fmaxf(__uint_as_float(r[0]), __uint_as_float(r[1])); }
DI float add16(float v) { const auto r = __builtin_amdgcn_permlane16_swap(__float_as_uint(v), __float_as_uint(v), false, false); return __uint_as_float(r[0]) + __uint_as_float(r[1]); }
DI float red4(float v) { return add32(add16(v)); }
DI float hi32(float v) { const auto r = __builtin_amdgcn_permlane32_swap(__float_as_uint(v), __float_as_uint(v), false, false); return __uint_as_float(r[1]); }
#define MFMA32(a, b, c) __builtin_amdgcn_mfma_f32_32x32x16_bf16((a), (b), (c), 0, 0, 0)
#define MFMA32K8(a, b, c) __builtin_amdgcn_mfma_f32_32x32x8bf16_1k((a), (b), (c), 0, 0, 0)
#define MFMA16(a, b, c) __builtin_amdgcn_mfma_f32_16x16x32_bf16((a), (b), (c), 0, 0, 0)

#define P1_POL ""
template <bool DEV> DI void glds16(const void* sbase, unsigned voff, unsigned lds_base) {
  unsigned sv;
  if constexpr (DEV)
    asm volatile("s_mov_b32 %0, m0\n\ts_mov_b32 m0, %3\n\ts_nop 0\n\tglobal_load_lds_dwordx4 %1, %2 sc1\n\ts_mov_b32 m0, %0"
                 : "=&s"(sv) : "v"(voff), "s"(sbase), "s"(lds_base) : "memory");
  else
    asm volatile("s_mov_b32 %0, m0\n\ts_mov_b32 m0, %3\n\ts_nop 0\n\tglobal_load_lds_dwordx4 %1, %2" P1_POL "\n\ts_mov_b32 m0, %0"
                 : "=&s"(sv) : "v"(voff), "s"(sbase), "s"(lds_base) : "memory");
}
template <int OFF, class T> DI void gld16(T& dst, const void* sbase, unsigned voff) {
  asm volatile("global_load_dwordx4 %0, %1, %2 offset:%3 sc1" : "=v"(dst) : "v"(voff), "s"(sbase), "i"(OFF) : "memory");
}
template <int OFF, class T> DI void gld8(T& dst, const void* sbase, unsigned voff) {
  asm volatile("global_load_dwordx2 %0, %1, %2 offset:%3 sc1" : "=v"(dst) : "v"(voff), "s"(sbase), "i"(OFF) : "memory");
}
template <int OFF> DI void gld4(float& dst, const void* sbase, unsigned voff) {
  asm volatile("global_load_dword %0, %1, %2 offset:%3 sc1" : "=v"(dst) : "v"(voff), "s"(sbase), "i"(OFF) : "memory");
}
DI void asm_warm(u32x4& dst, const void* sbase, unsigned voff) {
  asm volatile("global_load_dwordx4 %0, %1, %2 sc1" : "+v"(dst) : "v"(voff), "s"(sbase) : "memory");
}
template <int OFF> DI void gldu16(unsigned& dst, const void* sbase, unsigned voff) {
  asm volatile("global_load_ushort %0, %1, %2 offset:%3" : "=v"(dst) : "v"(voff), "s"(sbase), "i"(OFF) : "memory");
}
DI void st16(void* p, u32x4 v) { asm volatile("global_store_dwordx4 %0, %1, off sc1\n\ts_nop 1" :: "v"(p), "v"(v) : "memory"); }
DI void st8(void* p, u32x2 v) { asm volatile("global_store_dwordx2 %0, %1, off sc1" :: "v"(p), "v"(v) : "memory"); }
DI void st4(void* p, float v) { asm volatile("global_store_dword %0, %1, off sc1" :: "v"(p), "v"(v) : "memory"); }
DI void st8s(const void* sbase, unsigned voff, u32x2 v) { asm volatile("global_store_dwordx2 %0, %1, %2 sc1" :: "v"(voff), "v"(v), "s"(sbase) : "memory"); }
DI u32x2 nofill(unsigned a, unsigned b) { return (u32x2){min(a, 0xfffffffeu), min(b, 0xfffffffeu)}; }
DI unsigned umax4(u32x4 v) { return max(max(v[0], v[1]), max(v[2], v[3])); }
template <int N> DI void waitv() { asm volatile("s_waitcnt vmcnt(%0)" :: "i"(N) : "memory"); }
DI void bar() { asm volatile("s_waitcnt lgkmcnt(0)\n\ts_barrier" ::: "memory"); }
DI void setflag(char* flag, u32x2 tag, int lane) { if (lane < 8) st8(flag + lane * CTL_REP, tag); }
DI void publish(char* flag, u32x2 tag, int lane) { waitv<0>(); setflag(flag, tag, lane); }
DI void poll(const char* base, unsigned voff, u32x2 tag) {
#pragma unroll 1
  for (int spin = 0; spin < (1 << 17); ++spin) {
    u32x2 v;
    asm volatile("global_load_dwordx2 %0, %1, %2 sc1\n\ts_waitcnt vmcnt(0)" : "=v"(v) : "v"(voff), "s"(base) : "memory");
    if (__all(v[0] == tag[0] && v[1] == tag[1])) break;
    __builtin_amdgcn_s_sleep(1);
  }
}
DI void poll2(const char* base, unsigned voff0, unsigned voff1, u32x2 tag) {
#pragma unroll 1
  for (int spin = 0; spin < (1 << 17); ++spin) {
    u32x2 v, u;
    asm volatile("global_load_dwordx2 %0, %2, %4 sc1\n\tglobal_load_dwordx2 %1, %3, %4 sc1\n\ts_waitcnt vmcnt(0)" : "=&v"(v), "=&v"(u) : "v"(voff0), "v"(voff1), "s"(base) : "memory");
    if (__all(v[0] == tag[0] && v[1] == tag[1] && u[0] == tag[0] && u[1] == tag[1])) break;
    __builtin_amdgcn_s_sleep(1);
  }
}
DI void probe(const char* base, unsigned voff) {
#pragma unroll 1
  for (int spin = 0; spin < (1 << 17); ++spin) {
    unsigned v;
    asm volatile("global_load_dword %0, %1, %2 sc1\n\ts_waitcnt vmcnt(0)" : "=v"(v) : "v"(voff), "s"(base) : "memory");
    if (__all(v != 0xffffffffu)) break;
    __builtin_amdgcn_s_sleep(1);
  }
}
constexpr int t16off(int c0) { return (c0 >> 5) * 1024 + (c0 & 16) * 32; }
DI unsigned t16lane(int lane) { return (unsigned)((lane >> 5) * 256 + (lane & 15) * 16 + ((lane >> 4) & 1) * 8); }
DI int rotf(int f, int off, int n) { const int r = f + off; return r - (r >= n ? n : 0); }
DI void st16s(const void* sbase, unsigned voff, u32x4 v) { asm volatile("global_store_dwordx4 %0, %1, %2 sc1\n\ts_nop 1" :: "v"(voff), "v"(v), "s"(sbase) : "memory"); }
DI float row16_sum(float v) {
  v += __builtin_bit_cast(float, __builtin_amdgcn_update_dpp(0, __builtin_bit_cast(int, v), 0x128, 0xf, 0xf, false));
  v += __builtin_bit_cast(float, __builtin_amdgcn_update_dpp(0, __builtin_bit_cast(int, v), 0x124, 0xf, 0xf, false));
  v += __builtin_bit_cast(float, __builtin_amdgcn_update_dpp(0, __builtin_bit_cast(int, v), 0x122, 0xf, 0xf, false));
  v += __builtin_bit_cast(float, __builtin_amdgcn_update_dpp(0, __builtin_bit_cast(int, v), 0x121, 0xf, 0xf, false));
  return v;
}
#define ROT3(c) 0
#define ROT5(c) 0
DI int fresh_lane() { int l = __builtin_amdgcn_mbcnt_hi(~0u, __builtin_amdgcn_mbcnt_lo(~0u, 0u)); asm volatile("" : "+v"(l)); return l; }
template <class T> DI void pin(T& v) { asm volatile("" : "+v"(v)); }
DI bf16x8 lds128(unsigned addr) { return *(const LAS bf16x8*)(uintptr_t)addr; }
DI s16x4 lds64(unsigned addr) { return *(const LAS s16x4*)(uintptr_t)addr; }
DI s16x4 ldstr(unsigned addr) { return __builtin_amdgcn_ds_read_tr16_b64_v4i16((LAS s16x4*)(uintptr_t)addr); }
DI unsigned lds_addr_of(const void* p) { return (unsigned)(uintptr_t)(const LAS char*)p; }
#define STAMP_AT(kid, idx) ((void)0)
#define RETRY_COUNT(k) ((void)0)
#define WAITV_BAR(N) asm volatile("s_waitcnt vmcnt(" #N ") lgkmcnt(0)\n\ts_barrier" ::: "memory")
#define SB() __builtin_amdgcn_sched_barrier(0)
template <int N> using IC = std::integral_constant<int, N>;
template <class F, int... Is> DI void sfor_(F&& f, std::integer_sequence<int, Is...>) { (f(IC<Is>{}), ...); }
template <int N, class F> DI void sfor(F&& f) { sfor_(f, std::make_integer_sequence<int, N>{}); }
template <int N> DI void waitv_bar() { asm volatile("s_waitcnt vmcnt(%0) lgkmcnt(0)\n\ts_barrier" :: "i"(N) : "memory"); }

#define K1_LA_ 10
#define P3_G1 -1
#define K4_LA_ 5
#define K5_LA_ 24
constexpr int K1_STAGE = 32768, K1_NST = 4, K1_LA = K1_LA_;
template <bool ISV>
DI void p1_body(const u16* __restrict__ x, const u16* __restrict__ W, char* __restrict__ ws, const unsigned lds0, const int w, int rb, int cb) {
  const int lane = fresh_lane();
  const int wm = w >> 2, wn = w & 3;
  const int r32 = lane & 31, hh = lane >> 5;
  const unsigned a_voff0 = (unsigned)((lane >> 3) * 1280 + (((lane & 7) ^ ((lane >> 4) & 7)) << 4));
  const unsigned a_voff1 = (unsigned)((lane >> 3) * 1280 + (((lane & 7) ^ ((4 + (lane >> 4)) & 7)) << 4)) + 8 * 1280;
  const unsigned w_voff0 = (unsigned)((lane >> 3) * 3840 + (((lane & 7) ^ (((lane >> 4) & 1) << 2)) << 4));
  const unsigned w_voff1 = w_voff0 + 8 * 3840;
  const char* xs = (const char*)x + (size_t)(rb * 128 + w * 16) * 1280;
  const char* wsrc = (const char*)W + (size_t)((w & 3) * 16) * 3840 + (size_t)(cb * 128 + (w >> 2) * 64) * 2;
  const unsigned dst0 = lds0 + w * 2048;
  const int rot = 0;
  auto dma = [&](auto n_) {
    constexpr int n = n_, s = n >> 2, j = n & 3; constexpr unsigned so = (s % K1_NST) * K1_STAGE;
    const int sr = s + rot - (s + rot >= 10 ? 10 : 0);
    if constexpr (j == 0) glds16<false>(xs + sr * 128, a_voff0, dst0 + so);
    if constexpr (j == 1) glds16<false>(wsrc + (size_t)sr * 64 * 3840, w_voff0, dst0 + so + 16384);
    if constexpr (j == 2) glds16<false>(xs + sr * 128, a_voff1, dst0 + so + 1024);
    if constexpr (j == 3) glds16<false>(wsrc + (size_t)sr * 64 * 3840, w_voff1, dst0 + so + 16384 + 1024);
  };
  STAMP_AT(0, 0);
  sfor<K1_LA>([&](auto n) { dma(n); });
  const int ob = (wn >> 1) ^ wm;
  const int fsel = wn & 1;
  unsigned xa[2][4], xb[2][4], wa[2][2];
  {
    const int row = wm * 64 + fsel * 32 + r32;
    const int blk = (lane >> 4) & 1, q = (lane & 15) >> 2, p = lane & 3;
    const int c = (wn & 1) * 4 + blk * 2 + (p >> 1);
    const unsigned base = lds0 + (unsigned)(16384 + (wn >> 1) * 8192 + (8 * hh + q) * 128 + ((c ^ (((q >> 1) & 1) << 2)) << 4) + (p & 1) * 8);
#pragma unroll
    for (int hs = 0; hs < 2; ++hs) {
#pragma unroll
      for (int i = 0; i < 4; ++i) {
        xa[hs][i] = lds0 + hs * 65536 + (unsigned)(row * 128 + ((((i ^ ob) * 2 + hh) ^ ((row >> 1) & 7)) << 4));
        xb[hs][i] = xa[hs][i] + (fsel ? -4096 : 4096);
      }
      wa[hs][0] = base + hs * 65536 + ob * 2048; wa[hs][1] = base + hs * 65536 + (1 ^ ob) * 2048;
    }
  }
  f32x16 acc0 = {}, acc1 = {};
  float sxx = 0.f;
  f32x4 sx = {}, sw = {};
  bf16x8 sel = (bf16x8)(short)(((lane & 15) == ((lane >> 4) & 1)) ? 0x3f80 : 0); pin(sel);
  bf16x8 fx0[4], fx1[4], fw[4];
  auto rd = [&](auto i_) {
    constexpr int i = i_, s = i >> 2, p = i & 3, sl = s % K1_NST, hs = sl >> 1; constexpr unsigned so = (sl & 1) * K1_STAGE;
    fx0[p] = lds128(xa[hs][p] + so);
    const s16x4 wl = ldstr(wa[hs][p & 1] + so + (p >> 1) * 4096);
    const s16x4 wh = ldstr(wa[hs][p & 1] + so + (p >> 1) * 4096 + 512);
    fw[p] = __builtin_shufflevector(wl, wh, 0, 1, 2, 3, 4, 5, 6, 7);
    fx1[p] = lds128(xb[hs][p] + so);
  };
  const bool isq = cb < 5;
  const int pitch = isq ? 1280 : 2560;
  unsigned off[4];
  if (ISV) {
    const int n = cb * 128 + wn * 32 + r32 - 1280, head = n / 20, d0 = n - head * 20;
    const int d = (head & 1) ? (d0 < 4 ? d0 + 16 : d0 - 4) : d0;
    off[0] = (unsigned)(head * 163840 + 1280 + (hh * 20 + d) * 16); pin(off[0]);
  } else {
#pragma unroll
    for (int g = 0; g < 4; ++g) {
      const int n = cb * 128 + wn * 32 + 8 * g + 4 * hh - (isq ? 0 : 640), head = n / 20, d0 = n - head * 20;
      const int d = (head & 1) ? (d0 < 4 ? d0 + 16 : d0 - 4) : d0;
      off[g] = (unsigned)(head * 64 * pitch + (d < 16 ? (((d >> 3) * 32 + r32) * 16 + (d & 4) * 2) : (1024 + r32 * 8))); pin(off[g]);
    }
  }
  waitv_bar<K1_LA - 4>();
  STAMP_AT(0, 1);
  rd(IC<0>{}); rd(IC<1>{});
  sfor<40>([&](auto i_) {
    constexpr int i = i_, p = i & 3;
    if constexpr ((i + 2) % 4 == 0 && i + 2 < 40) { constexpr int last = (i - 1 + K1_LA < 39 ? i - 1 + K1_LA : 39); waitv_bar<(last - (i + 5) > 0 ? last - (i + 5) : 0)>(); }
    if constexpr (i + K1_LA < 40) dma(IC<i + K1_LA>{});
    if constexpr (i + 2 < 40) rd(IC<i + 2>{});
    SB();
    if (ISV) { acc0 = MFMA32(fx0[p], fw[p], acc0); acc1 = MFMA32(fx1[p], fw[p], acc1); }
    else     { acc0 = MFMA32(fw[p], fx0[p], acc0); acc1 = MFMA32(fw[p], fx1[p], acc1); }
    if constexpr ((p & 1) == 0) {
      sx = MFMA16(fx0[p], sel, sx);
      const u32x4 u = __builtin_bit_cast(u32x4, fx0[p]);
#pragma unroll
      for (int j = 0; j < 4; ++j) sxx = dot2(u[j], u[j], sxx);
      pin(sxx);
    } else {
      sw = MFMA16(fw[p], sel, sw); }
    SB();
  });
  STAMP_AT(0, 11);
  sxx = add32(sxx);
  LAS float* psum = (LAS float*)(uintptr_t)(lds0 + 65536);
  LAS float* psq = psum + 256;
  LAS float* csp = psum + 512;
  if (hh == 0) psq[(wn >> 1) * 128 + wm * 64 + fsel * 32 + r32] = sxx;
  if ((lane & 15) < 2) { const int o = (lane & 1) * 16 + (lane >> 4) * 4;
    *(LAS f32x4*)&psum[(wn >> 1) * 128 + wm * 64 + fsel * 32 + o] = sx; *(LAS f32x4*)&csp[wm * 128 + wn * 32 + o] = sw; }
  bar();
  STAMP_AT(0, 12);
  auto stat = [&](int rl) {
    const float mean = (psum[rl] + psum[128 + rl]) * (1.f / 640.f);
    const float var = fmaxf((psq[rl] + psq[128 + rl]) * (1.f / 640.f) - mean * mean, 0.f);
    return (f32x2){mean, __builtin_amdgcn_rsqf(var + LN_EPS)}; };
  if (ISV) {
    LAS f32x2* st = (LAS f32x2*)(uintptr_t)(lds0 + 65536 + 4096 + w * 512);
    st[lane] = stat(wm * 64 + lane);
    const int nl = wn * 32 + r32;
    const float c = csp[nl] + csp[128 + nl];
#pragma unroll
    for (int t = 0; t < 2; ++t) {
      const f32x16& a = t ? acc1 : acc0;
      const int rl = (t ^ fsel) * 32;
      unsigned pk[8];
#pragma unroll
      for (int g = 0; g < 4; ++g) {
        const f32x4 s01 = *(const LAS f32x4*)&st[rl + 8 * g + 4 * hh], s23 = *(const LAS f32x4*)&st[rl + 8 * g + 4 * hh + 2];
        pk[2 * g]     = pk_bf16(s01[1] * (a[4 * g] - s01[0] * c), s01[3] * (a[4 * g + 1] - s01[2] * c));
        pk[2 * g + 1] = pk_bf16(s23[1] * (a[4 * g + 2] - s23[0] * c), s23[3] * (a[4 * g + 3] - s23[2] * c));
      }
      const char* dst = ws + WS_KV + (size_t)(rb * 4 + wm * 2 + (rl >> 5)) * 2560;
      st16s(dst, off[0], (u32x4){pk[0], pk[1], pk[2], pk[3]});
      st16s(dst + 640, off[0], (u32x4){pk[4], pk[5], pk[6], pk[7]});
    }
  } else {
    const float sc = isq ? QSCALE : 1.f;
    const char* base = ws + (isq ? WS_Q : WS_KV);
    f32x4 c[4];
#pragma unroll
    for (int g = 0; g < 4; ++g) { const int nl = wn * 32 + 8 * g + 4 * hh; c[g] = *(const LAS f32x4*)&csp[nl] + *(const LAS f32x4*)&csp[128 + nl]; }
#pragma unroll
    for (int t = 0; t < 2; ++t) {
      const f32x16& a = t ? acc1 : acc0;
      const int rl = wm * 64 + (t ^ fsel) * 32;
      const f32x2 ms = stat(rl + r32);
      const float rs = ms.y * sc, mr = ms.x;
      const char* tb = base + (size_t)(rb * 4 + (rl >> 5)) * pitch;
#pragma unroll
      for (int g = 0; g < 4; ++g) {
        const unsigned p0 = pk_bf16(rs * (a[4 * g] - mr * c[g][0]), rs * (a[4 * g + 1] - mr * c[g][1]));
        const unsigned p1 = pk_bf16(rs * (a[4 * g + 2] - mr * c[g][2]), rs * (a[4 * g + 3] - mr * c[g][3]));
        st8s(tb, off[g], (u32x2){p0, p1});
      }
    }
  }
  STAMP_AT(0, 13);
}

DI void p1_repack(const u16* __restrict__ Wo, char* __restrict__ ws, const int w, int idx) {
  const int lane = fresh_lane();
  const unsigned pvo = (lane >> 4) * 8 * 1280 + (lane & 15) * 2;
  unsigned pw[7][8];
  sfor<7>([&](auto r) {
    const int f = idx * 50 + min(w + 8 * r, 49), cbk = f / 100, rem = f - cbk * 100, ks = rem / 5, ct = rem - ks * 5;
    const char* src = (const char*)(Wo + (size_t)(32 * ks) * 640 + 80 * cbk + 16 * ct);
    sfor<8>([&](auto i) { gldu16<(i & 3) * 1280>(pw[r][i], src + (i & ~3) * 1280, pvo); });
  });
  sfor<7>([&](auto r) {
    waitv<(6 - r) * 8>();
    sfor<8>([&](auto j) { pin(pw[r][j]); });
    const u32x4 v = {pw[r][0] | (pw[r][1] << 16), pw[r][2] | (pw[r][3] << 16), pw[r][4] | (pw[r][5] << 16), pw[r][6] | (pw[r][7] << 16)};
    pw[r][0] = v[0]; pw[r][1] = v[1]; pw[r][2] = v[2]; pw[r][3] = v[3];
  });
  sfor<7>([&](auto r) {
    if (w + 8 * r < 50) st16(ws + WS_WP + (size_t)(idx * 50 + w + 8 * r) * 1024 + lane * 16, (u32x4){pw[r][0], pw[r][1], pw[r][2], pw[r][3]});
  });
}

template <int OFF> DI void rdK(bf16x8& k0, s16x4& k1, unsigned a0, unsigned a1) {
  asm volatile("ds_read_b128 %0, %2 offset:%4\n\tds_read_b64 %1, %3 offset:%4" : "=&v"(k0), "=&v"(k1) : "v"(a0), "v"(a1), "i"(OFF));
}
template <int OFF> DI void rdV(bf16x8& v0, bf16x8& v1, unsigned a, unsigned long long msk) {
  asm volatile("s_mov_b64 exec, %5\n\tds_read_b128 %0, %2 offset:%3\n\tds_read_b128 %1, %2 offset:%4\n\ts_mov_b64 exec, -1"
               : "+v"(v0), "+v"(v1) : "v"(a), "i"(OFF), "i"(OFF + 640), "s"(msk));
}
#define EX2(v) __builtin_amdgcn_exp2f(v)
constexpr int wo_off(int p) { return (p < 60 ? p : p + 20) * 1024; }
constexpr int gu_off(int f) { return (f < 20 ? f + 60 : f < 60 ? f + 100 : f - 60) * 1024; }
constexpr int wd_off(int f) { return ((f / 20 + 4) % 8) * 20480 + (f % 20) * 1024; }
constexpr int CSF_OFF = 116 * 1024, XCH_OFF = 120 * 1024;

DI void p2_attn(char* __restrict__ ws, const u16* __restrict__ Wg, const u16* __restrict__ Wu, const u16* __restrict__ Wd,
                const unsigned lds0, const int w, const u32x2 tag, const int rb3, const int cb3, const int rb4, const int cb4) {
  const int lane = fresh_lane();
  const int kh = w >> 2, wq = w & 3;
  const int head = (blockIdx.x & 7) * 4 + ((blockIdx.x >> 3) & 3), qb = blockIdx.x >> 5;
  const int r32 = lane & 31, hh = lane >> 5;
  const char* kvg = ws + WS_KV + (size_t)head * 163840 + (size_t)kh * 81920 + wq * 1024;
  const unsigned voff = lane * 16;
  const unsigned kvl = lds0 + kh * 81920 + wq * 1024;
  char* ctl = ws + WS_CTL;
  const char* ctr = ctl + (blockIdx.x & 7) * CTL_REP;
  STAMP_AT(1, 0);
  {
    const int cfl = (20 * head) >> 5, cfh = (20 * head + 19) >> 5;
    const unsigned f0 = (unsigned)(((((lane & 32) ? 40 : 20) + ((lane & 16) ? cfh : cfl)) * 32 + kh * 16 + (lane & 15)) * 8);
    const unsigned f1 = (unsigned)((lane < 32 ? (((lane & 1) ? cfh : cfl) * 32 + qb * 4 + wq) : 1920 + cb3 * 16 + (lane & 15)) * 8);
    poll2(ctr + CTL_F1, f0, f1, tag);
  }
  STAMP_AT(1, 4);
#pragma unroll
  for (int i = 0; i < 4; ++i) glds16<true>(kvg + i * 4096, voff, kvl + i * 4096);
  bf16x8 q0[2]; s16x4 q1[2];
  {
    const char* qg = ws + WS_Q + ((size_t)head * 64 + qb * 8 + wq * 2) * 1280;
    gld16<0>(q0[0], qg, voff); gld16<1280>(q0[1], qg, voff); gld8<1024>(q1[0], qg, r32 * 8); gld8<2304>(q1[1], qg, r32 * 8);
  }
  const unsigned ka0 = lds0 + kh * 81920 + lane * 16, ka1 = lds0 + kh * 81920 + 1024 + r32 * 8, va = lds0 + kh * 81920 + 1280 + (hh * 20 + (r32 < 20 ? r32 : 0)) * 16;
  bf16x8 K0[2], V0[2], V1[2]; s16x4 K1[2];
  V0[0] = V0[1] = V1[0] = V1[1] = (bf16x8)(short)(r32 == 20 ? 0x3f80 : 0);
  pin(V0[0]); pin(V0[1]); pin(V1[0]); pin(V1[1]);
  WAITV_BAR(0);
  pin(q0[0]); pin(q0[1]); pin(q1[0]); pin(q1[1]);
  if (hh) { q1[0] = (s16x4){0, 0, 0, 0}; q1[1] = (s16x4){0, 0, 0, 0}; }
  STAMP_AT(1, 1);
  rdK<0>(K0[0], K1[0], ka0, ka1);
  const unsigned long long vmsk = 0x000fffff000fffffull;
  rdV<0>(V0[0], V1[0], va, vmsk);
  f32x16 cm[2];
  {
    const bf16x8 kf0 = lds128(lds0 + lane * 16); const s16x4 kf1 = lds64(lds0 + 1024 + r32 * 8);
#pragma unroll
    for (int t = 0; t < 2; ++t) {
      f32x16 s = MFMA32(kf0, q0[t], (f32x16){}); s = MFMA32K8(kf1, q1[t], s);
      float m = s[0];
#pragma unroll
      for (int i = 1; i < 16; ++i) m = fmaxf(m, s[i]);
      m = max32(m);
      cm[t] = (f32x16)(-m);
      pin(cm[t]);
    }
  }
  asm volatile("s_waitcnt lgkmcnt(0)" : "+v"(K0[0]), "+v"(K1[0]), "+v"(V0[0]), "+v"(V1[0]));
  f32x16 o[2] = {};
  f32x16 SA, SB_;
  u32x4 PA0, PA1, PB0 = {}, PB1 = {};
  SA = MFMA32(K0[0], q0[0], cm[0]); SA = MFMA32K8(K1[0], q1[0], SA);
  STAMP_AT(1, 2);
  pin(o[0]); pin(o[1]); pin(PB0); pin(PB1);
#define GRP(MF, Sc, Pc, i, j) do { MF; SB(); const float e0 = EX2(Sc[i]), e1 = EX2(Sc[i + 1]), e2 = EX2(Sc[i + 2]), e3 = EX2(Sc[i + 3]); \
    Pc[j] = pk_bf16(e0, e1); Pc[j + 1] = pk_bf16(e2, e3); pin(Pc); SB(); } while (0)
#define SLOT(QK, Sc, Pc0, Pc1, Sn, kb, jn, Pp0, Pp1, vb, jp) do { \
    GRP(if constexpr (QK) Sn = MFMA32(K0[kb], q0[jn], cm[jn]), Sc, Pc0, 0, 0); \
    GRP(if constexpr (QK) Sn = MFMA32K8(K1[kb], q1[jn], Sn), Sc, Pc0, 4, 2); \
    GRP(o[jp] = MFMA32(V0[vb], __builtin_bit_cast(bf16x8, Pp0), o[jp]), Sc, Pc1, 8, 0); \
    GRP(o[jp] = MFMA32(V1[vb], __builtin_bit_cast(bf16x8, Pp1), o[jp]), Sc, Pc1, 12, 2); } while (0)
#define LWAIT(kb, vb) asm volatile("s_waitcnt lgkmcnt(0)" : "+v"(K0[kb]), "+v"(K1[kb]), "+v"(V0[vb]), "+v"(V1[vb]))
  unsigned pw[3][8];
  const int gw = blockIdx.x * 8 + w, gwc = min(gw, 1919);
  const char* psrc[3];
#pragma unroll
  for (int r = 0; r < 2; ++r) {
    const int f = 2 * gwc + r, cbk = f / 120, rem = f - cbk * 120, ks = rem / 6, ct = rem - ks * 6;
    psrc[r] = (const char*)((ct < 3 ? Wg : Wu) + (size_t)(32 * ks) * 1536 + 48 * cbk + 16 * (ct < 3 ? ct : ct - 3));
  }
  { const int f = gwc, cbk = f / 240, rem = f - cbk * 240, ks = rem / 5, ct = rem - ks * 5;
    psrc[2] = (const char*)(Wd + (size_t)(32 * ks) * 640 + 80 * cbk + 16 * ct); }
  const unsigned pvo1 = (lane >> 4) * 8 * 3072 + (lane & 15) * 2, pvo2 = (lane >> 4) * 8 * 1280 + (lane & 15) * 2;
  const char* wog = ws + WS_WP + (size_t)cb3 * 102400;
  auto rstore = [&](auto r_, auto yv_) {
    constexpr int r = r_;
    waitv<yv_>();
    sfor<8>([&](auto j) { pin(pw[r][j]); });
    const u32x4 v = {pw[r][0] | (pw[r][1] << 16), pw[r][2] | (pw[r][3] << 16), pw[r][4] | (pw[r][5] << 16), pw[r][6] | (pw[r][7] << 16)};
    if (gw < 1920) {
      if constexpr (r < 2) {
        st16(ws + WS_WP + (size_t)(NFR_WO + 2 * gw + r) * 1024 + lane * 16, v);
        float c = 0.f;
#pragma unroll
        for (int j = 0; j < 4; ++j) c = dot2(v[j], 0x3f803f80u, c);
        c = red4(c);
        if (lane < 16) st4(ws + WS_CSP + (size_t)(2 * gw + r) * 64 + lane * 4, c);
      } else st16(ws + WS_WP + (size_t)(NFR_WO + NFR_GU + gw) * 1024 + lane * 16, v);
    }
  };
  const int qtile = qb * 8 + wq * 2 + kh, cg0 = head * 5 + (head & 1), cgt = (head & 1) ? head * 5 : head * 5 + 4;
  const char* ob = ws + WS_O + (size_t)qtile * 40960;
  const char* obs[3] = {ob + (cg0 >> 3) * 1024 + ((cg0 & 7) >> 1) * 256, ob + ((cg0 + 2) >> 3) * 1024 + (((cg0 + 2) & 7) >> 1) * 256,
                        ob + (cgt >> 3) * 1024 + ((cgt & 7) >> 1) * 256 + (cgt & 1) * 8};
  unsigned vo = (unsigned)((r32 >> 4) * 20480 + (r32 & 15) * 16 + hh * 8); pin(vo);
  u32x4 fill = {0xffffffffu, 0xffffffffu, 0xffffffffu, 0xffffffffu}; pin(fill);
  const u32x2 fill2 = {fill[0], fill[1]};
  u32x2 fa, fb, fc;
  const unsigned fvc = (unsigned)CTL_FR + (unsigned)((cb4 * 60 + min(lane, 59)) * 8);
  const unsigned fva = (unsigned)(hh ? CTL_F4 : CTL_F2) + (unsigned)(((rb3 * 2 + ((w & 3) >> 1)) * 32 + r32) * 8), fvb = lane < 32 ? (unsigned)CTL_F3 + (unsigned)(((rb4 * 16 + w * 2) * 8 + (lane & 15)) * 8) : (unsigned)CTL_FR + (unsigned)((cb3 * 240 + w * 30 + min(lane - 32, 29)) * 8);
  auto hook = [&](auto it_, auto i_) {
    constexpr int it = it_, i = i_, n = (it - 2) * 8 + i;
    if constexpr (n >= 0 && n < 2) st8s(obs[n], vo, fill2);
    if constexpr (n == 2) { if (!hh) st8s(obs[2], vo, fill2); }
    if constexpr (n >= 3 && n < 6) { const int L = (n - 3) * 64 + lane; st16s(ws + WS_G + (size_t)(rb4 * 16 + w * 2) * 49152 + cb4 * 1536, (unsigned)(L * 16 + (L >= 96 ? 49152 - 1536 : 0)), fill); }
    if constexpr (n >= 6 && n < 9) { if (w < 4) st16s(ws + WS_H + (size_t)(rb3 * 4 + w) * 20480 + cb3 * 2560, (unsigned)(min((n - 6) * 64 + lane, 159) * 16), fill); }
    if constexpr (n == 9) { if (w < 4 && lane < 16) st8s(ws + WS_HST + (size_t)(rb3 * 4 + w) * 1024 + (cb3 >> 1) * 256 + (cb3 & 1) * 8, (unsigned)(lane * 16), fill2); }
    if constexpr (it == 6 && i == 6) {
      waitv<6>();
      setflag(ctl + CTL_FR + gw * 8, tag, lane);
      setflag(ctl + CTL_F2 + (qtile * 32 + head) * 8, tag, lane);
      setflag(ctl + CTL_F4 + ((rb4 * 8 + w) * 32 + cb4) * 8, tag, lane);
      if (w < 4) setflag(ctl + CTL_F3 + ((rb3 * 4 + w) * 8 + cb3) * 8, tag, lane);
    }
    if constexpr (it == 7 && i == 6) { gld8<0>(fa, ctr, fva); gld8<0>(fb, ctr, fvb); gld8<0>(fc, ctr, fvc); }
    if constexpr (it < 2) glds16<true>(kvg + (size_t)(it * 8 + 4 + i) * 4096, voff, kvl + (it * 8 + 4 + i) * 4096);
    else if constexpr (it < 4) gldu16<(i & 1) * 3072>(pw[it - 2][i], psrc[it - 2] + (i & ~1) * 3072, pvo1);
    else if constexpr (it == 4) {
      gldu16<(i & 3) * 1280>(pw[2][i], psrc[2] + (i & ~3) * 1280, pvo2);
      if constexpr (i == 0) rstore(IC<0>{}, IC<9>{});
      if constexpr (i == 7) rstore(IC<1>{}, IC<8>{});
    } else if constexpr (it == 5) {
      if constexpr (i == 7) rstore(IC<2>{}, IC<2>{});
    } else {
      constexpr int j = (it - 6) * 8 + i;
      const char* src = wog + rotf(w + 8 * j, 20 * ROT3(cb3), 100) * 1024;
      if constexpr (j < 7) glds16<true>(src, voff, lds0 + w * 1024 + j * 8192);
      else if constexpr (j == 7) glds16<true>(src, voff, lds0 + w * 1024 + j * 8192 + (w >= 4 ? 20480 : 0));
      else if constexpr (j < 12) glds16<true>(src, voff, lds0 + w * 1024 + j * 8192 + 20480);
      else if constexpr (j == 12) { if (w < 4) glds16<true>(src, voff, lds0 + w * 1024 + j * 8192 + 20480); }
    }
    SB();
  };
  const unsigned ka0b = ka0 + 40960, ka1b = ka1 + 40960, vab = va + 40960;
  sfor<8>([&](auto it_) {
    constexpr int it = it_;
    if constexpr (it == 1) WAITV_BAR(6);
    if constexpr (it == 2) { WAITV_BAR(8); STAMP_AT(1, 3); }
    if constexpr (it == 4) asm volatile("s_waitcnt vmcnt(16)\n\ts_barrier" ::: "memory");
    if constexpr (it == 6) asm volatile("s_barrier" ::: "memory");
    if (((it ^ kh) & 1) == 0) __builtin_amdgcn_s_setprio(0); else __builtin_amdgcn_s_setprio(1);
    constexpr int o0 = (it & 3) * 10240;
    constexpr int on = it == 3 ? 0 : o0 + 10240;
    const unsigned a0 = it < 4 ? ka0 : ka0b, a1 = it < 4 ? ka1 : ka1b, av = it < 4 ? va : vab;
    const unsigned n0 = it < 3 ? ka0 : ka0b, n1 = it < 3 ? ka1 : ka1b, nv = it < 3 ? va : vab;
    rdK<o0 + 2560>(K0[1], K1[1], a0, a1); hook(it_, IC<0>{});                  SLOT(true, SA, PA0, PA1, SB_, 0, 1, PB0, PB1, 1, 1);
    LWAIT(1, 0); rdV<o0 + 2560>(V0[1], V1[1], av, vmsk); hook(it_, IC<1>{});   SLOT(true, SB_, PB0, PB1, SA, 1, 0, PA0, PA1, 0, 0);
    rdK<o0 + 5120>(K0[0], K1[0], a0, a1); hook(it_, IC<2>{});                  SLOT(true, SA, PA0, PA1, SB_, 1, 1, PB0, PB1, 0, 1);
    LWAIT(0, 1); rdV<o0 + 5120>(V0[0], V1[0], av, vmsk); hook(it_, IC<3>{});   SLOT(true, SB_, PB0, PB1, SA, 0, 0, PA0, PA1, 1, 0);
    rdK<o0 + 7680>(K0[1], K1[1], a0, a1); hook(it_, IC<4>{});                  SLOT(true, SA, PA0, PA1, SB_, 0, 1, PB0, PB1, 1, 1);
    LWAIT(1, 0); rdV<o0 + 7680>(V0[1], V1[1], av, vmsk); hook(it_, IC<5>{});   SLOT(true, SB_, PB0, PB1, SA, 1, 0, PA0, PA1, 0, 0);
    if constexpr (it < 7) rdK<on>(K0[0], K1[0], n0, n1);
    hook(it_, IC<6>{});                                                        SLOT(true, SA, PA0, PA1, SB_, 1, 1, PB0, PB1, 0, 1);
    LWAIT(0, 1);
    if constexpr (it < 7) rdV<on>(V0[0], V1[0], nv, vmsk);
    hook(it_, IC<7>{});                                                        SLOT(it < 7, SB_, PB0, PB1, SA, 0, 0, PA0, PA1, 1, 0);
  });
  __builtin_amdgcn_s_setprio(0);
  o[1] = MFMA32(V0[1], __builtin_bit_cast(bf16x8, PB0), o[1]);
  o[1] = MFMA32(V1[1], __builtin_bit_cast(bf16x8, PB1), o[1]);
  STAMP_AT(1, 9);
  bar();
  {
    LAS f32x4* xch = (LAS f32x4*)(uintptr_t)(lds0 + XCH_OFF + w * 3072);
    const f32x16& snd = kh ? o[0] : o[1];
#pragma unroll
    for (int g = 0; g < 3; ++g) xch[g * 64 + lane] = (f32x4){snd[4 * g], snd[4 * g + 1], snd[4 * g + 2], snd[4 * g + 3]};
  }
  bar();
  STAMP_AT(1, 10);
  float of[12];
  {
    const LAS f32x4* xch = (const LAS f32x4*)(uintptr_t)(lds0 + XCH_OFF + (w ^ 4) * 3072);
    const f32x16& own = kh ? o[1] : o[0];
#pragma unroll
    for (int g = 0; g < 3; ++g) { const f32x4 v = xch[g * 64 + lane];
#pragma unroll
      for (int j = 0; j < 4; ++j) of[4 * g + j] = own[4 * g + j] + v[j]; }
  }
  float l = hi32(of[8]);
  bool ok = (l > 8.67e-19f) && (l < 1.15e18f);
#pragma unroll
  for (int i = 0; i < 8; ++i) ok = ok && (fabsf(of[i]) < 1e37f);
  if (!hh) {
#pragma unroll
    for (int i = 8; i < 12; ++i) ok = ok && (fabsf(of[i]) < 1e37f);
  }
  if (__builtin_expect(!__all(ok), 0)) {
    __builtin_amdgcn_fence(__ATOMIC_ACQUIRE, "agent");
    const char* kv = ws + WS_KV + (size_t)head * 163840;
    const bf16x8 fq0 = kh ? q0[1] : q0[0];
    const s16x4 fq1 = kh ? q1[1] : q1[0];
    float m = -INFINITY;
    for (int t = 0; t < 64; ++t) {
      const bf16x8 kf0 = *(const bf16x8*)(kv + t * 2560 + lane * 16); const s16x4 kf1 = *(const s16x4*)(kv + t * 2560 + 1024 + r32 * 8);
      f32x16 s = MFMA32(kf0, fq0, (f32x16){}); s = MFMA32K8(kf1, fq1, s);
#pragma unroll
      for (int i = 0; i < 16; ++i) m = fmaxf(m, s[i]);
    }
    m = max32(m);
    f32x16 oo = {};
    const int vr = r32 < 20 ? r32 : 0;
    for (int t = 0; t < 64; ++t) {
      const bf16x8 kf0 = *(const bf16x8*)(kv + t * 2560 + lane * 16); const s16x4 kf1 = *(const s16x4*)(kv + t * 2560 + 1024 + r32 * 8);
      bf16x8 vf0 = *(const bf16x8*)(kv + t * 2560 + 1280 + (hh * 20 + vr) * 16), vf1 = *(const bf16x8*)(kv + t * 2560 + 1920 + (hh * 20 + vr) * 16);
      if (r32 >= 20) { vf0 = (bf16x8)(short)0x3f80; vf1 = vf0; }
      f32x16 s = MFMA32(kf0, fq0, (f32x16){}); s = MFMA32K8(kf1, fq1, s);
      u32x4 p0, p1;
#pragma unroll
      for (int i = 0; i < 4; ++i) {
        p0[i] = pk_bf16(EX2(s[2 * i] - m), EX2(s[2 * i + 1] - m));
        p1[i] = pk_bf16(EX2(s[8 + 2 * i] - m), EX2(s[8 + 2 * i + 1] - m));
      }
      oo = MFMA32(vf0, __builtin_bit_cast(bf16x8, p0), oo);
      oo = MFMA32(vf1, __builtin_bit_cast(bf16x8, p1), oo);
    }
#pragma unroll
    for (int i = 0; i < 12; ++i) of[i] = oo[i];
    l = hi32(of[8]);
  }
  const float rl = __builtin_amdgcn_rcpf(l);
  waitv<0>(); pin(fa); pin(fb); pin(fc);
#pragma unroll
  for (int g = 0; g < 3; ++g)
    if (g < 2 || !hh) st8s(obs[g], vo, nofill(pk_bf16(of[4 * g] * rl, of[4 * g + 1] * rl), pk_bf16(of[4 * g + 2] * rl, of[4 * g + 3] * rl)));
  STAMP_AT(1, 11);
  if (!__all(fa[0] == tag[0] && fa[1] == tag[1] && fb[0] == tag[0] && fb[1] == tag[1])) poll2(ctr, fva, fvb, tag);
  if (!__all(fc[0] == tag[0] && fc[1] == tag[1])) poll(ctr, fvc, tag);
  STAMP_AT(1, 12);
}

constexpr int x_none(int) { return 0; }
constexpr int x_p4(int i) { return ((i >= 2 && i < 7) || (i >= 10 && i < 18)) ? 1 : 0; }
template <int NSTEP, int NF, int LA, int LL, int NA, int ASTR, int (*BOFF)(int), int (*XCNT)(int), class PRE, class MM>
DI void gemm_res(const char* asrc, int rot, unsigned lds0, unsigned voff, PRE&& pre, MM&& mm) {
  bf16x8 a[NSTEP][NA];
  bf16x8 bf[LL + 1][NF];
  unsigned rd0 = lds0 + voff, rd1 = rd0 + 65536, rd2 = rd0 + 131072; pin(rd0); pin(rd1); pin(rd2);
  auto issue = [&](auto n_) { constexpr int n = n_; sfor<NA>([&](auto j) { gld16<(n & 3) * 1024>(a[n][j], asrc + rotf(n >> 2, rot, NSTEP / 4) * 4096 + j * ASTR, voff); }); };
  constexpr auto xsum = [](int lo, int hi) { int c = 0; for (int i = lo < 0 ? 0 : lo; i <= hi; ++i) c += XCNT(i); return c; };
  auto rd = [&](auto i_, auto f_) {
    constexpr int i = i_, f = f_, off = BOFF(i * NF + f);
    bf[i % (LL + 1)][f] = lds128((off < 65536 ? rd0 : off < 131072 ? rd1 : rd2) + (off & 65535));
  };
  auto check = [&](auto i_) {
    constexpr int i = i_;
    sfor<NA>([&](auto j) { pin(a[i][j]); });
    auto mx = [&]() { unsigned m = 0; sfor<NA>([&](auto j) { m = max(m, umax4(__builtin_bit_cast(u32x4, a[i][j]))); }); return m; };
    if (__builtin_expect(__any(mx() == 0xffffffffu), 0)) {
#pragma unroll 1
      for (int spin = 0; spin < (1 << 17); ++spin) {
        RETRY_COUNT(NSTEP + NF);
        issue(i_); waitv<0>(); sfor<NA>([&](auto j) { pin(a[i][j]); });
        if (!__any(mx() == 0xffffffffu)) break;
      }
    }
  };
  sfor<LA>([&](auto n) { issue(n); });
  pre(IC<-1>{});
  sfor<LL>([&](auto i) { sfor<NF>([&](auto f) { rd(i, f); }); });
  waitv<(LA - 1) * NA>(); check(IC<0>{});
  sfor<NSTEP>([&](auto i_) {
    constexpr int i = i_;
    pre(i_);
    SB();
    sfor<NF>([&](auto f_) {
      constexpr int f = f_;
      if constexpr (f == NF - 1 && i + 1 < NSTEP) {
        waitv<((i + LA < NSTEP ? i + LA : NSTEP - 1) - (i + 1)) * NA + xsum(i - LA + 2, i)>(); check(IC<i + 1>{}); SB(); }
      mm(f_, bf[i % (LL + 1)][f], a[i]);
      SB();
      if constexpr (i + LL < NSTEP) rd(IC<i + LL>{}, f_);
      if constexpr (f == 0 && i + LA < NSTEP) issue(IC<i + LA>{});
      SB();
    });
  });
}

__global__ __launch_bounds__(512, 2) void k_mega(const u16* __restrict__ x, const u16* __restrict__ Wqkv, const u16* __restrict__ Wo,
                                                 const u16* __restrict__ Wg, const u16* __restrict__ Wu, const u16* __restrict__ Wd,
                                                 char* __restrict__ ws, u16* __restrict__ out) {
  __shared__ __attribute__((aligned(16))) char smem[163840];
  const unsigned lds0 = lds_addr_of(smem);
  const int w = __builtin_amdgcn_readfirstlane(threadIdx.x >> 6);
  const int g8 = blockIdx.x & 7, i32 = blockIdx.x >> 3;
  char* ctl = ws + WS_CTL;
  const char* ctr = ctl + g8 * CTL_REP;
  unsigned cdum = 0;
  u32x2 ep;
  gld8<0>(ep, ctl, 0u);
  int f1idx;
  if (i32 < 30) {
    const int i = (g8 & 1) * 30 + i32, rb = (g8 >> 1) * 4 + (i & 3), cb = i >> 2;
    if (cb >= 10) p1_body<true>(x, Wqkv, ws, lds0, w, rb, cb);
    else p1_body<false>(x, Wqkv, ws, lds0, w, rb, cb);
    f1idx = (cb * 4 + (w & 3)) * 32 + rb * 2 + (w >> 2);
  } else {
    p1_repack(Wo, ws, w, blockIdx.x - 240);
    f1idx = 1920 + (blockIdx.x - 240) * 8 + w;
  }
  waitv<0>();
  pin(ep); pin(cdum);
  u32x2 tag;
  {
    const unsigned long long e = (((unsigned long long)__builtin_amdgcn_readfirstlane(ep[1]) << 32) | __builtin_amdgcn_readfirstlane(ep[0])) + 1ull;
    tag = (u32x2){(unsigned)e, (unsigned)(e >> 32)};
  }
  publish(ctl + CTL_F1 + f1idx * 8, tag, fresh_lane());
  bar();
  const int rb3 = g8 * 4 + (i32 >> 3), cb3 = i32 & 7;
  const int rb4 = (g8 >> 2) * 4 + (i32 >> 3), cb4 = (g8 & 3) * 8 + (i32 & 7);
  p2_attn(ws, Wg, Wu, Wd, lds0, w, tag, rb3, cb3, rb4, cb4);
  bar();
  const char* gug = ws + WS_WP + (size_t)NFR_WO * 1024 + (size_t)cb4 * 122880;
  const char* wdg = ws + WS_WP + (size_t)(NFR_WO + NFR_GU) * 1024 + (size_t)cb3 * 245760;
  const int rg4 = 24 * ROT3(cb4 & 7), rg5 = 20 * ROT5(cb3);
  u32x2 hp[5];
  if (w < 4) {
    const int lane = fresh_lane();
    const unsigned voff = lane * 16;
    const int row = rb3 * 64 + w * 16 + (lane & 15), col0 = cb3 * 80 + 4 * (lane >> 4);
    STAMP_AT(2, 0);
    u32x2 xr[5];
#pragma unroll
    for (int ct = 0; ct < 5; ++ct) xr[ct] = *(const u32x2*)(x + (size_t)row * 640 + col0 + 16 * ct);
    { const int hd = lane & 31, cgt = (hd & 1) ? hd * 5 : hd * 5 + 4;
      probe(ws + WS_O + (size_t)(rb3 * 4 + w) * 20480, (unsigned)((cgt >> 3) * 1024 + ((cgt & 7) >> 1) * 256 + (cgt & 1) * 8)); }
    sfor<5>([&](auto ct) { pin(xr[ct]); });
    STAMP_AT(2, 1);
    f32x4 acc[5] = {};
    gemm_res<20, 5, 20, 2, 1, 0, wo_off, x_none>(ws + WS_O + (size_t)(rb3 * 4 + w) * 20480, ROT3(cb3), lds0, voff,
      [&](auto i) { if constexpr (i == -1) STAMP_AT(2, 2); if constexpr (i == 1) STAMP_AT(2, 3); if constexpr (i == 10) STAMP_AT(2, 4);
        if constexpr (i == P3_G1 || i == 12) bar(); },
      [&](auto ct, const bf16x8& b, bf16x8 (&a)[1]) { acc[ct] = MFMA16(b, a[0], acc[ct]); });
    STAMP_AT(2, 6);
    float s1 = 0.f, s2 = 0.f;
    const char* hb = ws + WS_H + (size_t)(rb3 * 4 + w) * 20480;
    const unsigned vt = t16lane(lane);
#pragma unroll
    for (int ct = 0; ct < 5; ++ct) {
      hp[ct] = nofill(pk_bf16(bf_lo(xr[ct][0]) + acc[ct][0], bf_hi(xr[ct][0]) + acc[ct][1]), pk_bf16(bf_lo(xr[ct][1]) + acc[ct][2], bf_hi(xr[ct][1]) + acc[ct][3]));
      st8s(hb + t16off(cb3 * 80 + ct * 16), vt, hp[ct]);
      s1 = dot2(hp[ct][0], 0x3f803f80u, s1); s1 = dot2(hp[ct][1], 0x3f803f80u, s1);
      s2 = dot2(hp[ct][0], hp[ct][0], s2); s2 = dot2(hp[ct][1], hp[ct][1], s2);
    }
    s1 = red4(s1); s2 = red4(s2);
    if (lane < 16) st8s(ws + WS_HST + (size_t)(rb3 * 4 + w) * 1024 + (cb3 >> 1) * 256 + (cb3 & 1) * 8, (unsigned)(lane * 16), nofill(__float_as_uint(s1), __float_as_uint(s2)));
    bar();
    STAMP_AT(2, 7);
  } else {
    const int lane = fresh_lane(), v = w - 4;
    const unsigned voff = lane * 16;
    float cpart[10];
    const int cc = v * 24 + min(lane & 31, 23);
    sfor<10>([&](auto i) { gld4<i * 384>(cpart[i], ws + WS_CSP + (size_t)cb4 * 7680, (unsigned)((lane >> 5) * 3840 + cc * 4)); });
    waitv<0>();
    float cv = 0.f;
    sfor<10>([&](auto j) { pin(cpart[j]); cv += cpart[j]; });
    cv = add32(cv);
    bar();
    STAMP_AT(2, 2);
    sfor<5>([&](auto j) { glds16<true>(gug + rotf(v * 5 + j, rg4, 120) * 1024, voff, lds0 + gu_off(0) + (v * 5 + j) * 1024); });
    sfor<10>([&](auto j) { glds16<true>(gug + rotf(20 + v * 10 + j, rg4, 120) * 1024, voff, lds0 + gu_off(20) + (v * 10 + j) * 1024); });
    bar();
    sfor<15>([&](auto j) { glds16<true>(gug + rotf(60 + v * 15 + j, rg4, 120) * 1024, voff, lds0 + gu_off(60) + (v * 15 + j) * 1024); });
    STAMP_AT(2, 5);
    waitv<0>();
    bar();
    STAMP_AT(2, 6);
    if (lane < 24) ((LAS float*)(uintptr_t)(lds0 + CSF_OFF))[cc] = cv;
  }
  {
    const int lane = fresh_lane();
    const unsigned voff = lane * 16;
    const int r16 = lane & 15, g4 = lane >> 4;
    STAMP_AT(3, 0);
    f32x4 cs[6]; f32x2 ms[2];
    f32x4 hs[2];
#pragma unroll 1
    for (int spin = 0; spin < (1 << 17); ++spin) {
      gld16<0>(hs[0], ws + WS_HST + (size_t)(rb4 * 16 + w * 2) * 1024, voff);
      gld16<1024>(hs[1], ws + WS_HST + (size_t)(rb4 * 16 + w * 2) * 1024, voff);
      waitv<0>(); pin(hs[0]); pin(hs[1]);
      if (!__any(max(umax4(__builtin_bit_cast(u32x4, hs[0])), umax4(__builtin_bit_cast(u32x4, hs[1]))) == 0xffffffffu)) break;
      __builtin_amdgcn_s_sleep(1);
    }
    STAMP_AT(3, 1);
    f32x4 acc[2][6] = {};
    if (w < 4) __builtin_amdgcn_s_setprio(1);
    gemm_res<20, 6, K4_LA_, 1, 2, 20480, gu_off, x_p4>(ws + WS_H + (size_t)(rb4 * 16 + w * 2) * 20480, ROT3(cb4 & 7), lds0, voff,
      [&](auto i) { if constexpr (i == -1) STAMP_AT(3, 2); if constexpr (i == 1) STAMP_AT(3, 3); if constexpr (i == 10) STAMP_AT(3, 4);
        if constexpr (i == 9) bar();
        if constexpr (i == -1) sfor<2>([&](auto t) {
          const float s1 = red4(hs[t][0] + hs[t][2]), s2 = red4(hs[t][1] + hs[t][3]), mean = s1 * (1.f / 640.f);
          ms[t] = (f32x2){mean, __builtin_amdgcn_rsqf(fmaxf(s2 * (1.f / 640.f) - mean * mean, 0.f) + LN_EPS)}; pin(ms[t]); });
        if constexpr (i == 18) sfor<6>([&](auto ct) { cs[ct] = *(const LAS f32x4*)(uintptr_t)(lds0 + CSF_OFF + (ct * 16 + 4 * g4) * 4); });
        if constexpr (i >= 2 && i < 7) { const int f = (i - 2) * 8 + w - ((i == 6 && w >= 4) ? 4 : 0); glds16<true>(wdg + rotf(f, rg5, 240) * 1024, voff, lds0 + wd_off(0) + f * 1024); }
        if constexpr (i >= 10 && i < 15) { constexpr int n = i - 10; glds16<true>(wdg + rotf(40 + n * 8 + w, rg5, 240) * 1024, voff, lds0 + wd_off(40) + (n * 8 + w) * 1024); }
        if constexpr (i >= 15 && i < 18) { const int f = (i - 15) * 8 + w - ((i == 17 && w >= 4) ? 4 : 0); glds16<true>(wdg + rotf(140 + f, rg5, 240) * 1024, voff, lds0 + wd_off(140) + f * 1024); } },
      [&](auto ct, const bf16x8& b, bf16x8 (&a)[2]) { acc[0][ct] = MFMA16(b, a[0], acc[0][ct]); acc[1][ct] = MFMA16(b, a[1], acc[1][ct]); });
    STAMP_AT(3, 6);
    constexpr float GC0 = -2.f * 0.7978845608028654f * 1.4426950408889634f, GC1 = GC0 * 0.044715f;
    const char* gb = ws + WS_G + (size_t)(rb4 * 16 + w * 2) * 49152;
    const unsigned vt = t16lane(lane);
#pragma unroll
    for (int t = 0; t < 2; ++t) {
      const float nm = -ms[t].x, rstd = ms[t].y;
#pragma unroll
      for (int ct = 0; ct < 3; ++ct) {
        const f32x4 gv = (acc[t][ct] + cs[ct] * nm) * rstd, uv = (acc[t][3 + ct] + cs[3 + ct] * nm) * rstd;
        const f32x4 z = gv * (gv * gv * GC1 + GC0);
        f32x4 e;
#pragma unroll
        for (int j = 0; j < 4; ++j) e[j] = __builtin_amdgcn_exp2f(z[j]);
        e = e + 1.f;
#pragma unroll
        for (int j = 0; j < 4; ++j) e[j] = __builtin_amdgcn_rcpf(e[j]);
        const f32x4 r = gv * uv * e;
        st8s(gb + t * 49152 + t16off(cb4 * 48 + ct * 16), vt, nofill(pk_bf16(r[0], r[1]), pk_bf16(r[2], r[3])));
      }
    }
    __builtin_amdgcn_s_setprio(0);
    STAMP_AT(3, 7);
    waitv<6>();
    STAMP_AT(3, 8);
    bar();
  }
  if (w < 4) {
    const int lane = fresh_lane();
    const unsigned voff = lane * 16;
    const int row = rb3 * 64 + w * 16 + (lane & 15), col0 = cb3 * 80 + 4 * (lane >> 4);
    STAMP_AT(4, 0);
    probe(ws + WS_G + (size_t)(rb3 * 4 + w) * 49152, (unsigned)t16off((lane & 31) * 48 + 32));
    STAMP_AT(4, 1);
    f32x4 acc[5] = {};
    gemm_res<48, 5, K5_LA_, 2, 1, 0, wd_off, x_none>(ws + WS_G + (size_t)(rb3 * 4 + w) * 49152, ROT5(cb3), lds0, voff,
      [&](auto i) { if constexpr (i == 1) STAMP_AT(4, 3); if constexpr (i == 14) STAMP_AT(4, 4); if constexpr (i == 30) STAMP_AT(4, 5);
        if constexpr (i == -1 || i == 8 || i == 14 || i == 16 || i == 30 || i == 38) bar(); },
      [&](auto ct, const bf16x8& b, bf16x8 (&a)[1]) { acc[ct] = MFMA16(b, a[0], acc[ct]); });
    STAMP_AT(4, 12);
#pragma unroll
    for (int ct = 0; ct < 5; ++ct) {
      const unsigned p0 = pk_bf16(bf_lo(hp[ct][0]) + acc[ct][0], bf_hi(hp[ct][0]) + acc[ct][1]);
      const unsigned p1 = pk_bf16(bf_lo(hp[ct][1]) + acc[ct][2], bf_hi(hp[ct][1]) + acc[ct][3]);
      *(u32x2*)(out + (size_t)row * 640 + col0 + 16 * ct) = (u32x2){p0, p1};
    }
    STAMP_AT(4, 13);
  } else {
    const int lane = fresh_lane(), v = w - 4;
    const unsigned voff = lane * 16;
    glds16<true>(wdg + rotf(36 + v, rg5, 240) * 1024, voff, lds0 + wd_off(36) + v * 1024);
    waitv<0>(); bar();
    sfor<3>([&](auto g) { sfor<5>([&](auto j) { constexpr int f = (g + 4) * 20 + j; glds16<true>(wdg + rotf(f + v * 5, rg5, 240) * 1024, voff, lds0 + wd_off(f) + v * 5120); }); });
    bar();
    sfor<2>([&](auto g) { sfor<5>([&](auto j) { constexpr int f = (g + 8) * 20 + j; glds16<true>(wdg + rotf(f + v * 5, rg5, 240) * 1024, voff, lds0 + wd_off(f) + v * 5120); }); });
    waitv<10>(); bar();
    bar();
    sfor<2>([&](auto g) { sfor<5>([&](auto j) { constexpr int f = (g + 10) * 20 + j; glds16<true>(wdg + rotf(f + v * 5, rg5, 240) * 1024, voff, lds0 + wd_off(f) + v * 5120); }); });
    waitv<10>(); bar();
    waitv<0>(); bar();
    if (blockIdx.x == 0 && w == 4) {
      poll2(ctr + CTL_FR, (unsigned)(lane * 64), (unsigned)(4096 + lane * 64), tag);
      poll2(ctr + CTL_FR, (unsigned)(8192 + lane * 64), (unsigned)(12288 + lane * 64), tag);
      if (lane == 0) st8(ctl, tag);
    }
  }
}

extern "C" void kernel_launch(void* const* d_in, const int* in_sizes, int n_in, void* d_out, int out_size, void* d_ws, size_t ws_size,
                              hipStream_t stream) {
  k_mega<<<256, 512, 0, stream>>>((const u16*)d_in[0], (const u16*)d_in[1], (const u16*)d_in[2], (const u16*)d_in[4], (const u16*)d_in[5],
                                  (const u16*)d_in[6], (char*)d_ws, (u16*)d_out);
}
```

```cpp
#include <hip/hip_runtime.h>
#include <hip/hip_bf16.h>
#include <stdint.h>
#include <utility>
#define NO_ROT 1
#define NO_ROT2 1

typedef short bf16x8 __attribute__((ext_vector_type(8)));
typedef short s16x4 __attribute__((ext_vector_type(4)));
typedef float f32x16 __attribute__((ext_vector_type(16)));
typedef float f32x4 __attribute__((ext_vector_type(4)));
typedef float f32x2 __attribute__((ext_vector_type(2)));
typedef unsigned u32x4 __attribute__((ext_vector_type(4)));
typedef unsigned u32x2 __attribute__((ext_vector_type(2)));
typedef __bf16 bf2_t __attribute__((ext_vector_type(2)));
typedef unsigned short u16;
#define LAS __attribute__((address_space(3)))
#define DI __device__ __forceinline__

constexpr int S_ = 2048, D_ = 640, H_ = 32, DH = 20, F_ = 1536;
constexpr float LN_EPS = 1e-5f;
constexpr float QSCALE = 0.22360679774997896f * 1.4426950408889634f;

constexpr size_t WS_Q   = 0;
constexpr size_t WS_KV  = WS_Q + 2621440;
constexpr size_t WS_O   = WS_KV + 5242880;
constexpr size_t WS_H   = WS_O + 2621440;
constexpr size_t WS_HST = WS_H + 2621440;
constexpr size_t WS_G   = WS_HST + 131072;
constexpr size_t WS_WP  = WS_G + 6291456;
constexpr size_t WS_CSP = WS_WP + 6717440;
constexpr size_t WS_CTL = WS_CSP + 245760;
constexpr size_t CTL_F1 = 256;
constexpr size_t CTL_F2 = CTL_F1 + 2048 * 8;
constexpr size_t CTL_FR = CTL_F2 + 2048 * 8;
constexpr size_t CTL_F3 = CTL_FR + 2048 * 8;
constexpr size_t CTL_F4 = CTL_F3 + 1024 * 8;
constexpr size_t CTL_FD = CTL_F4 + 2048 * 8;
constexpr size_t CTL_REP = CTL_FD + 2048 * 8 - 256;
constexpr int NFR_WO = 800, NFR_GU = 3840, NFR_WD = 1920, NFR_ALL = NFR_WO + NFR_GU + NFR_WD;

DI unsigned pk_bf16(float lo, float hi) { f32x2 f = {lo, hi}; return __builtin_bit_cast(unsigned, __builtin_convertvector(f, bf2_t)); }
DI float bf_lo(unsigned u) { return __uint_as_float(u << 16); }
DI float bf_hi(unsigned u) { return __uint_as_float(u & 0xffff0000u); }
DI float dot2(unsigned a, unsigned b, float c) { return __builtin_amdgcn_fdot2_f32_bf16(__builtin_bit_cast(bf2_t, a), __builtin_bit_cast(bf2_t, b), c, false); }
DI float add32(float v) { const auto r = __builtin_amdgcn_permlane32_swap(__float_as_uint(v), __float_as_uint(v), false, false); return __uint_as_float(r[0]) + __uint_as_float(r[1]); }
DI float max32(float v) { const auto r = __builtin_amdgcn_permlane32_swap(__float_as_uint(v), __float_as_uint(v), false, false); return fmaxf(__uint_as_float(r[0]), __uint_as_float(r[1])); }
DI float add16(float v) { const auto r = __builtin_amdgcn_permlane16_swap(__float_as_uint(v), __float_as_uint(v), false, false); return __uint_as_float(r[0]) + __uint_as_float(r[1]); }
DI float red4(float v) { return add32(add16(v)); }
DI float hi32(float v) { const auto r = __builtin_amdgcn_permlane32_swap(__float_as_uint(v), __float_as_uint(v), false, false); return __uint_as_float(r[1]); }
#define MFMA32(a, b, c) __builtin_amdgcn_mfma_f32_32x32x16_bf16((a), (b), (c), 0, 0, 0)
#define MFMA32K8(a, b, c) __builtin_amdgcn_mfma_f32_32x32x8bf16_1k((a), (b), (c), 0, 0, 0)
#define MFMA16(a, b, c) __builtin_amdgcn_mfma_f32_16x16x32_bf16((a), (b), (c), 0, 0, 0)

DI void mfma16(f32x4& c, const bf16x8& a, const bf16x8& b) { asm volatile("v_mfma_f32_16x16x32_bf16 %0, %1, %2, %0" : "+v"(c) : "v"(a), "v"(b)); }
DI void acc_fence() { asm volatile("s_nop 7\n\ts_nop 3" ::: "memory"); }
template <bool DEV> DI void glds16(const void* sbase, unsigned voff, unsigned lds_base) {
  if constexpr (DEV) asm volatile("s_mov_b32 m0, %2\n\ts_nop 0\n\tglobal_load_lds_dwordx4 %0, %1 sc1" :: "v"(voff), "s"(sbase), "s"(lds_base) : "memory");
  else asm volatile("s_mov_b32 m0, %2\n\ts_nop 0\n\tglobal_load_lds_dwordx4 %0, %1" :: "v"(voff), "s"(sbase), "s"(lds_base) : "memory");
}
template <int OFF, class T> DI void gld16(T& dst, const void* sbase, unsigned voff) {
  asm volatile("global_load_dwordx4 %0, %1, %2 offset:%3 sc1" : "=v"(dst) : "v"(voff), "s"(sbase), "i"(OFF) : "memory");
}
template <int OFF, class T> DI void gld8(T& dst, const void* sbase, unsigned voff) {
  asm volatile("global_load_dwordx2 %0, %1, %2 offset:%3 sc1" : "=v"(dst) : "v"(voff), "s"(sbase), "i"(OFF) : "memory");
}
template <int OFF> DI void gld4(float& dst, const void* sbase, unsigned voff) {
  asm volatile("global_load_dword %0, %1, %2 offset:%3 sc1" : "=v"(dst) : "v"(voff), "s"(sbase), "i"(OFF) : "memory");
}
DI void asm_warm(u32x4& dst, const void* sbase, unsigned voff) {
  asm volatile("global_load_dwordx4 %0, %1, %2 sc1" : "+v"(dst) : "v"(voff), "s"(sbase) : "memory");
}
template <int OFF> DI void gldu16(unsigned& dst, const void* sbase, unsigned voff) {
  asm volatile("global_load_ushort %0, %1, %2 offset:%3" : "=v"(dst) : "v"(voff), "s"(sbase), "i"(OFF) : "memory");
}
DI void st16(void* p, u32x4 v) { asm volatile("global_store_dwordx4 %0, %1, off sc1\n\ts_nop 1" :: "v"(p), "v"(v) : "memory"); }
DI void st8(void* p, u32x2 v) { asm volatile("global_store_dwordx2 %0, %1, off sc1" :: "v"(p), "v"(v) : "memory"); }
DI void st4(void* p, float v) { asm volatile("global_store_dword %0, %1, off sc1" :: "v"(p), "v"(v) : "memory"); }
DI void st8s(const void* sbase, unsigned voff, u32x2 v) { asm volatile("global_store_dwordx2 %0, %1, %2 sc1" :: "v"(voff), "v"(v), "s"(sbase) : "memory"); }
DI u32x2 nofill(unsigned a, unsigned b) { return (u32x2){min(a, 0xfffffffeu), min(b, 0xfffffffeu)}; }
DI unsigned umax4(u32x4 v) { return max(max(v[0], v[1]), max(v[2], v[3])); }
template <int N> DI void waitv() { asm volatile("s_waitcnt vmcnt(%0)" :: "i"(N) : "memory"); }
DI void bar() { asm volatile("s_waitcnt lgkmcnt(0)\n\ts_barrier" ::: "memory"); }
DI void setflag(char* flag, u32x2 tag, int lane) { if (lane < 8) st8(flag + lane * CTL_REP, tag); }
DI void publish(char* flag, u32x2 tag, int lane) { waitv<0>(); setflag(flag, tag, lane); }
DI void poll(const char* base, unsigned voff, u32x2 tag) {
#pragma unroll 1
  for (int spin = 0; spin < (1 << 17); ++spin) {
    u32x2 v;
    asm volatile("global_load_dwordx2 %0, %1, %2 sc1\n\ts_waitcnt vmcnt(0)" : "=v"(v) : "v"(voff), "s"(base) : "memory");
    if (__all(v[0] == tag[0] && v[1] == tag[1])) break;
    __builtin_amdgcn_s_sleep(1);
  }
}
DI void poll2(const char* base, unsigned voff0, unsigned voff1, u32x2 tag) {
#pragma unroll 1
  for (int spin = 0; spin < (1 << 17); ++spin) {
    u32x2 v, u;
    asm volatile("global_load_dwordx2 %0, %2, %4 sc1\n\tglobal_load_dwordx2 %1, %3, %4 sc1\n\ts_waitcnt vmcnt(0)" : "=&v"(v), "=&v"(u) : "v"(voff0), "v"(voff1), "s"(base) : "memory");
    if (__all(v[0] == tag[0] && v[1] == tag[1] && u[0] == tag[0] && u[1] == tag[1])) break;
    __builtin_amdgcn_s_sleep(1);
  }
}
DI void probe(const char* base, unsigned voff) {
#pragma unroll 1
  for (int spin = 0; spin < (1 << 17); ++spin) {
    unsigned v;
    asm volatile("global_load_dword %0, %1, %2 sc1\n\ts_waitcnt vmcnt(0)" : "=v"(v) : "v"(voff), "s"(base) : "memory");
    if (__all(v != 0xffffffffu)) break;
    __builtin_amdgcn_s_sleep(1);
  }
}
constexpr int t16off(int c0) { return (c0 >> 5) * 1024 + (c0 & 16) * 32; }
DI unsigned t16lane(int lane) { return (unsigned)((lane >> 5) * 256 + (lane & 15) * 16 + ((lane >> 4) & 1) * 8); }
DI int rotf(int f, int off, int n) { const int r = f + off; return r - (r >= n ? n : 0); }
DI void st16s(const void* sbase, unsigned voff, u32x4 v) { asm volatile("global_store_dwordx4 %0, %1, %2 sc1\n\ts_nop 1" :: "v"(voff), "v"(v), "s"(sbase) : "memory"); }
DI float row16_sum(float v) {
  v += __builtin_bit_cast(float, __builtin_amdgcn_update_dpp(0, __builtin_bit_cast(int, v), 0x128, 0xf, 0xf, false));
  v += __builtin_bit_cast(float, __builtin_amdgcn_update_dpp(0, __builtin_bit_cast(int, v), 0x124, 0xf, 0xf, false));
  v += __builtin_bit_cast(float, __builtin_amdgcn_update_dpp(0, __builtin_bit_cast(int, v), 0x122, 0xf, 0xf, false));
  v += __builtin_bit_cast(float, __builtin_amdgcn_update_dpp(0, __builtin_bit_cast(int, v), 0x121, 0xf, 0xf, false));
  return v;
}
#define ROT3(c) 0
#define ROT5(c) 0
template <int KB> DI void code_warm(u32x4& dum, int idx, unsigned voff) {
  unsigned long long pc; asm volatile("s_getpc_b64 %0" : "=s"(pc));
  asm_warm(dum, (const char*)(pc & ~63ull) + min(idx, KB - 1) * 1024, voff);
}
#define CW1 40
#define CW2 29
DI int fresh_lane() { int l = __builtin_amdgcn_mbcnt_hi(~0u, __builtin_amdgcn_mbcnt_lo(~0u, 0u)); asm volatile("" : "+v"(l)); return l; }
template <class T> DI void pin(T& v) { asm volatile("" : "+v"(v)); }
DI bf16x8 lds128(unsigned addr) { return *(const LAS bf16x8*)(uintptr_t)addr; }
DI s16x4 lds64(unsigned addr) { return *(const LAS s16x4*)(uintptr_t)addr; }
DI s16x4 ldstr(unsigned addr) { return __builtin_amdgcn_ds_read_tr16_b64_v4i16((LAS s16x4*)(uintptr_t)addr); }
DI unsigned lds_addr_of(const void* p) { return (unsigned)(uintptr_t)(const LAS char*)p; }
#define STAMP_AT(kid, idx) ((void)0)
#define RETRY_COUNT(k) ((void)0)
#define WAITV_BAR(N) asm volatile("s_waitcnt vmcnt(" #N ") lgkmcnt(0)\n\ts_barrier" ::: "memory")
#define SB() __builtin_amdgcn_sched_barrier(0)
template <int N> using IC = std::integral_constant<int, N>;
template <class F, int... Is> DI void sfor_(F&& f, std::integer_sequence<int, Is...>) { (f(IC<Is>{}), ...); }
template <int N, class F> DI void sfor(F&& f) { sfor_(f, std::make_integer_sequence<int, N>{}); }
template <int N> DI void waitv_bar() { asm volatile("s_waitcnt vmcnt(%0) lgkmcnt(0)\n\ts_barrier" :: "i"(N) : "memory"); }

#define K1_LA_ 10
#define P3_G1 -1
#define K4_LA_ 6
#define K4_CG_ 2
#define K5_LA_ 16
constexpr int K1_STAGE = 32768, K1_NST = 4, K1_LA = K1_LA_;
static_assert(K1_LA <= 10, "slot reuse");
template <bool ISV>
DI void p1_body(const u16* __restrict__ x, const u16* __restrict__ W, char* __restrict__ ws, const unsigned lds0, const int w, int rb, int cb) {
  const int lane = fresh_lane();
  const int wm = w >> 2, wn = w & 3;
  const int r32 = lane & 31, hh = lane >> 5;
  const unsigned a_voff0 = (unsigned)((lane >> 3) * 1280 + (((lane & 7) ^ ((lane >> 4) & 7)) << 4));
  const unsigned a_voff1 = (unsigned)((lane >> 3) * 1280 + (((lane & 7) ^ ((4 + (lane >> 4)) & 7)) << 4)) + 8 * 1280;
  const unsigned w_voff0 = (unsigned)((lane >> 3) * 3840 + (((lane & 7) ^ (((lane >> 4) & 1) << 2)) << 4));
  const unsigned w_voff1 = w_voff0 + 8 * 3840;
  const char* xs = (const char*)x + (size_t)(rb * 128 + w * 16) * 1280;
  const char* wsrc = (const char*)W + (size_t)((w & 3) * 16) * 3840 + (size_t)(cb * 128 + (w >> 2) * 64) * 2;
  const unsigned dst0 = lds0 + w * 2048;
  const int rot = 0;
  auto dma = [&](auto n_) {
    constexpr int n = n_, s = n >> 2, j = n & 3; constexpr unsigned so = (s % K1_NST) * K1_STAGE;
    const int sr = s + rot - (s + rot >= 10 ? 10 : 0);
    if constexpr (j == 0) glds16<false>(xs + sr * 128, a_voff0, dst0 + so);
    if constexpr (j == 1) glds16<false>(wsrc + (size_t)sr * 64 * 3840, w_voff0, dst0 + so + 16384);
    if constexpr (j == 2) glds16<false>(xs + sr * 128, a_voff1, dst0 + so + 1024);
    if constexpr (j == 3) glds16<false>(wsrc + (size_t)sr * 64 * 3840, w_voff1, dst0 + so + 16384 + 1024);
  };
  STAMP_AT(0, 0);
  sfor<K1_LA>([&](auto n) { dma(n); });
  const int ob = (wn >> 1) ^ wm;
  const int fsel = wn & 1;
  unsigned xa[2][4], xb[2][4], wa[2][2];
  {
    const int row = wm * 64 + fsel * 32 + r32;
    const int blk = (lane >> 4) & 1, q = (lane & 15) >> 2, p = lane & 3;
    const int c = (wn & 1) * 4 + blk * 2 + (p >> 1);
    const unsigned base = lds0 + (unsigned)(16384 + (wn >> 1) * 8192 + (8 * hh + q) * 128 + ((c ^ (((q >> 1) & 1) << 2)) << 4) + (p & 1) * 8);
#pragma unroll
    for (int hs = 0; hs < 2; ++hs) {
#pragma unroll
      for (int i = 0; i < 4; ++i) {
        xa[hs][i] = lds0 + hs * 65536 + (unsigned)(row * 128 + ((((i ^ ob) * 2 + hh) ^ ((row >> 1) & 7)) << 4));
        xb[hs][i] = xa[hs][i] + (fsel ? -4096 : 4096);
      }
      wa[hs][0] = base + hs * 65536 + ob * 2048; wa[hs][1] = base + hs * 65536 + (1 ^ ob) * 2048;
    }
  }
  f32x16 acc0 = {}, acc1 = {};
  float sxx = 0.f;
  f32x4 sx = {}, sw = {};
  bf16x8 sel = (bf16x8)(short)(((lane & 15) == ((lane >> 4) & 1)) ? 0x3f80 : 0); pin(sel);
  bf16x8 fx0[4], fx1[4], fw[4];
  auto rd = [&](auto i_) {
    constexpr int i = i_, s = i >> 2, p = i & 3, sl = s % K1_NST, hs = sl >> 1; constexpr unsigned so = (sl & 1) * K1_STAGE;
    fx0[p] = lds128(xa[hs][p] + so);
    const s16x4 wl = ldstr(wa[hs][p & 1] + so + (p >> 1) * 4096);
    const s16x4 wh = ldstr(wa[hs][p & 1] + so + (p >> 1) * 4096 + 512);
    fw[p] = __builtin_shufflevector(wl, wh, 0, 1, 2, 3, 4, 5, 6, 7);
    fx1[p] = lds128(xb[hs][p] + so);
  };
  const bool isq = cb < 5;
  const int pitch = isq ? 1280 : 2560;
  unsigned off[4];
  if (ISV) {
    const int n = cb * 128 + wn * 32 + r32 - 1280, head = n / 20, d0 = n - head * 20;
    const int d = (head & 1) ? (d0 < 4 ? d0 + 16 : d0 - 4) : d0;
    off[0] = (unsigned)(head * 163840 + 1280 + (hh * 20 + d) * 16); pin(off[0]);
  } else {
#pragma unroll
    for (int g = 0; g < 4; ++g) {
      const int n = cb * 128 + wn * 32 + 8 * g + 4 * hh - (isq ? 0 : 640), head = n / 20, d0 = n - head * 20;
      const int d = (head & 1) ? (d0 < 4 ? d0 + 16 : d0 - 4) : d0;
      off[g] = (unsigned)(head * 64 * pitch + (d < 16 ? (((d >> 3) * 32 + r32) * 16 + (d & 4) * 2) : (1024 + r32 * 8))); pin(off[g]);
    }
  }
  waitv_bar<K1_LA - 4>();
  STAMP_AT(0, 1);
  rd(IC<0>{}); rd(IC<1>{});
  sfor<40>([&](auto i_) {
    constexpr int i = i_, p = i & 3;
    if constexpr ((i + 2) % 4 == 0 && i + 2 < 40) { constexpr int last = (i - 1 + K1_LA < 39 ? i - 1 + K1_LA : 39); waitv<(last - (i + 5) > 0 ? last - (i + 5) : 0)>(); asm volatile("s_barrier" ::: "memory"); }
    if constexpr (i + K1_LA < 40) dma(IC<i + K1_LA>{});
    if constexpr (i + 2 < 40) rd(IC<i + 2>{});
    SB();
    if (ISV) { acc0 = MFMA32(fx0[p], fw[p], acc0); acc1 = MFMA32(fx1[p], fw[p], acc1); }
    else     { acc0 = MFMA32(fw[p], fx0[p], acc0); acc1 = MFMA32(fw[p], fx1[p], acc1); }
    if constexpr ((p & 1) == 0) {
      sx = MFMA16(fx0[p], sel, sx);
      const u32x4 u = __builtin_bit_cast(u32x4, fx0[p]);
#pragma unroll
      for (int j = 0; j < 4; ++j) sxx = dot2(u[j], u[j], sxx);
      pin(sxx);
    } else {
      sw = MFMA16(fw[p], sel, sw); }
    SB();
  });
  STAMP_AT(0, 11);
  sxx = add32(sxx);
  LAS float* psum = (LAS float*)(uintptr_t)(lds0 + 65536);
  LAS float* psq = psum + 256;
  LAS float* csp = psum + 512;
  if (hh == 0) psq[(wn >> 1) * 128 + wm * 64 + fsel * 32 + r32] = sxx;
  if ((lane & 15) < 2) { const int o = (lane & 1) * 16 + (lane >> 4) * 4;
    *(LAS f32x4*)&psum[(wn >> 1) * 128 + wm * 64 + fsel * 32 + o] = sx; *(LAS f32x4*)&csp[wm * 128 + wn * 32 + o] = sw; }
  bar();
  STAMP_AT(0, 12);
  auto stat = [&](int rl) {
    const float mean = (psum[rl] + psum[128 + rl]) * (1.f / 640.f);
    const float var = fmaxf((psq[rl] + psq[128 + rl]) * (1.f / 640.f) - mean * mean, 0.f);
    return (f32x2){mean, __builtin_amdgcn_rsqf(var + LN_EPS)}; };
  if (ISV) {
    LAS f32x2* st = (LAS f32x2*)(uintptr_t)(lds0 + 65536 + 4096 + w * 512);
    st[lane] = stat(wm * 64 + lane);
    const int nl = wn * 32 + r32;
    const float c = csp[nl] + csp[128 + nl];
#pragma unroll
    for (int t = 0; t < 2; ++t) {
      const f32x16& a = t ? acc1 : acc0;
      const int rl = (t ^ fsel) * 32;
      unsigned pk[8];
#pragma unroll
      for (int g = 0; g < 4; ++g) {
        const f32x4 s01 = *(const LAS f32x4*)&st[rl + 8 * g + 4 * hh], s23 = *(const LAS f32x4*)&st[rl + 8 * g + 4 * hh + 2];
        pk[2 * g]     = pk_bf16(s01[1] * (a[4 * g] - s01[0] * c), s01[3] * (a[4 * g + 1] - s01[2] * c));
        pk[2 * g + 1] = pk_bf16(s23[1] * (a[4 * g + 2] - s23[0] * c), s23[3] * (a[4 * g + 3] - s23[2] * c));
      }
      const char* dst = ws + WS_KV + (size_t)(rb * 4 + wm * 2 + (rl >> 5)) * 2560;
      st16s(dst, off[0], (u32x4){pk[0], pk[1], pk[2], pk[3]});
      st16s(dst + 640, off[0], (u32x4){pk[4], pk[5], pk[6], pk[7]});
    }
  } else {
    const float sc = isq ? QSCALE : 1.f;
    const char* base = ws + (isq ? WS_Q : WS_KV);
    f32x4 c[4];
#pragma unroll
    for (int g = 0; g < 4; ++g) { const int nl = wn * 32 + 8 * g + 4 * hh; c[g] = *(const LAS f32x4*)&csp[nl] + *(const LAS f32x4*)&csp[128 + nl]; }
#pragma unroll
    for (int t = 0; t < 2; ++t) {
      const f32x16& a = t ? acc1 : acc0;
      const int rl = wm * 64 + (t ^ fsel) * 32;
      const f32x2 ms = stat(rl + r32);
      const float rs = ms.y * sc, mr = ms.x;
      const char* tb = base + (size_t)(rb * 4 + (rl >> 5)) * pitch;
#pragma unroll
      for (int g = 0; g < 4; ++g) {
        const unsigned p0 = pk_bf16(rs * (a[4 * g] - mr * c[g][0]), rs * (a[4 * g + 1] - mr * c[g][1]));
        const unsigned p1 = pk_bf16(rs * (a[4 * g + 2] - mr * c[g][2]), rs * (a[4 * g + 3] - mr * c[g][3]));
        st8s(tb, off[g], (u32x2){p0, p1});
      }
    }
  }
  STAMP_AT(0, 13);
}

DI void p1_repack(const u16* __restrict__ Wo, char* __restrict__ ws, const int w, int idx) {
  const int lane = fresh_lane();
  const unsigned pvo = (lane >> 4) * 8 * 1280 + (lane & 15) * 2;
  unsigned pw[7][8];
  sfor<7>([&](auto r) {
    const int f = idx * 50 + min(w + 8 * r, 49), cbk = f / 100, rem = f - cbk * 100, ks = rem / 5, ct = rem - ks * 5;
    const char* src = (const char*)(Wo + (size_t)(32 * ks) * 640 + 80 * cbk + 16 * ct);
    sfor<8>([&](auto i) { gldu16<(i & 3) * 1280>(pw[r][i], src + (i & ~3) * 1280, pvo); });
  });
  sfor<7>([&](auto r) {
    waitv<(6 - r) * 8>();
    sfor<8>([&](auto j) { pin(pw[r][j]); });
    const u32x4 v = {pw[r][0] | (pw[r][1] << 16), pw[r][2] | (pw[r][3] << 16), pw[r][4] | (pw[r][5] << 16), pw[r][6] | (pw[r][7] << 16)};
    pw[r][0] = v[0]; pw[r][1] = v[1]; pw[r][2] = v[2]; pw[r][3] = v[3];
  });
  sfor<7>([&](auto r) {
    if (w + 8 * r < 50) st16(ws + WS_WP + (size_t)(idx * 50 + w + 8 * r) * 1024 + lane * 16, (u32x4){pw[r][0], pw[r][1], pw[r][2], pw[r][3]});
  });
}

template <int OFF> DI void rdK(bf16x8& k0, s16x4& k1, unsigned a0, unsigned a1) {
  asm volatile("ds_read_b128 %0, %2 offset:%4\n\tds_read_b64 %1, %3 offset:%4" : "=&v"(k0), "=&v"(k1) : "v"(a0), "v"(a1), "i"(OFF));
}
template <int OFF> DI void rdV(bf16x8& v0, bf16x8& v1, unsigned a, unsigned long long msk) {
  asm volatile("s_mov_b64 exec, %5\n\tds_read_b128 %0, %2 offset:%3\n\tds_read_b128 %1, %2 offset:%4\n\ts_mov_b64 exec, -1"
               : "+v"(v0), "+v"(v1) : "v"(a), "i"(OFF), "i"(OFF + 640), "s"(msk));
}
#define EX2(v) __builtin_amdgcn_exp2f(v)
constexpr int wo_off(int p) { return (p < 60 ? p : p + 20) * 1024; }
constexpr int gu_off(int f) { return (f < 20 ? f + 60 : f < 60 ? f + 100 : f - 60) * 1024; }
constexpr int wd_off(int f) { return ((f / 20 + 4) % 8) * 20480 + (f % 20) * 1024; }
constexpr int CSF_OFF = 116 * 1024, XCH_OFF = 120 * 1024;

DI void p2_attn(char* __restrict__ ws, const u16* __restrict__ Wg, const u16* __restrict__ Wu, const u16* __restrict__ Wd,
                const unsigned lds0, const int w, const u32x2 tag, const int rb3, const int cb3, const int rb4, const int cb4) {
  const int lane = fresh_lane();
  const int kh = w >> 2, wq = w & 3;
  const int head = (blockIdx.x & 7) * 4 + ((blockIdx.x >> 3) & 3), qb = blockIdx.x >> 5;
  const int r32 = lane & 31, hh = lane >> 5;
  const char* kvg = ws + WS_KV + (size_t)head * 163840 + (size_t)kh * 81920 + wq * 1024;
  const unsigned voff = lane * 16;
  const unsigned kvl = lds0 + kh * 81920 + wq * 1024;
  char* ctl = ws + WS_CTL;
  const char* ctr = ctl + (blockIdx.x & 7) * CTL_REP;
  STAMP_AT(1, 0);
  unsigned f0;
  {
    const int cfl = (20 * head) >> 5, cfh = (20 * head + 19) >> 5;
    f0 = (unsigned)(((((lane & 32) ? 40 : 20) + ((lane & 16) ? cfh : cfl)) * 32 + kh * 16 + (lane & 15)) * 8);
    const unsigned f1 = (unsigned)((lane < 32 ? (((lane & 1) ? cfh : cfl) * 32 + qb * 4 + wq) : 1920 + cb3 * 16 + (lane & 15)) * 8);
    poll2(ctr + CTL_F1, f0 & ~(12u * 8u), f1, tag);
  }
  STAMP_AT(1, 4);
#pragma unroll
  for (int i = 0; i < 4; ++i) glds16<true>(kvg + i * 4096, voff, kvl + i * 4096);
  bf16x8 q0[2]; s16x4 q1[2];
  {
    const char* qg = ws + WS_Q + ((size_t)head * 64 + qb * 8 + wq * 2) * 1280;
    gld16<0>(q0[0], qg, voff); gld16<1280>(q0[1], qg, voff); gld8<1024>(q1[0], qg, r32 * 8); gld8<2304>(q1[1], qg, r32 * 8);
  }
  const unsigned ka0 = lds0 + kh * 81920 + lane * 16, ka1 = lds0 + kh * 81920 + 1024 + r32 * 8, va = lds0 + kh * 81920 + 1280 + (hh * 20 + (r32 < 20 ? r32 : 0)) * 16;
  bf16x8 K0[2], V0[2], V1[2]; s16x4 K1[2];
  V0[0] = V0[1] = V1[0] = V1[1] = (bf16x8)(short)(r32 == 20 ? 0x3f80 : 0);
  pin(V0[0]); pin(V0[1]); pin(V1[0]); pin(V1[1]);
  WAITV_BAR(0);
  u32x2 fr; gld8<0>(fr, ctr + CTL_F1, f0);
  pin(q0[0]); pin(q0[1]); pin(q1[0]); pin(q1[1]);
  if (hh) { q1[0] = (s16x4){0, 0, 0, 0}; q1[1] = (s16x4){0, 0, 0, 0}; }
  STAMP_AT(1, 1);
  rdK<0>(K0[0], K1[0], ka0, ka1);
  const unsigned long long vmsk = 0x000fffff000fffffull;
  rdV<0>(V0[0], V1[0], va, vmsk);
  f32x16 cm[2];
  {
    const bf16x8 kf0 = lds128(lds0 + lane * 16); const s16x4 kf1 = lds64(lds0 + 1024 + r32 * 8);
#pragma unroll
    for (int t = 0; t < 2; ++t) {
      f32x16 s = MFMA32(kf0, q0[t], (f32x16){}); s = MFMA32K8(kf1, q1[t], s);
      float m = s[0];
#pragma unroll
      for (int i = 1; i < 16; ++i) m = fmaxf(m, s[i]);
      m = max32(m);
      cm[t] = (f32x16)(-m);
      pin(cm[t]);
    }
  }
  asm volatile("s_waitcnt lgkmcnt(0)" : "+v"(K0[0]), "+v"(K1[0]), "+v"(V0[0]), "+v"(V1[0]));
  f32x16 o[2] = {};
  f32x16 SA, SB_;
  u32x4 PA0, PA1, PB0 = {}, PB1 = {};
  SA = MFMA32(K0[0], q0[0], cm[0]); SA = MFMA32K8(K1[0], q1[0], SA);
  waitv<0>(); pin(fr);
  if (!__all(fr[0] == tag[0] && fr[1] == tag[1])) poll(ctr + CTL_F1, f0, tag);
  STAMP_AT(1, 2);
  pin(o[0]); pin(o[1]); pin(PB0); pin(PB1);
#define GRP(MF, Sc, Pc, i, j) do { MF; SB(); const float e0 = EX2(Sc[i]), e1 = EX2(Sc[i + 1]), e2 = EX2(Sc[i + 2]), e3 = EX2(Sc[i + 3]); \
    Pc[j] = pk_bf16(e0, e1); Pc[j + 1] = pk_bf16(e2, e3); pin(Pc); SB(); } while (0)
#define SLOT(QK, Sc, Pc0, Pc1, Sn, kb, jn, Pp0, Pp1, vb, jp) do { \
    GRP(if constexpr (QK) Sn = MFMA32(K0[kb], q0[jn], cm[jn]), Sc, Pc0, 0, 0); \
    GRP(if constexpr (QK) Sn = MFMA32K8(K1[kb], q1[jn], Sn), Sc, Pc0, 4, 2); \
    GRP(o[jp] = MFMA32(V0[vb], __builtin_bit_cast(bf16x8, Pp0), o[jp]), Sc, Pc1, 8, 0); \
    GRP(o[jp] = MFMA32(V1[vb], __builtin_bit_cast(bf16x8, Pp1), o[jp]), Sc, Pc1, 12, 2); } while (0)
#define LWAIT(kb, vb) asm volatile("s_waitcnt lgkmcnt(0)" : "+v"(K0[kb]), "+v"(K1[kb]), "+v"(V0[vb]), "+v"(V1[vb]))
  unsigned pw[3][8];
  const int gw = blockIdx.x * 8 + w, gwc = min(gw, 1919);
  const char* psrc[3];
#pragma unroll
  for (int r = 0; r < 2; ++r) {
    const int f = 2 * gwc + r, cbk = f / 120, rem = f - cbk * 120, ks = rem / 6, ct = rem - ks * 6;
    psrc[r] = (const char*)((ct < 3 ? Wg : Wu) + (size_t)(32 * ks) * 1536 + 48 * cbk + 16 * (ct < 3 ? ct : ct - 3));
  }
  { const int f = gwc, cbk = f / 240, rem = f - cbk * 240, ks = rem / 5, ct = rem - ks * 5;
    psrc[2] = (const char*)(Wd + (size_t)(32 * ks) * 640 + 80 * cbk + 16 * ct); }
  const unsigned pvo1 = (lane >> 4) * 8 * 3072 + (lane & 15) * 2, pvo2 = (lane >> 4) * 8 * 1280 + (lane & 15) * 2;
  const char* wog = ws + WS_WP + (size_t)cb3 * 102400;
  auto rstore = [&](auto r_, auto yv_) {
    constexpr int r = r_;
    waitv<yv_>();
    sfor<8>([&](auto j) { pin(pw[r][j]); });
    const u32x4 v = {pw[r][0] | (pw[r][1] << 16), pw[r][2] | (pw[r][3] << 16), pw[r][4] | (pw[r][5] << 16), pw[r][6] | (pw[r][7] << 16)};
    if (gw < 1920) {
      if constexpr (r < 2) {
        st16(ws + WS_WP + (size_t)(NFR_WO + 2 * gw + r) * 1024 + lane * 16, v);
        float c = 0.f;
#pragma unroll
        for (int j = 0; j < 4; ++j) c = dot2(v[j], 0x3f803f80u, c);
        c = red4(c);
        if (lane < 16) st4(ws + WS_CSP + (size_t)(2 * gw + r) * 64 + lane * 4, c);
      } else st16(ws + WS_WP + (size_t)(NFR_WO + NFR_GU + gw) * 1024 + lane * 16, v);
    }
  };
  const int qtile = qb * 8 + wq * 2 + kh, cg0 = head * 5 + (head & 1), cgt = (head & 1) ? head * 5 : head * 5 + 4;
  const char* ob = ws + WS_O + (size_t)qtile * 40960;
  const char* obs[3] = {ob + (cg0 >> 3) * 1024 + ((cg0 & 7) >> 1) * 256, ob + ((cg0 + 2) >> 3) * 1024 + (((cg0 + 2) & 7) >> 1) * 256,
                        ob + (cgt >> 3) * 1024 + ((cgt & 7) >> 1) * 256 + (cgt & 1) * 8};
  unsigned vo = (unsigned)((r32 >> 4) * 20480 + (r32 & 15) * 16 + hh * 8); pin(vo);
  u32x4 fill = {0xffffffffu, 0xffffffffu, 0xffffffffu, 0xffffffffu}; pin(fill);
  const u32x2 fill2 = {fill[0], fill[1]};
  u32x4 cdum = {};
  u32x2 fa, fb, fc;
  const unsigned fvc = (unsigned)CTL_FR + (unsigned)((cb4 * 60 + min(lane, 59)) * 8);
  const unsigned fva = (unsigned)(hh ? CTL_F4 : CTL_F2) + (unsigned)(((rb3 * 2 + ((w & 3) >> 1)) * 32 + r32) * 8), fvb = (unsigned)CTL_F3 + (unsigned)(((rb4 * 16 + w * 2) * 8 + (lane & 15)) * 8);
  auto hook = [&](auto it_, auto i_) {
    constexpr int it = it_, i = i_, n = (it - 2) * 8 + i;
    if constexpr (n >= 0 && n < 2) st8s(obs[n], vo, fill2);
    if constexpr (n == 2) { if (!hh) st8s(obs[2], vo, fill2); }
    if constexpr (n >= 3 && n < 6) { const int L = (n - 3) * 64 + lane; st16s(ws + WS_G + (size_t)(rb4 * 16 + w * 2) * 49152 + cb4 * 1536, (unsigned)(L * 16 + (L >= 96 ? 49152 - 1536 : 0)), fill); }
    if constexpr (n >= 6 && n < 9) { if (w < 4) st16s(ws + WS_H + (size_t)(rb3 * 4 + w) * 20480 + cb3 * 2560, (unsigned)(min((n - 6) * 64 + lane, 159) * 16), fill); }
    if constexpr (n == 9) { if (w < 4 && lane < 16) st8s(ws + WS_HST + (size_t)(rb3 * 4 + w) * 1024 + (cb3 >> 1) * 256 + (cb3 & 1) * 8, (unsigned)(lane * 16), fill2); }
    if constexpr (it == 5 && i == 6) {
      waitv<0>();
      setflag(ctl + CTL_FR + gw * 8, tag, lane);
      setflag(ctl + CTL_F2 + (qtile * 32 + head) * 8, tag, lane);
      setflag(ctl + CTL_F4 + ((rb4 * 8 + w) * 32 + cb4) * 8, tag, lane);
      if (w < 4) setflag(ctl + CTL_F3 + ((rb3 * 4 + w) * 8 + cb3) * 8, tag, lane);
    }
    if constexpr (it == 7 && i == 5 && CW2 > 0) code_warm<CW2>(cdum, (blockIdx.x >> 3) * 2 + (w & 1), voff);
    if constexpr (it == 7 && i == 6) { gld8<0>(fa, ctr, fva); gld8<0>(fb, ctr, fvb); gld8<0>(fc, ctr, fvc); }
    if constexpr (it < 2) glds16<true>(kvg + (size_t)(it * 8 + 4 + i) * 4096, voff, kvl + (it * 8 + 4 + i) * 4096);
    else if constexpr (it < 4) gldu16<(i & 1) * 3072>(pw[it - 2][i], psrc[it - 2] + (i & ~1) * 3072, pvo1);
    else if constexpr (it == 4) {
      gldu16<(i & 3) * 1280>(pw[2][i], psrc[2] + (i & ~3) * 1280, pvo2);
      if constexpr (i == 0) rstore(IC<0>{}, IC<9>{});
      if constexpr (i == 7) rstore(IC<1>{}, IC<8>{});
    } else if constexpr (it == 5) {
      if constexpr (i == 7) rstore(IC<2>{}, IC<3>{});
    } else {
      constexpr int j = (it - 6) * 8 + i;
      const char* src = wog + rotf(w + 8 * j, 20 * ROT3(cb3), 100) * 1024;
      if constexpr (j < 7) glds16<true>(src, voff, lds0 + w * 1024 + j * 8192);
      else if constexpr (j == 7) glds16<true>(src, voff, lds0 + w * 1024 + j * 8192 + (w >= 4 ? 20480 : 0));
      else if constexpr (j < 12) glds16<true>(src, voff, lds0 + w * 1024 + j * 8192 + 20480);
      else if constexpr (j == 12) { if (w < 4) glds16<true>(src, voff, lds0 + w * 1024 + j * 8192 + 20480); }
    }
    SB();
  };
  const unsigned ka0b = ka0 + 40960, ka1b = ka1 + 40960, vab = va + 40960;
  sfor<8>([&](auto it_) {
    constexpr int it = it_;
    if constexpr (it == 1) WAITV_BAR(6);
    if constexpr (it == 2) { WAITV_BAR(8); STAMP_AT(1, 3); }
    if constexpr (it == 4) asm volatile("s_waitcnt vmcnt(16)\n\ts_barrier" ::: "memory");
    if constexpr (it == 6) asm volatile("s_barrier" ::: "memory");
    if (((it ^ kh) & 1) == 0) __builtin_amdgcn_s_setprio(0); else __builtin_amdgcn_s_setprio(1);
    constexpr int o0 = (it & 3) * 10240;
    constexpr int on = it == 3 ? 0 : o0 + 10240;
    const unsigned a0 = it < 4 ? ka0 : ka0b, a1 = it < 4 ? ka1 : ka1b, av = it < 4 ? va : vab;
    const unsigned n0 = it < 3 ? ka0 : ka0b, n1 = it < 3 ? ka1 : ka1b, nv = it < 3 ? va : vab;
    rdK<o0 + 2560>(K0[1], K1[1], a0, a1); hook(it_, IC<0>{});                  SLOT(true, SA, PA0, PA1, SB_, 0, 1, PB0, PB1, 1, 1);
    LWAIT(1, 0); rdV<o0 + 2560>(V0[1], V1[1], av, vmsk); hook(it_, IC<1>{});   SLOT(true, SB_, PB0, PB1, SA, 1, 0, PA0, PA1, 0, 0);
    rdK<o0 + 5120>(K0[0], K1[0], a0, a1); hook(it_, IC<2>{});                  SLOT(true, SA, PA0, PA1, SB_, 1, 1, PB0, PB1, 0, 1);
    LWAIT(0, 1); rdV<o0 + 5120>(V0[0], V1[0], av, vmsk); hook(it_, IC<3>{});   SLOT(true, SB_, PB0, PB1, SA, 0, 0, PA0, PA1, 1, 0);
    rdK<o0 + 7680>(K0[1], K1[1], a0, a1); hook(it_, IC<4>{});                  SLOT(true, SA, PA0, PA1, SB_, 0, 1, PB0, PB1, 1, 1);
    LWAIT(1, 0); rdV<o0 + 7680>(V0[1], V1[1], av, vmsk); hook(it_, IC<5>{});   SLOT(true, SB_, PB0, PB1, SA, 1, 0, PA0, PA1, 0, 0);
    if constexpr (it < 7) rdK<on>(K0[0], K1[0], n0, n1);
    hook(it_, IC<6>{});                                                        SLOT(true, SA, PA0, PA1, SB_, 1, 1, PB0, PB1, 0, 1);
    LWAIT(0, 1);
    if constexpr (it < 7) rdV<on>(V0[0], V1[0], nv, vmsk);
    hook(it_, IC<7>{});                                                        SLOT(it < 7, SB_, PB0, PB1, SA, 0, 0, PA0, PA1, 1, 0);
  });
  __builtin_amdgcn_s_setprio(0);
  o[1] = MFMA32(V0[1], __builtin_bit_cast(bf16x8, PB0), o[1]);
  o[1] = MFMA32(V1[1], __builtin_bit_cast(bf16x8, PB1), o[1]);
  STAMP_AT(1, 9);
  bar();
  {
    LAS f32x4* xch = (LAS f32x4*)(uintptr_t)(lds0 + XCH_OFF + w * 3072);
    const f32x16& snd = kh ? o[0] : o[1];
#pragma unroll
    for (int g = 0; g < 3; ++g) xch[g * 64 + lane] = (f32x4){snd[4 * g], snd[4 * g + 1], snd[4 * g + 2], snd[4 * g + 3]};
  }
  bar();
  STAMP_AT(1, 10);
  float of[12];
  {
    const LAS f32x4* xch = (const LAS f32x4*)(uintptr_t)(lds0 + XCH_OFF + (w ^ 4) * 3072);
    const f32x16& own = kh ? o[1] : o[0];
#pragma unroll
    for (int g = 0; g < 3; ++g) { const f32x4 v = xch[g * 64 + lane];
#pragma unroll
      for (int j = 0; j < 4; ++j) of[4 * g + j] = own[4 * g + j] + v[j]; }
  }
  float l = hi32(of[8]);
  bool ok = (l > 8.67e-19f) && (l < 1.15e18f);
#pragma unroll
  for (int i = 0; i < 8; ++i) ok = ok && (fabsf(of[i]) < 1e37f);
  if (!hh) {
#pragma unroll
    for (int i = 8; i < 12; ++i) ok = ok && (fabsf(of[i]) < 1e37f);
  }
  if (__builtin_expect(!__all(ok), 0)) {
    __builtin_amdgcn_fence(__ATOMIC_ACQUIRE, "agent");
    const char* kv = ws + WS_KV + (size_t)head * 163840;
    const bf16x8 fq0 = kh ? q0[1] : q0[0];
    const s16x4 fq1 = kh ? q1[1] : q1[0];
    float m = -INFINITY;
    for (int t = 0; t < 64; ++t) {
      const bf16x8 kf0 = *(const bf16x8*)(kv + t * 2560 + lane * 16); const s16x4 kf1 = *(const s16x4*)(kv + t * 2560 + 1024 + r32 * 8);
      f32x16 s = MFMA32(kf0, fq0, (f32x16){}); s = MFMA32K8(kf1, fq1, s);
#pragma unroll
      for (int i = 0; i < 16; ++i) m = fmaxf(m, s[i]);
    }
    m = max32(m);
    f32x16 oo = {};
    const int vr = r32 < 20 ? r32 : 0;
    for (int t = 0; t < 64; ++t) {
      const bf16x8 kf0 = *(const bf16x8*)(kv + t * 2560 + lane * 16); const s16x4 kf1 = *(const s16x4*)(kv + t * 2560 + 1024 + r32 * 8);
      bf16x8 vf0 = *(const bf16x8*)(kv + t * 2560 + 1280 + (hh * 20 + vr) * 16), vf1 = *(const bf16x8*)(kv + t * 2560 + 1920 + (hh * 20 + vr) * 16);
      if (r32 >= 20) { vf0 = (bf16x8)(short)0x3f80; vf1 = vf0; }
      f32x16 s = MFMA32(kf0, fq0, (f32x16){}); s = MFMA32K8(kf1, fq1, s);
      u32x4 p0, p1;
#pragma unroll
      for (int i = 0; i < 4; ++i) {
        p0[i] = pk_bf16(EX2(s[2 * i] - m), EX2(s[2 * i + 1] - m));
        p1[i] = pk_bf16(EX2(s[8 + 2 * i] - m), EX2(s[8 + 2 * i + 1] - m));
      }
      oo = MFMA32(vf0, __builtin_bit_cast(bf16x8, p0), oo);
      oo = MFMA32(vf1, __builtin_bit_cast(bf16x8, p1), oo);
    }
#pragma unroll
    for (int i = 0; i < 12; ++i) of[i] = oo[i];
    l = hi32(of[8]);
  }
  const float rl = __builtin_amdgcn_rcpf(l);
  waitv<0>(); pin(fa); pin(fb); pin(fc); pin(cdum);
  setflag(ctl + CTL_FD + gw * 8, tag, lane);
#pragma unroll
  for (int g = 0; g < 3; ++g)
    if (g < 2 || !hh) st8s(obs[g], vo, nofill(pk_bf16(of[4 * g] * rl, of[4 * g + 1] * rl), pk_bf16(of[4 * g + 2] * rl, of[4 * g + 3] * rl)));
  STAMP_AT(1, 11);
  if (!__all(fa[0] == tag[0] && fa[1] == tag[1] && fb[0] == tag[0] && fb[1] == tag[1])) poll2(ctr, fva, fvb, tag);
  if (!__all(fc[0] == tag[0] && fc[1] == tag[1])) poll(ctr, fvc, tag);
  STAMP_AT(1, 12);
}

constexpr int x_none(int) { return 0; }
constexpr int x_p4(int i) { return ((i >= 2 && i < 7) || (i >= 10 && i < 18)) ? 1 : 0; }
template <int NSTEP, int NF, int LA, int LL, int CG, int NA, int ASTR, int (*BOFF)(int), int (*XCNT)(int), class PRE, class MM>
DI void gemm_res(const char* asrc, int rot, unsigned lds0, unsigned voff, PRE&& pre, MM&& mm) {
  bf16x8 a[NSTEP][NA];
  bf16x8 bf[LL + 1][NF];
  unsigned rd0 = lds0 + voff, rd1 = rd0 + 65536, rd2 = rd0 + 131072; pin(rd0); pin(rd1); pin(rd2);
  auto issue = [&](auto n_) { constexpr int n = n_; sfor<NA>([&](auto j) { gld16<(n & 3) * 1024>(a[n][j], asrc + rotf(n >> 2, rot, NSTEP / 4) * 4096 + j * ASTR, voff); }); };
  constexpr auto xsum = [](int lo, int hi) { int c = 0; for (int i = lo < 0 ? 0 : lo; i <= hi; ++i) c += XCNT(i); return c; };
  auto rd = [&](auto i_, auto f_) {
    constexpr int i = i_, f = f_, off = BOFF(i * NF + f);
    bf[i % (LL + 1)][f] = lds128((off < 65536 ? rd0 : off < 131072 ? rd1 : rd2) + (off & 65535));
  };
  static_assert(NSTEP % CG == 0 && LA > CG, "groups");
  unsigned m = 0;
  auto fold = [&](auto n_) { constexpr int n = n_;
    sfor<NA>([&](auto j) { pin(a[n][j]); const u32x4 u = __builtin_bit_cast(u32x4, a[n][j]); m = max(max(m, u[0]), u[1]); m = max(max(m, u[2]), u[3]); });
    pin(m);
    if constexpr (n % CG == CG - 1) {
      if (__builtin_expect(__any(m == 0xffffffffu), 0)) {
#pragma unroll 1
        for (int spin = 0; spin < (1 << 17); ++spin) {
          RETRY_COUNT(NSTEP + NF);
          sfor<CG>([&](auto k) { issue(IC<n - CG + 1 + k>{}); });
          waitv<0>(); m = 0;
          sfor<CG>([&](auto k) { sfor<NA>([&](auto j) { pin(a[n - CG + 1 + k][j]); m = max(m, umax4(__builtin_bit_cast(u32x4, a[n - CG + 1 + k][j]))); }); });
          if (!__any(m == 0xffffffffu)) break;
        }
      }
    }
  };
  sfor<LA>([&](auto n) { issue(n); });
  pre(IC<-1>{});
  sfor<LL>([&](auto i) { sfor<NF>([&](auto f) { rd(i, f); }); });
  waitv<(LA - CG) * NA>(); sfor<CG>([&](auto n) { fold(n); });
  sfor<NSTEP>([&](auto i_) {
    constexpr int i = i_;
    pre(i_);
    SB();
    sfor<NF>([&](auto f_) {
      constexpr int f = f_;
      if constexpr (f == NF - 1 && i + CG < NSTEP) {
        waitv<((i + LA < NSTEP ? i + LA : NSTEP - 1) - (i + CG)) * NA + xsum(i + CG - LA + 1, i)>(); fold(IC<i + CG>{}); SB(); }
      mm(f_, bf[i % (LL + 1)][f], a[i]);
      SB();
      if constexpr (i + LL < NSTEP) rd(IC<i + LL>{}, f_);
      if constexpr (f == 0 && i + LA < NSTEP) issue(IC<i + LA>{});
      SB();
    });
  });
}

__global__ __launch_bounds__(512, 2) void k_mega(const u16* __restrict__ x, const u16* __restrict__ Wqkv, const u16* __restrict__ Wo,
                                                 const u16* __restrict__ Wg, const u16* __restrict__ Wu, const u16* __restrict__ Wd,
                                                 char* __restrict__ ws, u16* __restrict__ out) {
  __shared__ __attribute__((aligned(16))) char smem[163840];
  const unsigned lds0 = lds_addr_of(smem);
  const int w = __builtin_amdgcn_readfirstlane(threadIdx.x >> 6);
  const int g8 = blockIdx.x & 7, i32 = blockIdx.x >> 3;
  char* ctl = ws + WS_CTL;
  const char* ctr = ctl + g8 * CTL_REP;
  u32x4 cdum = {};
  if (CW1 > 0) code_warm<CW1>(cdum, i32 * 2 + (w & 1), (threadIdx.x & 63) * 16);
  u32x2 ep;
  gld8<0>(ep, ctl, 0u);
  int f1idx;
  if (i32 < 30) {
    const int i = (g8 & 1) * 30 + i32, rb = (g8 >> 1) * 4 + (i & 3), cb = i >> 2;
    if (cb >= 10) p1_body<true>(x, Wqkv, ws, lds0, w, rb, cb);
    else p1_body<false>(x, Wqkv, ws, lds0, w, rb, cb);
    f1idx = (cb * 4 + (w & 3)) * 32 + rb * 2 + (w >> 2);
  } else {
    p1_repack(Wo, ws, w, blockIdx.x - 240);
    f1idx = 1920 + (blockIdx.x - 240) * 8 + w;
  }
  waitv<0>();
  pin(ep); pin(cdum);
  u32x2 tag;
  {
    const unsigned long long e = (((unsigned long long)__builtin_amdgcn_readfirstlane(ep[1]) << 32) | __builtin_amdgcn_readfirstlane(ep[0])) + 1ull;
    tag = (u32x2){(unsigned)e, (unsigned)(e >> 32)};
  }
  publish(ctl + CTL_F1 + f1idx * 8, tag, fresh_lane());
  bar();
  const int rb3 = g8 * 4 + (i32 >> 3), cb3 = i32 & 7;
  const int rb4 = (g8 >> 2) * 4 + (i32 >> 3), cb4 = (g8 & 3) * 8 + (i32 & 7);
  p2_attn(ws, Wg, Wu, Wd, lds0, w, tag, rb3, cb3, rb4, cb4);
  bar();
  const char* gug = ws + WS_WP + (size_t)NFR_WO * 1024 + (size_t)cb4 * 122880;
  const char* wdg = ws + WS_WP + (size_t)(NFR_WO + NFR_GU) * 1024 + (size_t)cb3 * 245760;
  const int rg4 = 24 * ROT3(cb4 & 7), rg5 = 20 * ROT5(cb3);
  u32x2 hp[5];
  if (w < 4) {
    const int lane = fresh_lane();
    const unsigned voff = lane * 16;
    const int row = rb3 * 64 + w * 16 + (lane & 15), col0 = cb3 * 80 + 4 * (lane >> 4);
    STAMP_AT(2, 0);
    u32x2 xr[5];
#pragma unroll
    for (int ct = 0; ct < 5; ++ct) xr[ct] = *(const u32x2*)(x + (size_t)row * 640 + col0 + 16 * ct);
    { const int hd = lane & 31, cgt = (hd & 1) ? hd * 5 : hd * 5 + 4;
      probe(ws + WS_O + (size_t)(rb3 * 4 + w) * 20480, (unsigned)((cgt >> 3) * 1024 + ((cgt & 7) >> 1) * 256 + (cgt & 1) * 8)); }
    sfor<5>([&](auto ct) { pin(xr[ct]); });
    STAMP_AT(2, 1);
    f32x4 acc[5] = {};
    sfor<5>([&](auto ct) { pin(acc[ct]); });
    gemm_res<20, 5, 20, 2, 4, 1, 0, wo_off, x_none>(ws + WS_O + (size_t)(rb3 * 4 + w) * 20480, ROT3(cb3), lds0, voff,
      [&](auto i) { if constexpr (i == -1) STAMP_AT(2, 2); if constexpr (i == 1) STAMP_AT(2, 3); if constexpr (i == 10) STAMP_AT(2, 4);
        if constexpr (i == P3_G1 || i == 12) bar(); },
      [&](auto ct, const bf16x8& b, bf16x8 (&a)[1]) { mfma16(acc[ct], b, a[0]); });
    acc_fence(); sfor<5>([&](auto ct) { pin(acc[ct]); });
    STAMP_AT(2, 6);
    float s1 = 0.f, s2 = 0.f;
    const char* hb = ws + WS_H + (size_t)(rb3 * 4 + w) * 20480;
    const unsigned vt = t16lane(lane);
#pragma unroll
    for (int ct = 0; ct < 5; ++ct) {
      hp[ct] = nofill(pk_bf16(bf_lo(xr[ct][0]) + acc[ct][0], bf_hi(xr[ct][0]) + acc[ct][1]), pk_bf16(bf_lo(xr[ct][1]) + acc[ct][2], bf_hi(xr[ct][1]) + acc[ct][3]));
      st8s(hb + t16off(cb3 * 80 + ct * 16), vt, hp[ct]);
      s1 = dot2(hp[ct][0], 0x3f803f80u, s1); s1 = dot2(hp[ct][1], 0x3f803f80u, s1);
      s2 = dot2(hp[ct][0], hp[ct][0], s2); s2 = dot2(hp[ct][1], hp[ct][1], s2);
    }
    s1 = red4(s1); s2 = red4(s2);
    if (lane < 16) st8s(ws + WS_HST + (size_t)(rb3 * 4 + w) * 1024 + (cb3 >> 1) * 256 + (cb3 & 1) * 8, (unsigned)(lane * 16), nofill(__float_as_uint(s1), __float_as_uint(s2)));
    bar();
    STAMP_AT(2, 7);
  } else {
    const int lane = fresh_lane(), v = w - 4;
    const unsigned voff = lane * 16;
    float cpart[10];
    const int cc = v * 24 + min(lane & 31, 23);
    sfor<10>([&](auto i) { gld4<i * 384>(cpart[i], ws + WS_CSP + (size_t)cb4 * 7680, (unsigned)((lane >> 5) * 3840 + cc * 4)); });
    poll(ctr + CTL_FD, (unsigned)((cb3 * 240 + v * 60 + min(lane, 59)) * 8), tag);
    float cv = 0.f;
    sfor<10>([&](auto j) { pin(cpart[j]); cv += cpart[j]; });
    cv = add32(cv);
    bar();
    STAMP_AT(2, 2);
    sfor<5>([&](auto j) { glds16<true>(gug + rotf(v * 5 + j, rg4, 120) * 1024, voff, lds0 + gu_off(0) + (v * 5 + j) * 1024); });
    sfor<10>([&](auto j) { glds16<true>(gug + rotf(20 + v * 10 + j, rg4, 120) * 1024, voff, lds0 + gu_off(20) + (v * 10 + j) * 1024); });
    bar();
    sfor<15>([&](auto j) { glds16<true>(gug + rotf(60 + v * 15 + j, rg4, 120) * 1024, voff, lds0 + gu_off(60) + (v * 15 + j) * 1024); });
    STAMP_AT(2, 5);
    waitv<0>();
    bar();
    STAMP_AT(2, 6);
    if (lane < 24) ((LAS float*)(uintptr_t)(lds0 + CSF_OFF))[cc] = cv;
  }
  {
    const int lane = fresh_lane();
    const unsigned voff = lane * 16;
    const int r16 = lane & 15, g4 = lane >> 4;
    STAMP_AT(3, 0);
    f32x4 cs[6]; f32x2 ms[2];
    f32x4 hs[2];
#pragma unroll 1
    for (int spin = 0; spin < (1 << 17); ++spin) {
      gld16<0>(hs[0], ws + WS_HST + (size_t)(rb4 * 16 + w * 2) * 1024, voff);
      gld16<1024>(hs[1], ws + WS_HST + (size_t)(rb4 * 16 + w * 2) * 1024, voff);
      waitv<0>(); pin(hs[0]); pin(hs[1]);
      if (!__any(max(umax4(__builtin_bit_cast(u32x4, hs[0])), umax4(__builtin_bit_cast(u32x4, hs[1]))) == 0xffffffffu)) break;
      __builtin_amdgcn_s_sleep(1);
    }
    STAMP_AT(3, 1);
    f32x4 acc[2][6] = {};
    sfor<6>([&](auto ct) { pin(acc[0][ct]); pin(acc[1][ct]); });
    if (w < 4) __builtin_amdgcn_s_setprio(1);
    gemm_res<20, 6, K4_LA_, 1, K4_CG_, 2, 20480, gu_off, x_p4>(ws + WS_H + (size_t)(rb4 * 16 + w * 2) * 20480, ROT3(cb4 & 7), lds0, voff,
      [&](auto i) { if constexpr (i == -1) STAMP_AT(3, 2); if constexpr (i == 1) STAMP_AT(3, 3); if constexpr (i == 10) STAMP_AT(3, 4);
        if constexpr (i == 9) bar();
        if constexpr (i == -1) sfor<2>([&](auto t) {
          const float s1 = red4(hs[t][0] + hs[t][2]), s2 = red4(hs[t][1] + hs[t][3]), mean = s1 * (1.f / 640.f);
          ms[t] = (f32x2){mean, __builtin_amdgcn_rsqf(fmaxf(s2 * (1.f / 640.f) - mean * mean, 0.f) + LN_EPS)}; pin(ms[t]); });
        if constexpr (i == 18) sfor<6>([&](auto ct) { cs[ct] = *(const LAS f32x4*)(uintptr_t)(lds0 + CSF_OFF + (ct * 16 + 4 * g4) * 4); });
        if constexpr (i >= 2 && i < 7) { const int f = (i - 2) * 8 + w - ((i == 6 && w >= 4) ? 4 : 0); glds16<true>(wdg + rotf(f, rg5, 240) * 1024, voff, lds0 + wd_off(0) + f * 1024); }
        if constexpr (i >= 10 && i < 15) { constexpr int n = i - 10; glds16<true>(wdg + rotf(40 + n * 8 + w, rg5, 240) * 1024, voff, lds0 + wd_off(40) + (n * 8 + w) * 1024); }
        if constexpr (i >= 15 && i < 18) { const int f = (i - 15) * 8 + w - ((i == 17 && w >= 4) ? 4 : 0); glds16<true>(wdg + rotf(140 + f, rg5, 240) * 1024, voff, lds0 + wd_off(140) + f * 1024); } },
      [&](auto ct, const bf16x8& b, bf16x8 (&a)[2]) { mfma16(acc[0][ct], b, a[0]); mfma16(acc[1][ct], b, a[1]); });
    acc_fence(); sfor<6>([&](auto ct) { pin(acc[0][ct]); pin(acc[1][ct]); });
    STAMP_AT(3, 6);
    constexpr float GC0 = -2.f * 0.7978845608028654f * 1.4426950408889634f, GC1 = GC0 * 0.044715f;
    const char* gb = ws + WS_G + (size_t)(rb4 * 16 + w * 2) * 49152;
    const unsigned vt = t16lane(lane);
#pragma unroll
    for (int t = 0; t < 2; ++t) {
      const float nm = -ms[t].x, rstd = ms[t].y;
#pragma unroll
      for (int ct = 0; ct < 3; ++ct) {
        const f32x4 gv = (acc[t][ct] + cs[ct] * nm) * rstd, uv = (acc[t][3 + ct] + cs[3 + ct] * nm) * rstd;
        const f32x4 z = gv * (gv * gv * GC1 + GC0);
        f32x4 e;
#pragma unroll
        for (int j = 0; j < 4; ++j) e[j] = __builtin_amdgcn_exp2f(z[j]);
        e = e + 1.f;
#pragma unroll
        for (int j = 0; j < 4; ++j) e[j] = __builtin_amdgcn_rcpf(e[j]);
        const f32x4 r = gv * uv * e;
        st8s(gb + t * 49152 + t16off(cb4 * 48 + ct * 16), vt, nofill(pk_bf16(r[0], r[1]), pk_bf16(r[2], r[3])));
      }
    }
    __builtin_amdgcn_s_setprio(0);
    STAMP_AT(3, 7);
    waitv<6>();
    STAMP_AT(3, 8);
    bar();
  }
  if (w < 4) {
    const int lane = fresh_lane();
    const unsigned voff = lane * 16;
    const int row = rb3 * 64 + w * 16 + (lane & 15), col0 = cb3 * 80 + 4 * (lane >> 4);
    STAMP_AT(4, 0);
    probe(ws + WS_G + (size_t)(rb3 * 4 + w) * 49152, (unsigned)t16off((lane & 31) * 48 + 32));
    STAMP_AT(4, 1);
    f32x4 acc[5] = {};
    sfor<5>([&](auto ct) { pin(acc[ct]); });
    gemm_res<48, 5, K5_LA_, 2, 4, 1, 0, wd_off, x_none>(ws + WS_G + (size_t)(rb3 * 4 + w) * 49152, ROT5(cb3), lds0, voff,
      [&](auto i) { if constexpr (i == 1) STAMP_AT(4, 3); if constexpr (i == 14) STAMP_AT(4, 4); if constexpr (i == 30) STAMP_AT(4, 5);
        if constexpr (i == -1 || i == 8 || i == 14 || i == 16 || i == 30 || i == 38) bar(); },
      [&](auto ct, const bf16x8& b, bf16x8 (&a)[1]) { mfma16(acc[ct], b, a[0]); });
    acc_fence(); sfor<5>([&](auto ct) { pin(acc[ct]); });
    STAMP_AT(4, 12);
#pragma unroll
    for (int ct = 0; ct < 5; ++ct) {
      const unsigned p0 = pk_bf16(bf_lo(hp[ct][0]) + acc[ct][0], bf_hi(hp[ct][0]) + acc[ct][1]);
      const unsigned p1 = pk_bf16(bf_lo(hp[ct][1]) + acc[ct][2], bf_hi(hp[ct][1]) + acc[ct][3]);
      *(u32x2*)(out + (size_t)row * 640 + col0 + 16 * ct) = (u32x2){p0, p1};
    }
    STAMP_AT(4, 13);
  } else {
    const int lane = fresh_lane(), v = w - 4;
    const unsigned voff = lane * 16;
    glds16<true>(wdg + rotf(36 + v, rg5, 240) * 1024, voff, lds0 + wd_off(36) + v * 1024);
    waitv<0>(); bar();
    sfor<3>([&](auto g) { sfor<5>([&](auto j) { constexpr int f = (g + 4) * 20 + j; glds16<true>(wdg + rotf(f + v * 5, rg5, 240) * 1024, voff, lds0 + wd_off(f) + v * 5120); }); });
    bar();
    sfor<2>([&](auto g) { sfor<5>([&](auto j) { constexpr int f = (g + 8) * 20 + j; glds16<true>(wdg + rotf(f + v * 5, rg5, 240) * 1024, voff, lds0 + wd_off(f) + v * 5120); }); });
    waitv<10>(); bar();
    bar();
    sfor<2>([&](auto g) { sfor<5>([&](auto j) { constexpr int f = (g + 10) * 20 + j; glds16<true>(wdg + rotf(f + v * 5, rg5, 240) * 1024, voff, lds0 + wd_off(f) + v * 5120); }); });
    waitv<10>(); bar();
    waitv<0>(); bar();
    if (blockIdx.x == 0 && w == 4) {
      poll2(ctr + CTL_FR, (unsigned)(lane * 64), (unsigned)(4096 + lane * 64), tag);
      poll2(ctr + CTL_FR, (unsigned)(8192 + lane * 64), (unsigned)(12288 + lane * 64), tag);
      if (lane == 0) st8(ctl, tag);
    }
  }
}

extern "C" void kernel_launch(void* const* d_in, const int* in_sizes, int n_in, void* d_out, int out_size, void* d_ws, size_t ws_size,
                              hipStream_t stream) {
  k_mega<<<256, 512, 0, stream>>>((const u16*)d_in[0], (const u16*)d_in[1], (const u16*)d_in[2], (const u16*)d_in[4], (const u16*)d_in[5],
                                  (const u16*)d_in[6], (char*)d_ws, (u16*)d_out);
}
```

```cpp
#include <hip/hip_runtime.h>
#include <hip/hip_bf16.h>
#include <stdint.h>
#include <utility>

typedef short bf16x8 __attribute__((ext_vector_type(8)));
typedef short s16x4 __attribute__((ext_vector_type(4)));
typedef float f32x16 __attribute__((ext_vector_type(16)));
typedef float f32x4 __attribute__((ext_vector_type(4)));
typedef float f32x2 __attribute__((ext_vector_type(2)));
typedef unsigned u32x4 __attribute__((ext_vector_type(4)));
typedef unsigned u32x2 __attribute__((ext_vector_type(2)));
typedef __bf16 bf2_t __attribute__((ext_vector_type(2)));
typedef unsigned short u16;
#define LAS __attribute__((address_space(3)))
#define DI __device__ __forceinline__

constexpr int S_ = 2048, D_ = 640, H_ = 32, DH = 20, F_ = 1536;
constexpr float LN_EPS = 1e-5f;
constexpr float QSCALE = 0.22360679774997896f * 1.4426950408889634f;

constexpr size_t WS_Q   = 0;
constexpr size_t WS_KV  = WS_Q + 2621440;
constexpr size_t WS_O   = WS_KV + 5242880;
constexpr size_t WS_H   = WS_O + 2621440;
constexpr size_t WS_HST = WS_H + 2621440;
constexpr size_t WS_G   = WS_HST + 131072;
constexpr size_t WS_WP  = WS_G + 6291456;
constexpr size_t WS_CSP = WS_WP + 6717440;
constexpr size_t WS_CTL = WS_CSP + 245760;
constexpr size_t CTL_F1 = 256;
constexpr size_t CTL_F2 = CTL_F1 + 2048 * 8;
constexpr size_t CTL_FR = CTL_F2 + 2048 * 8;
constexpr size_t CTL_F3 = CTL_FR + 2048 * 8;
constexpr size_t CTL_F4 = CTL_F3 + 1024 * 8;
constexpr size_t CTL_FD = CTL_F4 + 2048 * 8;
constexpr size_t CTL_REP = CTL_FD + 2048 * 8 - 256;
constexpr int NFR_WO = 800, NFR_GU = 3840, NFR_WD = 1920, NFR_ALL = NFR_WO + NFR_GU + NFR_WD;

DI unsigned pk_bf16(float lo, float hi) { f32x2 f = {lo, hi}; return __builtin_bit_cast(unsigned, __builtin_convertvector(f, bf2_t)); }
DI float bf_lo(unsigned u) { return __uint_as_float(u << 16); }
DI float bf_hi(unsigned u) { return __uint_as_float(u & 0xffff0000u); }
DI float dot2(unsigned a, unsigned b, float c) { return __builtin_amdgcn_fdot2_f32_bf16(__builtin_bit_cast(bf2_t, a), __builtin_bit_cast(bf2_t, b), c, false); }
DI float add32(float v) { const auto r = __builtin_amdgcn_permlane32_swap(__float_as_uint(v), __float_as_uint(v), false, false); return __uint_as_float(r[0]) + __uint_as_float(r[1]); }
DI float max32(float v) { const auto r = __builtin_amdgcn_permlane32_swap(__float_as_uint(v), __float_as_uint(v), false, false); return fmaxf(__uint_as_float(r[0]), __uint_as_float(r[1])); }
DI float add16(float v) { const auto r = __builtin_amdgcn_permlane16_swap(__float_as_uint(v), __float_as_uint(v), false, false); return __uint_as_float(r[0]) + __uint_as_float(r[1]); }
DI float red4(float v) { return add32(add16(v)); }
DI float hi32(float v) { const auto r = __builtin_amdgcn_permlane32_swap(__float_as_uint(v), __float_as_uint(v), false, false); return __uint_as_float(r[1]); }
#define MFMA32(a, b, c) __builtin_amdgcn_mfma_f32_32x32x16_bf16((a), (b), (c), 0, 0, 0)
#define MFMA32K8(a, b, c) __builtin_amdgcn_mfma_f32_32x32x8bf16_1k((a), (b), (c), 0, 0, 0)
#define MFMA16(a, b, c) __builtin_amdgcn_mfma_f32_16x16x32_bf16((a), (b), (c), 0, 0, 0)

DI void mfma16(f32x4& c, const bf16x8& a, const bf16x8& b) { asm volatile("v_mfma_f32_16x16x32_bf16 %0, %1, %2, %0" : "+v"(c) : "v"(a), "v"(b)); }
DI void acc_fence() { asm volatile("s_nop 7\n\ts_nop 3" ::: "memory"); }
template <bool DEV> DI void glds16(const void* sbase, unsigned voff, unsigned lds_base) {
  if constexpr (DEV) asm volatile("s_mov_b32 m0, %2\n\ts_nop 0\n\tglobal_load_lds_dwordx4 %0, %1 sc1" :: "v"(voff), "s"(sbase), "s"(lds_base) : "memory");
  else asm volatile("s_mov_b32 m0, %2\n\ts_nop 0\n\tglobal_load_lds_dwordx4 %0, %1" :: "v"(voff), "s"(sbase), "s"(lds_base) : "memory");
}
template <int OFF, class T> DI void gld16(T& dst, const void* sbase, unsigned voff) {
  asm volatile("global_load_dwordx4 %0, %1, %2 offset:%3 sc1" : "=v"(dst) : "v"(voff), "s"(sbase), "i"(OFF) : "memory");
}
template <int OFF, class T> DI void gld8(T& dst, const void* sbase, unsigned voff) {
  asm volatile("global_load_dwordx2 %0, %1, %2 offset:%3 sc1" : "=v"(dst) : "v"(voff), "s"(sbase), "i"(OFF) : "memory");
}
template <int OFF> DI void gld4(float& dst, const void* sbase, unsigned voff) {
  asm volatile("global_load_dword %0, %1, %2 offset:%3 sc1" : "=v"(dst) : "v"(voff), "s"(sbase), "i"(OFF) : "memory");
}
template <int OFF> DI void gldu16(unsigned& dst, const void* sbase, unsigned voff) {
  asm volatile("global_load_ushort %0, %1, %2 offset:%3" : "=v"(dst) : "v"(voff), "s"(sbase), "i"(OFF) : "memory");
}
DI void st16(void* p, u32x4 v) { asm volatile("global_store_dwordx4 %0, %1, off sc1\n\ts_nop 1" :: "v"(p), "v"(v) : "memory"); }
DI void st8(void* p, u32x2 v) { asm volatile("global_store_dwordx2 %0, %1, off sc1" :: "v"(p), "v"(v) : "memory"); }
DI void st4(void* p, float v) { asm volatile("global_store_dword %0, %1, off sc1" :: "v"(p), "v"(v) : "memory"); }
DI void st8s(const void* sbase, unsigned voff, u32x2 v) { asm volatile("global_store_dwordx2 %0, %1, %2 sc1" :: "v"(voff), "v"(v), "s"(sbase) : "memory"); }
DI u32x2 nofill(unsigned a, unsigned b) { return (u32x2){min(a, 0xfffffffeu), min(b, 0xfffffffeu)}; }
DI unsigned umax4(u32x4 v) { return max(max(v[0], v[1]), max(v[2], v[3])); }
template <int N> DI void waitv() { asm volatile("s_waitcnt vmcnt(%0)" :: "i"(N) : "memory"); }
DI void bar() { asm volatile("s_waitcnt lgkmcnt(0)\n\ts_barrier" ::: "memory"); }
DI void setflag(char* flag, u32x2 tag, int lane) { if (lane < 8) st8(flag + lane * CTL_REP, tag); }
DI void publish(char* flag, u32x2 tag, int lane) { waitv<0>(); setflag(flag, tag, lane); }
DI void poll(const char* base, unsigned voff, u32x2 tag) {
#pragma unroll 1
  for (int spin = 0; spin < (1 << 17); ++spin) {
    u32x2 v;
    asm volatile("global_load_dwordx2 %0, %1, %2 sc1\n\ts_waitcnt vmcnt(0)" : "=v"(v) : "v"(voff), "s"(base) : "memory");
    if (__all(v[0] == tag[0] && v[1] == tag[1])) break;
    __builtin_amdgcn_s_sleep(1);
  }
}
DI void poll2(const char* base, unsigned voff0, unsigned voff1, u32x2 tag) {
#pragma unroll 1
  for (int spin = 0; spin < (1 << 17); ++spin) {
    u32x2 v, u;
    asm volatile("global_load_dwordx2 %0, %2, %4 sc1\n\tglobal_load_dwordx2 %1, %3, %4 sc1\n\ts_waitcnt vmcnt(0)" : "=&v"(v), "=&v"(u) : "v"(voff0), "v"(voff1), "s"(base) : "memory");
    if (__all(v[0] == tag[0] && v[1] == tag[1] && u[0] == tag[0] && u[1] == tag[1])) break;
    __builtin_amdgcn_s_sleep(1);
  }
}
DI void probe(const char* base, unsigned voff) {
#pragma unroll 1
  for (int spin = 0; spin < (1 << 17); ++spin) {
    unsigned v;
    asm volatile("global_load_dword %0, %1, %2 sc1\n\ts_waitcnt vmcnt(0)" : "=v"(v) : "v"(voff), "s"(base) : "memory");
    if (__all(v != 0xffffffffu)) break;
    __builtin_amdgcn_s_sleep(1);
  }
}
constexpr int t16off(int c0) { return (c0 >> 5) * 1024 + (c0 & 16) * 32; }
DI unsigned t16lane(int lane) { return (unsigned)((lane >> 5) * 256 + (lane & 15) * 16 + ((lane >> 4) & 1) * 8); }
DI void st16s(const void* sbase, unsigned voff, u32x4 v) { asm volatile("global_store_dwordx4 %0, %1, %2 sc1\n\ts_nop 1" :: "v"(voff), "v"(v), "s"(sbase) : "memory"); }
DI int fresh_lane() { int l = __builtin_amdgcn_mbcnt_hi(~0u, __builtin_amdgcn_mbcnt_lo(~0u, 0u)); asm volatile("" : "+v"(l)); return l; }
template <class T> DI void pins(T& v) { asm volatile("" : "+s"(v)); }
template <class T> DI void pin(T& v) { asm volatile("" : "+v"(v)); }
DI bf16x8 lds128(unsigned addr) { return *(const LAS bf16x8*)(uintptr_t)addr; }
DI s16x4 lds64(unsigned addr) { return *(const LAS s16x4*)(uintptr_t)addr; }
DI s16x4 ldstr(unsigned addr) { return __builtin_amdgcn_ds_read_tr16_b64_v4i16((LAS s16x4*)(uintptr_t)addr); }
DI unsigned lds_addr_of(const void* p) { return (unsigned)(uintptr_t)(const LAS char*)p; }
#define STAMP_AT(kid, idx) ((void)0)
#define RETRY_COUNT(k) ((void)0)
#define WAITV_BAR(N) asm volatile("s_waitcnt vmcnt(" #N ") lgkmcnt(0)\n\ts_barrier" ::: "memory")
#define SB() __builtin_amdgcn_sched_barrier(0)
template <int N> using IC = std::integral_constant<int, N>;
template <class F, int... Is> DI void sfor_(F&& f, std::integer_sequence<int, Is...>) { (f(IC<Is>{}), ...); }
template <int N, class F> DI void sfor(F&& f) { sfor_(f, std::make_integer_sequence<int, N>{}); }
template <int N> DI void waitv_bar() { asm volatile("s_waitcnt vmcnt(%0) lgkmcnt(0)\n\ts_barrier" :: "i"(N) : "memory"); }

#define K1_LA_ 10
#define P3_G1 -1
#define K4_LA_ 6
#define K4_CG_ 2
#define K5_LA_ 16
constexpr int K1_STAGE = 32768, K1_NST = 4, K1_LA = K1_LA_;
static_assert(K1_LA <= 10, "slot reuse");
template <bool ISV>
DI void p1_body(const u16* __restrict__ x, const u16* __restrict__ W, char* __restrict__ ws, const unsigned lds0, const int w, int rb, int cb) {
  const int lane = fresh_lane();
  const int wm = w >> 2, wn = w & 3;
  const int r32 = lane & 31, hh = lane >> 5;
  const unsigned a_voff0 = (unsigned)((lane >> 3) * 1280 + (((lane & 7) ^ ((lane >> 4) & 7)) << 4));
  const unsigned a_voff1 = (unsigned)((lane >> 3) * 1280 + (((lane & 7) ^ ((4 + (lane >> 4)) & 7)) << 4)) + 8 * 1280;
  const unsigned w_voff0 = (unsigned)((lane >> 3) * 3840 + (((lane & 7) ^ (((lane >> 4) & 1) << 2)) << 4));
  const unsigned w_voff1 = w_voff0 + 8 * 3840;
  const char* xs = (const char*)x + (size_t)(rb * 128 + w * 16) * 1280;
  const char* wsrc = (const char*)W + (size_t)((w & 3) * 16) * 3840 + (size_t)(cb * 128 + (w >> 2) * 64) * 2;
  const unsigned dst0 = lds0 + w * 2048;
  auto dma = [&](auto n_) {
    constexpr int n = n_, s = n >> 2, j = n & 3; constexpr unsigned so = (s % K1_NST) * K1_STAGE;
    if constexpr (j == 0) glds16<false>(xs + s * 128, a_voff0, dst0 + so);
    if constexpr (j == 1) glds16<false>(wsrc + (size_t)s * 64 * 3840, w_voff0, dst0 + so + 16384);
    if constexpr (j == 2) glds16<false>(xs + s * 128, a_voff1, dst0 + so + 1024);
    if constexpr (j == 3) glds16<false>(wsrc + (size_t)s * 64 * 3840, w_voff1, dst0 + so + 16384 + 1024);
  };
  STAMP_AT(0, 0);
  sfor<K1_LA>([&](auto n) { dma(n); });
  const int ob = (wn >> 1) ^ wm;
  const int fsel = wn & 1;
  unsigned xa[2][4], xb[2][4], wa[2][2];
  {
    const int row = wm * 64 + fsel * 32 + r32;
    const int blk = (lane >> 4) & 1, q = (lane & 15) >> 2, p = lane & 3;
    const int c = (wn & 1) * 4 + blk * 2 + (p >> 1);
    const unsigned base = lds0 + (unsigned)(16384 + (wn >> 1) * 8192 + (8 * hh + q) * 128 + ((c ^ (((q >> 1) & 1) << 2)) << 4) + (p & 1) * 8);
#pragma unroll
    for (int hs = 0; hs < 2; ++hs) {
#pragma unroll
      for (int i = 0; i < 4; ++i) {
        xa[hs][i] = lds0 + hs * 65536 + (unsigned)(row * 128 + ((((i ^ ob) * 2 + hh) ^ ((row >> 1) & 7)) << 4));
        xb[hs][i] = xa[hs][i] + (fsel ? -4096 : 4096);
      }
      wa[hs][0] = base + hs * 65536 + ob * 2048; wa[hs][1] = base + hs * 65536 + (1 ^ ob) * 2048;
    }
  }
  f32x16 acc0 = {}, acc1 = {};
  float sxx = 0.f;
  f32x4 sx = {}, sw = {};
  bf16x8 sel = (bf16x8)(short)(((lane & 15) == ((lane >> 4) & 1)) ? 0x3f80 : 0); pin(sel);
  bf16x8 fx0[4], fx1[4], fw[4];
  auto rd = [&](auto i_) {
    constexpr int i = i_, s = i >> 2, p = i & 3, sl = s % K1_NST, hs = sl >> 1; constexpr unsigned so = (sl & 1) * K1_STAGE;
    fx0[p] = lds128(xa[hs][p] + so);
    const s16x4 wl = ldstr(wa[hs][p & 1] + so + (p >> 1) * 4096);
    const s16x4 wh = ldstr(wa[hs][p & 1] + so + (p >> 1) * 4096 + 512);
    fw[p] = __builtin_shufflevector(wl, wh, 0, 1, 2, 3, 4, 5, 6, 7);
    fx1[p] = lds128(xb[hs][p] + so);
  };
  const bool isq = cb < 5;
  const int pitch = isq ? 1280 : 2560;
  unsigned off[4];
  if (ISV) {
    const int n = cb * 128 + wn * 32 + r32 - 1280, head = n / 20, d0 = n - head * 20;
    const int d = (head & 1) ? (d0 < 4 ? d0 + 16 : d0 - 4) : d0;
    off[0] = (unsigned)(head * 163840 + 1280 + (hh * 20 + d) * 16); pin(off[0]);
  } else {
#pragma unroll
    for (int g = 0; g < 4; ++g) {
      const int n = cb * 128 + wn * 32 + 8 * g + 4 * hh - (isq ? 0 : 640), head = n / 20, d0 = n - head * 20;
      const int d = (head & 1) ? (d0 < 4 ? d0 + 16 : d0 - 4) : d0;
      off[g] = (unsigned)(head * 64 * pitch + (d < 16 ? (((d >> 3) * 32 + r32) * 16 + (d & 4) * 2) : (1024 + r32 * 8))); pin(off[g]);
    }
  }
  waitv_bar<K1_LA - 4>();
  STAMP_AT(0, 1);
  rd(IC<0>{}); rd(IC<1>{});
  sfor<40>([&](auto i_) {
    constexpr int i = i_, p = i & 3;
    if constexpr ((i + 2) % 4 == 0 && i + 2 < 40) { constexpr int last = (i - 1 + K1_LA < 39 ? i - 1 + K1_LA : 39); waitv<(last - (i + 5) > 0 ? last - (i + 5) : 0)>(); asm volatile("s_barrier" ::: "memory"); }
    if constexpr (i + K1_LA < 40) dma(IC<i + K1_LA>{});
    if constexpr (i + 2 < 40) rd(IC<i + 2>{});
    SB();
    if (ISV) { acc0 = MFMA32(fx0[p], fw[p], acc0); acc1 = MFMA32(fx1[p], fw[p], acc1); }
    else     { acc0 = MFMA32(fw[p], fx0[p], acc0); acc1 = MFMA32(fw[p], fx1[p], acc1); }
    if constexpr ((p & 1) == 0) {
      sx = MFMA16(fx0[p], sel, sx);
      const u32x4 u = __builtin_bit_cast(u32x4, fx0[p]);
#pragma unroll
      for (int j = 0; j < 4; ++j) sxx = dot2(u[j], u[j], sxx);
      pin(sxx);
    } else {
      sw = MFMA16(fw[p], sel, sw); }
    SB();
  });
  STAMP_AT(0, 11);
  sxx = add32(sxx);
  LAS float* psum = (LAS float*)(uintptr_t)(lds0 + 65536);
  LAS float* psq = psum + 256;
  LAS float* csp = psum + 512;
  if (hh == 0) psq[(wn >> 1) * 128 + wm * 64 + fsel * 32 + r32] = sxx;
  if ((lane & 15) < 2) { const int o = (lane & 1) * 16 + (lane >> 4) * 4;
    *(LAS f32x4*)&psum[(wn >> 1) * 128 + wm * 64 + fsel * 32 + o] = sx; *(LAS f32x4*)&csp[wm * 128 + wn * 32 + o] = sw; }
  bar();
  STAMP_AT(0, 12);
  auto stat = [&](int rl) {
    const float mean = (psum[rl] + psum[128 + rl]) * (1.f / 640.f);
    const float var = fmaxf((psq[rl] + psq[128 + rl]) * (1.f / 640.f) - mean * mean, 0.f);
    return (f32x2){mean, __builtin_amdgcn_rsqf(var + LN_EPS)}; };
  if (ISV) {
    LAS f32x2* st = (LAS f32x2*)(uintptr_t)(lds0 + 65536 + 4096 + w * 512);
    st[lane] = stat(wm * 64 + lane);
    const int nl = wn * 32 + r32;
    const float c = csp[nl] + csp[128 + nl];
#pragma unroll
    for (int t = 0; t < 2; ++t) {
      const f32x16& a = t ? acc1 : acc0;
      const int rl = (t ^ fsel) * 32;
      unsigned pk[8];
#pragma unroll
      for (int g = 0; g < 4; ++g) {
        const f32x4 s01 = *(const LAS f32x4*)&st[rl + 8 * g + 4 * hh], s23 = *(const LAS f32x4*)&st[rl + 8 * g + 4 * hh + 2];
        pk[2 * g]     = pk_bf16(s01[1] * (a[4 * g] - s01[0] * c), s01[3] * (a[4 * g + 1] - s01[2] * c));
        pk[2 * g + 1] = pk_bf16(s23[1] * (a[4 * g + 2] - s23[0] * c), s23[3] * (a[4 * g + 3] - s23[2] * c));
      }
      const char* dst = ws + WS_KV + (size_t)(rb * 4 + wm * 2 + (rl >> 5)) * 2560;
      st16s(dst, off[0], (u32x4){pk[0], pk[1], pk[2], pk[3]});
      st16s(dst + 640, off[0], (u32x4){pk[4], pk[5], pk[6], pk[7]});
    }
  } else {
    const float sc = isq ? QSCALE : 1.f;
    const char* base = ws + (isq ? WS_Q : WS_KV);
    f32x4 c[4];
#pragma unroll
    for (int g = 0; g < 4; ++g) { const int nl = wn * 32 + 8 * g + 4 * hh; c[g] = *(const LAS f32x4*)&csp[nl] + *(const LAS f32x4*)&csp[128 + nl]; }
#pragma unroll
    for (int t = 0; t < 2; ++t) {
      const f32x16& a = t ? acc1 : acc0;
      const int rl = wm * 64 + (t ^ fsel) * 32;
      const f32x2 ms = stat(rl + r32);
      const float rs = ms.y * sc, mr = ms.x;
      const char* tb = base + (size_t)(rb * 4 + (rl >> 5)) * pitch;
#pragma unroll
      for (int g = 0; g < 4; ++g) {
        const unsigned p0 = pk_bf16(rs * (a[4 * g] - mr * c[g][0]), rs * (a[4 * g + 1] - mr * c[g][1]));
        const unsigned p1 = pk_bf16(rs * (a[4 * g + 2] - mr * c[g][2]), rs * (a[4 * g + 3] - mr * c[g][3]));
        st8s(tb, off[g], (u32x2){p0, p1});
      }
    }
  }
  STAMP_AT(0, 13);
}

DI void p1_repack(const u16* __restrict__ Wo, char* __restrict__ ws, const int w, int idx) {
  const int lane = fresh_lane();
  const unsigned pvo = (lane >> 4) * 8 * 1280 + (lane & 15) * 2;
  unsigned pw[7][8];
  sfor<7>([&](auto r) {
    const int f = idx * 50 + min(w + 8 * r, 49), cbk = f / 100, rem = f - cbk * 100, ks = rem / 5, ct = rem - ks * 5;
    const char* src = (const char*)(Wo + (size_t)(32 * ks) * 640 + 80 * cbk + 16 * ct);
    sfor<8>([&](auto i) { gldu16<(i & 3) * 1280>(pw[r][i], src + (i & ~3) * 1280, pvo); });
  });
  sfor<7>([&](auto r) {
    waitv<(6 - r) * 8>();
    sfor<8>([&](auto j) { pin(pw[r][j]); });
    const u32x4 v = {pw[r][0] | (pw[r][1] << 16), pw[r][2] | (pw[r][3] << 16), pw[r][4] | (pw[r][5] << 16), pw[r][6] | (pw[r][7] << 16)};
    pw[r][0] = v[0]; pw[r][1] = v[1]; pw[r][2] = v[2]; pw[r][3] = v[3];
  });
  sfor<7>([&](auto r) {
    if (w + 8 * r < 50) st16(ws + WS_WP + (size_t)(idx * 50 + w + 8 * r) * 1024 + lane * 16, (u32x4){pw[r][0], pw[r][1], pw[r][2], pw[r][3]});
  });
}

template <int OFF> DI void rdK(bf16x8& k0, s16x4& k1, unsigned a0, unsigned a1) {
  asm volatile("ds_read_b128 %0, %2 offset:%4\n\tds_read_b64 %1, %3 offset:%4" : "=&v"(k0), "=&v"(k1) : "v"(a0), "v"(a1), "i"(OFF));
}
template <int OFF> DI void rdV(bf16x8& v0, bf16x8& v1, unsigned a, unsigned long long msk) {
  asm volatile("s_mov_b64 exec, %5\n\tds_read_b128 %0, %2 offset:%3\n\tds_read_b128 %1, %2 offset:%4\n\ts_mov_b64 exec, -1"
               : "+v"(v0), "+v"(v1) : "v"(a), "i"(OFF), "i"(OFF + 640), "s"(msk));
}
#define EX2(v) __builtin_amdgcn_exp2f(v)
constexpr int wo_off(int p) { return (p < 60 ? p : p + 20) * 1024; }
constexpr int gu_off(int f) { return (f < 20 ? f + 60 : f < 60 ? f + 100 : f - 60) * 1024; }
constexpr int wd_off(int f) { return ((f / 20 + 4) % 8) * 20480 + (f % 20) * 1024; }
constexpr int CSF_OFF = 116 * 1024, XCH_OFF = 120 * 1024;

DI void p2_attn(char* __restrict__ ws, const u16* __restrict__ Wg, const u16* __restrict__ Wu, const u16* __restrict__ Wd,
                const unsigned lds0, const int w, const u32x2 tag, const int rb3, const int cb3, const int rb4, const int cb4) {
  const int lane = fresh_lane();
  const int kh = w >> 2, wq = w & 3;
  const int head = (blockIdx.x & 7) * 4 + ((blockIdx.x >> 3) & 3), qb = blockIdx.x >> 5;
  const int r32 = lane & 31, hh = lane >> 5;
  const char* kvg = ws + WS_KV + (size_t)head * 163840 + (size_t)kh * 81920 + wq * 1024;
  const unsigned voff = lane * 16;
  const unsigned kvl = lds0 + kh * 81920 + wq * 1024;
  char* ctl = ws + WS_CTL;
  const char* ctr = ctl + (blockIdx.x & 7) * CTL_REP;
  unsigned pw[3][8];
  const int gw = blockIdx.x * 8 + w, gwc = min(gw, 1919);
  const char* psrc[3];
#pragma unroll
  for (int r = 0; r < 2; ++r) {
    const int f = 2 * gwc + r, cbk = f / 120, rem = f - cbk * 120, ks = rem / 6, ct = rem - ks * 6;
    psrc[r] = (const char*)((ct < 3 ? Wg : Wu) + (size_t)(32 * ks) * 1536 + 48 * cbk + 16 * (ct < 3 ? ct : ct - 3));
  }
  { const int f = gwc, cbk = f / 240, rem = f - cbk * 240, ks = rem / 5, ct = rem - ks * 5;
    psrc[2] = (const char*)(Wd + (size_t)(32 * ks) * 640 + 80 * cbk + 16 * ct); }
  unsigned pvo1 = (lane >> 4) * 8 * 3072 + (lane & 15) * 2, pvo2 = (lane >> 4) * 8 * 1280 + (lane & 15) * 2; pin(pvo1); pin(pvo2);
  const char* wog = ws + WS_WP + (size_t)cb3 * 102400;
  const int qtile = qb * 8 + wq * 2 + kh, cg0 = head * 5 + (head & 1), cgt = (head & 1) ? head * 5 : head * 5 + 4;
  const char* ob = ws + WS_O + (size_t)qtile * 40960;
  const char* obs[3] = {ob + (cg0 >> 3) * 1024 + ((cg0 & 7) >> 1) * 256, ob + ((cg0 + 2) >> 3) * 1024 + (((cg0 + 2) & 7) >> 1) * 256,
                        ob + (cgt >> 3) * 1024 + ((cgt & 7) >> 1) * 256 + (cgt & 1) * 8};
  unsigned vo = (unsigned)((r32 >> 4) * 20480 + (r32 & 15) * 16 + hh * 8); pin(vo);
  u32x4 fill = {0xffffffffu, 0xffffffffu, 0xffffffffu, 0xffffffffu}; pin(fill);
  const u32x2 fill2 = {fill[0], fill[1]};
  u32x2 fa, fb, fc;
  const unsigned fvc = (unsigned)CTL_FR + (unsigned)((cb4 * 60 + min(lane, 59)) * 8);
  const unsigned fva = (unsigned)(hh ? CTL_F4 : CTL_F2) + (unsigned)(((rb3 * 2 + ((w & 3) >> 1)) * 32 + r32) * 8), fvb = (unsigned)CTL_F3 + (unsigned)(((rb4 * 16 + w * 2) * 8 + (lane & 15)) * 8);
  const char* gcl = ws + WS_G + (size_t)(rb4 * 16 + w * 2) * 49152 + cb4 * 1536;
  const char* hcl = ws + WS_H + (size_t)(rb3 * 4 + w) * 20480 + cb3 * 2560;
  const char* rdst = ws + WS_WP + (size_t)(NFR_WO + 2 * gw) * 1024;
  pins(psrc[0]); pins(psrc[1]); pins(psrc[2]); pins(obs[0]); pins(obs[1]); pins(obs[2]); pins(wog); pins(gcl); pins(hcl); pins(rdst);
  STAMP_AT(1, 0);
  unsigned f0;
  {
    const int cfl = (20 * head) >> 5, cfh = (20 * head + 19) >> 5;
    f0 = (unsigned)(((((lane & 32) ? 40 : 20) + ((lane & 16) ? cfh : cfl)) * 32 + kh * 16 + (lane & 15)) * 8);
    const unsigned f1 = (unsigned)((lane < 32 ? (((lane & 1) ? cfh : cfl) * 32 + qb * 4 + wq) : 1920 + cb3 * 16 + (lane & 15)) * 8);
    poll2(ctr + CTL_F1, f0 & ~(12u * 8u), f1, tag);
  }
  STAMP_AT(1, 4);
#pragma unroll
  for (int i = 0; i < 4; ++i) glds16<true>(kvg + i * 4096, voff, kvl + i * 4096);
  bf16x8 q0[2]; s16x4 q1[2];
  {
    const char* qg = ws + WS_Q + ((size_t)head * 64 + qb * 8 + wq * 2) * 1280;
    gld16<0>(q0[0], qg, voff); gld16<1280>(q0[1], qg, voff); gld8<1024>(q1[0], qg, r32 * 8); gld8<2304>(q1[1], qg, r32 * 8);
  }
  const unsigned ka0 = lds0 + kh * 81920 + lane * 16, ka1 = lds0 + kh * 81920 + 1024 + r32 * 8, va = lds0 + kh * 81920 + 1280 + (hh * 20 + (r32 < 20 ? r32 : 0)) * 16;
  bf16x8 K0[2], V0[2], V1[2]; s16x4 K1[2];
  V0[0] = V0[1] = V1[0] = V1[1] = (bf16x8)(short)(r32 == 20 ? 0x3f80 : 0);
  pin(V0[0]); pin(V0[1]); pin(V1[0]); pin(V1[1]);
  WAITV_BAR(0);
  u32x2 fr; gld8<0>(fr, ctr + CTL_F1, f0);
  pin(q0[0]); pin(q0[1]); pin(q1[0]); pin(q1[1]);
  if (hh) { q1[0] = (s16x4){0, 0, 0, 0}; q1[1] = (s16x4){0, 0, 0, 0}; }
  STAMP_AT(1, 1);
  rdK<0>(K0[0], K1[0], ka0, ka1);
  const unsigned long long vmsk = 0x000fffff000fffffull;
  rdV<0>(V0[0], V1[0], va, vmsk);
  f32x16 cm[2];
  {
    const bf16x8 kf0 = lds128(lds0 + lane * 16); const s16x4 kf1 = lds64(lds0 + 1024 + r32 * 8);
#pragma unroll
    for (int t = 0; t < 2; ++t) {
      f32x16 s = MFMA32(kf0, q0[t], (f32x16){}); s = MFMA32K8(kf1, q1[t], s);
      float m = s[0];
#pragma unroll
      for (int i = 1; i < 16; ++i) m = fmaxf(m, s[i]);
      m = max32(m);
      cm[t] = (f32x16)(-m);
      pin(cm[t]);
    }
  }
  asm volatile("s_waitcnt lgkmcnt(0)" : "+v"(K0[0]), "+v"(K1[0]), "+v"(V0[0]), "+v"(V1[0]));
  f32x16 o[2] = {};
  f32x16 SA, SB_;
  u32x4 PA0, PA1, PB0 = {}, PB1 = {};
  SA = MFMA32(K0[0], q0[0], cm[0]); SA = MFMA32K8(K1[0], q1[0], SA);
  waitv<0>(); pin(fr);
  if (!__all(fr[0] == tag[0] && fr[1] == tag[1])) poll(ctr + CTL_F1, f0, tag);
  STAMP_AT(1, 2);
  pin(o[0]); pin(o[1]); pin(PB0); pin(PB1);
#define GRP(MF, Sc, Pc, i, j) do { MF; SB(); const float e0 = EX2(Sc[i]), e1 = EX2(Sc[i + 1]), e2 = EX2(Sc[i + 2]), e3 = EX2(Sc[i + 3]); \
    Pc[j] = pk_bf16(e0, e1); Pc[j + 1] = pk_bf16(e2, e3); pin(Pc); SB(); } while (0)
#define SLOT(QK, Sc, Pc0, Pc1, Sn, kb, jn, Pp0, Pp1, vb, jp) do { \
    GRP(if constexpr (QK) Sn = MFMA32(K0[kb], q0[jn], cm[jn]), Sc, Pc0, 0, 0); \
    GRP(if constexpr (QK) Sn = MFMA32K8(K1[kb], q1[jn], Sn), Sc, Pc0, 4, 2); \
    GRP(o[jp] = MFMA32(V0[vb], __builtin_bit_cast(bf16x8, Pp0), o[jp]), Sc, Pc1, 8, 0); \
    GRP(o[jp] = MFMA32(V1[vb], __builtin_bit_cast(bf16x8, Pp1), o[jp]), Sc, Pc1, 12, 2); } while (0)
#define LWAIT(kb, vb) asm volatile("s_waitcnt lgkmcnt(0)" : "+v"(K0[kb]), "+v"(K1[kb]), "+v"(V0[vb]), "+v"(V1[vb]))
  auto rstore = [&](auto r_, auto yv_) {
    constexpr int r = r_;
    waitv<yv_>();
    sfor<8>([&](auto j) { pin(pw[r][j]); });
    const u32x4 v = {pw[r][0] | (pw[r][1] << 16), pw[r][2] | (pw[r][3] << 16), pw[r][4] | (pw[r][5] << 16), pw[r][6] | (pw[r][7] << 16)};
    if (gw < 1920) {
      if constexpr (r < 2) {
        st16s(rdst + r * 1024, voff, v);
        float c = 0.f;
#pragma unroll
        for (int j = 0; j < 4; ++j) c = dot2(v[j], 0x3f803f80u, c);
        c = red4(c);
        if (lane < 16) st4(ws + WS_CSP + (size_t)(2 * gw + r) * 64 + lane * 4, c);
      } else st16s(ws + WS_WP + (size_t)(NFR_WO + NFR_GU + gw) * 1024, voff, v);
    }
  };
  auto hook = [&](auto it_, auto i_) {
    constexpr int it = it_, i = i_, n = (it - 2) * 8 + i;
    if constexpr (n >= 0 && n < 2) st8s(obs[n], vo, fill2);
    if constexpr (n == 2) { if (!hh) st8s(obs[2], vo, fill2); }
    if constexpr (n >= 3 && n < 6) { const int L = (n - 3) * 64 + lane; st16s(gcl, (unsigned)(L * 16 + (L >= 96 ? 49152 - 1536 : 0)), fill); }
    if constexpr (n >= 6 && n < 9) { if (w < 4) st16s(hcl, (unsigned)(min((n - 6) * 64 + lane, 159) * 16), fill); }
    if constexpr (n == 9) { if (w < 4 && lane < 16) st8s(ws + WS_HST + (size_t)(rb3 * 4 + w) * 1024 + (cb3 >> 1) * 256 + (cb3 & 1) * 8, (unsigned)(lane * 16), fill2); }
    if constexpr (it == 5 && i == 6) {
      waitv<0>();
      setflag(ctl + CTL_FR + gw * 8, tag, lane);
      setflag(ctl + CTL_F2 + (qtile * 32 + head) * 8, tag, lane);
      setflag(ctl + CTL_F4 + ((rb4 * 8 + w) * 32 + cb4) * 8, tag, lane);
      if (w < 4) setflag(ctl + CTL_F3 + ((rb3 * 4 + w) * 8 + cb3) * 8, tag, lane);
    }
    if constexpr (it == 7 && i == 6) { gld8<0>(fa, ctr, fva); gld8<0>(fb, ctr, fvb); gld8<0>(fc, ctr, fvc); }
    if constexpr (it < 2) glds16<true>(kvg + (size_t)(it * 8 + 4 + i) * 4096, voff, kvl + (it * 8 + 4 + i) * 4096);
    else if constexpr (it < 4) gldu16<(i & 1) * 3072>(pw[it - 2][i], psrc[it - 2] + (i & ~1) * 3072, pvo1);
    else if constexpr (it == 4) {
      gldu16<(i & 3) * 1280>(pw[2][i], psrc[2] + (i & ~3) * 1280, pvo2);
      if constexpr (i == 0) rstore(IC<0>{}, IC<9>{});
      if constexpr (i == 7) rstore(IC<1>{}, IC<8>{});
    } else if constexpr (it == 5) {
      if constexpr (i == 7) rstore(IC<2>{}, IC<3>{});
    } else {
      constexpr int j = (it - 6) * 8 + i;
      const char* src = wog + (w + 8 * j) * 1024;
      if constexpr (j < 7) glds16<true>(src, voff, lds0 + w * 1024 + j * 8192);
      else if constexpr (j == 7) glds16<true>(src, voff, lds0 + w * 1024 + j * 8192 + (w >= 4 ? 20480 : 0));
      else if constexpr (j < 12) glds16<true>(src, voff, lds0 + w * 1024 + j * 8192 + 20480);
      else if constexpr (j == 12) { if (w < 4) glds16<true>(src, voff, lds0 + w * 1024 + j * 8192 + 20480); }
    }
    SB();
  };
  const unsigned ka0b = ka0 + 40960, ka1b = ka1 + 40960, vab = va + 40960;
  sfor<8>([&](auto it_) {
    constexpr int it = it_;
    if constexpr (it == 1) WAITV_BAR(6);
    if constexpr (it == 2) { WAITV_BAR(8); STAMP_AT(1, 3); }
    if constexpr (it == 4) asm volatile("s_waitcnt vmcnt(16)\n\ts_barrier" ::: "memory");
    if constexpr (it == 6) asm volatile("s_barrier" ::: "memory");
    if (((it ^ kh) & 1) == 0) __builtin_amdgcn_s_setprio(0); else __builtin_amdgcn_s_setprio(1);
    constexpr int o0 = (it & 3) * 10240;
    constexpr int on = it == 3 ? 0 : o0 + 10240;
    const unsigned a0 = it < 4 ? ka0 : ka0b, a1 = it < 4 ? ka1 : ka1b, av = it < 4 ? va : vab;
    const unsigned n0 = it < 3 ? ka0 : ka0b, n1 = it < 3 ? ka1 : ka1b, nv = it < 3 ? va : vab;
    rdK<o0 + 2560>(K0[1], K1[1], a0, a1); hook(it_, IC<0>{});                  SLOT(true, SA, PA0, PA1, SB_, 0, 1, PB0, PB1, 1, 1);
    LWAIT(1, 0); rdV<o0 + 2560>(V0[1], V1[1], av, vmsk); hook(it_, IC<1>{});   SLOT(true, SB_, PB0, PB1, SA, 1, 0, PA0, PA1, 0, 0);
    rdK<o0 + 5120>(K0[0], K1[0], a0, a1); hook(it_, IC<2>{});                  SLOT(true, SA, PA0, PA1, SB_, 1, 1, PB0, PB1, 0, 1);
    LWAIT(0, 1); rdV<o0 + 5120>(V0[0], V1[0], av, vmsk); hook(it_, IC<3>{});   SLOT(true, SB_, PB0, PB1, SA, 0, 0, PA0, PA1, 1, 0);
    rdK<o0 + 7680>(K0[1], K1[1], a0, a1); hook(it_, IC<4>{});                  SLOT(true, SA, PA0, PA1, SB_, 0, 1, PB0, PB1, 1, 1);
    LWAIT(1, 0); rdV<o0 + 7680>(V0[1], V1[1], av, vmsk); hook(it_, IC<5>{});   SLOT(true, SB_, PB0, PB1, SA, 1, 0, PA0, PA1, 0, 0);
    if constexpr (it < 7) rdK<on>(K0[0], K1[0], n0, n1);
    hook(it_, IC<6>{});                                                        SLOT(true, SA, PA0, PA1, SB_, 1, 1, PB0, PB1, 0, 1);
    LWAIT(0, 1);
    if constexpr (it < 7) rdV<on>(V0[0], V1[0], nv, vmsk);
    hook(it_, IC<7>{});                                                        SLOT(it < 7, SB_, PB0, PB1, SA, 0, 0, PA0, PA1, 1, 0);
  });
  __builtin_amdgcn_s_setprio(0);
  o[1] = MFMA32(V0[1], __builtin_bit_cast(bf16x8, PB0), o[1]);
  o[1] = MFMA32(V1[1], __builtin_bit_cast(bf16x8, PB1), o[1]);
  STAMP_AT(1, 9);
  bar();
  {
    LAS f32x4* xch = (LAS f32x4*)(uintptr_t)(lds0 + XCH_OFF + w * 3072);
    const f32x16& snd = kh ? o[0] : o[1];
#pragma unroll
    for (int g = 0; g < 3; ++g) xch[g * 64 + lane] = (f32x4){snd[4 * g], snd[4 * g + 1], snd[4 * g + 2], snd[4 * g + 3]};
  }
  bar();
  STAMP_AT(1, 10);
  float of[12];
  {
    const LAS f32x4* xch = (const LAS f32x4*)(uintptr_t)(lds0 + XCH_OFF + (w ^ 4) * 3072);
    const f32x16& own = kh ? o[1] : o[0];
#pragma unroll
    for (int g = 0; g < 3; ++g) { const f32x4 v = xch[g * 64 + lane];
#pragma unroll
      for (int j = 0; j < 4; ++j) of[4 * g + j] = own[4 * g + j] + v[j]; }
  }
  float l = hi32(of[8]);
  bool ok = (l > 8.67e-19f) && (l < 1.15e18f);
#pragma unroll
  for (int i = 0; i < 8; ++i) ok = ok && (fabsf(of[i]) < 1e37f);
  if (!hh) {
#pragma unroll
    for (int i = 8; i < 12; ++i) ok = ok && (fabsf(of[i]) < 1e37f);
  }
  if (__builtin_expect(!__all(ok), 0)) {
    __builtin_amdgcn_fence(__ATOMIC_ACQUIRE, "agent");
    const char* kv = ws + WS_KV + (size_t)head * 163840;
    const bf16x8 fq0 = kh ? q0[1] : q0[0];
    const s16x4 fq1 = kh ? q1[1] : q1[0];
    float m = -INFINITY;
    for (int t = 0; t < 64; ++t) {
      const bf16x8 kf0 = *(const bf16x8*)(kv + t * 2560 + lane * 16); const s16x4 kf1 = *(const s16x4*)(kv + t * 2560 + 1024 + r32 * 8);
      f32x16 s = MFMA32(kf0, fq0, (f32x16){}); s = MFMA32K8(kf1, fq1, s);
#pragma unroll
      for (int i = 0; i < 16; ++i) m = fmaxf(m, s[i]);
    }
    m = max32(m);
    f32x16 oo = {};
    const int vr = r32 < 20 ? r32 : 0;
    for (int t = 0; t < 64; ++t) {
      const bf16x8 kf0 = *(const bf16x8*)(kv + t * 2560 + lane * 16); const s16x4 kf1 = *(const s16x4*)(kv + t * 2560 + 1024 + r32 * 8);
      bf16x8 vf0 = *(const bf16x8*)(kv + t * 2560 + 1280 + (hh * 20 + vr) * 16), vf1 = *(const bf16x8*)(kv + t * 2560 + 1920 + (hh * 20 + vr) * 16);
      if (r32 >= 20) { vf0 = (bf16x8)(short)0x3f80; vf1 = vf0; }
      f32x16 s = MFMA32(kf0, fq0, (f32x16){}); s = MFMA32K8(kf1, fq1, s);
      u32x4 p0, p1;
#pragma unroll
      for (int i = 0; i < 4; ++i) {
        p0[i] = pk_bf16(EX2(s[2 * i] - m), EX2(s[2 * i + 1] - m));
        p1[i] = pk_bf16(EX2(s[8 + 2 * i] - m), EX2(s[8 + 2 * i + 1] - m));
      }
      oo = MFMA32(vf0, __builtin_bit_cast(bf16x8, p0), oo);
      oo = MFMA32(vf1, __builtin_bit_cast(bf16x8, p1), oo);
    }
#pragma unroll
    for (int i = 0; i < 12; ++i) of[i] = oo[i];
    l = hi32(of[8]);
  }
  const float rl = __builtin_amdgcn_rcpf(l);
  waitv<0>(); pin(fa); pin(fb); pin(fc);
  setflag(ctl + CTL_FD + gw * 8, tag, lane);
#pragma unroll
  for (int g = 0; g < 3; ++g)
    if (g < 2 || !hh) st8s(obs[g], vo, nofill(pk_bf16(of[4 * g] * rl, of[4 * g + 1] * rl), pk_bf16(of[4 * g + 2] * rl, of[4 * g + 3] * rl)));
  STAMP_AT(1, 11);
  if (!__all(fa[0] == tag[0] && fa[1] == tag[1] && fb[0] == tag[0] && fb[1] == tag[1])) poll2(ctr, fva, fvb, tag);
  if (!__all(fc[0] == tag[0] && fc[1] == tag[1])) poll(ctr, fvc, tag);
  STAMP_AT(1, 12);
}

constexpr int x_none(int) { return 0; }
constexpr int x_p4(int i) { return ((i >= 2 && i < 7) || (i >= 10 && i < 18)) ? 1 : 0; }
template <int NSTEP, int NF, int LA, int LL, int CG, int NA, int ASTR, int (*BOFF)(int), int (*XCNT)(int), class PRE, class MM>
DI void gemm_res(const char* asrc, unsigned lds0, unsigned voff, PRE&& pre, MM&& mm) {
  bf16x8 a[NSTEP][NA];
  bf16x8 bf[LL + 1][NF];
  unsigned rd0 = lds0 + voff, rd1 = rd0 + 65536, rd2 = rd0 + 131072; pin(rd0); pin(rd1); pin(rd2);
  auto issue = [&](auto n_) { constexpr int n = n_; sfor<NA>([&](auto j) { gld16<(n & 3) * 1024>(a[n][j], asrc + (n >> 2) * 4096 + j * ASTR, voff); }); };
  constexpr auto xsum = [](int lo, int hi) { int c = 0; for (int i = lo < 0 ? 0 : lo; i <= hi; ++i) c += XCNT(i); return c; };
  auto rd = [&](auto i_, auto f_) {
    constexpr int i = i_, f = f_, off = BOFF(i * NF + f);
    bf[i % (LL + 1)][f] = lds128((off < 65536 ? rd0 : off < 131072 ? rd1 : rd2) + (off & 65535));
  };
  static_assert(NSTEP % CG == 0 && LA > CG, "groups");
  unsigned m = 0;
  auto fold = [&](auto n_) { constexpr int n = n_;
    sfor<NA>([&](auto j) { pin(a[n][j]); const u32x4 u = __builtin_bit_cast(u32x4, a[n][j]); m = max(max(m, u[0]), u[1]); m = max(max(m, u[2]), u[3]); });
    pin(m);
    if constexpr (n % CG == CG - 1) {
      if (__builtin_expect(__any(m == 0xffffffffu), 0)) {
#pragma unroll 1
        for (int spin = 0; spin < (1 << 17); ++spin) {
          RETRY_COUNT(NSTEP + NF);
          sfor<CG>([&](auto k) { issue(IC<n - CG + 1 + k>{}); });
          waitv<0>(); m = 0;
          sfor<CG>([&](auto k) { sfor<NA>([&](auto j) { pin(a[n - CG + 1 + k][j]); m = max(m, umax4(__builtin_bit_cast(u32x4, a[n - CG + 1 + k][j]))); }); });
          if (!__any(m == 0xffffffffu)) break;
        }
      }
    }
  };
  sfor<LA>([&](auto n) { issue(n); });
  pre(IC<-1>{});
  sfor<LL>([&](auto i) { sfor<NF>([&](auto f) { rd(i, f); }); });
  waitv<(LA - CG) * NA>(); sfor<CG>([&](auto n) { fold(n); });
  sfor<NSTEP>([&](auto i_) {
    constexpr int i = i_;
    pre(i_);
    SB();
    sfor<NF>([&](auto f_) {
      constexpr int f = f_;
      if constexpr (f == NF - 1 && i + CG < NSTEP) {
        waitv<((i + LA < NSTEP ? i + LA : NSTEP - 1) - (i + CG)) * NA + xsum(i + CG - LA + 1, i)>(); fold(IC<i + CG>{}); SB(); }
      mm(f_, bf[i % (LL + 1)][f], a[i]);
      SB();
      if constexpr (i + LL < NSTEP) rd(IC<i + LL>{}, f_);
      if constexpr (f == 0 && i + LA < NSTEP) issue(IC<i + LA>{});
      SB();
    });
  });
}

__global__ __launch_bounds__(512, 2) void k_mega(const u16* __restrict__ x, const u16* __restrict__ Wqkv, const u16* __restrict__ Wo,
                                                 const u16* __restrict__ Wg, const u16* __restrict__ Wu, const u16* __restrict__ Wd,
                                                 char* __restrict__ ws, u16* __restrict__ out) {
  __shared__ __attribute__((aligned(16))) char smem[163840];
  const unsigned lds0 = lds_addr_of(smem);
  const int w = __builtin_amdgcn_readfirstlane(threadIdx.x >> 6);
  const int g8 = blockIdx.x & 7, i32 = blockIdx.x >> 3;
  char* ctl = ws + WS_CTL;
  const char* ctr = ctl + g8 * CTL_REP;
  u32x2 ep;
  gld8<0>(ep, ctl, 0u);
  int f1idx;
  if (i32 < 30) {
    const int i = (g8 & 1) * 30 + i32, rb = (g8 >> 1) * 4 + (i & 3), cb = i >> 2;
    if (cb >= 10) p1_body<true>(x, Wqkv, ws, lds0, w, rb, cb);
    else p1_body<false>(x, Wqkv, ws, lds0, w, rb, cb);
    f1idx = (cb * 4 + (w & 3)) * 32 + rb * 2 + (w >> 2);
  } else {
    p1_repack(Wo, ws, w, blockIdx.x - 240);
    f1idx = 1920 + (blockIdx.x - 240) * 8 + w;
  }
  waitv<0>();
  pin(ep);
  u32x2 tag;
  {
    const unsigned long long e = (((unsigned long long)__builtin_amdgcn_readfirstlane(ep[1]) << 32) | __builtin_amdgcn_readfirstlane(ep[0])) + 1ull;
    tag = (u32x2){(unsigned)e, (unsigned)(e >> 32)};
  }
  publish(ctl + CTL_F1 + f1idx * 8, tag, fresh_lane());
  bar();
  const int rb3 = g8 * 4 + (i32 >> 3), cb3 = i32 & 7;
  const int rb4 = (g8 >> 2) * 4 + (i32 >> 3), cb4 = (g8 & 3) * 8 + (i32 & 7);
  p2_attn(ws, Wg, Wu, Wd, lds0, w, tag, rb3, cb3, rb4, cb4);
  bar();
  const char* gug = ws + WS_WP + (size_t)NFR_WO * 1024 + (size_t)cb4 * 122880;
  const char* wdg = ws + WS_WP + (size_t)(NFR_WO + NFR_GU) * 1024 + (size_t)cb3 * 245760;
  u32x2 hp[5];
  if (w < 4) {
    const int lane = fresh_lane();
    const unsigned voff = lane * 16;
    const int row = rb3 * 64 + w * 16 + (lane & 15), col0 = cb3 * 80 + 4 * (lane >> 4);
    STAMP_AT(2, 0);
    u32x2 xr[5];
#pragma unroll
    for (int ct = 0; ct < 5; ++ct) xr[ct] = *(const u32x2*)(x + (size_t)row * 640 + col0 + 16 * ct);
    { const int hd = lane & 31, cgt = (hd & 1) ? hd * 5 : hd * 5 + 4;
      probe(ws + WS_O + (size_t)(rb3 * 4 + w) * 20480, (unsigned)((cgt >> 3) * 1024 + ((cgt & 7) >> 1) * 256 + (cgt & 1) * 8)); }
    sfor<5>([&](auto ct) { pin(xr[ct]); });
    STAMP_AT(2, 1);
    f32x4 acc[5] = {};
    sfor<5>([&](auto ct) { pin(acc[ct]); });
    gemm_res<20, 5, 20, 2, 4, 1, 0, wo_off, x_none>(ws + WS_O + (size_t)(rb3 * 4 + w) * 20480, lds0, voff,
      [&](auto i) { if constexpr (i == -1) STAMP_AT(2, 2); if constexpr (i == 1) STAMP_AT(2, 3); if constexpr (i == 10) STAMP_AT(2, 4);
        if constexpr (i == P3_G1 || i == 12) bar(); },
      [&](auto ct, const bf16x8& b, bf16x8 (&a)[1]) { mfma16(acc[ct], b, a[0]); });
    acc_fence(); sfor<5>([&](auto ct) { pin(acc[ct]); });
    STAMP_AT(2, 6);
    float s1 = 0.f, s2 = 0.f;
    const char* hb = ws + WS_H + (size_t)(rb3 * 4 + w) * 20480;
    const unsigned vt = t16lane(lane);
#pragma unroll
    for (int ct = 0; ct < 5; ++ct) {
      hp[ct] = nofill(pk_bf16(bf_lo(xr[ct][0]) + acc[ct][0], bf_hi(xr[ct][0]) + acc[ct][1]), pk_bf16(bf_lo(xr[ct][1]) + acc[ct][2], bf_hi(xr[ct][1]) + acc[ct][3]));
      st8s(hb + t16off(cb3 * 80 + ct * 16), vt, hp[ct]);
      s1 = dot2(hp[ct][0], 0x3f803f80u, s1); s1 = dot2(hp[ct][1], 0x3f803f80u, s1);
      s2 = dot2(hp[ct][0], hp[ct][0], s2); s2 = dot2(hp[ct][1], hp[ct][1], s2);
    }
    s1 = red4(s1); s2 = red4(s2);
    if (lane < 16) st8s(ws + WS_HST + (size_t)(rb3 * 4 + w) * 1024 + (cb3 >> 1) * 256 + (cb3 & 1) * 8, (unsigned)(lane * 16), nofill(__float_as_uint(s1), __float_as_uint(s2)));
    bar();
    STAMP_AT(2, 7);
  } else {
    const int lane = fresh_lane(), v = w - 4;
    const unsigned voff = lane * 16;
    float cpart[10];
    const int cc = v * 24 + min(lane & 31, 23);
    sfor<10>([&](auto i) { gld4<i * 384>(cpart[i], ws + WS_CSP + (size_t)cb4 * 7680, (unsigned)((lane >> 5) * 3840 + cc * 4)); });
    poll(ctr + CTL_FD, (unsigned)((cb3 * 240 + v * 60 + min(lane, 59)) * 8), tag);
    float cv = 0.f;
    sfor<10>([&](auto j) { pin(cpart[j]); cv += cpart[j]; });
    cv = add32(cv);
    bar();
    STAMP_AT(2, 2);
    sfor<5>([&](auto j) { glds16<true>(gug + (v * 5 + j) * 1024, voff, lds0 + gu_off(0) + (v * 5 + j) * 1024); });
    sfor<10>([&](auto j) { glds16<true>(gug + (20 + v * 10 + j) * 1024, voff, lds0 + gu_off(20) + (v * 10 + j) * 1024); });
    bar();
    sfor<15>([&](auto j) { glds16<true>(gug + (60 + v * 15 + j) * 1024, voff, lds0 + gu_off(60) + (v * 15 + j) * 1024); });
    STAMP_AT(2, 5);
    waitv<0>();
    bar();
    STAMP_AT(2, 6);
    if (lane < 24) ((LAS float*)(uintptr_t)(lds0 + CSF_OFF))[cc] = cv;
  }
  {
    const int lane = fresh_lane();
    const unsigned voff = lane * 16;
    const int r16 = lane & 15, g4 = lane >> 4;
    STAMP_AT(3, 0);
    f32x4 cs[6]; f32x2 ms[2];
    f32x4 hs[2];
#pragma unroll 1
    for (int spin = 0; spin < (1 << 17); ++spin) {
      gld16<0>(hs[0], ws + WS_HST + (size_t)(rb4 * 16 + w * 2) * 1024, voff);
      gld16<1024>(hs[1], ws + WS_HST + (size_t)(rb4 * 16 + w * 2) * 1024, voff);
      waitv<0>(); pin(hs[0]); pin(hs[1]);
      if (!__any(max(umax4(__builtin_bit_cast(u32x4, hs[0])), umax4(__builtin_bit_cast(u32x4, hs[1]))) == 0xffffffffu)) break;
      __builtin_amdgcn_s_sleep(1);
    }
    STAMP_AT(3, 1);
    f32x4 acc[2][6] = {};
    sfor<6>([&](auto ct) { pin(acc[0][ct]); pin(acc[1][ct]); });
    if (w < 4) __builtin_amdgcn_s_setprio(1);
    gemm_res<20, 6, K4_LA_, 1, K4_CG_, 2, 20480, gu_off, x_p4>(ws + WS_H + (size_t)(rb4 * 16 + w * 2) * 20480, lds0, voff,
      [&](auto i) { if constexpr (i == -1) STAMP_AT(3, 2); if constexpr (i == 1) STAMP_AT(3, 3); if constexpr (i == 10) STAMP_AT(3, 4);
        if constexpr (i == 9) bar();
        if constexpr (i == -1) sfor<2>([&](auto t) {
          const float s1 = red4(hs[t][0] + hs[t][2]), s2 = red4(hs[t][1] + hs[t][3]), mean = s1 * (1.f / 640.f);
          ms[t] = (f32x2){mean, __builtin_amdgcn_rsqf(fmaxf(s2 * (1.f / 640.f) - mean * mean, 0.f) + LN_EPS)}; pin(ms[t]); });
        if constexpr (i == 18) sfor<6>([&](auto ct) { cs[ct] = *(const LAS f32x4*)(uintptr_t)(lds0 + CSF_OFF + (ct * 16 + 4 * g4) * 4); });
        if constexpr (i >= 2 && i < 7) { const int f = (i - 2) * 8 + w - ((i == 6 && w >= 4) ? 4 : 0); glds16<true>(wdg + (f) * 1024, voff, lds0 + wd_off(0) + f * 1024); }
        if constexpr (i >= 10 && i < 15) { constexpr int n = i - 10; glds16<true>(wdg + (40 + n * 8 + w) * 1024, voff, lds0 + wd_off(40) + (n * 8 + w) * 1024); }
        if constexpr (i >= 15 && i < 18) { const int f = (i - 15) * 8 + w - ((i == 17 && w >= 4) ? 4 : 0); glds16<true>(wdg + (140 + f) * 1024, voff, lds0 + wd_off(140) + f * 1024); } },
      [&](auto ct, const bf16x8& b, bf16x8 (&a)[2]) { mfma16(acc[0][ct], b, a[0]); mfma16(acc[1][ct], b, a[1]); });
    acc_fence(); sfor<6>([&](auto ct) { pin(acc[0][ct]); pin(acc[1][ct]); });
    STAMP_AT(3, 6);
    constexpr float GC0 = -2.f * 0.7978845608028654f * 1.4426950408889634f, GC1 = GC0 * 0.044715f;
    const char* gb = ws + WS_G + (size_t)(rb4 * 16 + w * 2) * 49152;
    const unsigned vt = t16lane(lane);
#pragma unroll
    for (int t = 0; t < 2; ++t) {
      const float nm = -ms[t].x, rstd = ms[t].y;
#pragma unroll
      for (int ct = 0; ct < 3; ++ct) {
        const f32x4 gv = (acc[t][ct] + cs[ct] * nm) * rstd, uv = (acc[t][3 + ct] + cs[3 + ct] * nm) * rstd;
        const f32x4 z = gv * (gv * gv * GC1 + GC0);
        f32x4 e;
#pragma unroll
        for (int j = 0; j < 4; ++j) e[j] = __builtin_amdgcn_exp2f(z[j]);
        e = e + 1.f;
#pragma unroll
        for (int j = 0; j < 4; ++j) e[j] = __builtin_amdgcn_rcpf(e[j]);
        const f32x4 r = gv * uv * e;
        st8s(gb + t * 49152 + t16off(cb4 * 48 + ct * 16), vt, nofill(pk_bf16(r[0], r[1]), pk_bf16(r[2], r[3])));
      }
    }
    __builtin_amdgcn_s_setprio(0);
    STAMP_AT(3, 7);
    waitv<6>();
    STAMP_AT(3, 8);
    bar();
  }
  if (w < 4) {
    const int lane = fresh_lane();
    const unsigned voff = lane * 16;
    const int row = rb3 * 64 + w * 16 + (lane & 15), col0 = cb3 * 80 + 4 * (lane >> 4);
    STAMP_AT(4, 0);
    probe(ws + WS_G + (size_t)(rb3 * 4 + w) * 49152, (unsigned)t16off((lane & 31) * 48 + 32));
    STAMP_AT(4, 1);
    f32x4 acc[5] = {};
    sfor<5>([&](auto ct) { pin(acc[ct]); });
    gemm_res<48, 5, K5_LA_, 2, 4, 1, 0, wd_off, x_none>(ws + WS_G + (size_t)(rb3 * 4 + w) * 49152, lds0, voff,
      [&](auto i) { if constexpr (i == 1) STAMP_AT(4, 3); if constexpr (i == 14) STAMP_AT(4, 4); if constexpr (i == 30) STAMP_AT(4, 5);
        if constexpr (i == -1 || i == 8 || i == 14 || i == 16 || i == 30 || i == 38) bar(); },
      [&](auto ct, const bf16x8& b, bf16x8 (&a)[1]) { mfma16(acc[ct], b, a[0]); });
    acc_fence(); sfor<5>([&](auto ct) { pin(acc[ct]); });
    STAMP_AT(4, 12);
#pragma unroll
    for (int ct = 0; ct < 5; ++ct) {
      const unsigned p0 = pk_bf16(bf_lo(hp[ct][0]) + acc[ct][0], bf_hi(hp[ct][0]) + acc[ct][1]);
      const unsigned p1 = pk_bf16(bf_lo(hp[ct][1]) + acc[ct][2], bf_hi(hp[ct][1]) + acc[ct][3]);
      *(u32x2*)(out + (size_t)row * 640 + col0 + 16 * ct) = (u32x2){p0, p1};
    }
    STAMP_AT(4, 13);
  } else {
    const int lane = fresh_lane(), v = w - 4;
    const unsigned voff = lane * 16;
    glds16<true>(wdg + (36 + v) * 1024, voff, lds0 + wd_off(36) + v * 1024);
    waitv<0>(); bar();
    sfor<3>([&](auto g) { sfor<5>([&](auto j) { constexpr int f = (g + 4) * 20 + j; glds16<true>(wdg + (f + v * 5) * 1024, voff, lds0 + wd_off(f) + v * 5120); }); });
    bar();
    sfor<2>([&](auto g) { sfor<5>([&](auto j) { constexpr int f = (g + 8) * 20 + j; glds16<true>(wdg + (f + v * 5) * 1024, voff, lds0 + wd_off(f) + v * 5120); }); });
    waitv<10>(); bar();
    bar();
    sfor<2>([&](auto g) { sfor<5>([&](auto j) { constexpr int f = (g + 10) * 20 + j; glds16<true>(wdg + (f + v * 5) * 1024, voff, lds0 + wd_off(f) + v * 5120); }); });
    waitv<10>(); bar();
    waitv<0>(); bar();
    if (blockIdx.x == 0 && w == 4) {
      poll2(ctr + CTL_FR, (unsigned)(lane * 64), (unsigned)(4096 + lane * 64), tag);
      poll2(ctr + CTL_FR, (unsigned)(8192 + lane * 64), (unsigned)(12288 + lane * 64), tag);
      if (lane == 0) st8(ctl, tag);
    }
  }
}

extern "C" void kernel_launch(void* const* d_in, const int* in_sizes, int n_in, void* d_out, int out_size, void* d_ws, size_t ws_size,
                              hipStream_t stream) {
  k_mega<<<256, 512, 0, stream>>>((const u16*)d_in[0], (const u16*)d_in[1], (const u16*)d_in[2], (const u16*)d_in[4], (const u16*)d_in[5],
                                  (const u16*)d_in[6], (char*)d_ws, (u16*)d_out);
}
```

```cpp
#include <hip/hip_runtime.h>
#include <hip/hip_bf16.h>
#include <stdint.h>
#include <utility>

typedef short bf16x8 __attribute__((ext_vector_type(8)));
typedef short s16x4 __attribute__((ext_vector_type(4)));
typedef float f32x16 __attribute__((ext_vector_type(16)));
typedef float f32x4 __attribute__((ext_vector_type(4)));
typedef float f32x2 __attribute__((ext_vector_type(2)));
typedef unsigned u32x4 __attribute__((ext_vector_type(4)));
typedef unsigned u32x2 __attribute__((ext_vector_type(2)));
typedef __bf16 bf2_t __attribute__((ext_vector_type(2)));
typedef unsigned short u16;
#define LAS __attribute__((address_space(3)))
#define DI __device__ __forceinline__

constexpr int S_ = 2048, D_ = 640, H_ = 32, DH = 20, F_ = 1536;
constexpr float LN_EPS = 1e-5f;
constexpr float QSCALE = 0.22360679774997896f * 1.4426950408889634f;

constexpr size_t WS_Q   = 0;
constexpr size_t WS_KV  = WS_Q + 2621440;
constexpr size_t WS_O   = WS_KV + 5242880;
constexpr size_t WS_H   = WS_O + 2621440;
constexpr size_t WS_HST = WS_H + 2621440;
constexpr size_t WS_G   = WS_HST + 131072;
constexpr size_t WS_WP  = WS_G + 6291456;
constexpr size_t WS_CSP = WS_WP + 6717440;
constexpr size_t WS_RST = WS_CSP + 245760;
constexpr size_t WS_CTL = WS_RST + 32768;
constexpr size_t CTL_F1 = 256;
constexpr size_t CTL_F2 = CTL_F1 + 2048 * 8;
constexpr size_t CTL_FR = CTL_F2 + 2048 * 8;
constexpr size_t CTL_F3 = CTL_FR + 2048 * 8;
constexpr size_t CTL_F4 = CTL_F3 + 1024 * 8;
constexpr size_t CTL_FD = CTL_F4 + 2048 * 8;
constexpr size_t CTL_REP = CTL_FD + 2048 * 8 - 256;
constexpr int NFR_WO = 800, NFR_GU = 3840, NFR_WD = 1920, NFR_ALL = NFR_WO + NFR_GU + NFR_WD;

DI unsigned pk_bf16(float lo, float hi) { f32x2 f = {lo, hi}; return __builtin_bit_cast(unsigned, __builtin_convertvector(f, bf2_t)); }
DI float bf_lo(unsigned u) { return __uint_as_float(u << 16); }
DI float bf_hi(unsigned u) { return __uint_as_float(u & 0xffff0000u); }
DI float dot2(unsigned a, unsigned b, float c) { return __builtin_amdgcn_fdot2_f32_bf16(__builtin_bit_cast(bf2_t, a), __builtin_bit_cast(bf2_t, b), c, false); }
DI float add32(float v) { const auto r = __builtin_amdgcn_permlane32_swap(__float_as_uint(v), __float_as_uint(v), false, false); return __uint_as_float(r[0]) + __uint_as_float(r[1]); }
DI float max32(float v) { const auto r = __builtin_amdgcn_permlane32_swap(__float_as_uint(v), __float_as_uint(v), false, false); return fmaxf(__uint_as_float(r[0]), __uint_as_float(r[1])); }
DI float add16(float v) { const auto r = __builtin_amdgcn_permlane16_swap(__float_as_uint(v), __float_as_uint(v), false, false); return __uint_as_float(r[0]) + __uint_as_float(r[1]); }
DI float red4(float v) { return add32(add16(v)); }
DI float hi32(float v) { const auto r = __builtin_amdgcn_permlane32_swap(__float_as_uint(v), __float_as_uint(v), false, false); return __uint_as_float(r[1]); }
#define MFMA32(a, b, c) __builtin_amdgcn_mfma_f32_32x32x16_bf16((a), (b), (c), 0, 0, 0)
#define MFMA32K8(a, b, c) __builtin_amdgcn_mfma_f32_32x32x8bf16_1k((a), (b), (c), 0, 0, 0)
#define MFMA16(a, b, c) __builtin_amdgcn_mfma_f32_16x16x32_bf16((a), (b), (c), 0, 0, 0)

DI void mfma16(f32x4& c, const bf16x8& a, const bf16x8& b) { asm volatile("v_mfma_f32_16x16x32_bf16 %0, %1, %2, %0" : "+v"(c) : "v"(a), "v"(b)); }
DI void acc_fence() { asm volatile("s_nop 7\n\ts_nop 3" ::: "memory"); }
template <bool DEV> DI void glds16(const void* sbase, unsigned voff, unsigned lds_base) {
  if constexpr (DEV) asm volatile("s_mov_b32 m0, %2\n\ts_nop 0\n\tglobal_load_lds_dwordx4 %0, %1 sc1" :: "v"(voff), "s"(sbase), "s"(lds_base) : "memory");
  else asm volatile("s_mov_b32 m0, %2\n\ts_nop 0\n\tglobal_load_lds_dwordx4 %0, %1" :: "v"(voff), "s"(sbase), "s"(lds_base) : "memory");
}
template <int OFF, class T> DI void gld16(T& dst, const void* sbase, unsigned voff) {
  asm volatile("global_load_dwordx4 %0, %1, %2 offset:%3 sc1" : "=v"(dst) : "v"(voff), "s"(sbase), "i"(OFF) : "memory");
}
template <int OFF, class T> DI void gld8(T& dst, const void* sbase, unsigned voff) {
  asm volatile("global_load_dwordx2 %0, %1, %2 offset:%3 sc1" : "=v"(dst) : "v"(voff), "s"(sbase), "i"(OFF) : "memory");
}
template <int OFF> DI void gld4(float& dst, const void* sbase, unsigned voff) {
  asm volatile("global_load_dword %0, %1, %2 offset:%3 sc1" : "=v"(dst) : "v"(voff), "s"(sbase), "i"(OFF) : "memory");
}
template <int OFF> DI void gldu16(unsigned& dst, const void* sbase, unsigned voff) {
  asm volatile("global_load_ushort %0, %1, %2 offset:%3" : "=v"(dst) : "v"(voff), "s"(sbase), "i"(OFF) : "memory");
}
DI void st16(void* p, u32x4 v) { asm volatile("global_store_dwordx4 %0, %1, off sc1\n\ts_nop 1" :: "v"(p), "v"(v) : "memory"); }
DI void st8(void* p, u32x2 v) { asm volatile("global_store_dwordx2 %0, %1, off sc1" :: "v"(p), "v"(v) : "memory"); }
DI void st4(void* p, float v) { asm volatile("global_store_dword %0, %1, off sc1" :: "v"(p), "v"(v) : "memory"); }
DI void st8s(const void* sbase, unsigned voff, u32x2 v) { asm volatile("global_store_dwordx2 %0, %1, %2 sc1" :: "v"(voff), "v"(v), "s"(sbase) : "memory"); }
DI u32x2 nofill(unsigned a, unsigned b) { return (u32x2){min(a, 0xfffffffeu), min(b, 0xfffffffeu)}; }
DI unsigned umax4(u32x4 v) { return max(max(v[0], v[1]), max(v[2], v[3])); }
template <int N> DI void waitv() { asm volatile("s_waitcnt vmcnt(%0)" :: "i"(N) : "memory"); }
DI void bar() { asm volatile("s_waitcnt lgkmcnt(0)\n\ts_barrier" ::: "memory"); }
DI void setflag(char* flag, u32x2 tag, int lane) { if (lane < 8) st8(flag + lane * CTL_REP, tag); }
DI void publish(char* flag, u32x2 tag, int lane) { waitv<0>(); setflag(flag, tag, lane); }
DI void poll(const char* base, unsigned voff, u32x2 tag) {
#pragma unroll 1
  for (int spin = 0; spin < (1 << 17); ++spin) {
    u32x2 v;
    asm volatile("global_load_dwordx2 %0, %1, %2 sc1\n\ts_waitcnt vmcnt(0)" : "=v"(v) : "v"(voff), "s"(base) : "memory");
    if (__all(v[0] == tag[0] && v[1] == tag[1])) break;
    __builtin_amdgcn_s_sleep(1);
  }
}
DI void poll2(const char* base, unsigned voff0, unsigned voff1, u32x2 tag) {
#pragma unroll 1
  for (int spin = 0; spin < (1 << 17); ++spin) {
    u32x2 v, u;
    asm volatile("global_load_dwordx2 %0, %2, %4 sc1\n\tglobal_load_dwordx2 %1, %3, %4 sc1\n\ts_waitcnt vmcnt(0)" : "=&v"(v), "=&v"(u) : "v"(voff0), "v"(voff1), "s"(base) : "memory");
    if (__all(v[0] == tag[0] && v[1] == tag[1] && u[0] == tag[0] && u[1] == tag[1])) break;
    __builtin_amdgcn_s_sleep(1);
  }
}
DI void probe(const char* base, unsigned voff) {
#pragma unroll 1
  for (int spin = 0; spin < (1 << 17); ++spin) {
    unsigned v;
    asm volatile("global_load_dword %0, %1, %2 sc1\n\ts_waitcnt vmcnt(0)" : "=v"(v) : "v"(voff), "s"(base) : "memory");
    if (__all(v != 0xffffffffu)) break;
    __builtin_amdgcn_s_sleep(1);
  }
}
constexpr int t16off(int c0) { return (c0 >> 5) * 1024 + (c0 & 16) * 32; }
DI unsigned t16lane(int lane) { return (unsigned)((lane >> 5) * 256 + (lane & 15) * 16 + ((lane >> 4) & 1) * 8); }
DI void st16s(const void* sbase, unsigned voff, u32x4 v) { asm volatile("global_store_dwordx4 %0, %1, %2 sc1\n\ts_nop 1" :: "v"(voff), "v"(v), "s"(sbase) : "memory"); }
DI float row16_sum(float v) {
  v += __builtin_bit_cast(float, __builtin_amdgcn_update_dpp(0, __builtin_bit_cast(int, v), 0x128, 0xf, 0xf, false));
  v += __builtin_bit_cast(float, __builtin_amdgcn_update_dpp(0, __builtin_bit_cast(int, v), 0x124, 0xf, 0xf, false));
  v += __builtin_bit_cast(float, __builtin_amdgcn_update_dpp(0, __builtin_bit_cast(int, v), 0x122, 0xf, 0xf, false));
  v += __builtin_bit_cast(float, __builtin_amdgcn_update_dpp(0, __builtin_bit_cast(int, v), 0x121, 0xf, 0xf, false));
  return v;
}
DI u32x2 make_tag(u32x2 ep) {
  const unsigned long long e = (((unsigned long long)__builtin_amdgcn_readfirstlane(ep[1]) << 32) | __builtin_amdgcn_readfirstlane(ep[0])) + 1ull;
  return (u32x2){(unsigned)e, (unsigned)(e >> 32)};
}
struct RowStat { u32x4 a; unsigned b; };
DI void rs_load(RowStat& r, const u16* x, int row, int lane) {
  asm volatile("global_load_dwordx4 %0, %2, %4\n\tglobal_load_dword %1, %3, %4 offset:1024" : "=&v"(r.a), "=&v"(r.b) : "v"(lane * 16), "v"(lane * 4), "s"((const char*)x + (size_t)row * 1280) : "memory");
}
DI void rs_store(RowStat& r, char* ws, int row, int lane, u32x2 tag) {
  asm volatile("" : "+v"(r.a), "+v"(r.b));
  float s1 = dot2(r.b, 0x3f803f80u, 0.f), s2 = dot2(r.b, r.b, 0.f);
#pragma unroll
  for (int j = 0; j < 4; ++j) { s1 = dot2(r.a[j], 0x3f803f80u, s1); s2 = dot2(r.a[j], r.a[j], s2); }
  s1 = red4(row16_sum(s1)); s2 = red4(row16_sum(s2));
  const float mean = s1 * (1.f / 640.f), rstd = __builtin_amdgcn_rsqf(fmaxf(s2 * (1.f / 640.f) - mean * mean, 0.f) + LN_EPS);
  const unsigned m = __float_as_uint(mean), q = __float_as_uint(rstd);
  if (lane == 0) st16(ws + WS_RST + (size_t)row * 16, (u32x4){m, q, tag[0] ^ m, tag[1] ^ q});
}
DI int fresh_lane() { int l = __builtin_amdgcn_mbcnt_hi(~0u, __builtin_amdgcn_mbcnt_lo(~0u, 0u)); asm volatile("" : "+v"(l)); return l; }
template <class T> DI void pins(T& v) { asm volatile("" : "+s"(v)); }
template <class T> DI void pin(T& v) { asm volatile("" : "+v"(v)); }
DI bf16x8 lds128(unsigned addr) { return *(const LAS bf16x8*)(uintptr_t)addr; }
DI s16x4 lds64(unsigned addr) { return *(const LAS s16x4*)(uintptr_t)addr; }
DI s16x4 ldstr(unsigned addr) { return __builtin_amdgcn_ds_read_tr16_b64_v4i16((LAS s16x4*)(uintptr_t)addr); }
DI unsigned lds_addr_of(const void* p) { return (unsigned)(uintptr_t)(const LAS char*)p; }
#define STAMP_AT(kid, idx) ((void)0)
#define RETRY_COUNT(k) ((void)0)
#define WAITV_BAR(N) asm volatile("s_waitcnt vmcnt(" #N ") lgkmcnt(0)\n\ts_barrier" ::: "memory")
#define SB() __builtin_amdgcn_sched_barrier(0)
template <int N> using IC = std::integral_constant<int, N>;
template <class F, int... Is> DI void sfor_(F&& f, std::integer_sequence<int, Is...>) { (f(IC<Is>{}), ...); }
template <int N, class F> DI void sfor(F&& f) { sfor_(f, std::make_integer_sequence<int, N>{}); }
template <int N> DI void waitv_bar() { asm volatile("s_waitcnt vmcnt(%0) lgkmcnt(0)\n\ts_barrier" :: "i"(N) : "memory"); }

#define P3_G1 -1
#define K4_LA_ 6
#define K4_CG_ 2
#define K5_LA_ 16
constexpr int K1_STAGE = 32768;
DI void p1_load(const u16* __restrict__ x, const u16* __restrict__ W, const unsigned lds0, const int v, int rb, int cb) {
  const int lane = fresh_lane();
  const unsigned a_voff0 = (unsigned)((lane >> 3) * 1280 + (((lane & 7) ^ ((lane >> 4) & 7)) << 4));
  const unsigned a_voff1 = (unsigned)((lane >> 3) * 1280 + (((lane & 7) ^ ((4 + (lane >> 4)) & 7)) << 4)) + 8 * 1280;
  const unsigned w_voff0 = (unsigned)((lane >> 3) * 3840 + (((lane & 7) ^ (((lane >> 4) & 1) << 2)) << 4));
  const unsigned w_voff1 = w_voff0 + 8 * 3840;
  const char* xs = (const char*)x + (size_t)(rb * 128 + v * 32) * 1280;
  const char* wsrc = (const char*)W + (size_t)((v & 1) * 32) * 3840 + (size_t)(cb * 128 + (v >> 1) * 64) * 2;
  const int rot = ((((blockIdx.x >> 1) & 3) * 5 + (blockIdx.x & 1) * 10) >> 1) % 10;
  auto st = [&](auto s_) {
    constexpr int s0 = s_; const unsigned d = lds0 + (s0 % 5) * K1_STAGE + v * 4096;
    const int s = s0 + rot - (s0 + rot >= 10 ? 10 : 0);
    const char* xp = xs + s * 128; const char* wp = wsrc + (size_t)s * 64 * 3840;
    glds16<false>(xp, a_voff0, d); glds16<false>(wp, w_voff0, d + 16384);
    glds16<false>(xp, a_voff1, d + 1024); glds16<false>(wp, w_voff1, d + 16384 + 1024);
    glds16<false>(xp + 16 * 1280, a_voff0, d + 2048); glds16<false>(wp + 16 * 3840, w_voff0, d + 16384 + 2048);
    glds16<false>(xp + 16 * 1280, a_voff1, d + 3072); glds16<false>(wp + 16 * 3840, w_voff1, d + 16384 + 3072);
  };
  st(IC<0>{}); st(IC<1>{});
  sfor<10>([&](auto s_) { constexpr int s = s_;
    waitv<(s == 0 ? 8 : s <= 7 ? 16 : s == 8 ? 8 : 0)>(); asm volatile("s_barrier" ::: "memory");
    if constexpr (s == 0) { st(IC<2>{}); st(IC<3>{}); }
    else if constexpr (s + 3 < 10) st(IC<s + 3>{}); });
  bar();
}
template <bool ISV>
DI void p1_body(const u16* __restrict__ x, char* __restrict__ ws, const unsigned lds0, const int w, int rb, int cb, u32x2& tag) {
  const int lane = fresh_lane();
  u32x2 ep; gld8<0>(ep, ws + WS_CTL, 0u);
  RowStat rs[2];
  rs_load(rs[0], x, blockIdx.x * 8 + w * 2, lane); rs_load(rs[1], x, blockIdx.x * 8 + w * 2 + 1, lane);
  const int wm = w >> 1, wn = w & 1;
  const int r32 = lane & 31, hh = lane >> 5;
  STAMP_AT(0, 0);
  unsigned xa[3][4], wa[3][2][2];
  {
    const int row = wm * 64 + r32;
    const int blk = (lane >> 4) & 1, q = (lane & 15) >> 2, p = lane & 3;
#pragma unroll
    for (int hs = 0; hs < 3; ++hs) {
#pragma unroll
      for (int i = 0; i < 4; ++i) { xa[hs][i] = lds0 + hs * 65536 + (unsigned)(row * 128 + ((((i ^ wm) * 2 + hh) ^ ((row >> 1) & 7)) << 4)); pin(xa[hs][i]); }
#pragma unroll
      for (int j = 0; j < 2; ++j) {
        const unsigned base = lds0 + hs * 65536 + (unsigned)(16384 + wn * 8192 + (8 * hh + q) * 128 + (((j * 4 + blk * 2 + (p >> 1)) ^ (((q >> 1) & 1) << 2)) << 4) + (p & 1) * 8);
        wa[hs][j][0] = base + wm * 2048; wa[hs][j][1] = base + (1 ^ wm) * 2048; pin(wa[hs][j][0]); pin(wa[hs][j][1]);
      }
    }
  }
  f32x16 acc[2][2] = {};
  f32x4 sw[2] = {};
  u32x4 rec;
  bf16x8 sel = (bf16x8)(short)(((lane & 15) == ((lane >> 4) & 1)) ? 0x3f80 : 0); pin(sel);
  bf16x8 fx[2][2]; s16x4 fwl[2][2], fwh[2][2];
  auto rd = [&](auto i_, auto k_) {
    constexpr int i = i_, k = k_, s = i >> 2, p = i & 3, sl = s % 5, hs = sl >> 1; constexpr unsigned so = (sl & 1) * K1_STAGE;
    if constexpr (k == 0) fx[i & 1][0] = lds128(xa[hs][p] + so);
    if constexpr (k == 3) fx[i & 1][1] = lds128(xa[hs][p] + so + 4096);
    if constexpr (k == 1 || k == 2) { fwl[i & 1][k - 1] = ldstr(wa[hs][k - 1][p & 1] + so + (p >> 1) * 4096); fwh[i & 1][k - 1] = ldstr(wa[hs][k - 1][p & 1] + so + (p >> 1) * 4096 + 512); }
  };
  const bool isq = cb < 5;
  const int pitch = isq ? 1280 : 2560;
  unsigned off[2][ISV ? 1 : 4];
#pragma unroll
  for (int j = 0; j < 2; ++j) {
    if constexpr (ISV) {
      const int n = cb * 128 + wn * 64 + j * 32 + r32 - 1280, head = n / 20, d0 = n - head * 20;
      const int d = (head & 1) ? (d0 < 4 ? d0 + 16 : d0 - 4) : d0;
      off[j][0] = (unsigned)(head * 163840 + 1280 + (hh * 20 + d) * 16); pin(off[j][0]);
    } else {
#pragma unroll
      for (int g = 0; g < 4; ++g) {
        const int n = cb * 128 + wn * 64 + j * 32 + 8 * g + 4 * hh - (isq ? 0 : 640), head = n / 20, d0 = n - head * 20;
        const int d = (head & 1) ? (d0 < 4 ? d0 + 16 : d0 - 4) : d0;
        off[j][g] = (unsigned)(head * 64 * pitch + (d < 16 ? (((d >> 3) * 32 + r32) * 16 + (d & 4) * 2) : (1024 + r32 * 8))); pin(off[j][g]);
      }
    }
  }
  waitv<0>(); pin(ep); tag = make_tag(ep);
  rs_store(rs[0], ws, blockIdx.x * 8 + w * 2, lane, tag); rs_store(rs[1], ws, blockIdx.x * 8 + w * 2 + 1, lane, tag);
  asm volatile("s_barrier" ::: "memory");
  STAMP_AT(0, 1);
  sfor<4>([&](auto k) { rd(IC<0>{}, k); });
  sfor<40>([&](auto i_) {
    constexpr int i = i_, b = i & 1;
    if constexpr ((i & 3) == 3 && i < 39) asm volatile("s_barrier" ::: "memory");
    if constexpr (i == 30) gld16<0>(rec, ws + WS_RST + (size_t)(rb * 128 + wm * 64) * 16, (unsigned)(lane * 16));
    SB();
    const bf16x8 w0 = __builtin_shufflevector(fwl[b][0], fwh[b][0], 0, 1, 2, 3, 4, 5, 6, 7), w1 = __builtin_shufflevector(fwl[b][1], fwh[b][1], 0, 1, 2, 3, 4, 5, 6, 7);
    sfor<4>([&](auto k_) { constexpr int k = k_, t = k >> 1, j = (k ^ (k >> 1)) & 1;
      if constexpr (ISV) acc[t][j] = MFMA32(fx[b][t], j ? w1 : w0, acc[t][j]); else acc[t][j] = MFMA32(j ? w1 : w0, fx[b][t], acc[t][j]);
      SB();
      if constexpr (i < 39) rd(IC<i + 1>{}, k_);
      SB(); });
    if constexpr (b == 1) { sw[0] = MFMA16(w0, sel, sw[0]); sw[1] = MFMA16(w1, sel, sw[1]); }
    SB();
  });
  STAMP_AT(0, 11);
  LAS float* csp = (LAS float*)(uintptr_t)lds0;
  if ((lane & 15) < 2) sfor<2>([&](auto j) { *(LAS f32x4*)&csp[wm * 128 + wn * 64 + j * 32 + (lane & 1) * 16 + (lane >> 4) * 4] = sw[j]; });
  waitv<0>(); pin(rec);
#pragma unroll 1
  for (int spin = 0; spin < (1 << 17) && !__all((rec[2] ^ rec[0]) == tag[0] && (rec[3] ^ rec[1]) == tag[1]); ++spin) {
    gld16<0>(rec, ws + WS_RST + (size_t)(rb * 128 + wm * 64) * 16, (unsigned)(lane * 16)); waitv<0>(); pin(rec);
  }
  LAS f32x2* st = (LAS f32x2*)(uintptr_t)(lds0 + 4096 + w * 512);
  if constexpr (ISV) st[lane] = (f32x2){__uint_as_float(rec[0]), __uint_as_float(rec[1])};
  bar();
  STAMP_AT(0, 12);
  if constexpr (ISV) {
    float c[2];
#pragma unroll
    for (int j = 0; j < 2; ++j) c[j] = csp[wn * 64 + j * 32 + r32] + csp[128 + wn * 64 + j * 32 + r32];
#pragma unroll
    for (int t = 0; t < 2; ++t) {
      f32x4 s01[4], s23[4];
#pragma unroll
      for (int g = 0; g < 4; ++g) { s01[g] = *(const LAS f32x4*)&st[t * 32 + 8 * g + 4 * hh]; s23[g] = *(const LAS f32x4*)&st[t * 32 + 8 * g + 4 * hh + 2]; }
      const char* dst = ws + WS_KV + (size_t)(rb * 4 + wm * 2 + t) * 2560;
#pragma unroll
      for (int j = 0; j < 2; ++j) {
        const f32x16& a = acc[t][j];
        unsigned pk[8];
#pragma unroll
        for (int g = 0; g < 4; ++g) {
          pk[2 * g]     = pk_bf16(s01[g][1] * (a[4 * g] - s01[g][0] * c[j]), s01[g][3] * (a[4 * g + 1] - s01[g][2] * c[j]));
          pk[2 * g + 1] = pk_bf16(s23[g][1] * (a[4 * g + 2] - s23[g][0] * c[j]), s23[g][3] * (a[4 * g + 3] - s23[g][2] * c[j]));
        }
        st16s(dst, off[j][0], (u32x4){pk[0], pk[1], pk[2], pk[3]});
        st16s(dst + 640, off[j][0], (u32x4){pk[4], pk[5], pk[6], pk[7]});
      }
    }
  } else {
    const float sc = isq ? QSCALE : 1.f;
    const char* base = ws + (isq ? WS_Q : WS_KV) + (size_t)(rb * 4 + wm * 2) * pitch;
    const auto pm = __builtin_amdgcn_permlane32_swap(rec[0], rec[0], false, false), pr = __builtin_amdgcn_permlane32_swap(rec[1], rec[1], false, false);
#pragma unroll
    for (int j = 0; j < 2; ++j)
#pragma unroll
      for (int g = 0; g < 4; ++g) {
        const int nl = wn * 64 + j * 32 + 8 * g + 4 * hh;
        const f32x4 c = *(const LAS f32x4*)&csp[nl] + *(const LAS f32x4*)&csp[128 + nl];
#pragma unroll
        for (int t = 0; t < 2; ++t) {
          const f32x16& a = acc[t][j];
          const float rs_ = __uint_as_float(pr[t]) * sc, mr = __uint_as_float(pm[t]);
          st8s(base + t * pitch, off[j][g], (u32x2){pk_bf16(rs_ * (a[4 * g] - mr * c[0]), rs_ * (a[4 * g + 1] - mr * c[1])), pk_bf16(rs_ * (a[4 * g + 2] - mr * c[2]), rs_ * (a[4 * g + 3] - mr * c[3]))});
        }
      }
  }
  STAMP_AT(0, 13);
  waitv<0>();
  if (lane < 16) st8(ws + WS_CTL + CTL_F1 + ((cb * 4 + wn * 2 + (lane >> 3)) * 32 + rb * 2 + wm) * 8 + (lane & 7) * CTL_REP, tag);
}

DI void p1_repack(const u16* __restrict__ Wo, char* __restrict__ ws, const int w, int idx) {
  const int lane = fresh_lane();
  const unsigned pvo = (lane >> 4) * 8 * 1280 + (lane & 15) * 2;
  unsigned pw[7][8];
  sfor<7>([&](auto r) {
    const int f = idx * 50 + min(w + 8 * r, 49), cbk = f / 100, rem = f - cbk * 100, ks = rem / 5, ct = rem - ks * 5;
    const char* src = (const char*)(Wo + (size_t)(32 * ks) * 640 + 80 * cbk + 16 * ct);
    sfor<8>([&](auto i) { gldu16<(i & 3) * 1280>(pw[r][i], src + (i & ~3) * 1280, pvo); });
  });
  sfor<7>([&](auto r) {
    waitv<(6 - r) * 8>();
    sfor<8>([&](auto j) { pin(pw[r][j]); });
    const u32x4 v = {pw[r][0] | (pw[r][1] << 16), pw[r][2] | (pw[r][3] << 16), pw[r][4] | (pw[r][5] << 16), pw[r][6] | (pw[r][7] << 16)};
    pw[r][0] = v[0]; pw[r][1] = v[1]; pw[r][2] = v[2]; pw[r][3] = v[3];
  });
  sfor<7>([&](auto r) {
    if (w + 8 * r < 50) st16(ws + WS_WP + (size_t)(idx * 50 + w + 8 * r) * 1024 + lane * 16, (u32x4){pw[r][0], pw[r][1], pw[r][2], pw[r][3]});
  });
}

template <int OFF> DI void rdK(bf16x8& k0, s16x4& k1, unsigned a0, unsigned a1) {
  asm volatile("ds_read_b128 %0, %2 offset:%4\n\tds_read_b64 %1, %3 offset:%4" : "=&v"(k0), "=&v"(k1) : "v"(a0), "v"(a1), "i"(OFF));
}
template <int OFF> DI void rdV(bf16x8& v0, bf16x8& v1, unsigned a, unsigned long long msk) {
  asm volatile("s_mov_b64 exec, %5\n\tds_read_b128 %0, %2 offset:%3\n\tds_read_b128 %1, %2 offset:%4\n\ts_mov_b64 exec, -1"
               : "+v"(v0), "+v"(v1) : "v"(a), "i"(OFF), "i"(OFF + 640), "s"(msk));
}
#define EX2(v) __builtin_amdgcn_exp2f(v)
constexpr int wo_off(int p) { return (p < 60 ? p : p + 20) * 1024; }
constexpr int gu_off(int f) { return (f < 20 ? f + 60 : f < 60 ? f + 100 : f - 60) * 1024; }
constexpr int wd_off(int f) { return ((f / 20 + 4) % 8) * 20480 + (f % 20) * 1024; }
constexpr int CSF_OFF = 116 * 1024, XCH_OFF = 120 * 1024;

DI void p2_attn(char* __restrict__ ws, const u16* __restrict__ Wg, const u16* __restrict__ Wu, const u16* __restrict__ Wd,
                const unsigned lds0, const int w, const u32x2 tag, const int rb3, const int cb3, const int rb4, const int cb4) {
  const int lane = fresh_lane();
  const int kh = w >> 2, wq = w & 3;
  const int head = (blockIdx.x & 7) * 4 + ((blockIdx.x >> 3) & 3), qb = blockIdx.x >> 5;
  const int r32 = lane & 31, hh = lane >> 5;
  const char* kvg = ws + WS_KV + (size_t)head * 163840 + (size_t)kh * 81920 + wq * 1024;
  const unsigned voff = lane * 16;
  const unsigned kvl = lds0 + kh * 81920 + wq * 1024;
  char* ctl = ws + WS_CTL;
  const char* ctr = ctl + (blockIdx.x & 7) * CTL_REP;
  unsigned pw[3][8];
  const int gw = blockIdx.x * 8 + w, gwc = min(gw, 1919);
  const char* psrc[3];
#pragma unroll
  for (int r = 0; r < 2; ++r) {
    const int f = 2 * gwc + r, cbk = f / 120, rem = f - cbk * 120, ks = rem / 6, ct = rem - ks * 6;
    psrc[r] = (const char*)((ct < 3 ? Wg : Wu) + (size_t)(32 * ks) * 1536 + 48 * cbk + 16 * (ct < 3 ? ct : ct - 3));
  }
  { const int f = gwc, cbk = f / 240, rem = f - cbk * 240, ks = rem / 5, ct = rem - ks * 5;
    psrc[2] = (const char*)(Wd + (size_t)(32 * ks) * 640 + 80 * cbk + 16 * ct); }
  unsigned pvo1 = (lane >> 4) * 8 * 3072 + (lane & 15) * 2, pvo2 = (lane >> 4) * 8 * 1280 + (lane & 15) * 2; pin(pvo1); pin(pvo2);
  const char* wog = ws + WS_WP + (size_t)cb3 * 102400;
  const int qtile = qb * 8 + wq * 2 + kh, cg0 = head * 5 + (head & 1), cgt = (head & 1) ? head * 5 : head * 5 + 4;
  const char* ob = ws + WS_O + (size_t)qtile * 40960;
  const char* obs[3] = {ob + (cg0 >> 3) * 1024 + ((cg0 & 7) >> 1) * 256, ob + ((cg0 + 2) >> 3) * 1024 + (((cg0 + 2) & 7) >> 1) * 256,
                        ob + (cgt >> 3) * 1024 + ((cgt & 7) >> 1) * 256 + (cgt & 1) * 8};
  unsigned vo = (unsigned)((r32 >> 4) * 20480 + (r32 & 15) * 16 + hh * 8); pin(vo);
  u32x4 fill = {0xffffffffu, 0xffffffffu, 0xffffffffu, 0xffffffffu}; pin(fill);
  const u32x2 fill2 = {fill[0], fill[1]};
  u32x2 fa, fb, fc;
  const unsigned fvc = (unsigned)CTL_FR + (unsigned)((cb4 * 60 + min(lane, 59)) * 8);
  const unsigned fva = (unsigned)(hh ? CTL_F4 : CTL_F2) + (unsigned)(((rb3 * 2 + ((w & 3) >> 1)) * 32 + r32) * 8), fvb = (unsigned)CTL_F3 + (unsigned)(((rb4 * 16 + w * 2) * 8 + (lane & 15)) * 8);
  const char* gcl = ws + WS_G + (size_t)(rb4 * 16 + w * 2) * 49152 + cb4 * 1536;
  const char* hcl = ws + WS_H + (size_t)(rb3 * 4 + w) * 20480 + cb3 * 2560;
  const char* rdst = ws + WS_WP + (size_t)(NFR_WO + 2 * gw) * 1024;
  pins(psrc[0]); pins(psrc[1]); pins(psrc[2]); pins(obs[0]); pins(obs[1]); pins(obs[2]); pins(wog); pins(gcl); pins(hcl); pins(rdst);
  STAMP_AT(1, 0);
  unsigned f0;
  {
    const int cfl = (20 * head) >> 5, cfh = (20 * head + 19) >> 5;
    f0 = (unsigned)(((((lane & 32) ? 40 : 20) + ((lane & 16) ? cfh : cfl)) * 32 + kh * 16 + (lane & 15)) * 8);
    const unsigned f1 = (unsigned)((lane < 32 ? (((lane & 1) ? cfh : cfl) * 32 + qb * 4 + wq) : 1920 + cb3 * 16 + (lane & 15)) * 8);
    poll2(ctr + CTL_F1, f0 & ~(12u * 8u), f1, tag);
  }
  STAMP_AT(1, 4);
#pragma unroll
  for (int i = 0; i < 4; ++i) glds16<true>(kvg + i * 4096, voff, kvl + i * 4096);
  bf16x8 q0[2]; s16x4 q1[2];
  {
    const char* qg = ws + WS_Q + ((size_t)head * 64 + qb * 8 + wq * 2) * 1280;
    gld16<0>(q0[0], qg, voff); gld16<1280>(q0[1], qg, voff); gld8<1024>(q1[0], qg, r32 * 8); gld8<2304>(q1[1], qg, r32 * 8);
  }
  const unsigned ka0 = lds0 + kh * 81920 + lane * 16, ka1 = lds0 + kh * 81920 + 1024 + r32 * 8, va = lds0 + kh * 81920 + 1280 + (hh * 20 + (r32 < 20 ? r32 : 0)) * 16;
  bf16x8 K0[2], V0[2], V1[2]; s16x4 K1[2];
  V0[0] = V0[1] = V1[0] = V1[1] = (bf16x8)(short)(r32 == 20 ? 0x3f80 : 0);
  pin(V0[0]); pin(V0[1]); pin(V1[0]); pin(V1[1]);
  WAITV_BAR(0);
  u32x2 fr; gld8<0>(fr, ctr + CTL_F1, f0);
  pin(q0[0]); pin(q0[1]); pin(q1[0]); pin(q1[1]);
  if (hh) { q1[0] = (s16x4){0, 0, 0, 0}; q1[1] = (s16x4){0, 0, 0, 0}; }
  STAMP_AT(1, 1);
  rdK<0>(K0[0], K1[0], ka0, ka1);
  const unsigned long long vmsk = 0x000fffff000fffffull;
  rdV<0>(V0[0], V1[0], va, vmsk);
  f32x16 cm[2];
  {
    const bf16x8 kf0 = lds128(lds0 + lane * 16); const s16x4 kf1 = lds64(lds0 + 1024 + r32 * 8);
#pragma unroll
    for (int t = 0; t < 2; ++t) {
      f32x16 s = MFMA32(kf0, q0[t], (f32x16){}); s = MFMA32K8(kf1, q1[t], s);
      float m = s[0];
#pragma unroll
      for (int i = 1; i < 16; ++i) m = fmaxf(m, s[i]);
      m = max32(m);
      cm[t] = (f32x16)(-m);
      pin(cm[t]);
    }
  }
  asm volatile("s_waitcnt lgkmcnt(0)" : "+v"(K0[0]), "+v"(K1[0]), "+v"(V0[0]), "+v"(V1[0]));
  f32x16 o[2] = {};
  f32x16 SA, SB_;
  u32x4 PA0, PA1, PB0 = {}, PB1 = {};
  SA = MFMA32(K0[0], q0[0], cm[0]); SA = MFMA32K8(K1[0], q1[0], SA);
  waitv<0>(); pin(fr);
  if (!__all(fr[0] == tag[0] && fr[1] == tag[1])) poll(ctr + CTL_F1, f0, tag);
  STAMP_AT(1, 2);
  pin(o[0]); pin(o[1]); pin(PB0); pin(PB1);
#define GRP(MF, Sc, Pc, i, j) do { MF; SB(); const float e0 = EX2(Sc[i]), e1 = EX2(Sc[i + 1]), e2 = EX2(Sc[i + 2]), e3 = EX2(Sc[i + 3]); \
    Pc[j] = pk_bf16(e0, e1); Pc[j + 1] = pk_bf16(e2, e3); pin(Pc); SB(); } while (0)
#define SLOT(QK, Sc, Pc0, Pc1, Sn, kb, jn, Pp0, Pp1, vb, jp) do { \
    GRP(if constexpr (QK) Sn = MFMA32(K0[kb], q0[jn], cm[jn]), Sc, Pc0, 0, 0); \
    GRP(if constexpr (QK) Sn = MFMA32K8(K1[kb], q1[jn], Sn), Sc, Pc0, 4, 2); \
    GRP(o[jp] = MFMA32(V0[vb], __builtin_bit_cast(bf16x8, Pp0), o[jp]), Sc, Pc1, 8, 0); \
    GRP(o[jp] = MFMA32(V1[vb], __builtin_bit_cast(bf16x8, Pp1), o[jp]), Sc, Pc1, 12, 2); } while (0)
#define LWAIT(kb, vb) asm volatile("s_waitcnt lgkmcnt(0)" : "+v"(K0[kb]), "+v"(K1[kb]), "+v"(V0[vb]), "+v"(V1[vb]))
  auto rstore = [&](auto r_, auto yv_) {
    constexpr int r = r_;
    waitv<yv_>();
    sfor<8>([&](auto j) { pin(pw[r][j]); });
    const u32x4 v = {pw[r][0] | (pw[r][1] << 16), pw[r][2] | (pw[r][3] << 16), pw[r][4] | (pw[r][5] << 16), pw[r][6] | (pw[r][7] << 16)};
    if (gw < 1920) {
      if constexpr (r < 2) {
        st16s(rdst + r * 1024, voff, v);
        float c = 0.f;
#pragma unroll
        for (int j = 0; j < 4; ++j) c = dot2(v[j], 0x3f803f80u, c);
        c = red4(c);
        if (lane < 16) st4(ws + WS_CSP + (size_t)(2 * gw + r) * 64 + lane * 4, c);
      } else st16s(ws + WS_WP + (size_t)(NFR_WO + NFR_GU + gw) * 1024, voff, v);
    }
  };
  auto hook = [&](auto it_, auto i_) {
    constexpr int it = it_, i = i_, n = (it - 2) * 8 + i;
    if constexpr (n >= 0 && n < 2) st8s(obs[n], vo, fill2);
    if constexpr (n == 2) { if (!hh) st8s(obs[2], vo, fill2); }
    if constexpr (n >= 3 && n < 6) { const int L = (n - 3) * 64 + lane; st16s(gcl, (unsigned)(L * 16 + (L >= 96 ? 49152 - 1536 : 0)), fill); }
    if constexpr (n >= 6 && n < 9) { if (w < 4) st16s(hcl, (unsigned)(min((n - 6) * 64 + lane, 159) * 16), fill); }
    if constexpr (n == 9) { if (w < 4 && lane < 16) st8s(ws + WS_HST + (size_t)(rb3 * 4 + w) * 1024 + (cb3 >> 1) * 256 + (cb3 & 1) * 8, (unsigned)(lane * 16), fill2); }
    if constexpr (it == 5 && i == 6) {
      waitv<0>();
      setflag(ctl + CTL_FR + gw * 8, tag, lane);
      setflag(ctl + CTL_F2 + (qtile * 32 + head) * 8, tag, lane);
      setflag(ctl + CTL_F4 + ((rb4 * 8 + w) * 32 + cb4) * 8, tag, lane);
      if (w < 4) setflag(ctl + CTL_F3 + ((rb3 * 4 + w) * 8 + cb3) * 8, tag, lane);
    }
    if constexpr (it == 7 && i == 6) { gld8<0>(fa, ctr, fva); gld8<0>(fb, ctr, fvb); gld8<0>(fc, ctr, fvc); }
    if constexpr (it < 2) glds16<true>(kvg + (size_t)(it * 8 + 4 + i) * 4096, voff, kvl + (it * 8 + 4 + i) * 4096);
    else if constexpr (it < 4) gldu16<(i & 1) * 3072>(pw[it - 2][i], psrc[it - 2] + (i & ~1) * 3072, pvo1);
    else if constexpr (it == 4) {
      gldu16<(i & 3) * 1280>(pw[2][i], psrc[2] + (i & ~3) * 1280, pvo2);
      if constexpr (i == 0) rstore(IC<0>{}, IC<9>{});
      if constexpr (i == 7) rstore(IC<1>{}, IC<8>{});
    } else if constexpr (it == 5) {
      if constexpr (i == 7) rstore(IC<2>{}, IC<3>{});
    } else {
      constexpr int j = (it - 6) * 8 + i;
      const char* src = wog + (w + 8 * j) * 1024;
      if constexpr (j < 7) glds16<true>(src, voff, lds0 + w * 1024 + j * 8192);
      else if constexpr (j == 7) glds16<true>(src, voff, lds0 + w * 1024 + j * 8192 + (w >= 4 ? 20480 : 0));
      else if constexpr (j < 12) glds16<true>(src, voff, lds0 + w * 1024 + j * 8192 + 20480);
      else if constexpr (j == 12) { if (w < 4) glds16<true>(src, voff, lds0 + w * 1024 + j * 8192 + 20480); }
    }
    SB();
  };
  const unsigned ka0b = ka0 + 40960, ka1b = ka1 + 40960, vab = va + 40960;
  sfor<8>([&](auto it_) {
    constexpr int it = it_;
    if constexpr (it == 1) WAITV_BAR(6);
    if constexpr (it == 2) { WAITV_BAR(8); STAMP_AT(1, 3); }
    if constexpr (it == 4) asm volatile("s_waitcnt vmcnt(16)\n\ts_barrier" ::: "memory");
    if constexpr (it == 6) asm volatile("s_barrier" ::: "memory");
    if (((it ^ kh) & 1) == 0) __builtin_amdgcn_s_setprio(0); else __builtin_amdgcn_s_setprio(1);
    constexpr int o0 = (it & 3) * 10240;
    constexpr int on = it == 3 ? 0 : o0 + 10240;
    const unsigned a0 = it < 4 ? ka0 : ka0b, a1 = it < 4 ? ka1 : ka1b, av = it < 4 ? va : vab;
    const unsigned n0 = it < 3 ? ka0 : ka0b, n1 = it < 3 ? ka1 : ka1b, nv = it < 3 ? va : vab;
    rdK<o0 + 2560>(K0[1], K1[1], a0, a1); hook(it_, IC<0>{});                  SLOT(true, SA, PA0, PA1, SB_, 0, 1, PB0, PB1, 1, 1);
    LWAIT(1, 0); rdV<o0 + 2560>(V0[1], V1[1], av, vmsk); hook(it_, IC<1>{});   SLOT(true, SB_, PB0, PB1, SA, 1, 0, PA0, PA1, 0, 0);
    rdK<o0 + 5120>(K0[0], K1[0], a0, a1); hook(it_, IC<2>{});                  SLOT(true, SA, PA0, PA1, SB_, 1, 1, PB0, PB1, 0, 1);
    LWAIT(0, 1); rdV<o0 + 5120>(V0[0], V1[0], av, vmsk); hook(it_, IC<3>{});   SLOT(true, SB_, PB0, PB1, SA, 0, 0, PA0, PA1, 1, 0);
    rdK<o0 + 7680>(K0[1], K1[1], a0, a1); hook(it_, IC<4>{});                  SLOT(true, SA, PA0, PA1, SB_, 0, 1, PB0, PB1, 1, 1);
    LWAIT(1, 0); rdV<o0 + 7680>(V0[1], V1[1], av, vmsk); hook(it_, IC<5>{});   SLOT(true, SB_, PB0, PB1, SA, 1, 0, PA0, PA1, 0, 0);
    if constexpr (it < 7) rdK<on>(K0[0], K1[0], n0, n1);
    hook(it_, IC<6>{});                                                        SLOT(true, SA, PA0, PA1, SB_, 1, 1, PB0, PB1, 0, 1);
    LWAIT(0, 1);
    if constexpr (it < 7) rdV<on>(V0[0], V1[0], nv, vmsk);
    hook(it_, IC<7>{});                                                        SLOT(it < 7, SB_, PB0, PB1, SA, 0, 0, PA0, PA1, 1, 0);
  });
  __builtin_amdgcn_s_setprio(0);
  o[1] = MFMA32(V0[1], __builtin_bit_cast(bf16x8, PB0), o[1]);
  o[1] = MFMA32(V1[1], __builtin_bit_cast(bf16x8, PB1), o[1]);
  STAMP_AT(1, 9);
  bar();
  {
    LAS f32x4* xch = (LAS f32x4*)(uintptr_t)(lds0 + XCH_OFF + w * 3072);
    const f32x16& snd = kh ? o[0] : o[1];
#pragma unroll
    for (int g = 0; g < 3; ++g) xch[g * 64 + lane] = (f32x4){snd[4 * g], snd[4 * g + 1], snd[4 * g + 2], snd[4 * g + 3]};
  }
  bar();
  STAMP_AT(1, 10);
  float of[12];
  {
    const LAS f32x4* xch = (const LAS f32x4*)(uintptr_t)(lds0 + XCH_OFF + (w ^ 4) * 3072);
    const f32x16& own = kh ? o[1] : o[0];
#pragma unroll
    for (int g = 0; g < 3; ++g) { const f32x4 v = xch[g * 64 + lane];
#pragma unroll
      for (int j = 0; j < 4; ++j) of[4 * g + j] = own[4 * g + j] + v[j]; }
  }
  float l = hi32(of[8]);
  bool ok = (l > 8.67e-19f) && (l < 1.15e18f);
#pragma unroll
  for (int i = 0; i < 8; ++i) ok = ok && (fabsf(of[i]) < 1e37f);
  if (!hh) {
#pragma unroll
    for (int i = 8; i < 12; ++i) ok = ok && (fabsf(of[i]) < 1e37f);
  }
  if (__builtin_expect(!__all(ok), 0)) {
    __builtin_amdgcn_fence(__ATOMIC_ACQUIRE, "agent");
    const char* kv = ws + WS_KV + (size_t)head * 163840;
    const bf16x8 fq0 = kh ? q0[1] : q0[0];
    const s16x4 fq1 = kh ? q1[1] : q1[0];
    float m = -INFINITY;
    for (int t = 0; t < 64; ++t) {
      const bf16x8 kf0 = *(const bf16x8*)(kv + t * 2560 + lane * 16); const s16x4 kf1 = *(const s16x4*)(kv + t * 2560 + 1024 + r32 * 8);
      f32x16 s = MFMA32(kf0, fq0, (f32x16){}); s = MFMA32K8(kf1, fq1, s);
#pragma unroll
      for (int i = 0; i < 16; ++i) m = fmaxf(m, s[i]);
    }
    m = max32(m);
    f32x16 oo = {};
    const int vr = r32 < 20 ? r32 : 0;
    for (int t = 0; t < 64; ++t) {
      const bf16x8 kf0 = *(const bf16x8*)(kv + t * 2560 + lane * 16); const s16x4 kf1 = *(const s16x4*)(kv + t * 2560 + 1024 + r32 * 8);
      bf16x8 vf0 = *(const bf16x8*)(kv + t * 2560 + 1280 + (hh * 20 + vr) * 16), vf1 = *(const bf16x8*)(kv + t * 2560 + 1920 + (hh * 20 + vr) * 16);
      if (r32 >= 20) { vf0 = (bf16x8)(short)0x3f80; vf1 = vf0; }
      f32x16 s = MFMA32(kf0, fq0, (f32x16){}); s = MFMA32K8(kf1, fq1, s);
      u32x4 p0, p1;
#pragma unroll
      for (int i = 0; i < 4; ++i) {
        p0[i] = pk_bf16(EX2(s[2 * i] - m), EX2(s[2 * i + 1] - m));
        p1[i] = pk_bf16(EX2(s[8 + 2 * i] - m), EX2(s[8 + 2 * i + 1] - m));
      }
      oo = MFMA32(vf0, __builtin_bit_cast(bf16x8, p0), oo);
      oo = MFMA32(vf1, __builtin_bit_cast(bf16x8, p1), oo);
    }
#pragma unroll
    for (int i = 0; i < 12; ++i) of[i] = oo[i];
    l = hi32(of[8]);
  }
  const float rl = __builtin_amdgcn_rcpf(l);
  waitv<0>(); pin(fa); pin(fb); pin(fc);
  setflag(ctl + CTL_FD + gw * 8, tag, lane);
#pragma unroll
  for (int g = 0; g < 3; ++g)
    if (g < 2 || !hh) st8s(obs[g], vo, nofill(pk_bf16(of[4 * g] * rl, of[4 * g + 1] * rl), pk_bf16(of[4 * g + 2] * rl, of[4 * g + 3] * rl)));
  STAMP_AT(1, 11);
  if (!__all(fa[0] == tag[0] && fa[1] == tag[1] && fb[0] == tag[0] && fb[1] == tag[1])) poll2(ctr, fva, fvb, tag);
  if (!__all(fc[0] == tag[0] && fc[1] == tag[1])) poll(ctr, fvc, tag);
  STAMP_AT(1, 12);
}

constexpr int x_none(int) { return 0; }
constexpr int x_p4(int i) { return ((i >= 2 && i < 7) || (i >= 10 && i < 18)) ? 1 : 0; }
template <int NSTEP, int NF, int LA, int LL, int CG, int NA, int ASTR, int (*BOFF)(int), int (*XCNT)(int), class PRE, class MM>
DI void gemm_res(const char* asrc, unsigned lds0, unsigned voff, PRE&& pre, MM&& mm) {
  bf16x8 a[NSTEP][NA];
  bf16x8 bf[LL + 1][NF];
  unsigned rd0 = lds0 + voff, rd1 = rd0 + 65536, rd2 = rd0 + 131072; pin(rd0); pin(rd1); pin(rd2);
  auto issue = [&](auto n_) { constexpr int n = n_; sfor<NA>([&](auto j) { gld16<(n & 3) * 1024>(a[n][j], asrc + (n >> 2) * 4096 + j * ASTR, voff); }); };
  constexpr auto xsum = [](int lo, int hi) { int c = 0; for (int i = lo < 0 ? 0 : lo; i <= hi; ++i) c += XCNT(i); return c; };
  auto rd = [&](auto i_, auto f_) {
    constexpr int i = i_, f = f_, off = BOFF(i * NF + f);
    bf[i % (LL + 1)][f] = lds128((off < 65536 ? rd0 : off < 131072 ? rd1 : rd2) + (off & 65535));
  };
  static_assert(NSTEP % CG == 0 && LA > CG, "groups");
  unsigned m = 0;
  auto fold = [&](auto n_) { constexpr int n = n_;
    sfor<NA>([&](auto j) { pin(a[n][j]); const u32x4 u = __builtin_bit_cast(u32x4, a[n][j]); m = max(max(m, u[0]), u[1]); m = max(max(m, u[2]), u[3]); });
    pin(m);
    if constexpr (n % CG == CG - 1) {
      if (__builtin_expect(__any(m == 0xffffffffu), 0)) {
#pragma unroll 1
        for (int spin = 0; spin < (1 << 17); ++spin) {
          RETRY_COUNT(NSTEP + NF);
          sfor<CG>([&](auto k) { issue(IC<n - CG + 1 + k>{}); });
          waitv<0>(); m = 0;
          sfor<CG>([&](auto k) { sfor<NA>([&](auto j) { pin(a[n - CG + 1 + k][j]); m = max(m, umax4(__builtin_bit_cast(u32x4, a[n - CG + 1 + k][j]))); }); });
          if (!__any(m == 0xffffffffu)) break;
        }
      }
    }
  };
  sfor<LA>([&](auto n) { issue(n); });
  pre(IC<-1>{});
  sfor<LL>([&](auto i) { sfor<NF>([&](auto f) { rd(i, f); }); });
  waitv<(LA - CG) * NA>(); sfor<CG>([&](auto n) { fold(n); });
  sfor<NSTEP>([&](auto i_) {
    constexpr int i = i_;
    pre(i_);
    SB();
    sfor<NF>([&](auto f_) {
      constexpr int f = f_;
      if constexpr (f == NF - 1 && i + CG < NSTEP) {
        waitv<((i + LA < NSTEP ? i + LA : NSTEP - 1) - (i + CG)) * NA + xsum(i + CG - LA + 1, i)>(); fold(IC<i + CG>{}); SB(); }
      mm(f_, bf[i % (LL + 1)][f], a[i]);
      SB();
      if constexpr (i + LL < NSTEP) rd(IC<i + LL>{}, f_);
      if constexpr (f == 0 && i + LA < NSTEP) issue(IC<i + LA>{});
      SB();
    });
  });
}

__global__ __launch_bounds__(512, 2) void k_mega(const u16* __restrict__ x, const u16* __restrict__ Wqkv, const u16* __restrict__ Wo,
                                                 const u16* __restrict__ Wg, const u16* __restrict__ Wu, const u16* __restrict__ Wd,
                                                 char* __restrict__ ws, u16* __restrict__ out) {
  __shared__ __attribute__((aligned(16))) char smem[163840];
  const unsigned lds0 = lds_addr_of(smem);
  const int w = __builtin_amdgcn_readfirstlane(threadIdx.x >> 6);
  const int g8 = blockIdx.x & 7, i32 = blockIdx.x >> 3;
  char* ctl = ws + WS_CTL;
  const char* ctr = ctl + g8 * CTL_REP;
  u32x2 tag;
  if (i32 < 30) {
    const int i = (g8 & 1) * 30 + i32, rb = (g8 >> 1) * 4 + (i & 3), cb = i >> 2;
    if (w >= 4) { p1_load(x, Wqkv, lds0, w - 4, rb, cb); u32x2 ep; gld8<0>(ep, ctl, 0u); waitv<0>(); pin(ep); tag = make_tag(ep); }
    else if (cb >= 10) p1_body<true>(x, ws, lds0, w, rb, cb, tag);
    else p1_body<false>(x, ws, lds0, w, rb, cb, tag);
  } else {
    { RowStat rs; u32x2 ep; const int lane = fresh_lane(); gld8<0>(ep, ctl, 0u); rs_load(rs, x, blockIdx.x * 8 + w, lane); waitv<0>(); pin(ep); tag = make_tag(ep);
      rs_store(rs, ws, blockIdx.x * 8 + w, lane, tag); }
    p1_repack(Wo, ws, w, blockIdx.x - 240);
    publish(ctl + CTL_F1 + (1920 + (blockIdx.x - 240) * 8 + w) * 8, tag, fresh_lane());
  }
  bar();
  const int rb3 = g8 * 4 + (i32 >> 3), cb3 = i32 & 7;
  const int rb4 = (g8 >> 2) * 4 + (i32 >> 3), cb4 = (g8 & 3) * 8 + (i32 & 7);
  p2_attn(ws, Wg, Wu, Wd, lds0, w, tag, rb3, cb3, rb4, cb4);
  bar();
  const char* gug = ws + WS_WP + (size_t)NFR_WO * 1024 + (size_t)cb4 * 122880;
  const char* wdg = ws + WS_WP + (size_t)(NFR_WO + NFR_GU) * 1024 + (size_t)cb3 * 245760;
  u32x2 hp[5];
  if (w < 4) {
    const int lane = fresh_lane();
    const unsigned voff = lane * 16;
    const int row = rb3 * 64 + w * 16 + (lane & 15), col0 = cb3 * 80 + 4 * (lane >> 4);
    STAMP_AT(2, 0);
    u32x2 xr[5];
#pragma unroll
    for (int ct = 0; ct < 5; ++ct) xr[ct] = *(const u32x2*)(x + (size_t)row * 640 + col0 + 16 * ct);
    { const int hd = lane & 31, cgt = (hd & 1) ? hd * 5 : hd * 5 + 4;
      probe(ws + WS_O + (size_t)(rb3 * 4 + w) * 20480, (unsigned)((cgt >> 3) * 1024 + ((cgt & 7) >> 1) * 256 + (cgt & 1) * 8)); }
    sfor<5>([&](auto ct) { pin(xr[ct]); });
    STAMP_AT(2, 1);
    f32x4 acc[5] = {};
    sfor<5>([&](auto ct) { pin(acc[ct]); });
    gemm_res<20, 5, 20, 2, 4, 1, 0, wo_off, x_none>(ws + WS_O + (size_t)(rb3 * 4 + w) * 20480, lds0, voff,
      [&](auto i) { if constexpr (i == -1) STAMP_AT(2, 2); if constexpr (i == 1) STAMP_AT(2, 3); if constexpr (i == 10) STAMP_AT(2, 4);
        if constexpr (i == P3_G1 || i == 12) bar(); },
      [&](auto ct, const bf16x8& b, bf16x8 (&a)[1]) { mfma16(acc[ct], b, a[0]); });
    acc_fence(); sfor<5>([&](auto ct) { pin(acc[ct]); });
    STAMP_AT(2, 6);
    float s1 = 0.f, s2 = 0.f;
    const char* hb = ws + WS_H + (size_t)(rb3 * 4 + w) * 20480;
    const unsigned vt = t16lane(lane);
#pragma unroll
    for (int ct = 0; ct < 5; ++ct) {
      hp[ct] = nofill(pk_bf16(bf_lo(xr[ct][0]) + acc[ct][0], bf_hi(xr[ct][0]) + acc[ct][1]), pk_bf16(bf_lo(xr[ct][1]) + acc[ct][2], bf_hi(xr[ct][1]) + acc[ct][3]));
      st8s(hb + t16off(cb3 * 80 + ct * 16), vt, hp[ct]);
      s1 = dot2(hp[ct][0], 0x3f803f80u, s1); s1 = dot2(hp[ct][1], 0x3f803f80u, s1);
      s2 = dot2(hp[ct][0], hp[ct][0], s2); s2 = dot2(hp[ct][1], hp[ct][1], s2);
    }
    s1 = red4(s1); s2 = red4(s2);
    if (lane < 16) st8s(ws + WS_HST + (size_t)(rb3 * 4 + w) * 1024 + (cb3 >> 1) * 256 + (cb3 & 1) * 8, (unsigned)(lane * 16), nofill(__float_as_uint(s1), __float_as_uint(s2)));
    bar();
    STAMP_AT(2, 7);
  } else {
    const int lane = fresh_lane(), v = w - 4;
    const unsigned voff = lane * 16;
    float cpart[10];
    const int cc = v * 24 + min(lane & 31, 23);
    sfor<10>([&](auto i) { gld4<i * 384>(cpart[i], ws + WS_CSP, (unsigned)(cb4 * 7680 + (lane >> 5) * 3840 + cc * 4)); });
    poll(ctr + CTL_FD, (unsigned)((cb3 * 240 + v * 60 + min(lane, 59)) * 8), tag);
    float cv = 0.f;
    sfor<10>([&](auto j) { pin(cpart[j]); cv += cpart[j]; });
    cv = add32(cv);
    bar();
    STAMP_AT(2, 2);
    sfor<5>([&](auto j) { glds16<true>(gug + (v * 5 + j) * 1024, voff, lds0 + gu_off(0) + (v * 5 + j) * 1024); });
    sfor<10>([&](auto j) { glds16<true>(gug + (20 + v * 10 + j) * 1024, voff, lds0 + gu_off(20) + (v * 10 + j) * 1024); });
    bar();
    sfor<15>([&](auto j) { glds16<true>(gug + (60 + v * 15 + j) * 1024, voff, lds0 + gu_off(60) + (v * 15 + j) * 1024); });
    STAMP_AT(2, 5);
    waitv<0>();
    bar();
    STAMP_AT(2, 6);
    if (lane < 24) ((LAS float*)(uintptr_t)(lds0 + CSF_OFF))[cc] = cv;
  }
  {
    const int lane = fresh_lane();
    const unsigned voff = lane * 16;
    const int r16 = lane & 15, g4 = lane >> 4;
    STAMP_AT(3, 0);
    f32x4 cs[6]; f32x2 ms[2];
    f32x4 hs[2];
#pragma unroll 1
    for (int spin = 0; spin < (1 << 17); ++spin) {
      gld16<0>(hs[0], ws + WS_HST + (size_t)(rb4 * 16 + w * 2) * 1024, voff);
      gld16<1024>(hs[1], ws + WS_HST + (size_t)(rb4 * 16 + w * 2) * 1024, voff);
      waitv<0>(); pin(hs[0]); pin(hs[1]);
      if (!__any(max(umax4(__builtin_bit_cast(u32x4, hs[0])), umax4(__builtin_bit_cast(u32x4, hs[1]))) == 0xffffffffu)) break;
      __builtin_amdgcn_s_sleep(1);
    }
    STAMP_AT(3, 1);
    f32x4 acc[2][6] = {};
    sfor<6>([&](auto ct) { pin(acc[0][ct]); pin(acc[1][ct]); });
    if (w < 4) __builtin_amdgcn_s_setprio(1);
    gemm_res<20, 6, K4_LA_, 1, K4_CG_, 2, 20480, gu_off, x_p4>(ws + WS_H + (size_t)(rb4 * 16 + w * 2) * 20480, lds0, voff,
      [&](auto i) { if constexpr (i == -1) STAMP_AT(3, 2); if constexpr (i == 1) STAMP_AT(3, 3); if constexpr (i == 10) STAMP_AT(3, 4);
        if constexpr (i == 9) bar();
        if constexpr (i == -1) sfor<2>([&](auto t) {
          const float s1 = red4(hs[t][0] + hs[t][2]), s2 = red4(hs[t][1] + hs[t][3]), mean = s1 * (1.f / 640.f);
          ms[t] = (f32x2){mean, __builtin_amdgcn_rsqf(fmaxf(s2 * (1.f / 640.f) - mean * mean, 0.f) + LN_EPS)}; pin(ms[t]); });
        if constexpr (i == 18) sfor<6>([&](auto ct) { cs[ct] = *(const LAS f32x4*)(uintptr_t)(lds0 + CSF_OFF + (ct * 16 + 4 * g4) * 4); });
        if constexpr (i >= 2 && i < 7) { const int f = (i - 2) * 8 + w - ((i == 6 && w >= 4) ? 4 : 0); glds16<true>(wdg + (f) * 1024, voff, lds0 + wd_off(0) + f * 1024); }
        if constexpr (i >= 10 && i < 15) { constexpr int n = i - 10; glds16<true>(wdg + (40 + n * 8 + w) * 1024, voff, lds0 + wd_off(40) + (n * 8 + w) * 1024); }
        if constexpr (i >= 15 && i < 18) { const int f = (i - 15) * 8 + w - ((i == 17 && w >= 4) ? 4 : 0); glds16<true>(wdg + (140 + f) * 1024, voff, lds0 + wd_off(140) + f * 1024); } },
      [&](auto ct, const bf16x8& b, bf16x8 (&a)[2]) { mfma16(acc[0][ct], b, a[0]); mfma16(acc[1][ct], b, a[1]); });
    acc_fence(); sfor<6>([&](auto ct) { pin(acc[0][ct]); pin(acc[1][ct]); });
    STAMP_AT(3, 6);
    constexpr float GC0 = -2.f * 0.7978845608028654f * 1.4426950408889634f, GC1 = GC0 * 0.044715f;
    const char* gb = ws + WS_G + (size_t)(rb4 * 16 + w * 2) * 49152;
    const unsigned vt = t16lane(lane);
#pragma unroll
    for (int t = 0; t < 2; ++t) {
      const float nm = -ms[t].x, rstd = ms[t].y;
#pragma unroll
      for (int ct = 0; ct < 3; ++ct) {
        const f32x4 gv = (acc[t][ct] + cs[ct] * nm) * rstd, uv = (acc[t][3 + ct] + cs[3 + ct] * nm) * rstd;
        const f32x4 z = gv * (gv * gv * GC1 + GC0);
        f32x4 e;
#pragma unroll
        for (int j = 0; j < 4; ++j) e[j] = __builtin_amdgcn_exp2f(z[j]);
        e = e + 1.f;
#pragma unroll
        for (int j = 0; j < 4; ++j) e[j] = __builtin_amdgcn_rcpf(e[j]);
        const f32x4 r = gv * uv * e;
        st8s(gb + t * 49152 + t16off(cb4 * 48 + ct * 16), vt, nofill(pk_bf16(r[0], r[1]), pk_bf16(r[2], r[3])));
      }
    }
    __builtin_amdgcn_s_setprio(0);
    STAMP_AT(3, 7);
    waitv<6>();
    STAMP_AT(3, 8);
    bar();
  }
  if (w < 4) {
    const int lane = fresh_lane();
    const unsigned voff = lane * 16;
    const int row = rb3 * 64 + w * 16 + (lane & 15), col0 = cb3 * 80 + 4 * (lane >> 4);
    STAMP_AT(4, 0);
    probe(ws + WS_G + (size_t)(rb3 * 4 + w) * 49152, (unsigned)t16off((lane & 31) * 48 + 32));
    STAMP_AT(4, 1);
    f32x4 acc[5] = {};
    sfor<5>([&](auto ct) { pin(acc[ct]); });
    gemm_res<48, 5, K5_LA_, 2, 4, 1, 0, wd_off, x_none>(ws + WS_G + (size_t)(rb3 * 4 + w) * 49152, lds0, voff,
      [&](auto i) { if constexpr (i == 1) STAMP_AT(4, 3); if constexpr (i == 14) STAMP_AT(4, 4); if constexpr (i == 30) STAMP_AT(4, 5);
        if constexpr (i == -1 || i == 8 || i == 14 || i == 16 || i == 30 || i == 38) bar(); },
      [&](auto ct, const bf16x8& b, bf16x8 (&a)[1]) { mfma16(acc[ct], b, a[0]); });
    acc_fence(); sfor<5>([&](auto ct) { pin(acc[ct]); });
    STAMP_AT(4, 12);
#pragma unroll
    for (int ct = 0; ct < 5; ++ct) {
      const unsigned p0 = pk_bf16(bf_lo(hp[ct][0]) + acc[ct][0], bf_hi(hp[ct][0]) + acc[ct][1]);
      const unsigned p1 = pk_bf16(bf_lo(hp[ct][1]) + acc[ct][2], bf_hi(hp[ct][1]) + acc[ct][3]);
      *(u32x2*)(out + (size_t)row * 640 + col0 + 16 * ct) = (u32x2){p0, p1};
    }
    STAMP_AT(4, 13);
  } else {
    const int lane = fresh_lane(), v = w - 4;
    const unsigned voff = lane * 16;
    glds16<true>(wdg + (36 + v) * 1024, voff, lds0 + wd_off(36) + v * 1024);
    waitv<0>(); bar();
    sfor<3>([&](auto g) { sfor<5>([&](auto j) { constexpr int f = (g + 4) * 20 + j; glds16<true>(wdg + (f + v * 5) * 1024, voff, lds0 + wd_off(f) + v * 5120); }); });
    bar();
    sfor<2>([&](auto g) { sfor<5>([&](auto j) { constexpr int f = (g + 8) * 20 + j; glds16<true>(wdg + (f + v * 5) * 1024, voff, lds0 + wd_off(f) + v * 5120); }); });
    waitv<10>(); bar();
    bar();
    sfor<2>([&](auto g) { sfor<5>([&](auto j) { constexpr int f = (g + 10) * 20 + j; glds16<true>(wdg + (f + v * 5) * 1024, voff, lds0 + wd_off(f) + v * 5120); }); });
    waitv<10>(); bar();
    waitv<0>(); bar();
    if (blockIdx.x == 0 && w == 4) {
      poll2(ctr + CTL_FR, (unsigned)(lane * 64), (unsigned)(4096 + lane * 64), tag);
      poll2(ctr + CTL_FR, (unsigned)(8192 + lane * 64), (unsigned)(12288 + lane * 64), tag);
      if (lane == 0) st8(ctl, tag);
    }
  }
}

extern "C" void kernel_launch(void* const* d_in, const int* in_sizes, int n_in, void* d_out, int out_size, void* d_ws, size_t ws_size,
                              hipStream_t stream) {
  k_mega<<<256, 512, 0, stream>>>((const u16*)d_in[0], (const u16*)d_in[1], (const u16*)d_in[2], (const u16*)d_in[4], (const u16*)d_in[5],
                                  (const u16*)d_in[6], (char*)d_ws, (u16*)d_out);
}
```

```cpp
#include <hip/hip_runtime.h>
#include <hip/hip_bf16.h>
#include <stdint.h>
#include <utility>

typedef short bf16x8 __attribute__((ext_vector_type(8)));
typedef short s16x4 __attribute__((ext_vector_type(4)));
typedef float f32x16 __attribute__((ext_vector_type(16)));
typedef float f32x4 __attribute__((ext_vector_type(4)));
typedef float f32x2 __attribute__((ext_vector_type(2)));
typedef unsigned u32x4 __attribute__((ext_vector_type(4)));
typedef unsigned u32x2 __attribute__((ext_vector_type(2)));
typedef __bf16 bf2_t __attribute__((ext_vector_type(2)));
typedef unsigned short u16;
#define LAS __attribute__((address_space(3)))
#define DI __device__ __forceinline__

constexpr float LN_EPS = 1e-5f;
constexpr float QSCALE = 0.22360679774997896f * 1.4426950408889634f;

constexpr size_t WS_Q   = 0;
constexpr size_t WS_KV  = WS_Q + 2621440;
constexpr size_t WS_O   = WS_KV + 5242880;
constexpr size_t WS_H   = WS_O + 2621440;
constexpr size_t WS_HST = WS_H + 2621440;
constexpr size_t WS_G   = WS_HST + 131072;
constexpr size_t WS_WP  = WS_G + 6291456;
constexpr size_t WS_CSP = WS_WP + 6717440;
constexpr size_t WS_RST = WS_CSP + 245760;
constexpr size_t WS_CTL = WS_RST + 32768;
constexpr size_t CTL_F1 = 256;
constexpr size_t CTL_F2 = CTL_F1 + 2048 * 8;
constexpr size_t CTL_FR = CTL_F2 + 2048 * 8;
constexpr size_t CTL_F3 = CTL_FR + 2048 * 8;
constexpr size_t CTL_F4 = CTL_F3 + 1024 * 8;
constexpr size_t CTL_FD = CTL_F4 + 2048 * 8;
constexpr size_t CTL_REP = CTL_FD + 2048 * 8 - 256;
constexpr int NFR_WO = 800, NFR_GU = 3840, NFR_WD = 1920;

DI unsigned pk_bf16(float lo, float hi) { f32x2 f = {lo, hi}; return __builtin_bit_cast(unsigned, __builtin_convertvector(f, bf2_t)); }
DI float bf_lo(unsigned u) { return __uint_as_float(u << 16); }
DI float bf_hi(unsigned u) { return __uint_as_float(u & 0xffff0000u); }
DI float dot2(unsigned a, unsigned b, float c) { return __builtin_amdgcn_fdot2_f32_bf16(__builtin_bit_cast(bf2_t, a), __builtin_bit_cast(bf2_t, b), c, false); }
DI float add32(float v) { const auto r = __builtin_amdgcn_permlane32_swap(__float_as_uint(v), __float_as_uint(v), false, false); return __uint_as_float(r[0]) + __uint_as_float(r[1]); }
DI float max32(float v) { const auto r = __builtin_amdgcn_permlane32_swap(__float_as_uint(v), __float_as_uint(v), false, false); return fmaxf(__uint_as_float(r[0]), __uint_as_float(r[1])); }
DI float add16(float v) { const auto r = __builtin_amdgcn_permlane16_swap(__float_as_uint(v), __float_as_uint(v), false, false); return __uint_as_float(r[0]) + __uint_as_float(r[1]); }
DI float red4(float v) { return add32(add16(v)); }
DI float hi32(float v) { const auto r = __builtin_amdgcn_permlane32_swap(__float_as_uint(v), __float_as_uint(v), false, false); return __uint_as_float(r[1]); }
#define MFMA32(a, b, c) __builtin_amdgcn_mfma_f32_32x32x16_bf16((a), (b), (c), 0, 0, 0)
#define MFMA32K8(a, b, c) __builtin_amdgcn_mfma_f32_32x32x8bf16_1k((a), (b), (c), 0, 0, 0)
#define MFMA16(a, b, c) __builtin_amdgcn_mfma_f32_16x16x32_bf16((a), (b), (c), 0, 0, 0)

DI void mfma16(f32x4& c, const bf16x8& a, const bf16x8& b) { asm volatile("v_mfma_f32_16x16x32_bf16 %0, %1, %2, %0" : "+v"(c) : "v"(a), "v"(b)); }
DI void acc_fence() { asm volatile("s_nop 7\n\ts_nop 3" ::: "memory"); }
template <bool DEV> DI void glds16(const void* sbase, unsigned voff, unsigned lds_base) {
  if constexpr (DEV) asm volatile("s_mov_b32 m0, %2\n\ts_nop 0\n\tglobal_load_lds_dwordx4 %0, %1 sc1" :: "v"(voff), "s"(sbase), "s"(lds_base) : "memory");
  else asm volatile("s_mov_b32 m0, %2\n\ts_nop 0\n\tglobal_load_lds_dwordx4 %0, %1" :: "v"(voff), "s"(sbase), "s"(lds_base) : "memory");
}
template <int OFF, class T> DI void gld16(T& dst, const void* sbase, unsigned voff) {
  asm volatile("global_load_dwordx4 %0, %1, %2 offset:%3 sc1" : "=v"(dst) : "v"(voff), "s"(sbase), "i"(OFF) : "memory");
}
template <int OFF, class T> DI void gld8(T& dst, const void* sbase, unsigned voff) {
  asm volatile("global_load_dwordx2 %0, %1, %2 offset:%3 sc1" : "=v"(dst) : "v"(voff), "s"(sbase), "i"(OFF) : "memory");
}
template <int OFF> DI void gld4(float& dst, const void* sbase, unsigned voff) {
  asm volatile("global_load_dword %0, %1, %2 offset:%3 sc1" : "=v"(dst) : "v"(voff), "s"(sbase), "i"(OFF) : "memory");
}
template <int OFF> DI void gldu16(unsigned& dst, const void* sbase, unsigned voff) {
  asm volatile("global_load_ushort %0, %1, %2 offset:%3" : "=v"(dst) : "v"(voff), "s"(sbase), "i"(OFF) : "memory");
}
DI void st16(void* p, u32x4 v) { asm volatile("global_store_dwordx4 %0, %1, off sc1\n\ts_nop 1" :: "v"(p), "v"(v) : "memory"); }
DI void st8(void* p, u32x2 v) { asm volatile("global_store_dwordx2 %0, %1, off sc1" :: "v"(p), "v"(v) : "memory"); }
DI void st4(void* p, float v) { asm volatile("global_store_dword %0, %1, off sc1" :: "v"(p), "v"(v) : "memory"); }
DI void st8s(const void* sbase, unsigned voff, u32x2 v) { asm volatile("global_store_dwordx2 %0, %1, %2 sc1" :: "v"(voff), "v"(v), "s"(sbase) : "memory"); }
DI u32x2 nofill(unsigned a, unsigned b) { return (u32x2){min(a, 0xfffffffeu), min(b, 0xfffffffeu)}; }
DI unsigned umax4(u32x4 v) { return max(max(v[0], v[1]), max(v[2], v[3])); }
template <int N> DI void waitv() { asm volatile("s_waitcnt vmcnt(%0)" :: "i"(N) : "memory"); }
DI void bar() { asm volatile("s_waitcnt lgkmcnt(0)\n\ts_barrier" ::: "memory"); }
DI void setflag(char* flag, u32x2 tag, int lane) { if (lane < 8) st8(flag + lane * CTL_REP, tag); }
DI void publish(char* flag, u32x2 tag, int lane) { waitv<0>(); setflag(flag, tag, lane); }
DI void poll(const char* base, unsigned voff, u32x2 tag) {
#pragma unroll 1
  for (int spin = 0; spin < (1 << 17); ++spin) {
    u32x2 v;
    asm volatile("global_load_dwordx2 %0, %1, %2 sc1\n\ts_waitcnt vmcnt(0)" : "=v"(v) : "v"(voff), "s"(base) : "memory");
    if (__all(v[0] == tag[0] && v[1] == tag[1])) break;
    __builtin_amdgcn_s_sleep(1);
  }
}
DI void poll2(const char* base, unsigned voff0, unsigned voff1, u32x2 tag) {
#pragma unroll 1
  for (int spin = 0; spin < (1 << 17); ++spin) {
    u32x2 v, u;
    asm volatile("global_load_dwordx2 %0, %2, %4 sc1\n\tglobal_load_dwordx2 %1, %3, %4 sc1\n\ts_waitcnt vmcnt(0)" : "=&v"(v), "=&v"(u) : "v"(voff0), "v"(voff1), "s"(base) : "memory");
    if (__all(v[0] == tag[0] && v[1] == tag[1] && u[0] == tag[0] && u[1] == tag[1])) break;
    __builtin_amdgcn_s_sleep(1);
  }
}
DI void probe(const char* base, unsigned voff) {
#pragma unroll 1
  for (int spin = 0; spin < (1 << 17); ++spin) {
    unsigned v;
    asm volatile("global_load_dword %0, %1, %2 sc1\n\ts_waitcnt vmcnt(0)" : "=v"(v) : "v"(voff), "s"(base) : "memory");
    if (__all(v != 0xffffffffu)) break;
    __builtin_amdgcn_s_sleep(1);
  }
}
constexpr int t16off(int c0) { return (c0 >> 5) * 1024 + (c0 & 16) * 32; }
DI unsigned t16lane(int lane) { return (unsigned)((lane >> 5) * 256 + (lane & 15) * 16 + ((lane >> 4) & 1) * 8); }
DI void st16s(const void* sbase, unsigned voff, u32x4 v) { asm volatile("global_store_dwordx4 %0, %1, %2 sc1\n\ts_nop 1" :: "v"(voff), "v"(v), "s"(sbase) : "memory"); }
DI float row16_sum(float v) {
  v += __builtin_bit_cast(float, __builtin_amdgcn_update_dpp(0, __builtin_bit_cast(int, v), 0x128, 0xf, 0xf, false));
  v += __builtin_bit_cast(float, __builtin_amdgcn_update_dpp(0, __builtin_bit_cast(int, v), 0x124, 0xf, 0xf, false));
  v += __builtin_bit_cast(float, __builtin_amdgcn_update_dpp(0, __builtin_bit_cast(int, v), 0x122, 0xf, 0xf, false));
  v += __builtin_bit_cast(float, __builtin_amdgcn_update_dpp(0, __builtin_bit_cast(int, v), 0x121, 0xf, 0xf, false));
  return v;
}
DI u32x2 make_tag(u32x2 ep) {
  const unsigned long long e = (((unsigned long long)__builtin_amdgcn_readfirstlane(ep[1]) << 32) | __builtin_amdgcn_readfirstlane(ep[0])) + 1ull;
  return (u32x2){(unsigned)e, (unsigned)(e >> 32)};
}
constexpr unsigned RS_K0 = 0x9e3779b9u, RS_K1 = 0x7f4a7c15u;
struct RowStat { u32x4 a; unsigned b; };
DI void rs_load(RowStat& r, const u16* x, int row, int lane) {
  asm volatile("global_load_dwordx4 %0, %2, %4\n\tglobal_load_dword %1, %3, %4 offset:1024" : "=&v"(r.a), "=&v"(r.b) : "v"(lane * 16), "v"(lane * 4), "s"((const char*)x + (size_t)row * 1280) : "memory");
}
DI void rs_store(RowStat& r, char* ws, int row, int lane, u32x2 tag) {
  asm volatile("" : "+v"(r.a), "+v"(r.b));
  float s1 = dot2(r.b, 0x3f803f80u, 0.f), s2 = dot2(r.b, r.b, 0.f);
#pragma unroll
  for (int j = 0; j < 4; ++j) { s1 = dot2(r.a[j], 0x3f803f80u, s1); s2 = dot2(r.a[j], r.a[j], s2); }
  s1 = red4(row16_sum(s1)); s2 = red4(row16_sum(s2));
  const float mean = s1 * (1.f / 640.f), rstd = __builtin_amdgcn_rsqf(fmaxf(s2 * (1.f / 640.f) - mean * mean, 0.f) + LN_EPS);
  const unsigned m = __float_as_uint(mean), q = __float_as_uint(rstd);
  if (lane == 0) st16(ws + WS_RST + (size_t)row * 16, (u32x4){m, q, tag[0] ^ m ^ RS_K0, tag[1] ^ q ^ RS_K1});
}
DI int fresh_lane() { int l = __builtin_amdgcn_mbcnt_hi(~0u, __builtin_amdgcn_mbcnt_lo(~0u, 0u)); asm volatile("" : "+v"(l)); return l; }
template <class T> DI void pins(T& v) { asm volatile("" : "+s"(v)); }
template <class T> DI void pin(T& v) { asm volatile("" : "+v"(v)); }
DI bf16x8 lds128(unsigned addr) { return *(const LAS bf16x8*)(uintptr_t)addr; }
DI s16x4 lds64(unsigned addr) { return *(const LAS s16x4*)(uintptr_t)addr; }
DI s16x4 ldstr(unsigned addr) { return __builtin_amdgcn_ds_read_tr16_b64_v4i16((LAS s16x4*)(uintptr_t)addr); }
DI unsigned lds_addr_of(const void* p) { return (unsigned)(uintptr_t)(const LAS char*)p; }
#define STAMP_AT(kid, idx) ((void)0)
#define RETRY_COUNT(k) ((void)0)
#define WAITV_BAR(N) asm volatile("s_waitcnt vmcnt(" #N ") lgkmcnt(0)\n\ts_barrier" ::: "memory")
#define SB() __builtin_amdgcn_sched_barrier(0)
template <int N> using IC = std::integral_constant<int, N>;
template <class F, int... Is> DI void sfor_(F&& f, std::integer_sequence<int, Is...>) { (f(IC<Is>{}), ...); }
template <int N, class F> DI void sfor(F&& f) { sfor_(f, std::make_integer_sequence<int, N>{}); }

constexpr int P3_G1 = -1, K4_LA_ = 6, K4_CG_ = 2, K5_LA_ = 16;
constexpr int K1_STAGE = 32768;
DI void p1_load(const u16* __restrict__ x, const u16* __restrict__ W, const unsigned lds0, const int v, int rb, int cb) {
  const int lane = fresh_lane();
  const unsigned a_voff0 = (unsigned)((lane >> 3) * 1280 + (((lane & 7) ^ ((lane >> 4) & 7)) << 4));
  const unsigned a_voff1 = (unsigned)((lane >> 3) * 1280 + (((lane & 7) ^ ((4 + (lane >> 4)) & 7)) << 4)) + 8 * 1280;
  const unsigned w_voff0 = (unsigned)((lane >> 3) * 3840 + (((lane & 7) ^ (((lane >> 4) & 1) << 2)) << 4));
  const unsigned w_voff1 = w_voff0 + 8 * 3840;
  const char* xs = (const char*)x + (size_t)(rb * 128 + v * 32) * 1280;
  const char* wsrc = (const char*)W + (size_t)((v & 1) * 32) * 3840 + (size_t)(cb * 128 + (v >> 1) * 64) * 2;
  const int rot = ((((blockIdx.x >> 1) & 3) * 5 + (blockIdx.x & 1) * 10) >> 1) % 10;
  auto st = [&](auto s_) {
    constexpr int s0 = s_; const unsigned d = lds0 + (s0 % 5) * K1_STAGE + v * 4096;
    const int s = s0 + rot - (s0 + rot >= 10 ? 10 : 0);
    const char* xp = xs + s * 128; const char* wp = wsrc + (size_t)s * 64 * 3840;
    glds16<false>(xp, a_voff0, d); glds16<false>(wp, w_voff0, d + 16384);
    glds16<false>(xp, a_voff1, d + 1024); glds16<false>(wp, w_voff1, d + 16384 + 1024);
    glds16<false>(xp + 16 * 1280, a_voff0, d + 2048); glds16<false>(wp + 16 * 3840, w_voff0, d + 16384 + 2048);
    glds16<false>(xp + 16 * 1280, a_voff1, d + 3072); glds16<false>(wp + 16 * 3840, w_voff1, d + 16384 + 3072);
  };
  st(IC<0>{}); st(IC<1>{});
  sfor<10>([&](auto s_) { constexpr int s = s_;
    waitv<(s == 0 ? 8 : s <= 7 ? 16 : s == 8 ? 8 : 0)>(); asm volatile("s_barrier" ::: "memory");
    if constexpr (s == 0) { st(IC<2>{}); st(IC<3>{}); }
    else if constexpr (s + 3 < 10) st(IC<s + 3>{}); });
  bar();
}
template <bool ISV>
DI void p1_body(const u16* __restrict__ x, char* __restrict__ ws, const unsigned lds0, const int w, int rb, int cb, u32x2& tag) {
  const int lane = fresh_lane();
  u32x2 ep; gld8<0>(ep, ws + WS_CTL, 0u);
  RowStat rs[2];
  rs_load(rs[0], x, blockIdx.x * 8 + w * 2, lane); rs_load(rs[1], x, blockIdx.x * 8 + w * 2 + 1, lane);
  const int wm = w >> 1, wn = w & 1;
  const int r32 = lane & 31, hh = lane >> 5;
  STAMP_AT(0, 0);
  unsigned xa[3][4], wa[3][2][2];
  {
    const int row = wm * 64 + r32;
    const int blk = (lane >> 4) & 1, q = (lane & 15) >> 2, p = lane & 3;
#pragma unroll
    for (int hs = 0; hs < 3; ++hs) {
#pragma unroll
      for (int i = 0; i < 4; ++i) { xa[hs][i] = lds0 + hs * 65536 + (unsigned)(row * 128 + ((((i ^ wm) * 2 + hh) ^ ((row >> 1) & 7)) << 4)); pin(xa[hs][i]); }
#pragma unroll
      for (int j = 0; j < 2; ++j) {
        const unsigned base = lds0 + hs * 65536 + (unsigned)(16384 + wn * 8192 + (8 * hh + q) * 128 + (((j * 4 + blk * 2 + (p >> 1)) ^ (((q >> 1) & 1) << 2)) << 4) + (p & 1) * 8);
        wa[hs][j][0] = base + wm * 2048; wa[hs][j][1] = base + (1 ^ wm) * 2048; pin(wa[hs][j][0]); pin(wa[hs][j][1]);
      }
    }
  }
  f32x16 acc[2][2] = {};
  f32x4 sw[2] = {};
  u32x4 rec;
  bf16x8 sel = (bf16x8)(short)(((lane & 15) == ((lane >> 4) & 1)) ? 0x3f80 : 0); pin(sel);
  bf16x8 fx[2][2]; s16x4 fwl[2][2], fwh[2][2];
  auto rd = [&](auto i_, auto k_) {
    constexpr int i = i_, k = k_, s = i >> 2, p = i & 3, sl = s % 5, hs = sl >> 1; constexpr unsigned so = (sl & 1) * K1_STAGE;
    if constexpr (k == 0) fx[i & 1][0] = lds128(xa[hs][p] + so);
    if constexpr (k == 3) fx[i & 1][1] = lds128(xa[hs][p] + so + 4096);
    if constexpr (k == 1 || k == 2) { fwl[i & 1][k - 1] = ldstr(wa[hs][k - 1][p & 1] + so + (p >> 1) * 4096); fwh[i & 1][k - 1] = ldstr(wa[hs][k - 1][p & 1] + so + (p >> 1) * 4096 + 512); }
  };
  const bool isq = cb < 5;
  const int pitch = isq ? 1280 : 2560;
  unsigned off[2][ISV ? 1 : 4];
#pragma unroll
  for (int j = 0; j < 2; ++j) {
    if constexpr (ISV) {
      const int n = cb * 128 + wn * 64 + j * 32 + r32 - 1280, head = n / 20, d0 = n - head * 20;
      const int d = (head & 1) ? (d0 < 4 ? d0 + 16 : d0 - 4) : d0;
      off[j][0] = (unsigned)(head * 163840 + 1280 + (hh * 20 + d) * 16); pin(off[j][0]);
    } else {
#pragma unroll
      for (int g = 0; g < 4; ++g) {
        const int n = cb * 128 + wn * 64 + j * 32 + 8 * g + 4 * hh - (isq ? 0 : 640), head = n / 20, d0 = n - head * 20;
        const int d = (head & 1) ? (d0 < 4 ? d0 + 16 : d0 - 4) : d0;
        off[j][g] = (unsigned)(head * 64 * pitch + (d < 16 ? (((d >> 3) * 32 + r32) * 16 + (d & 4) * 2) : (1024 + r32 * 8))); pin(off[j][g]);
      }
    }
  }
  waitv<0>(); pin(ep); tag = make_tag(ep);
  rs_store(rs[0], ws, blockIdx.x * 8 + w * 2, lane, tag); rs_store(rs[1], ws, blockIdx.x * 8 + w * 2 + 1, lane, tag);
  asm volatile("s_barrier" ::: "memory");
  STAMP_AT(0, 1);
  sfor<4>([&](auto k) { rd(IC<0>{}, k); });
  sfor<40>([&](auto i_) {
    constexpr int i = i_, b = i & 1;
    if constexpr ((i & 3) == 3 && i < 39) asm volatile("s_barrier" ::: "memory");
    if constexpr (i == 30) gld16<0>(rec, ws + WS_RST + (size_t)(rb * 128 + wm * 64) * 16, (unsigned)(lane * 16));
    SB();
    const bf16x8 w0 = __builtin_shufflevector(fwl[b][0], fwh[b][0], 0, 1, 2, 3, 4, 5, 6, 7), w1 = __builtin_shufflevector(fwl[b][1], fwh[b][1], 0, 1, 2, 3, 4, 5, 6, 7);
    sfor<4>([&](auto k_) { constexpr int k = k_, t = k >> 1, j = (k ^ (k >> 1)) & 1;
      if constexpr (ISV) acc[t][j] = MFMA32(fx[b][t], j ? w1 : w0, acc[t][j]); else acc[t][j] = MFMA32(j ? w1 : w0, fx[b][t], acc[t][j]);
      SB();
      if constexpr (i < 39) rd(IC<i + 1>{}, k_);
      SB(); });
    if constexpr (b == 1) { sw[0] = MFMA16(w0, sel, sw[0]); sw[1] = MFMA16(w1, sel, sw[1]); }
    SB();
  });
  STAMP_AT(0, 11);
  LAS float* csp = (LAS float*)(uintptr_t)lds0;
  if ((lane & 15) < 2) sfor<2>([&](auto j) { *(LAS f32x4*)&csp[wm * 128 + wn * 64 + j * 32 + (lane & 1) * 16 + (lane >> 4) * 4] = sw[j]; });
  waitv<0>(); pin(rec);
#pragma unroll 1
  for (int spin = 0; spin < (1 << 17) && !__all((rec[2] ^ rec[0] ^ RS_K0) == tag[0] && (rec[3] ^ rec[1] ^ RS_K1) == tag[1]); ++spin) {
    gld16<0>(rec, ws + WS_RST + (size_t)(rb * 128 + wm * 64) * 16, (unsigned)(lane * 16)); waitv<0>(); pin(rec);
  }
  LAS f32x2* st = (LAS f32x2*)(uintptr_t)(lds0 + 4096 + w * 512);
  if constexpr (ISV) st[lane] = (f32x2){__uint_as_float(rec[0]), __uint_as_float(rec[1])};
  bar();
  STAMP_AT(0, 12);
  if constexpr (ISV) {
    float c[2];
#pragma unroll
    for (int j = 0; j < 2; ++j) c[j] = csp[wn * 64 + j * 32 + r32] + csp[128 + wn * 64 + j * 32 + r32];
#pragma unroll
    for (int t = 0; t < 2; ++t) {
      f32x4 s01[4], s23[4];
#pragma unroll
      for (int g = 0; g < 4; ++g) { s01[g] = *(const LAS f32x4*)&st[t * 32 + 8 * g + 4 * hh]; s23[g] = *(const LAS f32x4*)&st[t * 32 + 8 * g + 4 * hh + 2]; }
      const char* dst = ws + WS_KV + (size_t)(rb * 4 + wm * 2 + t) * 2560;
#pragma unroll
      for (int j = 0; j < 2; ++j) {
        const f32x16& a = acc[t][j];
        unsigned pk[8];
#pragma unroll
        for (int g = 0; g < 4; ++g) {
          pk[2 * g]     = pk_bf16(s01[g][1] * (a[4 * g] - s01[g][0] * c[j]), s01[g][3] * (a[4 * g + 1] - s01[g][2] * c[j]));
          pk[2 * g + 1] = pk_bf16(s23[g][1] * (a[4 * g + 2] - s23[g][0] * c[j]), s23[g][3] * (a[4 * g + 3] - s23[g][2] * c[j]));
        }
        st16s(dst, off[j][0], (u32x4){pk[0], pk[1], pk[2], pk[3]});
        st16s(dst + 640, off[j][0], (u32x4){pk[4], pk[5], pk[6], pk[7]});
      }
    }
  } else {
    const float sc = isq ? QSCALE : 1.f;
    const char* base = ws + (isq ? WS_Q : WS_KV) + (size_t)(rb * 4 + wm * 2) * pitch;
    const auto pm = __builtin_amdgcn_permlane32_swap(rec[0], rec[0], false, false), pr = __builtin_amdgcn_permlane32_swap(rec[1], rec[1], false, false);
#pragma unroll
    for (int j = 0; j < 2; ++j)
#pragma unroll
      for (int g = 0; g < 4; ++g) {
        const int nl = wn * 64 + j * 32 + 8 * g + 4 * hh;
        const f32x4 c = *(const LAS f32x4*)&csp[nl] + *(const LAS f32x4*)&csp[128 + nl];
#pragma unroll
        for (int t = 0; t < 2; ++t) {
          const f32x16& a = acc[t][j];
          const float rs_ = __uint_as_float(pr[t]) * sc, mr = __uint_as_float(pm[t]);
          st8s(base + t * pitch, off[j][g], (u32x2){pk_bf16(rs_ * (a[4 * g] - mr * c[0]), rs_ * (a[4 * g + 1] - mr * c[1])), pk_bf16(rs_ * (a[4 * g + 2] - mr * c[2]), rs_ * (a[4 * g + 3] - mr * c[3]))});
        }
      }
  }
  STAMP_AT(0, 13);
  waitv<0>();
  if (lane < 16) st8(ws + WS_CTL + CTL_F1 + ((cb * 4 + wn * 2 + (lane >> 3)) * 32 + rb * 2 + wm) * 8 + (lane & 7) * CTL_REP, tag);
}

DI void p1_repack(const u16* __restrict__ Wo, char* __restrict__ ws, const int w, int idx) {
  const int lane = fresh_lane();
  const unsigned pvo = (lane >> 4) * 8 * 1280 + (lane & 15) * 2;
  unsigned pw[7][8];
  sfor<7>([&](auto r) {
    const int f = idx * 50 + min(w + 8 * r, 49), cbk = f / 100, rem = f - cbk * 100, ks = rem / 5, ct = rem - ks * 5;
    const char* src = (const char*)(Wo + (size_t)(32 * ks) * 640 + 80 * cbk + 16 * ct);
    sfor<8>([&](auto i) { gldu16<(i & 3) * 1280>(pw[r][i], src + (i & ~3) * 1280, pvo); });
  });
  sfor<7>([&](auto r) {
    waitv<(6 - r) * 8>();
    sfor<8>([&](auto j) { pin(pw[r][j]); });
    const u32x4 v = {pw[r][0] | (pw[r][1] << 16), pw[r][2] | (pw[r][3] << 16), pw[r][4] | (pw[r][5] << 16), pw[r][6] | (pw[r][7] << 16)};
    pw[r][0] = v[0]; pw[r][1] = v[1]; pw[r][2] = v[2]; pw[r][3] = v[3];
  });
  sfor<7>([&](auto r) {
    if (w + 8 * r < 50) st16(ws + WS_WP + (size_t)(idx * 50 + w + 8 * r) * 1024 + lane * 16, (u32x4){pw[r][0], pw[r][1], pw[r][2], pw[r][3]});
  });
}

template <int OFF> DI void rdK(bf16x8& k0, s16x4& k1, unsigned a0, unsigned a1) {
  asm volatile("ds_read_b128 %0, %2 offset:%4\n\tds_read_b64 %1, %3 offset:%4" : "=&v"(k0), "=&v"(k1) : "v"(a0), "v"(a1), "i"(OFF));
}
template <int OFF> DI void rdV(bf16x8& v0, bf16x8& v1, unsigned a, unsigned long long msk) {
  asm volatile("s_mov_b64 exec, %5\n\tds_read_b128 %0, %2 offset:%3\n\tds_read_b128 %1, %2 offset:%4\n\ts_mov_b64 exec, -1"
               : "+v"(v0), "+v"(v1) : "v"(a), "i"(OFF), "i"(OFF + 640), "s"(msk));
}
#define EX2(v) __builtin_amdgcn_exp2f(v)
constexpr int wo_off(int p) { return (p < 60 ? p : p + 20) * 1024; }
constexpr int gu_off(int f) { return (f < 20 ? f + 60 : f < 60 ? f + 100 : f - 60) * 1024; }
constexpr int wd_off(int f) { return ((f / 20 + 4) % 8) * 20480 + (f % 20) * 1024; }
constexpr int CSF_OFF = 116 * 1024, XCH_OFF = 120 * 1024;

DI void p2_attn(char* __restrict__ ws, const u16* __restrict__ Wg, const u16* __restrict__ Wu, const u16* __restrict__ Wd,
                const unsigned lds0, const int w, const u32x2 tag, const int rb3, const int cb3, const int rb4, const int cb4) {
  const int lane = fresh_lane();
  const int kh = w >> 2, wq = w & 3;
  const int head = (blockIdx.x & 7) * 4 + ((blockIdx.x >> 3) & 3), qb = blockIdx.x >> 5;
  const int r32 = lane & 31, hh = lane >> 5;
  const char* kvg = ws + WS_KV + (size_t)head * 163840 + (size_t)kh * 81920 + wq * 1024;
  const unsigned voff = lane * 16;
  const unsigned kvl = lds0 + kh * 81920 + wq * 1024;
  char* ctl = ws + WS_CTL;
  const char* ctr = ctl + (blockIdx.x & 7) * CTL_REP;
  unsigned pw[3][8];
  const int gw = blockIdx.x * 8 + w, gwc = min(gw, 1919);
  const char* psrc[3];
#pragma unroll
  for (int r = 0; r < 2; ++r) {
    const int f = 2 * gwc + r, cbk = f / 120, rem = f - cbk * 120, ks = rem / 6, ct = rem - ks * 6;
    psrc[r] = (const char*)((ct < 3 ? Wg : Wu) + (size_t)(32 * ks) * 1536 + 48 * cbk + 16 * (ct < 3 ? ct : ct - 3));
  }
  { const int f = gwc, cbk = f / 240, rem = f - cbk * 240, ks = rem / 5, ct = rem - ks * 5;
    psrc[2] = (const char*)(Wd + (size_t)(32 * ks) * 640 + 80 * cbk + 16 * ct); }
  unsigned pvo1 = (lane >> 4) * 8 * 3072 + (lane & 15) * 2, pvo2 = (lane >> 4) * 8 * 1280 + (lane & 15) * 2; pin(pvo1); pin(pvo2);
  const char* wog = ws + WS_WP + (size_t)cb3 * 102400;
  const int qtile = qb * 8 + wq * 2 + kh, cg0 = head * 5 + (head & 1), cgt = (head & 1) ? head * 5 : head * 5 + 4;
  const char* ob = ws + WS_O + (size_t)qtile * 40960;
  const char* obs[3] = {ob + (cg0 >> 3) * 1024 + ((cg0 & 7) >> 1) * 256, ob + ((cg0 + 2) >> 3) * 1024 + (((cg0 + 2) & 7) >> 1) * 256,
                        ob + (cgt >> 3) * 1024 + ((cgt & 7) >> 1) * 256 + (cgt & 1) * 8};
  unsigned vo = (unsigned)((r32 >> 4) * 20480 + (r32 & 15) * 16 + hh * 8); pin(vo);
  u32x4 fill = {0xffffffffu, 0xffffffffu, 0xffffffffu, 0xffffffffu}; pin(fill);
  const u32x2 fill2 = {fill[0], fill[1]};
  u32x2 fa, fb, fc;
  const unsigned fvc = (unsigned)CTL_FR + (unsigned)((cb4 * 60 + min(lane, 59)) * 8);
  const unsigned fva = (unsigned)(hh ? CTL_F4 : CTL_F2) + (unsigned)(((rb3 * 2 + ((w & 3) >> 1)) * 32 + r32) * 8), fvb = (unsigned)CTL_F3 + (unsigned)(((rb4 * 16 + w * 2) * 8 + (lane & 15)) * 8);
  const char* gcl = ws + WS_G + (size_t)(rb4 * 16 + w * 2) * 49152 + cb4 * 1536;
  const char* hcl = ws + WS_H + (size_t)(rb3 * 4 + w) * 20480 + cb3 * 2560;
  const char* rdst = ws + WS_WP + (size_t)(NFR_WO + 2 * gw) * 1024;
  pins(psrc[0]); pins(psrc[1]); pins(psrc[2]); pins(obs[0]); pins(obs[1]); pins(obs[2]); pins(wog); pins(gcl); pins(hcl); pins(rdst);
  STAMP_AT(1, 0);
  unsigned f0;
  {
    const int cfl = (20 * head) >> 5, cfh = (20 * head + 19) >> 5;
    f0 = (unsigned)(((((lane & 32) ? 40 : 20) + ((lane & 16) ? cfh : cfl)) * 32 + kh * 16 + (lane & 15)) * 8);
    const unsigned f1 = (unsigned)((lane < 32 ? (((lane & 1) ? cfh : cfl) * 32 + qb * 4 + wq) : 1920 + cb3 * 16 + (lane & 15)) * 8);
    poll2(ctr + CTL_F1, f0 & ~(12u * 8u), f1, tag);
  }
  STAMP_AT(1, 4);
#pragma unroll
  for (int i = 0; i < 4; ++i) glds16<true>(kvg + i * 4096, voff, kvl + i * 4096);
  bf16x8 q0[2]; s16x4 q1[2];
  {
    const char* qg = ws + WS_Q + ((size_t)head * 64 + qb * 8 + wq * 2) * 1280;
    gld16<0>(q0[0], qg, voff); gld16<1280>(q0[1], qg, voff); gld8<1024>(q1[0], qg, r32 * 8); gld8<2304>(q1[1], qg, r32 * 8);
  }
  const unsigned ka0 = lds0 + kh * 81920 + lane * 16, ka1 = lds0 + kh * 81920 + 1024 + r32 * 8, va = lds0 + kh * 81920 + 1280 + (hh * 20 + (r32 < 20 ? r32 : 0)) * 16;
  bf16x8 K0[2], V0[2], V1[2]; s16x4 K1[2];
  V0[0] = V0[1] = V1[0] = V1[1] = (bf16x8)(short)(r32 == 20 ? 0x3f80 : 0);
  pin(V0[0]); pin(V0[1]); pin(V1[0]); pin(V1[1]);
  WAITV_BAR(0);
  u32x2 fr; gld8<0>(fr, ctr + CTL_F1, f0);
  pin(q0[0]); pin(q0[1]); pin(q1[0]); pin(q1[1]);
  if (hh) { q1[0] = (s16x4){0, 0, 0, 0}; q1[1] = (s16x4){0, 0, 0, 0}; }
  STAMP_AT(1, 1);
  rdK<0>(K0[0], K1[0], ka0, ka1);
  const unsigned long long vmsk = 0x000fffff000fffffull;
  rdV<0>(V0[0], V1[0], va, vmsk);
  f32x16 cm[2];
  {
    const bf16x8 kf0 = lds128(lds0 + lane * 16); const s16x4 kf1 = lds64(lds0 + 1024 + r32 * 8);
#pragma unroll
    for (int t = 0; t < 2; ++t) {
      f32x16 s = MFMA32(kf0, q0[t], (f32x16){}); s = MFMA32K8(kf1, q1[t], s);
      float m = s[0];
#pragma unroll
      for (int i = 1; i < 16; ++i) m = fmaxf(m, s[i]);
      m = max32(m);
      cm[t] = (f32x16)(-m);
      pin(cm[t]);
    }
  }
  asm volatile("s_waitcnt lgkmcnt(0)" : "+v"(K0[0]), "+v"(K1[0]), "+v"(V0[0]), "+v"(V1[0]));
  f32x16 o[2] = {};
  f32x16 SA, SB_;
  u32x4 PA0, PA1, PB0 = {}, PB1 = {};
  SA = MFMA32(K0[0], q0[0], cm[0]); SA = MFMA32K8(K1[0], q1[0], SA);
  waitv<0>(); pin(fr);
  if (!__all(fr[0] == tag[0] && fr[1] == tag[1])) poll(ctr + CTL_F1, f0, tag);
  STAMP_AT(1, 2);
  pin(o[0]); pin(o[1]); pin(PB0); pin(PB1);
#define GRP(MF, Sc, Pc, i, j) do { MF; SB(); const float e0 = EX2(Sc[i]), e1 = EX2(Sc[i + 1]), e2 = EX2(Sc[i + 2]), e3 = EX2(Sc[i + 3]); \
    Pc[j] = pk_bf16(e0, e1); Pc[j + 1] = pk_bf16(e2, e3); pin(Pc); SB(); } while (0)
#define SLOT(QK, Sc, Pc0, Pc1, Sn, kb, jn, Pp0, Pp1, vb, jp) do { \
    GRP(if constexpr (QK) Sn = MFMA32(K0[kb], q0[jn], cm[jn]), Sc, Pc0, 0, 0); \
    GRP(if constexpr (QK) Sn = MFMA32K8(K1[kb], q1[jn], Sn), Sc, Pc0, 4, 2); \
    GRP(o[jp] = MFMA32(V0[vb], __builtin_bit_cast(bf16x8, Pp0), o[jp]), Sc, Pc1, 8, 0); \
    GRP(o[jp] = MFMA32(V1[vb], __builtin_bit_cast(bf16x8, Pp1), o[jp]), Sc, Pc1, 12, 2); } while (0)
#define LWAIT(kb, vb) asm volatile("s_waitcnt lgkmcnt(0)" : "+v"(K0[kb]), "+v"(K1[kb]), "+v"(V0[vb]), "+v"(V1[vb]))
  auto rstore = [&](auto r_, auto yv_) {
    constexpr int r = r_;
    waitv<yv_>();
    sfor<8>([&](auto j) { pin(pw[r][j]); });
    const u32x4 v = {pw[r][0] | (pw[r][1] << 16), pw[r][2] | (pw[r][3] << 16), pw[r][4] | (pw[r][5] << 16), pw[r][6] | (pw[r][7] << 16)};
    if (gw < 1920) {
      if constexpr (r < 2) {
        st16s(rdst + r * 1024, voff, v);
        float c = 0.f;
#pragma unroll
        for (int j = 0; j < 4; ++j) c = dot2(v[j], 0x3f803f80u, c);
        c = red4(c);
        if (lane < 16) st4(ws + WS_CSP + (size_t)(2 * gw + r) * 64 + lane * 4, c);
      } else st16s(ws + WS_WP + (size_t)(NFR_WO + NFR_GU + gw) * 1024, voff, v);
    }
  };
  auto hook = [&](auto it_, auto i_) {
    constexpr int it = it_, i = i_, n = (it - 2) * 8 + i;
    if constexpr (n >= 0 && n < 2) st8s(obs[n], vo, fill2);
    if constexpr (n == 2) { if (!hh) st8s(obs[2], vo, fill2); }
    if constexpr (n >= 3 && n < 6) { const int L = (n - 3) * 64 + lane; st16s(gcl, (unsigned)(L * 16 + (L >= 96 ? 49152 - 1536 : 0)), fill); }
    if constexpr (n >= 6 && n < 9) { if (w < 4) st16s(hcl, (unsigned)(min((n - 6) * 64 + lane, 159) * 16), fill); }
    if constexpr (n == 9) { if (w < 4 && lane < 16) st8s(ws + WS_HST + (size_t)(rb3 * 4 + w) * 1024 + (cb3 >> 1) * 256 + (cb3 & 1) * 8, (unsigned)(lane * 16), fill2); }
    if constexpr (it == 5 && i == 6) {
      waitv<0>();
      setflag(ctl + CTL_FR + gw * 8, tag, lane);
      setflag(ctl + CTL_F2 + (qtile * 32 + head) * 8, tag, lane);
      setflag(ctl + CTL_F4 + ((rb4 * 8 + w) * 32 + cb4) * 8, tag, lane);
      if (w < 4) setflag(ctl + CTL_F3 + ((rb3 * 4 + w) * 8 + cb3) * 8, tag, lane);
    }
    if constexpr (it == 7 && i == 6) { gld8<0>(fa, ctr, fva); gld8<0>(fb, ctr, fvb); gld8<0>(fc, ctr, fvc); }
    if constexpr (it < 2) glds16<true>(kvg + (size_t)(it * 8 + 4 + i) * 4096, voff, kvl + (it * 8 + 4 + i) * 4096);
    else if constexpr (it < 4) gldu16<(i & 1) * 3072>(pw[it - 2][i], psrc[it - 2] + (i & ~1) * 3072, pvo1);
    else if constexpr (it == 4) {
      gldu16<(i & 3) * 1280>(pw[2][i], psrc[2] + (i & ~3) * 1280, pvo2);
      if constexpr (i == 0) rstore(IC<0>{}, IC<9>{});
      if constexpr (i == 7) rstore(IC<1>{}, IC<8>{});
    } else if constexpr (it == 5) {
      if constexpr (i == 7) rstore(IC<2>{}, IC<3>{});
    } else {
      constexpr int j = (it - 6) * 8 + i;
      const char* src = wog + (w + 8 * j) * 1024;
      if constexpr (j < 7) glds16<true>(src, voff, lds0 + w * 1024 + j * 8192);
      else if constexpr (j == 7) glds16<true>(src, voff, lds0 + w * 1024 + j * 8192 + (w >= 4 ? 20480 : 0));
      else if constexpr (j < 12) glds16<true>(src, voff, lds0 + w * 1024 + j * 8192 + 20480);
      else if constexpr (j == 12) { if (w < 4) glds16<true>(src, voff, lds0 + w * 1024 + j * 8192 + 20480); }
    }
    SB();
  };
  const unsigned ka0b = ka0 + 40960, ka1b = ka1 + 40960, vab = va + 40960;
  sfor<8>([&](auto it_) {
    constexpr int it = it_;
    if constexpr (it == 1) WAITV_BAR(6);
    if constexpr (it == 2) { WAITV_BAR(8); STAMP_AT(1, 3); }
    if constexpr (it == 4) asm volatile("s_waitcnt vmcnt(16)\n\ts_barrier" ::: "memory");
    if constexpr (it == 6) asm volatile("s_barrier" ::: "memory");
    if (((it ^ kh) & 1) == 0) __builtin_amdgcn_s_setprio(0); else __builtin_amdgcn_s_setprio(1);
    constexpr int o0 = (it & 3) * 10240;
    constexpr int on = it == 3 ? 0 : o0 + 10240;
    const unsigned a0 = it < 4 ? ka0 : ka0b, a1 = it < 4 ? ka1 : ka1b, av = it < 4 ? va : vab;
    const unsigned n0 = it < 3 ? ka0 : ka0b, n1 = it < 3 ? ka1 : ka1b, nv = it < 3 ? va : vab;
    rdK<o0 + 2560>(K0[1], K1[1], a0, a1); hook(it_, IC<0>{});                  SLOT(true, SA, PA0, PA1, SB_, 0, 1, PB0, PB1, 1, 1);
    LWAIT(1, 0); rdV<o0 + 2560>(V0[1], V1[1], av, vmsk); hook(it_, IC<1>{});   SLOT(true, SB_, PB0, PB1, SA, 1, 0, PA0, PA1, 0, 0);
    rdK<o0 + 5120>(K0[0], K1[0], a0, a1); hook(it_, IC<2>{});                  SLOT(true, SA, PA0, PA1, SB_, 1, 1, PB0, PB1, 0, 1);
    LWAIT(0, 1); rdV<o0 + 5120>(V0[0], V1[0], av, vmsk); hook(it_, IC<3>{});   SLOT(true, SB_, PB0, PB1, SA, 0, 0, PA0, PA1, 1, 0);
    rdK<o0 + 7680>(K0[1], K1[1], a0, a1); hook(it_, IC<4>{});                  SLOT(true, SA, PA0, PA1, SB_, 0, 1, PB0, PB1, 1, 1);
    LWAIT(1, 0); rdV<o0 + 7680>(V0[1], V1[1], av, vmsk); hook(it_, IC<5>{});   SLOT(true, SB_, PB0, PB1, SA, 1, 0, PA0, PA1, 0, 0);
    if constexpr (it < 7) rdK<on>(K0[0], K1[0], n0, n1);
    hook(it_, IC<6>{});                                                        SLOT(true, SA, PA0, PA1, SB_, 1, 1, PB0, PB1, 0, 1);
    LWAIT(0, 1);
    if constexpr (it < 7) rdV<on>(V0[0], V1[0], nv, vmsk);
    hook(it_, IC<7>{});                                                        SLOT(it < 7, SB_, PB0, PB1, SA, 0, 0, PA0, PA1, 1, 0);
  });
  __builtin_amdgcn_s_setprio(0);
  o[1] = MFMA32(V0[1], __builtin_bit_cast(bf16x8, PB0), o[1]);
  o[1] = MFMA32(V1[1], __builtin_bit_cast(bf16x8, PB1), o[1]);
  STAMP_AT(1, 9);
  bar();
  {
    LAS f32x4* xch = (LAS f32x4*)(uintptr_t)(lds0 + XCH_OFF + w * 3072);
    const f32x16& snd = kh ? o[0] : o[1];
#pragma unroll
    for (int g = 0; g < 3; ++g) xch[g * 64 + lane] = (f32x4){snd[4 * g], snd[4 * g + 1], snd[4 * g + 2], snd[4 * g + 3]};
  }
  bar();
  STAMP_AT(1, 10);
  float of[12];
  {
    const LAS f32x4* xch = (const LAS f32x4*)(uintptr_t)(lds0 + XCH_OFF + (w ^ 4) * 3072);
    const f32x16& own = kh ? o[1] : o[0];
#pragma unroll
    for (int g = 0; g < 3; ++g) { const f32x4 v = xch[g * 64 + lane];
#pragma unroll
      for (int j = 0; j < 4; ++j) of[4 * g + j] = own[4 * g + j] + v[j]; }
  }
  float l = hi32(of[8]);
  bool ok = (l > 8.67e-19f) && (l < 1.15e18f);
#pragma unroll
  for (int i = 0; i < 8; ++i) ok = ok && (fabsf(of[i]) < 1e37f);
  if (!hh) {
#pragma unroll
    for (int i = 8; i < 12; ++i) ok = ok && (fabsf(of[i]) < 1e37f);
  }
  if (__builtin_expect(!__all(ok), 0)) {
    __builtin_amdgcn_fence(__ATOMIC_ACQUIRE, "agent");
    const char* kv = ws + WS_KV + (size_t)head * 163840;
    const bf16x8 fq0 = kh ? q0[1] : q0[0];
    const s16x4 fq1 = kh ? q1[1] : q1[0];
    float m = -INFINITY;
    for (int t = 0; t < 64; ++t) {
      const bf16x8 kf0 = *(const bf16x8*)(kv + t * 2560 + lane * 16); const s16x4 kf1 = *(const s16x4*)(kv + t * 2560 + 1024 + r32 * 8);
      f32x16 s = MFMA32(kf0, fq0, (f32x16){}); s = MFMA32K8(kf1, fq1, s);
#pragma unroll
      for (int i = 0; i < 16; ++i) m = fmaxf(m, s[i]);
    }
    m = max32(m);
    f32x16 oo = {};
    const int vr = r32 < 20 ? r32 : 0;
    for (int t = 0; t < 64; ++t) {
      const bf16x8 kf0 = *(const bf16x8*)(kv + t * 2560 + lane * 16); const s16x4 kf1 = *(const s16x4*)(kv + t * 2560 + 1024 + r32 * 8);
      bf16x8 vf0 = *(const bf16x8*)(kv + t * 2560 + 1280 + (hh * 20 + vr) * 16), vf1 = *(const bf16x8*)(kv + t * 2560 + 1920 + (hh * 20 + vr) * 16);
      if (r32 >= 20) { vf0 = (bf16x8)(short)0x3f80; vf1 = vf0; }
      f32x16 s = MFMA32(kf0, fq0, (f32x16){}); s = MFMA32K8(kf1, fq1, s);
      u32x4 p0, p1;
#pragma unroll
      for (int i = 0; i < 4; ++i) {
        p0[i] = pk_bf16(EX2(s[2 * i] - m), EX2(s[2 * i + 1] - m));
        p1[i] = pk_bf16(EX2(s[8 + 2 * i] - m), EX2(s[8 + 2 * i + 1] - m));
      }
      oo = MFMA32(vf0, __builtin_bit_cast(bf16x8, p0), oo);
      oo = MFMA32(vf1, __builtin_bit_cast(bf16x8, p1), oo);
    }
#pragma unroll
    for (int i = 0; i < 12; ++i) of[i] = oo[i];
    l = hi32(of[8]);
  }
  const float rl = __builtin_amdgcn_rcpf(l);
  waitv<0>(); pin(fa); pin(fb); pin(fc);
  setflag(ctl + CTL_FD + gw * 8, tag, lane);
#pragma unroll
  for (int g = 0; g < 3; ++g)
    if (g < 2 || !hh) st8s(obs[g], vo, nofill(pk_bf16(of[4 * g] * rl, of[4 * g + 1] * rl), pk_bf16(of[4 * g + 2] * rl, of[4 * g + 3] * rl)));
  STAMP_AT(1, 11);
  if (!__all(fa[0] == tag[0] && fa[1] == tag[1] && fb[0] == tag[0] && fb[1] == tag[1])) poll2(ctr, fva, fvb, tag);
  if (!__all(fc[0] == tag[0] && fc[1] == tag[1])) poll(ctr, fvc, tag);
  STAMP_AT(1, 12);
}

constexpr int x_none(int) { return 0; }
constexpr int x_p4(int i) { return ((i >= 2 && i < 7) || (i >= 10 && i < 18)) ? 1 : 0; }
template <int NSTEP, int NF, int LA, int LL, int CG, int NA, int ASTR, int (*BOFF)(int), int (*XCNT)(int), class PRE, class MM>
DI void gemm_res(const char* asrc, unsigned lds0, unsigned voff, PRE&& pre, MM&& mm) {
  bf16x8 a[NSTEP][NA];
  bf16x8 bf[LL + 1][NF];
  unsigned rd0 = lds0 + voff, rd1 = rd0 + 65536, rd2 = rd0 + 131072; pin(rd0); pin(rd1); pin(rd2);
  auto issue = [&](auto n_) { constexpr int n = n_; sfor<NA>([&](auto j) { gld16<(n & 3) * 1024>(a[n][j], asrc + (n >> 2) * 4096 + j * ASTR, voff); }); };
  constexpr auto xsum = [](int lo, int hi) { int c = 0; for (int i = lo < 0 ? 0 : lo; i <= hi; ++i) c += XCNT(i); return c; };
  auto rd = [&](auto i_, auto f_) {
    constexpr int i = i_, f = f_, off = BOFF(i * NF + f);
    bf[i % (LL + 1)][f] = lds128((off < 65536 ? rd0 : off < 131072 ? rd1 : rd2) + (off & 65535));
  };
  static_assert(NSTEP % CG == 0 && LA > CG, "groups");
  unsigned m = 0;
  auto fold = [&](auto n_) { constexpr int n = n_;
    sfor<NA>([&](auto j) { pin(a[n][j]); const u32x4 u = __builtin_bit_cast(u32x4, a[n][j]); m = max(max(m, u[0]), u[1]); m = max(max(m, u[2]), u[3]); });
    pin(m);
    if constexpr (n % CG == CG - 1) {
      if (__builtin_expect(__any(m == 0xffffffffu), 0)) {
#pragma unroll 1
        for (int spin = 0; spin < (1 << 17); ++spin) {
          RETRY_COUNT(NSTEP + NF);
          sfor<CG>([&](auto k) { issue(IC<n - CG + 1 + k>{}); });
          waitv<0>(); m = 0;
          sfor<CG>([&](auto k) { sfor<NA>([&](auto j) { pin(a[n - CG + 1 + k][j]); m = max(m, umax4(__builtin_bit_cast(u32x4, a[n - CG + 1 + k][j]))); }); });
          if (!__any(m == 0xffffffffu)) break;
        }
      }
    }
  };
  sfor<LA>([&](auto n) { issue(n); });
  pre(IC<-1>{});
  sfor<LL>([&](auto i) { sfor<NF>([&](auto f) { rd(i, f); }); });
  waitv<(LA - CG) * NA>(); sfor<CG>([&](auto n) { fold(n); });
  sfor<NSTEP>([&](auto i_) {
    constexpr int i = i_;
    pre(i_);
    SB();
    sfor<NF>([&](auto f_) {
      constexpr int f = f_;
      if constexpr (f == NF - 1 && i + CG < NSTEP) {
        waitv<((i + LA < NSTEP ? i + LA : NSTEP - 1) - (i + CG)) * NA + xsum(i + CG - LA + 1, i)>(); fold(IC<i + CG>{}); SB(); }
      mm(f_, bf[i % (LL + 1)][f], a[i]);
      SB();
      if constexpr (i + LL < NSTEP) rd(IC<i + LL>{}, f_);
      if constexpr (f == 0 && i + LA < NSTEP) issue(IC<i + LA>{});
      SB();
    });
  });
}

__global__ __launch_bounds__(512, 2) void k_mega(const u16* __restrict__ x, const u16* __restrict__ Wqkv, const u16* __restrict__ Wo,
                                                 const u16* __restrict__ Wg, const u16* __restrict__ Wu, const u16* __restrict__ Wd,
                                                 char* __restrict__ ws, u16* __restrict__ out) {
  __shared__ __attribute__((aligned(16))) char smem[163840];
  const unsigned lds0 = lds_addr_of(smem);
  const int w = __builtin_amdgcn_readfirstlane(threadIdx.x >> 6);
  const int g8 = blockIdx.x & 7, i32 = blockIdx.x >> 3;
  char* ctl = ws + WS_CTL;
  const char* ctr = ctl + g8 * CTL_REP;
  u32x2 tag;
  if (i32 < 30) {
    const int i = (g8 & 1) * 30 + i32, rb = (g8 >> 1) * 4 + (i & 3), cb = i >> 2;
    if (w >= 4) { p1_load(x, Wqkv, lds0, w - 4, rb, cb); u32x2 ep; gld8<0>(ep, ctl, 0u); waitv<0>(); pin(ep); tag = make_tag(ep); }
    else if (cb >= 10) p1_body<true>(x, ws, lds0, w, rb, cb, tag);
    else p1_body<false>(x, ws, lds0, w, rb, cb, tag);
  } else {
    { RowStat rs; u32x2 ep; const int lane = fresh_lane(); gld8<0>(ep, ctl, 0u); rs_load(rs, x, blockIdx.x * 8 + w, lane); waitv<0>(); pin(ep); tag = make_tag(ep);
      rs_store(rs, ws, blockIdx.x * 8 + w, lane, tag); }
    p1_repack(Wo, ws, w, blockIdx.x - 240);
    publish(ctl + CTL_F1 + (1920 + (blockIdx.x - 240) * 8 + w) * 8, tag, fresh_lane());
  }
  bar();
  const int rb3 = g8 * 4 + (i32 >> 3), cb3 = i32 & 7;
  const int rb4 = (g8 >> 2) * 4 + (i32 >> 3), cb4 = (g8 & 3) * 8 + (i32 & 7);
  p2_attn(ws, Wg, Wu, Wd, lds0, w, tag, rb3, cb3, rb4, cb4);
  bar();
  const char* gug = ws + WS_WP + (size_t)NFR_WO * 1024 + (size_t)cb4 * 122880;
  const char* wdg = ws + WS_WP + (size_t)(NFR_WO + NFR_GU) * 1024 + (size_t)cb3 * 245760;
  u32x2 hp[5];
  if (w < 4) {
    const int lane = fresh_lane();
    const unsigned voff = lane * 16;
    const int row = rb3 * 64 + w * 16 + (lane & 15), col0 = cb3 * 80 + 4 * (lane >> 4);
    STAMP_AT(2, 0);
    u32x2 xr[5];
#pragma unroll
    for (int ct = 0; ct < 5; ++ct) xr[ct] = *(const u32x2*)(x + (size_t)row * 640 + col0 + 16 * ct);
    { const int hd = lane & 31, cgt = (hd & 1) ? hd * 5 : hd * 5 + 4;
      probe(ws + WS_O + (size_t)(rb3 * 4 + w) * 20480, (unsigned)((cgt >> 3) * 1024 + ((cgt & 7) >> 1) * 256 + (cgt & 1) * 8)); }
    sfor<5>([&](auto ct) { pin(xr[ct]); });
    STAMP_AT(2, 1);
    f32x4 acc[5] = {};
    sfor<5>([&](auto ct) { pin(acc[ct]); });
    gemm_res<20, 5, 20, 2, 4, 1, 0, wo_off, x_none>(ws + WS_O + (size_t)(rb3 * 4 + w) * 20480, lds0, voff,
      [&](auto i) { if constexpr (i == -1) STAMP_AT(2, 2); if constexpr (i == 1) STAMP_AT(2, 3); if constexpr (i == 10) STAMP_AT(2, 4);
        if constexpr (i == P3_G1 || i == 12) bar(); },
      [&](auto ct, const bf16x8& b, bf16x8 (&a)[1]) { mfma16(acc[ct], b, a[0]); });
    acc_fence(); sfor<5>([&](auto ct) { pin(acc[ct]); });
    STAMP_AT(2, 6);
    float s1 = 0.f, s2 = 0.f;
    const char* hb = ws + WS_H + (size_t)(rb3 * 4 + w) * 20480;
    const unsigned vt = t16lane(lane);
#pragma unroll
    for (int ct = 0; ct < 5; ++ct) {
      hp[ct] = nofill(pk_bf16(bf_lo(xr[ct][0]) + acc[ct][0], bf_hi(xr[ct][0]) + acc[ct][1]), pk_bf16(bf_lo(xr[ct][1]) + acc[ct][2], bf_hi(xr[ct][1]) + acc[ct][3]));
      st8s(hb + t16off(cb3 * 80 + ct * 16), vt, hp[ct]);
      s1 = dot2(hp[ct][0], 0x3f803f80u, s1); s1 = dot2(hp[ct][1], 0x3f803f80u, s1);
      s2 = dot2(hp[ct][0], hp[ct][0], s2); s2 = dot2(hp[ct][1], hp[ct][1], s2);
    }
    s1 = red4(s1); s2 = red4(s2);
    if (lane < 16) st8s(ws + WS_HST + (size_t)(rb3 * 4 + w) * 1024 + (cb3 >> 1) * 256 + (cb3 & 1) * 8, (unsigned)(lane * 16), nofill(__float_as_uint(s1), __float_as_uint(s2)));
    bar();
    STAMP_AT(2, 7);
  } else {
    const int lane = fresh_lane(), v = w - 4;
    const unsigned voff = lane * 16;
    float cpart[10];
    const int cc = v * 24 + min(lane & 31, 23);
    sfor<10>([&](auto i) { gld4<i * 384>(cpart[i], ws + WS_CSP, (unsigned)(cb4 * 7680 + (lane >> 5) * 3840 + cc * 4)); });
    poll(ctr + CTL_FD, (unsigned)((cb3 * 240 + v * 60 + min(lane, 59)) * 8), tag);
    float cv = 0.f;
    sfor<10>([&](auto j) { pin(cpart[j]); cv += cpart[j]; });
    cv = add32(cv);
    bar();
    STAMP_AT(2, 2);
    sfor<5>([&](auto j) { glds16<true>(gug + (v * 5 + j) * 1024, voff, lds0 + gu_off(0) + (v * 5 + j) * 1024); });
    sfor<10>([&](auto j) { glds16<true>(gug + (20 + v * 10 + j) * 1024, voff, lds0 + gu_off(20) + (v * 10 + j) * 1024); });
    bar();
    sfor<15>([&](auto j) { glds16<true>(gug + (60 + v * 15 + j) * 1024, voff, lds0 + gu_off(60) + (v * 15 + j) * 1024); });
    STAMP_AT(2, 5);
    waitv<0>();
    bar();
    STAMP_AT(2, 6);
    if (lane < 24) ((LAS float*)(uintptr_t)(lds0 + CSF_OFF))[cc] = cv;
  }
  {
    const int lane = fresh_lane();
    const unsigned voff = lane * 16;
    const int r16 = lane & 15, g4 = lane >> 4;
    STAMP_AT(3, 0);
    f32x4 cs[6]; f32x2 ms[2];
    f32x4 hs[2];
#pragma unroll 1
    for (int spin = 0; spin < (1 << 17); ++spin) {
      gld16<0>(hs[0], ws + WS_HST + (size_t)(rb4 * 16 + w * 2) * 1024, voff);
      gld16<1024>(hs[1], ws + WS_HST + (size_t)(rb4 * 16 + w * 2) * 1024, voff);
      waitv<0>(); pin(hs[0]); pin(hs[1]);
      if (!__any(max(umax4(__builtin_bit_cast(u32x4, hs[0])), umax4(__builtin_bit_cast(u32x4, hs[1]))) == 0xffffffffu)) break;
      __builtin_amdgcn_s_sleep(1);
    }
    STAMP_AT(3, 1);
    f32x4 acc[2][6] = {};
    sfor<6>([&](auto ct) { pin(acc[0][ct]); pin(acc[1][ct]); });
    if (w < 4) __builtin_amdgcn_s_setprio(1);
    gemm_res<20, 6, K4_LA_, 1, K4_CG_, 2, 20480, gu_off, x_p4>(ws + WS_H + (size_t)(rb4 * 16 + w * 2) * 20480, lds0, voff,
      [&](auto i) { if constexpr (i == -1) STAMP_AT(3, 2); if constexpr (i == 1) STAMP_AT(3, 3); if constexpr (i == 10) STAMP_AT(3, 4);
        if constexpr (i == 9) bar();
        if constexpr (i == -1) sfor<2>([&](auto t) {
          const float s1 = red4(hs[t][0] + hs[t][2]), s2 = red4(hs[t][1] + hs[t][3]), mean = s1 * (1.f / 640.f);
          ms[t] = (f32x2){mean, __builtin_amdgcn_rsqf(fmaxf(s2 * (1.f / 640.f) - mean * mean, 0.f) + LN_EPS)}; pin(ms[t]); });
        if constexpr (i == 18) sfor<6>([&](auto ct) { cs[ct] = *(const LAS f32x4*)(uintptr_t)(lds0 + CSF_OFF + (ct * 16 + 4 * g4) * 4); });
        if constexpr (i >= 2 && i < 7) { const int f = (i - 2) * 8 + w - ((i == 6 && w >= 4) ? 4 : 0); glds16<true>(wdg + (f) * 1024, voff, lds0 + wd_off(0) + f * 1024); }
        if constexpr (i >= 10 && i < 15) { constexpr int n = i - 10; glds16<true>(wdg + (40 + n * 8 + w) * 1024, voff, lds0 + wd_off(40) + (n * 8 + w) * 1024); }
        if constexpr (i >= 15 && i < 18) { const int f = (i - 15) * 8 + w - ((i == 17 && w >= 4) ? 4 : 0); glds16<true>(wdg + (140 + f) * 1024, voff, lds0 + wd_off(140) + f * 1024); } },
      [&](auto ct, const bf16x8& b, bf16x8 (&a)[2]) { mfma16(acc[0][ct], b, a[0]); mfma16(acc[1][ct], b, a[1]); });
    acc_fence(); sfor<6>([&](auto ct) { pin(acc[0][ct]); pin(acc[1][ct]); });
    STAMP_AT(3, 6);
    constexpr float GC0 = -2.f * 0.7978845608028654f * 1.4426950408889634f, GC1 = GC0 * 0.044715f;
    const char* gb = ws + WS_G + (size_t)(rb4 * 16 + w * 2) * 49152;
    const unsigned vt = t16lane(lane);
#pragma unroll
    for (int t = 0; t < 2; ++t) {
      const float nm = -ms[t].x, rstd = ms[t].y;
#pragma unroll
      for (int ct = 0; ct < 3; ++ct) {
        const f32x4 gv = (acc[t][ct] + cs[ct] * nm) * rstd, uv = (acc[t][3 + ct] + cs[3 + ct] * nm) * rstd;
        const f32x4 z = gv * (gv * gv * GC1 + GC0);
        f32x4 e;
#pragma unroll
        for (int j = 0; j < 4; ++j) e[j] = __builtin_amdgcn_exp2f(z[j]);
        e = e + 1.f;
#pragma unroll
        for (int j = 0; j < 4; ++j) e[j] = __builtin_amdgcn_rcpf(e[j]);
        const f32x4 r = gv * uv * e;
        st8s(gb + t * 49152 + t16off(cb4 * 48 + ct * 16), vt, nofill(pk_bf16(r[0], r[1]), pk_bf16(r[2], r[3])));
      }
    }
    __builtin_amdgcn_s_setprio(0);
    STAMP_AT(3, 7);
    waitv<6>();
    STAMP_AT(3, 8);
    bar();
  }
  if (w < 4) {
    const int lane = fresh_lane();
    const unsigned voff = lane * 16;
    const int row = rb3 * 64 + w * 16 + (lane & 15), col0 = cb3 * 80 + 4 * (lane >> 4);
    STAMP_AT(4, 0);
    probe(ws + WS_G + (size_t)(rb3 * 4 + w) * 49152, (unsigned)t16off((lane & 31) * 48 + 32));
    STAMP_AT(4, 1);
    f32x4 acc[5] = {};
    sfor<5>([&](auto ct) { pin(acc[ct]); });
    gemm_res<48, 5, K5_LA_, 2, 4, 1, 0, wd_off, x_none>(ws + WS_G + (size_t)(rb3 * 4 + w) * 49152, lds0, voff,
      [&](auto i) { if constexpr (i == 1) STAMP_AT(4, 3); if constexpr (i == 14) STAMP_AT(4, 4); if constexpr (i == 30) STAMP_AT(4, 5);
        if constexpr (i == -1 || i == 8 || i == 14 || i == 16 || i == 30 || i == 38) bar(); },
      [&](auto ct, const bf16x8& b, bf16x8 (&a)[1]) { mfma16(acc[ct], b, a[0]); });
    acc_fence(); sfor<5>([&](auto ct) { pin(acc[ct]); });
    STAMP_AT(4, 12);
#pragma unroll
    for (int ct = 0; ct < 5; ++ct) {
      const unsigned p0 = pk_bf16(bf_lo(hp[ct][0]) + acc[ct][0], bf_hi(hp[ct][0]) + acc[ct][1]);
      const unsigned p1 = pk_bf16(bf_lo(hp[ct][1]) + acc[ct][2], bf_hi(hp[ct][1]) + acc[ct][3]);
      *(u32x2*)(out + (size_t)row * 640 + col0 + 16 * ct) = (u32x2){p0, p1};
    }
    STAMP_AT(4, 13);
  } else {
    const int lane = fresh_lane(), v = w - 4;
    const unsigned voff = lane * 16;
    glds16<true>(wdg + (36 + v) * 1024, voff, lds0 + wd_off(36) + v * 1024);
    waitv<0>(); bar();
    sfor<3>([&](auto g) { sfor<5>([&](auto j) { constexpr int f = (g + 4) * 20 + j; glds16<true>(wdg + (f + v * 5) * 1024, voff, lds0 + wd_off(f) + v * 5120); }); });
    bar();
    sfor<2>([&](auto g) { sfor<5>([&](auto j) { constexpr int f = (g + 8) * 20 + j; glds16<true>(wdg + (f + v * 5) * 1024, voff, lds0 + wd_off(f) + v * 5120); }); });
    waitv<10>(); bar();
    bar();
    sfor<2>([&](auto g) { sfor<5>([&](auto j) { constexpr int f = (g + 10) * 20 + j; glds16<true>(wdg + (f + v * 5) * 1024, voff, lds0 + wd_off(f) + v * 5120); }); });
    waitv<10>(); bar();
    waitv<0>(); bar();
    if (blockIdx.x == 0 && w == 4) {
      poll2(ctr + CTL_FR, (unsigned)(lane * 64), (unsigned)(4096 + lane * 64), tag);
      poll2(ctr + CTL_FR, (unsigned)(8192 + lane * 64), (unsigned)(12288 + lane * 64), tag);
      if (lane == 0) st8(ctl, tag);
    }
  }
}

extern "C" void kernel_launch(void* const* d_in, const int* in_sizes, int n_in, void* d_out, int out_size, void* d_ws, size_t ws_size,
                              hipStream_t stream) {
  k_mega<<<256, 512, 0, stream>>>((const u16*)d_in[0], (const u16*)d_in[1], (const u16*)d_in[2], (const u16*)d_in[4], (const u16*)d_in[5],
                                  (const u16*)d_in[6], (char*)d_ws, (u16*)d_out);
}
```

```cpp
#include <hip/hip_runtime.h>
#include <hip/hip_bf16.h>
#include <stdint.h>
#include <utility>
#define CW1 30
#define CW1S 0
#define CW2 28

typedef short bf16x8 __attribute__((ext_vector_type(8)));
typedef short s16x4 __attribute__((ext_vector_type(4)));
typedef float f32x16 __attribute__((ext_vector_type(16)));
typedef float f32x4 __attribute__((ext_vector_type(4)));
typedef float f32x2 __attribute__((ext_vector_type(2)));
typedef unsigned u32x4 __attribute__((ext_vector_type(4)));
typedef unsigned u32x2 __attribute__((ext_vector_type(2)));
typedef __bf16 bf2_t __attribute__((ext_vector_type(2)));
typedef unsigned short u16;
#define LAS __attribute__((address_space(3)))
#define DI __device__ __forceinline__

constexpr float LN_EPS = 1e-5f;
constexpr float QSCALE = 0.22360679774997896f * 1.4426950408889634f;

constexpr size_t WS_Q   = 0;
constexpr size_t WS_KV  = WS_Q + 2621440;
constexpr size_t WS_O   = WS_KV + 5242880;
constexpr size_t WS_H   = WS_O + 2621440;
constexpr size_t WS_HST = WS_H + 2621440;
constexpr size_t WS_G   = WS_HST + 131072;
constexpr size_t WS_WP  = WS_G + 6291456;
constexpr size_t WS_CSP = WS_WP + 6717440;
constexpr size_t WS_RST = WS_CSP + 245760;
constexpr size_t WS_CTL = WS_RST + 32768;
constexpr size_t CTL_F1 = 256;
constexpr size_t CTL_F2 = CTL_F1 + 2048 * 8;
constexpr size_t CTL_FR = CTL_F2 + 2048 * 8;
constexpr size_t CTL_F3 = CTL_FR + 2048 * 8;
constexpr size_t CTL_F4 = CTL_F3 + 1024 * 8;
constexpr size_t CTL_FD = CTL_F4 + 2048 * 8;
constexpr size_t CTL_REP = CTL_FD + 2048 * 8 - 256;
constexpr int NFR_WO = 800, NFR_GU = 3840, NFR_WD = 1920;

DI unsigned pk_bf16(float lo, float hi) { f32x2 f = {lo, hi}; return __builtin_bit_cast(unsigned, __builtin_convertvector(f, bf2_t)); }
DI float bf_lo(unsigned u) { return __uint_as_float(u << 16); }
DI float bf_hi(unsigned u) { return __uint_as_float(u & 0xffff0000u); }
DI float dot2(unsigned a, unsigned b, float c) { return __builtin_amdgcn_fdot2_f32_bf16(__builtin_bit_cast(bf2_t, a), __builtin_bit_cast(bf2_t, b), c, false); }
DI float add32(float v) { const auto r = __builtin_amdgcn_permlane32_swap(__float_as_uint(v), __float_as_uint(v), false, false); return __uint_as_float(r[0]) + __uint_as_float(r[1]); }
DI float max32(float v) { const auto r = __builtin_amdgcn_permlane32_swap(__float_as_uint(v), __float_as_uint(v), false, false); return fmaxf(__uint_as_float(r[0]), __uint_as_float(r[1])); }
DI float add16(float v) { const auto r = __builtin_amdgcn_permlane16_swap(__float_as_uint(v), __float_as_uint(v), false, false); return __uint_as_float(r[0]) + __uint_as_float(r[1]); }
DI float red4(float v) { return add32(add16(v)); }
DI float hi32(float v) { const auto r = __builtin_amdgcn_permlane32_swap(__float_as_uint(v), __float_as_uint(v), false, false); return __uint_as_float(r[1]); }
#define MFMA32(a, b, c) __builtin_amdgcn_mfma_f32_32x32x16_bf16((a), (b), (c), 0, 0, 0)
#define MFMA32K8(a, b, c) __builtin_amdgcn_mfma_f32_32x32x8bf16_1k((a), (b), (c), 0, 0, 0)
#define MFMA16(a, b, c) __builtin_amdgcn_mfma_f32_16x16x32_bf16((a), (b), (c), 0, 0, 0)

DI void mfma16(f32x4& c, const bf16x8& a, const bf16x8& b) { asm volatile("v_mfma_f32_16x16x32_bf16 %0, %1, %2, %0" : "+v"(c) : "v"(a), "v"(b)); }
DI void acc_fence() { asm volatile("s_nop 7\n\ts_nop 3" ::: "memory"); }
template <bool DEV> DI void glds16(const void* sbase, unsigned voff, unsigned lds_base) {
  if constexpr (DEV) asm volatile("s_mov_b32 m0, %2\n\ts_nop 0\n\tglobal_load_lds_dwordx4 %0, %1 sc1" :: "v"(voff), "s"(sbase), "s"(lds_base) : "memory");
  else asm volatile("s_mov_b32 m0, %2\n\ts_nop 0\n\tglobal_load_lds_dwordx4 %0, %1" :: "v"(voff), "s"(sbase), "s"(lds_base) : "memory");
}
template <int OFF, class T> DI void gld16(T& dst, const void* sbase, unsigned voff) {
  asm volatile("global_load_dwordx4 %0, %1, %2 offset:%3 sc1" : "=v"(dst) : "v"(voff), "s"(sbase), "i"(OFF) : "memory");
}
template <int OFF, class T> DI void gld8(T& dst, const void* sbase, unsigned voff) {
  asm volatile("global_load_dwordx2 %0, %1, %2 offset:%3 sc1" : "=v"(dst) : "v"(voff), "s"(sbase), "i"(OFF) : "memory");
}
template <int OFF> DI void gld4(float& dst, const void* sbase, unsigned voff) {
  asm volatile("global_load_dword %0, %1, %2 offset:%3 sc1" : "=v"(dst) : "v"(voff), "s"(sbase), "i"(OFF) : "memory");
}
template <int OFF> DI void gldu16(unsigned& dst, const void* sbase, unsigned voff) {
  asm volatile("global_load_ushort %0, %1, %2 offset:%3" : "=v"(dst) : "v"(voff), "s"(sbase), "i"(OFF) : "memory");
}
DI void st16(void* p, u32x4 v) { asm volatile("global_store_dwordx4 %0, %1, off sc1\n\ts_nop 1" :: "v"(p), "v"(v) : "memory"); }
DI void st8(void* p, u32x2 v) { asm volatile("global_store_dwordx2 %0, %1, off sc1" :: "v"(p), "v"(v) : "memory"); }
DI void st4(void* p, float v) { asm volatile("global_store_dword %0, %1, off sc1" :: "v"(p), "v"(v) : "memory"); }
DI void st8s(const void* sbase, unsigned voff, u32x2 v) { asm volatile("global_store_dwordx2 %0, %1, %2 sc1" :: "v"(voff), "v"(v), "s"(sbase) : "memory"); }
DI u32x2 nofill(unsigned a, unsigned b) { return (u32x2){min(a, 0xfffffffeu), min(b, 0xfffffffeu)}; }
DI unsigned umax4(u32x4 v) { return max(max(v[0], v[1]), max(v[2], v[3])); }
template <int N> DI void waitv() { asm volatile("s_waitcnt vmcnt(%0)" :: "i"(N) : "memory"); }
DI void bar() { asm volatile("s_waitcnt lgkmcnt(0)\n\ts_barrier" ::: "memory"); }
DI void setflag(char* flag, u32x2 tag, int lane) { if (lane < 8) st8(flag + lane * CTL_REP, tag); }
DI void publish(char* flag, u32x2 tag, int lane) { waitv<0>(); setflag(flag, tag, lane); }
DI void poll(const char* base, unsigned voff, u32x2 tag) {
#pragma unroll 1
  for (int spin = 0; spin < (1 << 17); ++spin) {
    u32x2 v;
    asm volatile("global_load_dwordx2 %0, %1, %2 sc1\n\ts_waitcnt vmcnt(0)" : "=v"(v) : "v"(voff), "s"(base) : "memory");
    if (__all(v[0] == tag[0] && v[1] == tag[1])) break;
    __builtin_amdgcn_s_sleep(1);
  }
}
DI void poll2(const char* base, unsigned voff0, unsigned voff1, u32x2 tag) {
#pragma unroll 1
  for (int spin = 0; spin < (1 << 17); ++spin) {
    u32x2 v, u;
    asm volatile("global_load_dwordx2 %0, %2, %4 sc1\n\tglobal_load_dwordx2 %1, %3, %4 sc1\n\ts_waitcnt vmcnt(0)" : "=&v"(v), "=&v"(u) : "v"(voff0), "v"(voff1), "s"(base) : "memory");
    if (__all(v[0] == tag[0] && v[1] == tag[1] && u[0] == tag[0] && u[1] == tag[1])) break;
    __builtin_amdgcn_s_sleep(1);
  }
}
DI void probe(const char* base, unsigned voff) {
#pragma unroll 1
  for (int spin = 0; spin < (1 << 17); ++spin) {
    unsigned v;
    asm volatile("global_load_dword %0, %1, %2 sc1\n\ts_waitcnt vmcnt(0)" : "=v"(v) : "v"(voff), "s"(base) : "memory");
    if (__all(v != 0xffffffffu)) break;
    __builtin_amdgcn_s_sleep(1);
  }
}
constexpr int t16off(int c0) { return (c0 >> 5) * 1024 + (c0 & 16) * 32; }
DI unsigned t16lane(int lane) { return (unsigned)((lane >> 5) * 256 + (lane & 15) * 16 + ((lane >> 4) & 1) * 8); }
DI void st16s(const void* sbase, unsigned voff, u32x4 v) { asm volatile("global_store_dwordx4 %0, %1, %2 sc1\n\ts_nop 1" :: "v"(voff), "v"(v), "s"(sbase) : "memory"); }
DI float row16_sum(float v) {
  v += __builtin_bit_cast(float, __builtin_amdgcn_update_dpp(0, __builtin_bit_cast(int, v), 0x128, 0xf, 0xf, false));
  v += __builtin_bit_cast(float, __builtin_amdgcn_update_dpp(0, __builtin_bit_cast(int, v), 0x124, 0xf, 0xf, false));
  v += __builtin_bit_cast(float, __builtin_amdgcn_update_dpp(0, __builtin_bit_cast(int, v), 0x122, 0xf, 0xf, false));
  v += __builtin_bit_cast(float, __builtin_amdgcn_update_dpp(0, __builtin_bit_cast(int, v), 0x121, 0xf, 0xf, false));
  return v;
}
DI u32x2 make_tag(u32x2 ep) {
  const unsigned long long e = (((unsigned long long)__builtin_amdgcn_readfirstlane(ep[1]) << 32) | __builtin_amdgcn_readfirstlane(ep[0])) + 1ull;
  return (u32x2){(unsigned)e, (unsigned)(e >> 32)};
}
constexpr unsigned RS_K0 = 0x9e3779b9u, RS_K1 = 0x7f4a7c15u;
struct RowStat { u32x4 a; unsigned b; };
DI void rs_load(RowStat& r, const u16* x, int row, int lane) {
  asm volatile("global_load_dwordx4 %0, %2, %4\n\tglobal_load_dword %1, %3, %4 offset:1024" : "=&v"(r.a), "=&v"(r.b) : "v"(lane * 16), "v"(lane * 4), "s"((const char*)x + (size_t)row * 1280) : "memory");
}
DI void rs_store(RowStat& r, char* ws, int row, int lane, u32x2 tag) {
  asm volatile("" : "+v"(r.a), "+v"(r.b));
  float s1 = dot2(r.b, 0x3f803f80u, 0.f), s2 = dot2(r.b, r.b, 0.f);
#pragma unroll
  for (int j = 0; j < 4; ++j) { s1 = dot2(r.a[j], 0x3f803f80u, s1); s2 = dot2(r.a[j], r.a[j], s2); }
  s1 = red4(row16_sum(s1)); s2 = red4(row16_sum(s2));
  const float mean = s1 * (1.f / 640.f), rstd = __builtin_amdgcn_rsqf(fmaxf(s2 * (1.f / 640.f) - mean * mean, 0.f) + LN_EPS);
  const unsigned m = __float_as_uint(mean), q = __float_as_uint(rstd);
  if (lane == 0) st16(ws + WS_RST + (size_t)row * 16, (u32x4){m, q, tag[0] ^ m ^ RS_K0, tag[1] ^ q ^ RS_K1});
}
template <int SKIP, int KB> DI void code_warm(u32x4& dum, int idx, unsigned voff) {
  unsigned long long pc; asm volatile("s_getpc_b64 %0" : "=s"(pc));
  asm volatile("global_load_dwordx4 %0, %1, %2" : "+v"(dum) : "v"(voff), "s"((const char*)(pc & ~63ull) + (SKIP + min(idx, KB - 1)) * 1024) : "memory");
}
DI int fresh_lane() { int l = __builtin_amdgcn_mbcnt_hi(~0u, __builtin_amdgcn_mbcnt_lo(~0u, 0u)); asm volatile("" : "+v"(l)); return l; }
template <class T> DI void pins(T& v) { asm volatile("" : "+s"(v)); }
template <class T> DI void pin(T& v) { asm volatile("" : "+v"(v)); }
DI bf16x8 lds128(unsigned addr) { return *(const LAS bf16x8*)(uintptr_t)addr; }
DI s16x4 lds64(unsigned addr) { return *(const LAS s16x4*)(uintptr_t)addr; }
DI s16x4 ldstr(unsigned addr) { return __builtin_amdgcn_ds_read_tr16_b64_v4i16((LAS s16x4*)(uintptr_t)addr); }
DI unsigned lds_addr_of(const void* p) { return (unsigned)(uintptr_t)(const LAS char*)p; }
#define STAMP_AT(kid, idx) ((void)0)
#define RETRY_COUNT(k) ((void)0)
#define WAITV_BAR(N) asm volatile("s_waitcnt vmcnt(" #N ") lgkmcnt(0)\n\ts_barrier" ::: "memory")
#define SB() __builtin_amdgcn_sched_barrier(0)
template <int N> using IC = std::integral_constant<int, N>;
template <class F, int... Is> DI void sfor_(F&& f, std::integer_sequence<int, Is...>) { (f(IC<Is>{}), ...); }
template <int N, class F> DI void sfor(F&& f) { sfor_(f, std::make_integer_sequence<int, N>{}); }

constexpr int P3_G1 = -1, K4_LA_ = 6, K4_CG_ = 2, K5_LA_ = 16;
constexpr int K1_STAGE = 32768;
DI void p1_load(const u16* __restrict__ x, const u16* __restrict__ W, const unsigned lds0, const int v, int rb, int cb) {
  const int lane = fresh_lane();
  const unsigned a_voff0 = (unsigned)((lane >> 3) * 1280 + (((lane & 7) ^ ((lane >> 4) & 7)) << 4));
  const unsigned a_voff1 = (unsigned)((lane >> 3) * 1280 + (((lane & 7) ^ ((4 + (lane >> 4)) & 7)) << 4)) + 8 * 1280;
  const unsigned w_voff0 = (unsigned)((lane >> 3) * 3840 + (((lane & 7) ^ (((lane >> 4) & 1) << 2)) << 4));
  const unsigned w_voff1 = w_voff0 + 8 * 3840;
  const char* xs = (const char*)x + (size_t)(rb * 128 + v * 32) * 1280;
  const char* wsrc = (const char*)W + (size_t)((v & 1) * 32) * 3840 + (size_t)(cb * 128 + (v >> 1) * 64) * 2;
  const int rot = ((((blockIdx.x >> 1) & 3) * 5 + (blockIdx.x & 1) * 10) >> 1) % 10;
  auto st = [&](auto s_) {
    constexpr int s0 = s_; const unsigned d = lds0 + (s0 % 5) * K1_STAGE + v * 4096;
    const int s = s0 + rot - (s0 + rot >= 10 ? 10 : 0);
    const char* xp = xs + s * 128; const char* wp = wsrc + (size_t)s * 64 * 3840;
    glds16<false>(xp, a_voff0, d); glds16<false>(wp, w_voff0, d + 16384);
    glds16<false>(xp, a_voff1, d + 1024); glds16<false>(wp, w_voff1, d + 16384 + 1024);
    glds16<false>(xp + 16 * 1280, a_voff0, d + 2048); glds16<false>(wp + 16 * 3840, w_voff0, d + 16384 + 2048);
    glds16<false>(xp + 16 * 1280, a_voff1, d + 3072); glds16<false>(wp + 16 * 3840, w_voff1, d + 16384 + 3072);
  };
  u32x4 cdum = {};
  st(IC<0>{}); st(IC<1>{});
  sfor<10>([&](auto s_) { constexpr int s = s_;
    waitv<(s == 0 ? 8 : s <= 7 ? 16 : s == 8 ? 8 : 0)>(); asm volatile("s_barrier" ::: "memory");
    if constexpr (s == 7 && CW1 > 0) { if (v == 0) code_warm<CW1S, CW1>(cdum, blockIdx.x >> 3, (unsigned)(lane * 16)); }
    if constexpr (s == 0) { st(IC<2>{}); st(IC<3>{}); }
    else if constexpr (s + 3 < 10) st(IC<s + 3>{}); });
  bar();
  waitv<0>(); pin(cdum);
}
template <bool ISV>
DI void p1_body(const u16* __restrict__ x, char* __restrict__ ws, const unsigned lds0, const int w, int rb, int cb, u32x2& tag) {
  const int lane = fresh_lane();
  u32x2 ep; gld8<0>(ep, ws + WS_CTL, 0u);
  RowStat rs[2];
  rs_load(rs[0], x, blockIdx.x * 8 + w * 2, lane); rs_load(rs[1], x, blockIdx.x * 8 + w * 2 + 1, lane);
  const int wm = w >> 1, wn = w & 1;
  const int r32 = lane & 31, hh = lane >> 5;
  STAMP_AT(0, 0);
  unsigned xa[3][4], wa[3][2][2];
  {
    const int row = wm * 64 + r32;
    const int blk = (lane >> 4) & 1, q = (lane & 15) >> 2, p = lane & 3;
#pragma unroll
    for (int hs = 0; hs < 3; ++hs) {
#pragma unroll
      for (int i = 0; i < 4; ++i) { xa[hs][i] = lds0 + hs * 65536 + (unsigned)(row * 128 + ((((i ^ wm) * 2 + hh) ^ ((row >> 1) & 7)) << 4)); pin(xa[hs][i]); }
#pragma unroll
      for (int j = 0; j < 2; ++j) {
        const unsigned base = lds0 + hs * 65536 + (unsigned)(16384 + wn * 8192 + (8 * hh + q) * 128 + (((j * 4 + blk * 2 + (p >> 1)) ^ (((q >> 1) & 1) << 2)) << 4) + (p & 1) * 8);
        wa[hs][j][0] = base + wm * 2048; wa[hs][j][1] = base + (1 ^ wm) * 2048; pin(wa[hs][j][0]); pin(wa[hs][j][1]);
      }
    }
  }
  f32x16 acc[2][2] = {};
  f32x4 sw[2] = {};
  u32x4 rec;
  bf16x8 sel = (bf16x8)(short)(((lane & 15) == ((lane >> 4) & 1)) ? 0x3f80 : 0); pin(sel);
  bf16x8 fx[2][2]; s16x4 fwl[2][2], fwh[2][2];
  auto rd = [&](auto i_, auto k_) {
    constexpr int i = i_, k = k_, s = i >> 2, p = i & 3, sl = s % 5, hs = sl >> 1; constexpr unsigned so = (sl & 1) * K1_STAGE;
    if constexpr (k == 0) fx[i & 1][0] = lds128(xa[hs][p] + so);
    if constexpr (k == 3) fx[i & 1][1] = lds128(xa[hs][p] + so + 4096);
    if constexpr (k == 1 || k == 2) { fwl[i & 1][k - 1] = ldstr(wa[hs][k - 1][p & 1] + so + (p >> 1) * 4096); fwh[i & 1][k - 1] = ldstr(wa[hs][k - 1][p & 1] + so + (p >> 1) * 4096 + 512); }
  };
  const bool isq = cb < 5;
  const int pitch = isq ? 1280 : 2560;
  unsigned off[2][ISV ? 1 : 4];
#pragma unroll
  for (int j = 0; j < 2; ++j) {
    if constexpr (ISV) {
      const int n = cb * 128 + wn * 64 + j * 32 + r32 - 1280, head = n / 20, d0 = n - head * 20;
      const int d = (head & 1) ? (d0 < 4 ? d0 + 16 : d0 - 4) : d0;
      off[j][0] = (unsigned)(head * 163840 + 1280 + (hh * 20 + d) * 16); pin(off[j][0]);
    } else {
#pragma unroll
      for (int g = 0; g < 4; ++g) {
        const int n = cb * 128 + wn * 64 + j * 32 + 8 * g + 4 * hh - (isq ? 0 : 640), head = n / 20, d0 = n - head * 20;
        const int d = (head & 1) ? (d0 < 4 ? d0 + 16 : d0 - 4) : d0;
        off[j][g] = (unsigned)(head * 64 * pitch + (d < 16 ? (((d >> 3) * 32 + r32) * 16 + (d & 4) * 2) : (1024 + r32 * 8))); pin(off[j][g]);
      }
    }
  }
  waitv<0>(); pin(ep); tag = make_tag(ep);
  rs_store(rs[0], ws, blockIdx.x * 8 + w * 2, lane, tag); rs_store(rs[1], ws, blockIdx.x * 8 + w * 2 + 1, lane, tag);
  asm volatile("s_barrier" ::: "memory");
  STAMP_AT(0, 1);
  sfor<4>([&](auto k) { rd(IC<0>{}, k); });
  sfor<40>([&](auto i_) {
    constexpr int i = i_, b = i & 1;
    if constexpr ((i & 3) == 3 && i < 39) asm volatile("s_barrier" ::: "memory");
    if constexpr (i == 30) gld16<0>(rec, ws + WS_RST + (size_t)(rb * 128 + wm * 64) * 16, (unsigned)(lane * 16));
    SB();
    const bf16x8 w0 = __builtin_shufflevector(fwl[b][0], fwh[b][0], 0, 1, 2, 3, 4, 5, 6, 7), w1 = __builtin_shufflevector(fwl[b][1], fwh[b][1], 0, 1, 2, 3, 4, 5, 6, 7);
    sfor<4>([&](auto k_) { constexpr int k = k_, t = k >> 1, j = (k ^ (k >> 1)) & 1;
      if constexpr (ISV) acc[t][j] = MFMA32(fx[b][t], j ? w1 : w0, acc[t][j]); else acc[t][j] = MFMA32(j ? w1 : w0, fx[b][t], acc[t][j]);
      SB();
      if constexpr (i < 39) rd(IC<i + 1>{}, k_);
      SB(); });
    if constexpr (b == 1) { sw[0] = MFMA16(w0, sel, sw[0]); sw[1] = MFMA16(w1, sel, sw[1]); }
    SB();
  });
  STAMP_AT(0, 11);
  LAS float* csp = (LAS float*)(uintptr_t)lds0;
  if ((lane & 15) < 2) sfor<2>([&](auto j) { *(LAS f32x4*)&csp[wm * 128 + wn * 64 + j * 32 + (lane & 1) * 16 + (lane >> 4) * 4] = sw[j]; });
  waitv<0>(); pin(rec);
#pragma unroll 1
  for (int spin = 0; spin < (1 << 17) && !__all((rec[2] ^ rec[0] ^ RS_K0) == tag[0] && (rec[3] ^ rec[1] ^ RS_K1) == tag[1]); ++spin) {
    gld16<0>(rec, ws + WS_RST + (size_t)(rb * 128 + wm * 64) * 16, (unsigned)(lane * 16)); waitv<0>(); pin(rec);
  }
  LAS f32x2* st = (LAS f32x2*)(uintptr_t)(lds0 + 4096 + w * 512);
  if constexpr (ISV) st[lane] = (f32x2){__uint_as_float(rec[0]), __uint_as_float(rec[1])};
  bar();
  STAMP_AT(0, 12);
  if constexpr (ISV) {
    float c[2];
#pragma unroll
    for (int j = 0; j < 2; ++j) c[j] = csp[wn * 64 + j * 32 + r32] + csp[128 + wn * 64 + j * 32 + r32];
#pragma unroll
    for (int t = 0; t < 2; ++t) {
      f32x4 s01[4], s23[4];
#pragma unroll
      for (int g = 0; g < 4; ++g) { s01[g] = *(const LAS f32x4*)&st[t * 32 + 8 * g + 4 * hh]; s23[g] = *(const LAS f32x4*)&st[t * 32 + 8 * g + 4 * hh + 2]; }
      const char* dst = ws + WS_KV + (size_t)(rb * 4 + wm * 2 + t) * 2560;
#pragma unroll
      for (int j = 0; j < 2; ++j) {
        const f32x16& a = acc[t][j];
        unsigned pk[8];
#pragma unroll
        for (int g = 0; g < 4; ++g) {
          pk[2 * g]     = pk_bf16(s01[g][1] * (a[4 * g] - s01[g][0] * c[j]), s01[g][3] * (a[4 * g + 1] - s01[g][2] * c[j]));
          pk[2 * g + 1] = pk_bf16(s23[g][1] * (a[4 * g + 2] - s23[g][0] * c[j]), s23[g][3] * (a[4 * g + 3] - s23[g][2] * c[j]));
        }
        st16s(dst, off[j][0], (u32x4){pk[0], pk[1], pk[2], pk[3]});
        st16s(dst + 640, off[j][0], (u32x4){pk[4], pk[5], pk[6], pk[7]});
      }
    }
  } else {
    const float sc = isq ? QSCALE : 1.f;
    const char* base = ws + (isq ? WS_Q : WS_KV) + (size_t)(rb * 4 + wm * 2) * pitch;
    const auto pm = __builtin_amdgcn_permlane32_swap(rec[0], rec[0], false, false), pr = __builtin_amdgcn_permlane32_swap(rec[1], rec[1], false, false);
#pragma unroll
    for (int j = 0; j < 2; ++j)
#pragma unroll
      for (int g = 0; g < 4; ++g) {
        const int nl = wn * 64 + j * 32 + 8 * g + 4 * hh;
        const f32x4 c = *(const LAS f32x4*)&csp[nl] + *(const LAS f32x4*)&csp[128 + nl];
#pragma unroll
        for (int t = 0; t < 2; ++t) {
          const f32x16& a = acc[t][j];
          const float rs_ = __uint_as_float(pr[t]) * sc, mr = __uint_as_float(pm[t]);
          st8s(base + t * pitch, off[j][g], (u32x2){pk_bf16(rs_ * (a[4 * g] - mr * c[0]), rs_ * (a[4 * g + 1] - mr * c[1])), pk_bf16(rs_ * (a[4 * g + 2] - mr * c[2]), rs_ * (a[4 * g + 3] - mr * c[3]))});
        }
      }
  }
  STAMP_AT(0, 13);
  waitv<0>();
  if (lane < 16) st8(ws + WS_CTL + CTL_F1 + ((cb * 4 + wn * 2 + (lane >> 3)) * 32 + rb * 2 + wm) * 8 + (lane & 7) * CTL_REP, tag);
}

DI void p1_repack(const u16* __restrict__ Wo, char* __restrict__ ws, const int w, int idx) {
  const int lane = fresh_lane();
  const unsigned pvo = (lane >> 4) * 8 * 1280 + (lane & 15) * 2;
  unsigned pw[7][8];
  sfor<7>([&](auto r) {
    const int f = idx * 50 + min(w + 8 * r, 49), cbk = f / 100, rem = f - cbk * 100, ks = rem / 5, ct = rem - ks * 5;
    const char* src = (const char*)(Wo + (size_t)(32 * ks) * 640 + 80 * cbk + 16 * ct);
    sfor<8>([&](auto i) { gldu16<(i & 3) * 1280>(pw[r][i], src + (i & ~3) * 1280, pvo); });
  });
  sfor<7>([&](auto r) {
    waitv<(6 - r) * 8>();
    sfor<8>([&](auto j) { pin(pw[r][j]); });
    const u32x4 v = {pw[r][0] | (pw[r][1] << 16), pw[r][2] | (pw[r][3] << 16), pw[r][4] | (pw[r][5] << 16), pw[r][6] | (pw[r][7] << 16)};
    pw[r][0] = v[0]; pw[r][1] = v[1]; pw[r][2] = v[2]; pw[r][3] = v[3];
  });
  sfor<7>([&](auto r) {
    if (w + 8 * r < 50) st16(ws + WS_WP + (size_t)(idx * 50 + w + 8 * r) * 1024 + lane * 16, (u32x4){pw[r][0], pw[r][1], pw[r][2], pw[r][3]});
  });
}

template <int OFF> DI void rdK(bf16x8& k0, s16x4& k1, unsigned a0, unsigned a1) {
  asm volatile("ds_read_b128 %0, %2 offset:%4\n\tds_read_b64 %1, %3 offset:%4" : "=&v"(k0), "=&v"(k1) : "v"(a0), "v"(a1), "i"(OFF));
}
template <int OFF> DI void rdV(bf16x8& v0, bf16x8& v1, unsigned a, unsigned long long msk) {
  asm volatile("s_mov_b64 exec, %5\n\tds_read_b128 %0, %2 offset:%3\n\tds_read_b128 %1, %2 offset:%4\n\ts_mov_b64 exec, -1"
               : "+v"(v0), "+v"(v1) : "v"(a), "i"(OFF), "i"(OFF + 640), "s"(msk));
}
#define EX2(v) __builtin_amdgcn_exp2f(v)
constexpr int wo_off(int p) { return (p < 60 ? p : p + 20) * 1024; }
constexpr int gu_off(int f) { return (f < 20 ? f + 60 : f < 60 ? f + 100 : f - 60) * 1024; }
constexpr int wd_off(int f) { return ((f / 20 + 4) % 8) * 20480 + (f % 20) * 1024; }
constexpr int CSF_OFF = 116 * 1024, XCH_OFF = 120 * 1024;

DI void p2_attn(char* __restrict__ ws, const u16* __restrict__ Wg, const u16* __restrict__ Wu, const u16* __restrict__ Wd,
                const unsigned lds0, const int w, const u32x2 tag, const int rb3, const int cb3, const int rb4, const int cb4) {
  const int lane = fresh_lane();
  const int kh = w >> 2, wq = w & 3;
  const int head = (blockIdx.x & 7) * 4 + ((blockIdx.x >> 3) & 3), qb = blockIdx.x >> 5;
  const int r32 = lane & 31, hh = lane >> 5;
  const char* kvg = ws + WS_KV + (size_t)head * 163840 + (size_t)kh * 81920 + wq * 1024;
  const unsigned voff = lane * 16;
  const unsigned kvl = lds0 + kh * 81920 + wq * 1024;
  char* ctl = ws + WS_CTL;
  const char* ctr = ctl + (blockIdx.x & 7) * CTL_REP;
  unsigned pw[3][8];
  const int gw = blockIdx.x * 8 + w, gwc = min(gw, 1919);
  const char* psrc[3];
#pragma unroll
  for (int r = 0; r < 2; ++r) {
    const int f = 2 * gwc + r, cbk = f / 120, rem = f - cbk * 120, ks = rem / 6, ct = rem - ks * 6;
    psrc[r] = (const char*)((ct < 3 ? Wg : Wu) + (size_t)(32 * ks) * 1536 + 48 * cbk + 16 * (ct < 3 ? ct : ct - 3));
  }
  { const int f = gwc, cbk = f / 240, rem = f - cbk * 240, ks = rem / 5, ct = rem - ks * 5;
    psrc[2] = (const char*)(Wd + (size_t)(32 * ks) * 640 + 80 * cbk + 16 * ct); }
  unsigned pvo1 = (lane >> 4) * 8 * 3072 + (lane & 15) * 2, pvo2 = (lane >> 4) * 8 * 1280 + (lane & 15) * 2; pin(pvo1); pin(pvo2);
  const char* wog = ws + WS_WP + (size_t)cb3 * 102400;
  const int qtile = qb * 8 + wq * 2 + kh, cg0 = head * 5 + (head & 1), cgt = (head & 1) ? head * 5 : head * 5 + 4;
  const char* ob = ws + WS_O + (size_t)qtile * 40960;
  const char* obs[3] = {ob + (cg0 >> 3) * 1024 + ((cg0 & 7) >> 1) * 256, ob + ((cg0 + 2) >> 3) * 1024 + (((cg0 + 2) & 7) >> 1) * 256,
                        ob + (cgt >> 3) * 1024 + ((cgt & 7) >> 1) * 256 + (cgt & 1) * 8};
  unsigned vo = (unsigned)((r32 >> 4) * 20480 + (r32 & 15) * 16 + hh * 8); pin(vo);
  u32x4 fill = {0xffffffffu, 0xffffffffu, 0xffffffffu, 0xffffffffu}; pin(fill);
  const u32x2 fill2 = {fill[0], fill[1]};
  u32x4 cdum = {};
  u32x2 fa, fb, fc;
  const unsigned fvc = (unsigned)CTL_FR + (unsigned)((cb4 * 60 + min(lane, 59)) * 8);
  const unsigned fva = (unsigned)(hh ? CTL_F4 : CTL_F2) + (unsigned)(((rb3 * 2 + ((w & 3) >> 1)) * 32 + r32) * 8), fvb = (unsigned)CTL_F3 + (unsigned)(((rb4 * 16 + w * 2) * 8 + (lane & 15)) * 8);
  const char* gcl = ws + WS_G + (size_t)(rb4 * 16 + w * 2) * 49152 + cb4 * 1536;
  const char* hcl = ws + WS_H + (size_t)(rb3 * 4 + w) * 20480 + cb3 * 2560;
  const char* rdst = ws + WS_WP + (size_t)(NFR_WO + 2 * gw) * 1024;
  pins(psrc[0]); pins(psrc[1]); pins(psrc[2]); pins(obs[0]); pins(obs[1]); pins(obs[2]); pins(wog); pins(gcl); pins(hcl); pins(rdst);
  STAMP_AT(1, 0);
  unsigned f0;
  {
    const int cfl = (20 * head) >> 5, cfh = (20 * head + 19) >> 5;
    f0 = (unsigned)(((((lane & 32) ? 40 : 20) + ((lane & 16) ? cfh : cfl)) * 32 + kh * 16 + (lane & 15)) * 8);
    const unsigned f1 = (unsigned)((lane < 32 ? (((lane & 1) ? cfh : cfl) * 32 + qb * 4 + wq) : 1920 + cb3 * 16 + (lane & 15)) * 8);
    poll2(ctr + CTL_F1, f0 & ~(12u * 8u), f1, tag);
  }
  STAMP_AT(1, 4);
#pragma unroll
  for (int i = 0; i < 4; ++i) glds16<true>(kvg + i * 4096, voff, kvl + i * 4096);
  bf16x8 q0[2]; s16x4 q1[2];
  {
    const char* qg = ws + WS_Q + ((size_t)head * 64 + qb * 8 + wq * 2) * 1280;
    gld16<0>(q0[0], qg, voff); gld16<1280>(q0[1], qg, voff); gld8<1024>(q1[0], qg, r32 * 8); gld8<2304>(q1[1], qg, r32 * 8);
  }
  const unsigned ka0 = lds0 + kh * 81920 + lane * 16, ka1 = lds0 + kh * 81920 + 1024 + r32 * 8, va = lds0 + kh * 81920 + 1280 + (hh * 20 + (r32 < 20 ? r32 : 0)) * 16;
  bf16x8 K0[2], V0[2], V1[2]; s16x4 K1[2];
  V0[0] = V0[1] = V1[0] = V1[1] = (bf16x8)(short)(r32 == 20 ? 0x3f80 : 0);
  pin(V0[0]); pin(V0[1]); pin(V1[0]); pin(V1[1]);
  WAITV_BAR(0);
  u32x2 fr; gld8<0>(fr, ctr + CTL_F1, f0);
  pin(q0[0]); pin(q0[1]); pin(q1[0]); pin(q1[1]);
  if (hh) { q1[0] = (s16x4){0, 0, 0, 0}; q1[1] = (s16x4){0, 0, 0, 0}; }
  STAMP_AT(1, 1);
  rdK<0>(K0[0], K1[0], ka0, ka1);
  const unsigned long long vmsk = 0x000fffff000fffffull;
  rdV<0>(V0[0], V1[0], va, vmsk);
  f32x16 cm[2];
  {
    const bf16x8 kf0 = lds128(lds0 + lane * 16); const s16x4 kf1 = lds64(lds0 + 1024 + r32 * 8);
#pragma unroll
    for (int t = 0; t < 2; ++t) {
      f32x16 s = MFMA32(kf0, q0[t], (f32x16){}); s = MFMA32K8(kf1, q1[t], s);
      float m = s[0];
#pragma unroll
      for (int i = 1; i < 16; ++i) m = fmaxf(m, s[i]);
      m = max32(m);
      cm[t] = (f32x16)(-m);
      pin(cm[t]);
    }
  }
  asm volatile("s_waitcnt lgkmcnt(0)" : "+v"(K0[0]), "+v"(K1[0]), "+v"(V0[0]), "+v"(V1[0]));
  f32x16 o[2] = {};
  f32x16 SA, SB_;
  u32x4 PA0, PA1, PB0 = {}, PB1 = {};
  SA = MFMA32(K0[0], q0[0], cm[0]); SA = MFMA32K8(K1[0], q1[0], SA);
  waitv<0>(); pin(fr);
  if (!__all(fr[0] == tag[0] && fr[1] == tag[1])) poll(ctr + CTL_F1, f0, tag);
  STAMP_AT(1, 2);
  pin(o[0]); pin(o[1]); pin(PB0); pin(PB1);
#define GRP(MF, Sc, Pc, i, j) do { MF; SB(); const float e0 = EX2(Sc[i]), e1 = EX2(Sc[i + 1]), e2 = EX2(Sc[i + 2]), e3 = EX2(Sc[i + 3]); \
    Pc[j] = pk_bf16(e0, e1); Pc[j + 1] = pk_bf16(e2, e3); pin(Pc); SB(); } while (0)
#define SLOT(QK, Sc, Pc0, Pc1, Sn, kb, jn, Pp0, Pp1, vb, jp) do { \
    GRP(if constexpr (QK) Sn = MFMA32(K0[kb], q0[jn], cm[jn]), Sc, Pc0, 0, 0); \
    GRP(if constexpr (QK) Sn = MFMA32K8(K1[kb], q1[jn], Sn), Sc, Pc0, 4, 2); \
    GRP(o[jp] = MFMA32(V0[vb], __builtin_bit_cast(bf16x8, Pp0), o[jp]), Sc, Pc1, 8, 0); \
    GRP(o[jp] = MFMA32(V1[vb], __builtin_bit_cast(bf16x8, Pp1), o[jp]), Sc, Pc1, 12, 2); } while (0)
#define LWAIT(kb, vb) asm volatile("s_waitcnt lgkmcnt(0)" : "+v"(K0[kb]), "+v"(K1[kb]), "+v"(V0[vb]), "+v"(V1[vb]))
  auto rstore = [&](auto r_, auto yv_) {
    constexpr int r = r_;
    waitv<yv_>();
    sfor<8>([&](auto j) { pin(pw[r][j]); });
    const u32x4 v = {pw[r][0] | (pw[r][1] << 16), pw[r][2] | (pw[r][3] << 16), pw[r][4] | (pw[r][5] << 16), pw[r][6] | (pw[r][7] << 16)};
    if (gw < 1920) {
      if constexpr (r < 2) {
        st16s(rdst + r * 1024, voff, v);
        float c = 0.f;
#pragma unroll
        for (int j = 0; j < 4; ++j) c = dot2(v[j], 0x3f803f80u, c);
        c = red4(c);
        if (lane < 16) st4(ws + WS_CSP + (size_t)(2 * gw + r) * 64 + lane * 4, c);
      } else st16s(ws + WS_WP + (size_t)(NFR_WO + NFR_GU + gw) * 1024, voff, v);
    }
  };
  auto hook = [&](auto it_, auto i_) {
    constexpr int it = it_, i = i_, n = (it - 2) * 8 + i;
    if constexpr (n >= 0 && n < 2) st8s(obs[n], vo, fill2);
    if constexpr (n == 2) { if (!hh) st8s(obs[2], vo, fill2); }
    if constexpr (n >= 3 && n < 6) { const int L = (n - 3) * 64 + lane; st16s(gcl, (unsigned)(L * 16 + (L >= 96 ? 49152 - 1536 : 0)), fill); }
    if constexpr (n >= 6 && n < 9) { if (w < 4) st16s(hcl, (unsigned)(min((n - 6) * 64 + lane, 159) * 16), fill); }
    if constexpr (n == 9) { if (w < 4 && lane < 16) st8s(ws + WS_HST + (size_t)(rb3 * 4 + w) * 1024 + (cb3 >> 1) * 256 + (cb3 & 1) * 8, (unsigned)(lane * 16), fill2); }
    if constexpr (it == 5 && i == 6) {
      waitv<0>();
      setflag(ctl + CTL_FR + gw * 8, tag, lane);
      setflag(ctl + CTL_F2 + (qtile * 32 + head) * 8, tag, lane);
      setflag(ctl + CTL_F4 + ((rb4 * 8 + w) * 32 + cb4) * 8, tag, lane);
      if (w < 4) setflag(ctl + CTL_F3 + ((rb3 * 4 + w) * 8 + cb3) * 8, tag, lane);
    }
    if constexpr (it == 6 && i == 0 && CW2 > 0) { if (w == 0) code_warm<3, CW2>(cdum, blockIdx.x >> 3, voff); }
    if constexpr (it == 7 && i == 6) { gld8<0>(fa, ctr, fva); gld8<0>(fb, ctr, fvb); gld8<0>(fc, ctr, fvc); }
    if constexpr (it < 2) glds16<true>(kvg + (size_t)(it * 8 + 4 + i) * 4096, voff, kvl + (it * 8 + 4 + i) * 4096);
    else if constexpr (it < 4) gldu16<(i & 1) * 3072>(pw[it - 2][i], psrc[it - 2] + (i & ~1) * 3072, pvo1);
    else if constexpr (it == 4) {
      gldu16<(i & 3) * 1280>(pw[2][i], psrc[2] + (i & ~3) * 1280, pvo2);
      if constexpr (i == 0) rstore(IC<0>{}, IC<9>{});
      if constexpr (i == 7) rstore(IC<1>{}, IC<8>{});
    } else if constexpr (it == 5) {
      if constexpr (i == 7) rstore(IC<2>{}, IC<3>{});
    } else {
      constexpr int j = (it - 6) * 8 + i;
      const char* src = wog + (w + 8 * j) * 1024;
      if constexpr (j < 7) glds16<true>(src, voff, lds0 + w * 1024 + j * 8192);
      else if constexpr (j == 7) glds16<true>(src, voff, lds0 + w * 1024 + j * 8192 + (w >= 4 ? 20480 : 0));
      else if constexpr (j < 12) glds16<true>(src, voff, lds0 + w * 1024 + j * 8192 + 20480);
      else if constexpr (j == 12) { if (w < 4) glds16<true>(src, voff, lds0 + w * 1024 + j * 8192 + 20480); }
    }
    SB();
  };
  const unsigned ka0b = ka0 + 40960, ka1b = ka1 + 40960, vab = va + 40960;
  sfor<8>([&](auto it_) {
    constexpr int it = it_;
    if constexpr (it == 1) WAITV_BAR(6);
    if constexpr (it == 2) { WAITV_BAR(8); STAMP_AT(1, 3); }
    if constexpr (it == 4) asm volatile("s_waitcnt vmcnt(16)\n\ts_barrier" ::: "memory");
    if constexpr (it == 6) asm volatile("s_barrier" ::: "memory");
    if (((it ^ kh) & 1) == 0) __builtin_amdgcn_s_setprio(0); else __builtin_amdgcn_s_setprio(1);
    constexpr int o0 = (it & 3) * 10240;
    constexpr int on = it == 3 ? 0 : o0 + 10240;
    const unsigned a0 = it < 4 ? ka0 : ka0b, a1 = it < 4 ? ka1 : ka1b, av = it < 4 ? va : vab;
    const unsigned n0 = it < 3 ? ka0 : ka0b, n1 = it < 3 ? ka1 : ka1b, nv = it < 3 ? va : vab;
    rdK<o0 + 2560>(K0[1], K1[1], a0, a1); hook(it_, IC<0>{});                  SLOT(true, SA, PA0, PA1, SB_, 0, 1, PB0, PB1, 1, 1);
    LWAIT(1, 0); rdV<o0 + 2560>(V0[1], V1[1], av, vmsk); hook(it_, IC<1>{});   SLOT(true, SB_, PB0, PB1, SA, 1, 0, PA0, PA1, 0, 0);
    rdK<o0 + 5120>(K0[0], K1[0], a0, a1); hook(it_, IC<2>{});                  SLOT(true, SA, PA0, PA1, SB_, 1, 1, PB0, PB1, 0, 1);
    LWAIT(0, 1); rdV<o0 + 5120>(V0[0], V1[0], av, vmsk); hook(it_, IC<3>{});   SLOT(true, SB_, PB0, PB1, SA, 0, 0, PA0, PA1, 1, 0);
    rdK<o0 + 7680>(K0[1], K1[1], a0, a1); hook(it_, IC<4>{});                  SLOT(true, SA, PA0, PA1, SB_, 0, 1, PB0, PB1, 1, 1);
    LWAIT(1, 0); rdV<o0 + 7680>(V0[1], V1[1], av, vmsk); hook(it_, IC<5>{});   SLOT(true, SB_, PB0, PB1, SA, 1, 0, PA0, PA1, 0, 0);
    if constexpr (it < 7) rdK<on>(K0[0], K1[0], n0, n1);
    hook(it_, IC<6>{});                                                        SLOT(true, SA, PA0, PA1, SB_, 1, 1, PB0, PB1, 0, 1);
    LWAIT(0, 1);
    if constexpr (it < 7) rdV<on>(V0[0], V1[0], nv, vmsk);
    hook(it_, IC<7>{});                                                        SLOT(it < 7, SB_, PB0, PB1, SA, 0, 0, PA0, PA1, 1, 0);
  });
  __builtin_amdgcn_s_setprio(0);
  o[1] = MFMA32(V0[1], __builtin_bit_cast(bf16x8, PB0), o[1]);
  o[1] = MFMA32(V1[1], __builtin_bit_cast(bf16x8, PB1), o[1]);
  STAMP_AT(1, 9);
  bar();
  {
    LAS f32x4* xch = (LAS f32x4*)(uintptr_t)(lds0 + XCH_OFF + w * 3072);
    const f32x16& snd = kh ? o[0] : o[1];
#pragma unroll
    for (int g = 0; g < 3; ++g) xch[g * 64 + lane] = (f32x4){snd[4 * g], snd[4 * g + 1], snd[4 * g + 2], snd[4 * g + 3]};
  }
  bar();
  STAMP_AT(1, 10);
  float of[12];
  {
    const LAS f32x4* xch = (const LAS f32x4*)(uintptr_t)(lds0 + XCH_OFF + (w ^ 4) * 3072);
    const f32x16& own = kh ? o[1] : o[0];
#pragma unroll
    for (int g = 0; g < 3; ++g) { const f32x4 v = xch[g * 64 + lane];
#pragma unroll
      for (int j = 0; j < 4; ++j) of[4 * g + j] = own[4 * g + j] + v[j]; }
  }
  float l = hi32(of[8]);
  bool ok = (l > 8.67e-19f) && (l < 1.15e18f);
#pragma unroll
  for (int i = 0; i < 8; ++i) ok = ok && (fabsf(of[i]) < 1e37f);
  if (!hh) {
#pragma unroll
    for (int i = 8; i < 12; ++i) ok = ok && (fabsf(of[i]) < 1e37f);
  }
  if (__builtin_expect(!__all(ok), 0)) {
    __builtin_amdgcn_fence(__ATOMIC_ACQUIRE, "agent");
    const char* kv = ws + WS_KV + (size_t)head * 163840;
    const bf16x8 fq0 = kh ? q0[1] : q0[0];
    const s16x4 fq1 = kh ? q1[1] : q1[0];
    float m = -INFINITY;
    for (int t = 0; t < 64; ++t) {
      const bf16x8 kf0 = *(const bf16x8*)(kv + t * 2560 + lane * 16); const s16x4 kf1 = *(const s16x4*)(kv + t * 2560 + 1024 + r32 * 8);
      f32x16 s = MFMA32(kf0, fq0, (f32x16){}); s = MFMA32K8(kf1, fq1, s);
#pragma unroll
      for (int i = 0; i < 16; ++i) m = fmaxf(m, s[i]);
    }
    m = max32(m);
    f32x16 oo = {};
    const int vr = r32 < 20 ? r32 : 0;
    for (int t = 0; t < 64; ++t) {
      const bf16x8 kf0 = *(const bf16x8*)(kv + t * 2560 + lane * 16); const s16x4 kf1 = *(const s16x4*)(kv + t * 2560 + 1024 + r32 * 8);
      bf16x8 vf0 = *(const bf16x8*)(kv + t * 2560 + 1280 + (hh * 20 + vr) * 16), vf1 = *(const bf16x8*)(kv + t * 2560 + 1920 + (hh * 20 + vr) * 16);
      if (r32 >= 20) { vf0 = (bf16x8)(short)0x3f80; vf1 = vf0; }
      f32x16 s = MFMA32(kf0, fq0, (f32x16){}); s = MFMA32K8(kf1, fq1, s);
      u32x4 p0, p1;
#pragma unroll
      for (int i = 0; i < 4; ++i) {
        p0[i] = pk_bf16(EX2(s[2 * i] - m), EX2(s[2 * i + 1] - m));
        p1[i] = pk_bf16(EX2(s[8 + 2 * i] - m), EX2(s[8 + 2 * i + 1] - m));
      }
      oo = MFMA32(vf0, __builtin_bit_cast(bf16x8, p0), oo);
      oo = MFMA32(vf1, __builtin_bit_cast(bf16x8, p1), oo);
    }
#pragma unroll
    for (int i = 0; i < 12; ++i) of[i] = oo[i];
    l = hi32(of[8]);
  }
  const float rl = __builtin_amdgcn_rcpf(l);
  waitv<0>(); pin(fa); pin(fb); pin(fc); pin(cdum);
  setflag(ctl + CTL_FD + gw * 8, tag, lane);
#pragma unroll
  for (int g = 0; g < 3; ++g)
    if (g < 2 || !hh) st8s(obs[g], vo, nofill(pk_bf16(of[4 * g] * rl, of[4 * g + 1] * rl), pk_bf16(of[4 * g + 2] * rl, of[4 * g + 3] * rl)));
  STAMP_AT(1, 11);
  if (!__all(fa[0] == tag[0] && fa[1] == tag[1] && fb[0] == tag[0] && fb[1] == tag[1])) poll2(ctr, fva, fvb, tag);
  if (!__all(fc[0] == tag[0] && fc[1] == tag[1])) poll(ctr, fvc, tag);
  STAMP_AT(1, 12);
}

constexpr int x_none(int) { return 0; }
constexpr int x_p4(int i) { return ((i >= 2 && i < 7) || (i >= 10 && i < 18)) ? 1 : 0; }
template <int NSTEP, int NF, int LA, int LL, int CG, int NA, int ASTR, int (*BOFF)(int), int (*XCNT)(int), class PRE, class MM>
DI void gemm_res(const char* asrc, unsigned lds0, unsigned voff, PRE&& pre, MM&& mm) {
  bf16x8 a[NSTEP][NA];
  bf16x8 bf[LL + 1][NF];
  unsigned rd0 = lds0 + voff, rd1 = rd0 + 65536, rd2 = rd0 + 131072; pin(rd0); pin(rd1); pin(rd2);
  auto issue = [&](auto n_) { constexpr int n = n_; sfor<NA>([&](auto j) { gld16<(n & 3) * 1024>(a[n][j], asrc + (n >> 2) * 4096 + j * ASTR, voff); }); };
  constexpr auto xsum = [](int lo, int hi) { int c = 0; for (int i = lo < 0 ? 0 : lo; i <= hi; ++i) c += XCNT(i); return c; };
  auto rd = [&](auto i_, auto f_) {
    constexpr int i = i_, f = f_, off = BOFF(i * NF + f);
    bf[i % (LL + 1)][f] = lds128((off < 65536 ? rd0 : off < 131072 ? rd1 : rd2) + (off & 65535));
  };
  static_assert(NSTEP % CG == 0 && LA > CG, "groups");
  unsigned m = 0;
  auto fold = [&](auto n_) { constexpr int n = n_;
    sfor<NA>([&](auto j) { pin(a[n][j]); const u32x4 u = __builtin_bit_cast(u32x4, a[n][j]); m = max(max(m, u[0]), u[1]); m = max(max(m, u[2]), u[3]); });
    pin(m);
    if constexpr (n % CG == CG - 1) {
      if (__builtin_expect(__any(m == 0xffffffffu), 0)) {
#pragma unroll 1
        for (int spin = 0; spin < (1 << 17); ++spin) {
          RETRY_COUNT(NSTEP + NF);
          sfor<CG>([&](auto k) { issue(IC<n - CG + 1 + k>{}); });
          waitv<0>(); m = 0;
          sfor<CG>([&](auto k) { sfor<NA>([&](auto j) { pin(a[n - CG + 1 + k][j]); m = max(m, umax4(__builtin_bit_cast(u32x4, a[n - CG + 1 + k][j]))); }); });
          if (!__any(m == 0xffffffffu)) break;
        }
      }
    }
  };
  sfor<LA>([&](auto n) { issue(n); });
  pre(IC<-1>{});
  sfor<LL>([&](auto i) { sfor<NF>([&](auto f) { rd(i, f); }); });
  waitv<(LA - CG) * NA>(); sfor<CG>([&](auto n) { fold(n); });
  sfor<NSTEP>([&](auto i_) {
    constexpr int i = i_;
    pre(i_);
    SB();
    sfor<NF>([&](auto f_) {
      constexpr int f = f_;
      if constexpr (f == NF - 1 && i + CG < NSTEP) {
        waitv<((i + LA < NSTEP ? i + LA : NSTEP - 1) - (i + CG)) * NA + xsum(i + CG - LA + 1, i)>(); fold(IC<i + CG>{}); SB(); }
      mm(f_, bf[i % (LL + 1)][f], a[i]);
      SB();
      if constexpr (i + LL < NSTEP) rd(IC<i + LL>{}, f_);
      if constexpr (f == 0 && i + LA < NSTEP) issue(IC<i + LA>{});
      SB();
    });
  });
}

__global__ __launch_bounds__(512, 2) void k_mega(const u16* __restrict__ x, const u16* __restrict__ Wqkv, const u16* __restrict__ Wo,
                                                 const u16* __restrict__ Wg, const u16* __restrict__ Wu, const u16* __restrict__ Wd,
                                                 char* __restrict__ ws, u16* __restrict__ out) {
  __shared__ __attribute__((aligned(16))) char smem[163840];
  const unsigned lds0 = lds_addr_of(smem);
  const int w = __builtin_amdgcn_readfirstlane(threadIdx.x >> 6);
  const int g8 = blockIdx.x & 7, i32 = blockIdx.x >> 3;
  char* ctl = ws + WS_CTL;
  const char* ctr = ctl + g8 * CTL_REP;
  u32x2 tag;
  if (i32 < 30) {
    const int i = (g8 & 1) * 30 + i32, rb = (g8 >> 1) * 4 + (i & 3), cb = i >> 2;
    if (w >= 4) { p1_load(x, Wqkv, lds0, w - 4, rb, cb); u32x2 ep; gld8<0>(ep, ctl, 0u); waitv<0>(); pin(ep); tag = make_tag(ep); }
    else if (cb >= 10) p1_body<true>(x, ws, lds0, w, rb, cb, tag);
    else p1_body<false>(x, ws, lds0, w, rb, cb, tag);
  } else {
    { RowStat rs; u32x2 ep; const int lane = fresh_lane(); gld8<0>(ep, ctl, 0u); rs_load(rs, x, blockIdx.x * 8 + w, lane); waitv<0>(); pin(ep); tag = make_tag(ep);
      rs_store(rs, ws, blockIdx.x * 8 + w, lane, tag); }
    p1_repack(Wo, ws, w, blockIdx.x - 240);
    publish(ctl + CTL_F1 + (1920 + (blockIdx.x - 240) * 8 + w) * 8, tag, fresh_lane());
  }
  bar();
  const int rb3 = g8 * 4 + (i32 >> 3), cb3 = i32 & 7;
  const int rb4 = (g8 >> 2) * 4 + (i32 >> 3), cb4 = (g8 & 3) * 8 + (i32 & 7);
  p2_attn(ws, Wg, Wu, Wd, lds0, w, tag, rb3, cb3, rb4, cb4);
  bar();
  const char* gug = ws + WS_WP + (size_t)NFR_WO * 1024 + (size_t)cb4 * 122880;
  const char* wdg = ws + WS_WP + (size_t)(NFR_WO + NFR_GU) * 1024 + (size_t)cb3 * 245760;
  u32x2 hp[5];
  if (w < 4) {
    const int lane = fresh_lane();
    const unsigned voff = lane * 16;
    const int row = rb3 * 64 + w * 16 + (lane & 15), col0 = cb3 * 80 + 4 * (lane >> 4);
    STAMP_AT(2, 0);
    u32x2 xr[5];
#pragma unroll
    for (int ct = 0; ct < 5; ++ct) xr[ct] = *(const u32x2*)(x + (size_t)row * 640 + col0 + 16 * ct);
    { const int hd = lane & 31, cgt = (hd & 1) ? hd * 5 : hd * 5 + 4;
      probe(ws + WS_O + (size_t)(rb3 * 4 + w) * 20480, (unsigned)((cgt >> 3) * 1024 + ((cgt & 7) >> 1) * 256 + (cgt & 1) * 8)); }
    sfor<5>([&](auto ct) { pin(xr[ct]); });
    STAMP_AT(2, 1);
    f32x4 acc[5] = {};
    sfor<5>([&](auto ct) { pin(acc[ct]); });
    gemm_res<20, 5, 20, 2, 4, 1, 0, wo_off, x_none>(ws + WS_O + (size_t)(rb3 * 4 + w) * 20480, lds0, voff,
      [&](auto i) { if constexpr (i == -1) STAMP_AT(2, 2); if constexpr (i == 1) STAMP_AT(2, 3); if constexpr (i == 10) STAMP_AT(2, 4);
        if constexpr (i == P3_G1 || i == 12) bar(); },
      [&](auto ct, const bf16x8& b, bf16x8 (&a)[1]) { mfma16(acc[ct], b, a[0]); });
    acc_fence(); sfor<5>([&](auto ct) { pin(acc[ct]); });
    STAMP_AT(2, 6);
    float s1 = 0.f, s2 = 0.f;
    const char* hb = ws + WS_H + (size_t)(rb3 * 4 + w) * 20480;
    const unsigned vt = t16lane(lane);
#pragma unroll
    for (int ct = 0; ct < 5; ++ct) {
      hp[ct] = nofill(pk_bf16(bf_lo(xr[ct][0]) + acc[ct][0], bf_hi(xr[ct][0]) + acc[ct][1]), pk_bf16(bf_lo(xr[ct][1]) + acc[ct][2], bf_hi(xr[ct][1]) + acc[ct][3]));
      st8s(hb + t16off(cb3 * 80 + ct * 16), vt, hp[ct]);
      s1 = dot2(hp[ct][0], 0x3f803f80u, s1); s1 = dot2(hp[ct][1], 0x3f803f80u, s1);
      s2 = dot2(hp[ct][0], hp[ct][0], s2); s2 = dot2(hp[ct][1], hp[ct][1], s2);
    }
    s1 = red4(s1); s2 = red4(s2);
    if (lane < 16) st8s(ws + WS_HST + (size_t)(rb3 * 4 + w) * 1024 + (cb3 >> 1) * 256 + (cb3 & 1) * 8, (unsigned)(lane * 16), nofill(__float_as_uint(s1), __float_as_uint(s2)));
    bar();
    STAMP_AT(2, 7);
  } else {
    const int lane = fresh_lane(), v = w - 4;
    const unsigned voff = lane * 16;
    float cpart[10];
    const int cc = v * 24 + min(lane & 31, 23);
    sfor<10>([&](auto i) { gld4<i * 384>(cpart[i], ws + WS_CSP, (unsigned)(cb4 * 7680 + (lane >> 5) * 3840 + cc * 4)); });
    poll(ctr + CTL_FD, (unsigned)((cb3 * 240 + v * 60 + min(lane, 59)) * 8), tag);
    float cv = 0.f;
    sfor<10>([&](auto j) { pin(cpart[j]); cv += cpart[j]; });
    cv = add32(cv);
    bar();
    STAMP_AT(2, 2);
    sfor<5>([&](auto j) { glds16<true>(gug + (v * 5 + j) * 1024, voff, lds0 + gu_off(0) + (v * 5 + j) * 1024); });
    sfor<10>([&](auto j) { glds16<true>(gug + (20 + v * 10 + j) * 1024, voff, lds0 + gu_off(20) + (v * 10 + j) * 1024); });
    bar();
    sfor<15>([&](auto j) { glds16<true>(gug + (60 + v * 15 + j) * 1024, voff, lds0 + gu_off(60) + (v * 15 + j) * 1024); });
    STAMP_AT(2, 5);
    waitv<0>();
    bar();
    STAMP_AT(2, 6);
    if (lane < 24) ((LAS float*)(uintptr_t)(lds0 + CSF_OFF))[cc] = cv;
  }
  {
    const int lane = fresh_lane();
    const unsigned voff = lane * 16;
    const int r16 = lane & 15, g4 = lane >> 4;
    STAMP_AT(3, 0);
    f32x4 cs[6]; f32x2 ms[2];
    f32x4 hs[2];
#pragma unroll 1
    for (int spin = 0; spin < (1 << 17); ++spin) {
      gld16<0>(hs[0], ws + WS_HST + (size_t)(rb4 * 16 + w * 2) * 1024, voff);
      gld16<1024>(hs[1], ws + WS_HST + (size_t)(rb4 * 16 + w * 2) * 1024, voff);
      waitv<0>(); pin(hs[0]); pin(hs[1]);
      if (!__any(max(umax4(__builtin_bit_cast(u32x4, hs[0])), umax4(__builtin_bit_cast(u32x4, hs[1]))) == 0xffffffffu)) break;
      __builtin_amdgcn_s_sleep(1);
    }
    STAMP_AT(3, 1);
    f32x4 acc[2][6] = {};
    sfor<6>([&](auto ct) { pin(acc[0][ct]); pin(acc[1][ct]); });
    if (w < 4) __builtin_amdgcn_s_setprio(1);
    gemm_res<20, 6, K4_LA_, 1, K4_CG_, 2, 20480, gu_off, x_p4>(ws + WS_H + (size_t)(rb4 * 16 + w * 2) * 20480, lds0, voff,
      [&](auto i) { if constexpr (i == -1) STAMP_AT(3, 2); if constexpr (i == 1) STAMP_AT(3, 3); if constexpr (i == 10) STAMP_AT(3, 4);
        if constexpr (i == 9) bar();
        if constexpr (i == -1) sfor<2>([&](auto t) {
          const float s1 = red4(hs[t][0] + hs[t][2]), s2 = red4(hs[t][1] + hs[t][3]), mean = s1 * (1.f / 640.f);
          ms[t] = (f32x2){mean, __builtin_amdgcn_rsqf(fmaxf(s2 * (1.f / 640.f) - mean * mean, 0.f) + LN_EPS)}; pin(ms[t]); });
        if constexpr (i == 18) sfor<6>([&](auto ct) { cs[ct] = *(const LAS f32x4*)(uintptr_t)(lds0 + CSF_OFF + (ct * 16 + 4 * g4) * 4); });
        if constexpr (i >= 2 && i < 7) { const int f = (i - 2) * 8 + w - ((i == 6 && w >= 4) ? 4 : 0); glds16<true>(wdg + (f) * 1024, voff, lds0 + wd_off(0) + f * 1024); }
        if constexpr (i >= 10 && i < 15) { constexpr int n = i - 10; glds16<true>(wdg + (40 + n * 8 + w) * 1024, voff, lds0 + wd_off(40) + (n * 8 + w) * 1024); }
        if constexpr (i >= 15 && i < 18) { const int f = (i - 15) * 8 + w - ((i == 17 && w >= 4) ? 4 : 0); glds16<true>(wdg + (140 + f) * 1024, voff, lds0 + wd_off(140) + f * 1024); } },
      [&](auto ct, const bf16x8& b, bf16x8 (&a)[2]) { mfma16(acc[0][ct], b, a[0]); mfma16(acc[1][ct], b, a[1]); });
    acc_fence(); sfor<6>([&](auto ct) { pin(acc[0][ct]); pin(acc[1][ct]); });
    STAMP_AT(3, 6);
    constexpr float GC0 = -2.f * 0.7978845608028654f * 1.4426950408889634f, GC1 = GC0 * 0.044715f;
    const char* gb = ws + WS_G + (size_t)(rb4 * 16 + w * 2) * 49152;
    const unsigned vt = t16lane(lane);
#pragma unroll
    for (int t = 0; t < 2; ++t) {
      const float nm = -ms[t].x, rstd = ms[t].y;
#pragma unroll
      for (int ct = 0; ct < 3; ++ct) {
        const f32x4 gv = (acc[t][ct] + cs[ct] * nm) * rstd, uv = (acc[t][3 + ct] + cs[3 + ct] * nm) * rstd;
        const f32x4 z = gv * (gv * gv * GC1 + GC0);
        f32x4 e;
#pragma unroll
        for (int j = 0; j < 4; ++j) e[j] = __builtin_amdgcn_exp2f(z[j]);
        e = e + 1.f;
#pragma unroll
        for (int j = 0; j < 4; ++j) e[j] = __builtin_amdgcn_rcpf(e[j]);
        const f32x4 r = gv * uv * e;
        st8s(gb + t * 49152 + t16off(cb4 * 48 + ct * 16), vt, nofill(pk_bf16(r[0], r[1]), pk_bf16(r[2], r[3])));
      }
    }
    __builtin_amdgcn_s_setprio(0);
    STAMP_AT(3, 7);
    waitv<6>();
    STAMP_AT(3, 8);
    bar();
  }
  if (w < 4) {
    const int lane = fresh_lane();
    const unsigned voff = lane * 16;
    const int row = rb3 * 64 + w * 16 + (lane & 15), col0 = cb3 * 80 + 4 * (lane >> 4);
    STAMP_AT(4, 0);
    probe(ws + WS_G + (size_t)(rb3 * 4 + w) * 49152, (unsigned)t16off((lane & 31) * 48 + 32));
    STAMP_AT(4, 1);
    f32x4 acc[5] = {};
    sfor<5>([&](auto ct) { pin(acc[ct]); });
    gemm_res<48, 5, K5_LA_, 2, 4, 1, 0, wd_off, x_none>(ws + WS_G + (size_t)(rb3 * 4 + w) * 49152, lds0, voff,
      [&](auto i) { if constexpr (i == 1) STAMP_AT(4, 3); if constexpr (i == 14) STAMP_AT(4, 4); if constexpr (i == 30) STAMP_AT(4, 5);
        if constexpr (i == -1 || i == 8 || i == 14 || i == 16 || i == 30 || i == 38) bar(); },
      [&](auto ct, const bf16x8& b, bf16x8 (&a)[1]) { mfma16(acc[ct], b, a[0]); });
    acc_fence(); sfor<5>([&](auto ct) { pin(acc[ct]); });
    STAMP_AT(4, 12);
#pragma unroll
    for (int ct = 0; ct < 5; ++ct) {
      const unsigned p0 = pk_bf16(bf_lo(hp[ct][0]) + acc[ct][0], bf_hi(hp[ct][0]) + acc[ct][1]);
      const unsigned p1 = pk_bf16(bf_lo(hp[ct][1]) + acc[ct][2], bf_hi(hp[ct][1]) + acc[ct][3]);
      *(u32x2*)(out + (size_t)row * 640 + col0 + 16 * ct) = (u32x2){p0, p1};
    }
    STAMP_AT(4, 13);
  } else {
    const int lane = fresh_lane(), v = w - 4;
    const unsigned voff = lane * 16;
    glds16<true>(wdg + (36 + v) * 1024, voff, lds0 + wd_off(36) + v * 1024);
    waitv<0>(); bar();
    sfor<3>([&](auto g) { sfor<5>([&](auto j) { constexpr int f = (g + 4) * 20 + j; glds16<true>(wdg + (f + v * 5) * 1024, voff, lds0 + wd_off(f) + v * 5120); }); });
    bar();
    sfor<2>([&](auto g) { sfor<5>([&](auto j) { constexpr int f = (g + 8) * 20 + j; glds16<true>(wdg + (f + v * 5) * 1024, voff, lds0 + wd_off(f) + v * 5120); }); });
    waitv<10>(); bar();
    bar();
    sfor<2>([&](auto g) { sfor<5>([&](auto j) { constexpr int f = (g + 10) * 20 + j; glds16<true>(wdg + (f + v * 5) * 1024, voff, lds0 + wd_off(f) + v * 5120); }); });
    waitv<10>(); bar();
    waitv<0>(); bar();
    if (blockIdx.x == 0 && w == 4) {
      poll2(ctr + CTL_FR, (unsigned)(lane * 64), (unsigned)(4096 + lane * 64), tag);
      poll2(ctr + CTL_FR, (unsigned)(8192 + lane * 64), (unsigned)(12288 + lane * 64), tag);
      if (lane == 0) st8(ctl, tag);
    }
  }
}

extern "C" void kernel_launch(void* const* d_in, const int* in_sizes, int n_in, void* d_out, int out_size, void* d_ws, size_t ws_size,
                              hipStream_t stream) {
  k_mega<<<256, 512, 0, stream>>>((const u16*)d_in[0], (const u16*)d_in[1], (const u16*)d_in[2], (const u16*)d_in[4], (const u16*)d_in[5],
                                  (const u16*)d_in[6], (char*)d_ws, (u16*)d_out);
}
```

```cpp
#include <hip/hip_runtime.h>
#include <hip/hip_bf16.h>
#include <stdint.h>
#include <utility>

typedef short bf16x8 __attribute__((ext_vector_type(8)));
typedef short s16x4 __attribute__((ext_vector_type(4)));
typedef float f32x16 __attribute__((ext_vector_type(16)));
typedef float f32x4 __attribute__((ext_vector_type(4)));
typedef float f32x2 __attribute__((ext_vector_type(2)));
typedef unsigned u32x4 __attribute__((ext_vector_type(4)));
typedef unsigned u32x2 __attribute__((ext_vector_type(2)));
typedef __bf16 bf2_t __attribute__((ext_vector_type(2)));
typedef unsigned short u16;
#define LAS __attribute__((address_space(3)))
#define DI __device__ __forceinline__

constexpr float LN_EPS = 1e-5f;
constexpr float QSCALE = 0.22360679774997896f * 1.4426950408889634f;

constexpr size_t WS_Q   = 0;
constexpr size_t WS_KV  = WS_Q + 2621440;
constexpr size_t WS_O   = WS_KV + 5242880;
constexpr size_t WS_H   = WS_O + 2621440;
constexpr size_t WS_HST = WS_H + 2621440;
constexpr size_t WS_G   = WS_HST + 131072;
constexpr size_t WS_WP  = WS_G + 6291456;
constexpr size_t WS_CSP = WS_WP + 6717440;
constexpr size_t WS_RST = WS_CSP + 245760;
constexpr size_t WS_CTL = WS_RST + 32768;
constexpr size_t CTL_F1 = 256;
constexpr size_t CTL_F2 = CTL_F1 + 2048 * 8;
constexpr size_t CTL_FR = CTL_F2 + 2048 * 8;
constexpr size_t CTL_F3 = CTL_FR + 2048 * 8;
constexpr size_t CTL_F4 = CTL_F3 + 1024 * 8;
constexpr size_t CTL_FD = CTL_F4 + 2048 * 8;
constexpr size_t CTL_REP = CTL_FD + 2048 * 8 - 256;
constexpr int NFR_WO = 800, NFR_GU = 3840, NFR_WD = 1920;

DI unsigned pk_bf16(float lo, float hi) { f32x2 f = {lo, hi}; return __builtin_bit_cast(unsigned, __builtin_convertvector(f, bf2_t)); }
DI float bf_lo(unsigned u) { return __uint_as_float(u << 16); }
DI float bf_hi(unsigned u) { return __uint_as_float(u & 0xffff0000u); }
DI float dot2(unsigned a, unsigned b, float c) { return __builtin_amdgcn_fdot2_f32_bf16(__builtin_bit_cast(bf2_t, a), __builtin_bit_cast(bf2_t, b), c, false); }
DI float add32(float v) { const auto r = __builtin_amdgcn_permlane32_swap(__float_as_uint(v), __float_as_uint(v), false, false); return __uint_as_float(r[0]) + __uint_as_float(r[1]); }
DI float max32(float v) { const auto r = __builtin_amdgcn_permlane32_swap(__float_as_uint(v), __float_as_uint(v), false, false); return fmaxf(__uint_as_float(r[0]), __uint_as_float(r[1])); }
DI float add16(float v) { const auto r = __builtin_amdgcn_permlane16_swap(__float_as_uint(v), __float_as_uint(v), false, false); return __uint_as_float(r[0]) + __uint_as_float(r[1]); }
DI float red4(float v) { return add32(add16(v)); }
DI float hi32(float v) { const auto r = __builtin_amdgcn_permlane32_swap(__float_as_uint(v), __float_as_uint(v), false, false); return __uint_as_float(r[1]); }
#define MFMA32(a, b, c) __builtin_amdgcn_mfma_f32_32x32x16_bf16((a), (b), (c), 0, 0, 0)
#define MFMA32K8(a, b, c) __builtin_amdgcn_mfma_f32_32x32x8bf16_1k((a), (b), (c), 0, 0, 0)
#define MFMA16(a, b, c) __builtin_amdgcn_mfma_f32_16x16x32_bf16((a), (b), (c), 0, 0, 0)

DI void mfma16(f32x4& c, const bf16x8& a, const bf16x8& b) { asm volatile("v_mfma_f32_16x16x32_bf16 %0, %1, %2, %0" : "+v"(c) : "v"(a), "v"(b)); }
DI void acc_fence() { asm volatile("s_nop 7\n\ts_nop 3" ::: "memory"); }
template <bool DEV> DI void glds16(const void* sbase, unsigned voff, unsigned lds_base) {
  if constexpr (DEV) asm volatile("s_mov_b32 m0, %2\n\ts_nop 0\n\tglobal_load_lds_dwordx4 %0, %1 sc1" :: "v"(voff), "s"(sbase), "s"(lds_base) : "memory");
  else asm volatile("s_mov_b32 m0, %2\n\ts_nop 0\n\tglobal_load_lds_dwordx4 %0, %1" :: "v"(voff), "s"(sbase), "s"(lds_base) : "memory");
}
template <int OFF, class T> DI void gld16(T& dst, const void* sbase, unsigned voff) {
  asm volatile("global_load_dwordx4 %0, %1, %2 offset:%3 sc1" : "=v"(dst) : "v"(voff), "s"(sbase), "i"(OFF) : "memory");
}
template <int OFF, class T> DI void gld8(T& dst, const void* sbase, unsigned voff) {
  asm volatile("global_load_dwordx2 %0, %1, %2 offset:%3 sc1" : "=v"(dst) : "v"(voff), "s"(sbase), "i"(OFF) : "memory");
}
template <int OFF> DI void gld4(float& dst, const void* sbase, unsigned voff) {
  asm volatile("global_load_dword %0, %1, %2 offset:%3 sc1" : "=v"(dst) : "v"(voff), "s"(sbase), "i"(OFF) : "memory");
}
template <int OFF> DI void gldu16(unsigned& dst, const void* sbase, unsigned voff) {
  asm volatile("global_load_ushort %0, %1, %2 offset:%3" : "=v"(dst) : "v"(voff), "s"(sbase), "i"(OFF) : "memory");
}
DI void st16(void* p, u32x4 v) { asm volatile("global_store_dwordx4 %0, %1, off sc1\n\ts_nop 1" :: "v"(p), "v"(v) : "memory"); }
DI void st8(void* p, u32x2 v) { asm volatile("global_store_dwordx2 %0, %1, off sc1" :: "v"(p), "v"(v) : "memory"); }
DI void st4(void* p, float v) { asm volatile("global_store_dword %0, %1, off sc1" :: "v"(p), "v"(v) : "memory"); }
DI void st8s(const void* sbase, unsigned voff, u32x2 v) { asm volatile("global_store_dwordx2 %0, %1, %2 sc1" :: "v"(voff), "v"(v), "s"(sbase) : "memory"); }
DI u32x2 nofill(unsigned a, unsigned b) { return (u32x2){min(a, 0xfffffffeu), min(b, 0xfffffffeu)}; }
DI unsigned umax4(u32x4 v) { return max(max(v[0], v[1]), max(v[2], v[3])); }
template <int N> DI void waitv() { asm volatile("s_waitcnt vmcnt(%0)" :: "i"(N) : "memory"); }
DI void bar() { asm volatile("s_waitcnt lgkmcnt(0)\n\ts_barrier" ::: "memory"); }
DI void setflag(char* flag, u32x2 tag, int lane) { if (lane < 8) st8(flag + lane * CTL_REP, tag); }
DI void publish(char* flag, u32x2 tag, int lane) { waitv<0>(); setflag(flag, tag, lane); }
DI void poll(const char* base, unsigned voff, u32x2 tag) {
#pragma unroll 1
  for (int spin = 0; spin < (1 << 17); ++spin) {
    u32x2 v;
    asm volatile("global_load_dwordx2 %0, %1, %2 sc1\n\ts_waitcnt vmcnt(0)" : "=v"(v) : "v"(voff), "s"(base) : "memory");
    if (__all(v[0] == tag[0] && v[1] == tag[1])) break;
    __builtin_amdgcn_s_sleep(1);
  }
}
DI void poll2(const char* base, unsigned voff0, unsigned voff1, u32x2 tag) {
#pragma unroll 1
  for (int spin = 0; spin < (1 << 17); ++spin) {
    u32x2 v, u;
    asm volatile("global_load_dwordx2 %0, %2, %4 sc1\n\tglobal_load_dwordx2 %1, %3, %4 sc1\n\ts_waitcnt vmcnt(0)" : "=&v"(v), "=&v"(u) : "v"(voff0), "v"(voff1), "s"(base) : "memory");
    if (__all(v[0] == tag[0] && v[1] == tag[1] && u[0] == tag[0] && u[1] == tag[1])) break;
    __builtin_amdgcn_s_sleep(1);
  }
}
DI void probe(const char* base, unsigned voff) {
#pragma unroll 1
  for (int spin = 0; spin < (1 << 17); ++spin) {
    unsigned v;
    asm volatile("global_load_dword %0, %1, %2 sc1\n\ts_waitcnt vmcnt(0)" : "=v"(v) : "v"(voff), "s"(base) : "memory");
    if (__all(v != 0xffffffffu)) break;
    __builtin_amdgcn_s_sleep(1);
  }
}
constexpr int t16off(int c0) { return (c0 >> 5) * 1024 + (c0 & 16) * 32; }
DI unsigned t16lane(int lane) { return (unsigned)((lane >> 5) * 256 + (lane & 15) * 16 + ((lane >> 4) & 1) * 8); }
DI void st16s(const void* sbase, unsigned voff, u32x4 v) { asm volatile("global_store_dwordx4 %0, %1, %2 sc1\n\ts_nop 1" :: "v"(voff), "v"(v), "s"(sbase) : "memory"); }
DI float row16_sum(float v) {
  v += __builtin_bit_cast(float, __builtin_amdgcn_update_dpp(0, __builtin_bit_cast(int, v), 0x128, 0xf, 0xf, false));
  v += __builtin_bit_cast(float, __builtin_amdgcn_update_dpp(0, __builtin_bit_cast(int, v), 0x124, 0xf, 0xf, false));
  v += __builtin_bit_cast(float, __builtin_amdgcn_update_dpp(0, __builtin_bit_cast(int, v), 0x122, 0xf, 0xf, false));
  v += __builtin_bit_cast(float, __builtin_amdgcn_update_dpp(0, __builtin_bit_cast(int, v), 0x121, 0xf, 0xf, false));
  return v;
}
DI u32x2 make_tag(u32x2 ep) {
  const unsigned long long e = (((unsigned long long)__builtin_amdgcn_readfirstlane(ep[1]) << 32) | __builtin_amdgcn_readfirstlane(ep[0])) + 1ull;
  return (u32x2){(unsigned)e, (unsigned)(e >> 32)};
}
constexpr unsigned RS_K0 = 0x9e3779b9u, RS_K1 = 0x7f4a7c15u;
struct RowStat { u32x4 a; unsigned b; };
DI void rowstat_get(RowStat& r, const u16* x, int row, int lane) {
  asm volatile("global_load_dwordx4 %0, %2, %4\n\tglobal_load_dword %1, %3, %4 offset:1024" : "=&v"(r.a), "=&v"(r.b) : "v"(lane * 16), "v"(lane * 4), "s"((const char*)x + (size_t)row * 1280) : "memory");
}
DI void rowstat_put(RowStat& r, char* ws, int row, int lane, u32x2 tag) {
  asm volatile("" : "+v"(r.a), "+v"(r.b));
  float s1 = dot2(r.b, 0x3f803f80u, 0.f), s2 = dot2(r.b, r.b, 0.f);
#pragma unroll
  for (int j = 0; j < 4; ++j) { s1 = dot2(r.a[j], 0x3f803f80u, s1); s2 = dot2(r.a[j], r.a[j], s2); }
  s1 = red4(row16_sum(s1)); s2 = red4(row16_sum(s2));
  const float mean = s1 * (1.f / 640.f), rstd = __builtin_amdgcn_rsqf(fmaxf(s2 * (1.f / 640.f) - mean * mean, 0.f) + LN_EPS);
  const unsigned m = __float_as_uint(mean), q = __float_as_uint(rstd);
  if (lane == 0) st16(ws + WS_RST + (size_t)row * 16, (u32x4){m, q, tag[0] ^ m ^ RS_K0, tag[1] ^ q ^ RS_K1});
}
DI int fresh_lane() { int l = __builtin_amdgcn_mbcnt_hi(~0u, __builtin_amdgcn_mbcnt_lo(~0u, 0u)); asm volatile("" : "+v"(l)); return l; }
template <class T> DI void pins(T& v) { asm volatile("" : "+s"(v)); }
template <class T> DI void pin(T& v) { asm volatile("" : "+v"(v)); }
DI bf16x8 lds128(unsigned addr) { return *(const LAS bf16x8*)(uintptr_t)addr; }
DI s16x4 lds64(unsigned addr) { return *(const LAS s16x4*)(uintptr_t)addr; }
DI s16x4 ldstr(unsigned addr) { return __builtin_amdgcn_ds_read_tr16_b64_v4i16((LAS s16x4*)(uintptr_t)addr); }
DI unsigned lds_addr_of(const void* p) { return (unsigned)(uintptr_t)(const LAS char*)p; }
#define STAMP_AT(kid, idx) ((void)0)
#define RETRY_COUNT(k) ((void)0)
#define WAITV_BAR(N) asm volatile("s_waitcnt vmcnt(" #N ") lgkmcnt(0)\n\ts_barrier" ::: "memory")
#define SB() __builtin_amdgcn_sched_barrier(0)
template <int N> using IC = std::integral_constant<int, N>;
template <class F, int... Is> DI void sfor_(F&& f, std::integer_sequence<int, Is...>) { (f(IC<Is>{}), ...); }
template <int N, class F> DI void sfor(F&& f) { sfor_(f, std::make_integer_sequence<int, N>{}); }

constexpr int P3_G1 = -1, K4_LA_ = 6, K4_CG_ = 2, K5_LA_ = 16;
constexpr int K1_STAGE = 32768;
DI void p1_load(const u16* __restrict__ x, const u16* __restrict__ W, const unsigned lds0, const int v, int rb, int cb) {
  const int lane = fresh_lane();
  const unsigned a_voff0 = (unsigned)((lane >> 3) * 1280 + (((lane & 7) ^ ((lane >> 4) & 7)) << 4));
  const unsigned a_voff1 = (unsigned)((lane >> 3) * 1280 + (((lane & 7) ^ ((4 + (lane >> 4)) & 7)) << 4)) + 8 * 1280;
  const unsigned w_voff0 = (unsigned)((lane >> 3) * 3840 + (((lane & 7) ^ (((lane >> 4) & 1) << 2)) << 4));
  const unsigned w_voff1 = w_voff0 + 8 * 3840;
  const char* xs = (const char*)x + (size_t)(rb * 128 + v * 32) * 1280;
  const char* wsrc = (const char*)W + (size_t)((v & 1) * 32) * 3840 + (size_t)(cb * 128 + (v >> 1) * 64) * 2;
  const int rot = ((((blockIdx.x >> 1) & 3) * 5 + (blockIdx.x & 1) * 10) >> 1) % 10;
  auto st = [&](auto s_) {
    constexpr int s0 = s_; const unsigned d = lds0 + (s0 % 5) * K1_STAGE + v * 4096;
    const int s = s0 + rot - (s0 + rot >= 10 ? 10 : 0);
    const char* xp = xs + s * 128; const char* wp = wsrc + (size_t)s * 64 * 3840;
    glds16<false>(xp, a_voff0, d); glds16<false>(wp, w_voff0, d + 16384);
    glds16<false>(xp, a_voff1, d + 1024); glds16<false>(wp, w_voff1, d + 16384 + 1024);
    glds16<false>(xp + 16 * 1280, a_voff0, d + 2048); glds16<false>(wp + 16 * 3840, w_voff0, d + 16384 + 2048);
    glds16<false>(xp + 16 * 1280, a_voff1, d + 3072); glds16<false>(wp + 16 * 3840, w_voff1, d + 16384 + 3072);
  };
  st(IC<0>{}); st(IC<1>{});
  sfor<10>([&](auto s_) { constexpr int s = s_;
    waitv<(s == 0 ? 8 : s <= 7 ? 16 : s == 8 ? 8 : 0)>(); asm volatile("s_barrier" ::: "memory");
    if constexpr (s == 0) { st(IC<2>{}); st(IC<3>{}); }
    else if constexpr (s + 3 < 10) st(IC<s + 3>{}); });
  bar();
}
template <bool ISV>
DI void p1_body(const u16* __restrict__ x, char* __restrict__ ws, const unsigned lds0, const int w, int rb, int cb, u32x2& tag) {
  const int lane = fresh_lane();
  u32x2 ep; gld8<0>(ep, ws + WS_CTL, 0u);
  RowStat rs[2];
  rowstat_get(rs[0], x, blockIdx.x * 8 + w * 2, lane); rowstat_get(rs[1], x, blockIdx.x * 8 + w * 2 + 1, lane);
  const int wm = w >> 1, wn = w & 1;
  const int r32 = lane & 31, hh = lane >> 5;
  STAMP_AT(0, 0);
  unsigned xa[3][4], wa[3][2][2];
  {
    const int row = wm * 64 + r32;
    const int blk = (lane >> 4) & 1, q = (lane & 15) >> 2, p = lane & 3;
#pragma unroll
    for (int hs = 0; hs < 3; ++hs) {
#pragma unroll
      for (int i = 0; i < 4; ++i) { xa[hs][i] = lds0 + hs * 65536 + (unsigned)(row * 128 + ((((i ^ wm) * 2 + hh) ^ ((row >> 1) & 7)) << 4)); pin(xa[hs][i]); }
#pragma unroll
      for (int j = 0; j < 2; ++j) {
        const unsigned base = lds0 + hs * 65536 + (unsigned)(16384 + wn * 8192 + (8 * hh + q) * 128 + (((j * 4 + blk * 2 + (p >> 1)) ^ (((q >> 1) & 1) << 2)) << 4) + (p & 1) * 8);
        wa[hs][j][0] = base + wm * 2048; wa[hs][j][1] = base + (1 ^ wm) * 2048; pin(wa[hs][j][0]); pin(wa[hs][j][1]);
      }
    }
  }
  f32x16 acc[2][2] = {};
  f32x4 sw[2] = {};
  u32x4 rec;
  bf16x8 sel = (bf16x8)(short)(((lane & 15) == ((lane >> 4) & 1)) ? 0x3f80 : 0); pin(sel);
  bf16x8 fx[2][2]; s16x4 fwl[2][2], fwh[2][2];
  auto rd = [&](auto i_, auto k_) {
    constexpr int i = i_, k = k_, s = i >> 2, p = i & 3, sl = s % 5, hs = sl >> 1; constexpr unsigned so = (sl & 1) * K1_STAGE;
    if constexpr (k == 0) fx[i & 1][0] = lds128(xa[hs][p] + so);
    if constexpr (k == 3) fx[i & 1][1] = lds128(xa[hs][p] + so + 4096);
    if constexpr (k == 1 || k == 2) { fwl[i & 1][k - 1] = ldstr(wa[hs][k - 1][p & 1] + so + (p >> 1) * 4096); fwh[i & 1][k - 1] = ldstr(wa[hs][k - 1][p & 1] + so + (p >> 1) * 4096 + 512); }
  };
  const bool isq = cb < 5;
  const int pitch = isq ? 1280 : 2560;
  unsigned off[2][ISV ? 1 : 4];
#pragma unroll
  for (int j = 0; j < 2; ++j) {
    if constexpr (ISV) {
      const int n = cb * 128 + wn * 64 + j * 32 + r32 - 1280, head = n / 20, d0 = n - head * 20;
      const int d = (head & 1) ? (d0 < 4 ? d0 + 16 : d0 - 4) : d0;
      off[j][0] = (unsigned)(head * 163840 + 1280 + (hh * 20 + d) * 16); pin(off[j][0]);
    } else {
#pragma unroll
      for (int g = 0; g < 4; ++g) {
        const int n = cb * 128 + wn * 64 + j * 32 + 8 * g + 4 * hh - (isq ? 0 : 640), head = n / 20, d0 = n - head * 20;
        const int d = (head & 1) ? (d0 < 4 ? d0 + 16 : d0 - 4) : d0;
        off[j][g] = (unsigned)(head * 64 * pitch + (d < 16 ? (((d >> 3) * 32 + r32) * 16 + (d & 4) * 2) : (1024 + r32 * 8))); pin(off[j][g]);
      }
    }
  }
  waitv<0>(); pin(ep); tag = make_tag(ep);
  rowstat_put(rs[0], ws, blockIdx.x * 8 + w * 2, lane, tag); rowstat_put(rs[1], ws, blockIdx.x * 8 + w * 2 + 1, lane, tag);
  asm volatile("s_barrier" ::: "memory");
  STAMP_AT(0, 1);
  sfor<4>([&](auto k) { rd(IC<0>{}, k); });
  sfor<40>([&](auto i_) {
    constexpr int i = i_, b = i & 1;
    if constexpr ((i & 3) == 3 && i < 39) asm volatile("s_barrier" ::: "memory");
    if constexpr (i == 30) gld16<0>(rec, ws + WS_RST + (size_t)(rb * 128 + wm * 64) * 16, (unsigned)(lane * 16));
    SB();
    const bf16x8 w0 = __builtin_shufflevector(fwl[b][0], fwh[b][0], 0, 1, 2, 3, 4, 5, 6, 7), w1 = __builtin_shufflevector(fwl[b][1], fwh[b][1], 0, 1, 2, 3, 4, 5, 6, 7);
    sfor<4>([&](auto k_) { constexpr int k = k_, t = k >> 1, j = (k ^ (k >> 1)) & 1;
      if constexpr (ISV) acc[t][j] = MFMA32(fx[b][t], j ? w1 : w0, acc[t][j]); else acc[t][j] = MFMA32(j ? w1 : w0, fx[b][t], acc[t][j]);
      SB();
      if constexpr (i < 39) rd(IC<i + 1>{}, k_);
      SB(); });
    if constexpr (b == 1) { sw[0] = MFMA16(w0, sel, sw[0]); sw[1] = MFMA16(w1, sel, sw[1]); }
    SB();
  });
  STAMP_AT(0, 11);
  LAS float* csp = (LAS float*)(uintptr_t)lds0;
  if ((lane & 15) < 2) sfor<2>([&](auto j) { *(LAS f32x4*)&csp[wm * 128 + wn * 64 + j * 32 + (lane & 1) * 16 + (lane >> 4) * 4] = sw[j]; });
  waitv<0>(); pin(rec);
#pragma unroll 1
  for (int spin = 0; spin < (1 << 17) && !__all((rec[2] ^ rec[0] ^ RS_K0) == tag[0] && (rec[3] ^ rec[1] ^ RS_K1) == tag[1]); ++spin) {
    gld16<0>(rec, ws + WS_RST + (size_t)(rb * 128 + wm * 64) * 16, (unsigned)(lane * 16)); waitv<0>(); pin(rec);
  }
  LAS f32x2* st = (LAS f32x2*)(uintptr_t)(lds0 + 4096 + w * 512);
  if constexpr (ISV) st[lane] = (f32x2){__uint_as_float(rec[0]), __uint_as_float(rec[1])};
  bar();
  STAMP_AT(0, 12);
  if constexpr (ISV) {
    float c[2];
#pragma unroll
    for (int j = 0; j < 2; ++j) c[j] = csp[wn * 64 + j * 32 + r32] + csp[128 + wn * 64 + j * 32 + r32];
#pragma unroll
    for (int t = 0; t < 2; ++t) {
      f32x4 s01[4], s23[4];
#pragma unroll
      for (int g = 0; g < 4; ++g) { s01[g] = *(const LAS f32x4*)&st[t * 32 + 8 * g + 4 * hh]; s23[g] = *(const LAS f32x4*)&st[t * 32 + 8 * g + 4 * hh + 2]; }
      const char* dst = ws + WS_KV + (size_t)(rb * 4 + wm * 2 + t) * 2560;
#pragma unroll
      for (int j = 0; j < 2; ++j) {
        const f32x16& a = acc[t][j];
        unsigned pk[8];
#pragma unroll
        for (int g = 0; g < 4; ++g) {
          pk[2 * g]     = pk_bf16(s01[g][1] * (a[4 * g] - s01[g][0] * c[j]), s01[g][3] * (a[4 * g + 1] - s01[g][2] * c[j]));
          pk[2 * g + 1] = pk_bf16(s23[g][1] * (a[4 * g + 2] - s23[g][0] * c[j]), s23[g][3] * (a[4 * g + 3] - s23[g][2] * c[j]));
        }
        st16s(dst, off[j][0], (u32x4){pk[0], pk[1], pk[2], pk[3]});
        st16s(dst + 640, off[j][0], (u32x4){pk[4], pk[5], pk[6], pk[7]});
      }
    }
  } else {
    const float sc = isq ? QSCALE : 1.f;
    const char* base = ws + (isq ? WS_Q : WS_KV) + (size_t)(rb * 4 + wm * 2) * pitch;
    const auto pm = __builtin_amdgcn_permlane32_swap(rec[0], rec[0], false, false), pr = __builtin_amdgcn_permlane32_swap(rec[1], rec[1], false, false);
#pragma unroll
    for (int j = 0; j < 2; ++j)
#pragma unroll
      for (int g = 0; g < 4; ++g) {
        const int nl = wn * 64 + j * 32 + 8 * g + 4 * hh;
        const f32x4 c = *(const LAS f32x4*)&csp[nl] + *(const LAS f32x4*)&csp[128 + nl];
#pragma unroll
        for (int t = 0; t < 2; ++t) {
          const f32x16& a = acc[t][j];
          const float rs_ = __uint_as_float(pr[t]) * sc, mr = __uint_as_float(pm[t]);
          st8s(base + t * pitch, off[j][g], (u32x2){pk_bf16(rs_ * (a[4 * g] - mr * c[0]), rs_ * (a[4 * g + 1] - mr * c[1])), pk_bf16(rs_ * (a[4 * g + 2] - mr * c[2]), rs_ * (a[4 * g + 3] - mr * c[3]))});
        }
      }
  }
  STAMP_AT(0, 13);
  waitv<0>();
  if (lane < 16) st8(ws + WS_CTL + CTL_F1 + ((cb * 4 + wn * 2 + (lane >> 3)) * 32 + rb * 2 + wm) * 8 + (lane & 7) * CTL_REP, tag);
}

DI void p1_repack(const u16* __restrict__ Wo, char* __restrict__ ws, const int w, int idx) {
  const int lane = fresh_lane();
  const unsigned pvo = (lane >> 4) * 8 * 1280 + (lane & 15) * 2;
  unsigned pw[7][8];
  sfor<7>([&](auto r) {
    const int f = idx * 50 + min(w + 8 * r, 49), cbk = f / 100, rem = f - cbk * 100, ks = rem / 5, ct = rem - ks * 5;
    const char* src = (const char*)(Wo + (size_t)(32 * ks) * 640 + 80 * cbk + 16 * ct);
    sfor<8>([&](auto i) { gldu16<(i & 3) * 1280>(pw[r][i], src + (i & ~3) * 1280, pvo); });
  });
  sfor<7>([&](auto r) {
    waitv<(6 - r) * 8>();
    sfor<8>([&](auto j) { pin(pw[r][j]); });
    const u32x4 v = {pw[r][0] | (pw[r][1] << 16), pw[r][2] | (pw[r][3] << 16), pw[r][4] | (pw[r][5] << 16), pw[r][6] | (pw[r][7] << 16)};
    pw[r][0] = v[0]; pw[r][1] = v[1]; pw[r][2] = v[2]; pw[r][3] = v[3];
  });
  sfor<7>([&](auto r) {
    if (w + 8 * r < 50) st16(ws + WS_WP + (size_t)(idx * 50 + w + 8 * r) * 1024 + lane * 16, (u32x4){pw[r][0], pw[r][1], pw[r][2], pw[r][3]});
  });
}

template <int OFF> DI void rdK(bf16x8& k0, s16x4& k1, unsigned a0, unsigned a1) {
  asm volatile("ds_read_b128 %0, %2 offset:%4\n\tds_read_b64 %1, %3 offset:%4" : "=&v"(k0), "=&v"(k1) : "v"(a0), "v"(a1), "i"(OFF));
}
template <int OFF> DI void rdV(bf16x8& v0, bf16x8& v1, unsigned a, unsigned long long msk) {
  asm volatile("s_mov_b64 exec, %5\n\tds_read_b128 %0, %2 offset:%3\n\tds_read_b128 %1, %2 offset:%4\n\ts_mov_b64 exec, -1"
               : "+v"(v0), "+v"(v1) : "v"(a), "i"(OFF), "i"(OFF + 640), "s"(msk));
}
#define EX2(v) __builtin_amdgcn_exp2f(v)
constexpr int wo_off(int p) { return (p < 60 ? p : p + 20) * 1024; }
constexpr int gu_off(int f) { return (f < 20 ? f + 60 : f < 60 ? f + 100 : f - 60) * 1024; }
constexpr int wd_off(int f) { return ((f / 20 + 4) % 8) * 20480 + (f % 20) * 1024; }
constexpr int CSF_OFF = 116 * 1024, XCH_OFF = 120 * 1024;

DI void p2_attn(char* __restrict__ ws, const u16* __restrict__ Wg, const u16* __restrict__ Wu, const u16* __restrict__ Wd,
                const unsigned lds0, const int w, const u32x2 tag, const int rb3, const int cb3, const int rb4, const int cb4) {
  const int lane = fresh_lane();
  const int kh = w >> 2, wq = w & 3;
  const int head = (blockIdx.x & 7) * 4 + ((blockIdx.x >> 3) & 3), qb = blockIdx.x >> 5;
  const int r32 = lane & 31, hh = lane >> 5;
  const char* kvg = ws + WS_KV + (size_t)head * 163840 + (size_t)kh * 81920 + wq * 1024;
  const unsigned voff = lane * 16;
  const unsigned kvl = lds0 + kh * 81920 + wq * 1024;
  char* ctl = ws + WS_CTL;
  const char* ctr = ctl + (blockIdx.x & 7) * CTL_REP;
  unsigned pw[3][8];
  const int gw = blockIdx.x * 8 + w, gwc = min(gw, 1919);
  const char* psrc[3];
#pragma unroll
  for (int r = 0; r < 2; ++r) {
    const int f = 2 * gwc + r, cbk = f / 120, rem = f - cbk * 120, ks = rem / 6, ct = rem - ks * 6;
    psrc[r] = (const char*)((ct < 3 ? Wg : Wu) + (size_t)(32 * ks) * 1536 + 48 * cbk + 16 * (ct < 3 ? ct : ct - 3));
  }
  { const int f = gwc, cbk = f / 240, rem = f - cbk * 240, ks = rem / 5, ct = rem - ks * 5;
    psrc[2] = (const char*)(Wd + (size_t)(32 * ks) * 640 + 80 * cbk + 16 * ct); }
  unsigned pvo1 = (lane >> 4) * 8 * 3072 + (lane & 15) * 2, pvo2 = (lane >> 4) * 8 * 1280 + (lane & 15) * 2; pin(pvo1); pin(pvo2);
  const char* wog = ws + WS_WP + (size_t)cb3 * 102400;
  const int qtile = qb * 8 + wq * 2 + kh, cg0 = head * 5 + (head & 1), cgt = (head & 1) ? head * 5 : head * 5 + 4;
  const char* ob = ws + WS_O + (size_t)qtile * 40960;
  const char* obs[3] = {ob + (cg0 >> 3) * 1024 + ((cg0 & 7) >> 1) * 256, ob + ((cg0 + 2) >> 3) * 1024 + (((cg0 + 2) & 7) >> 1) * 256,
                        ob + (cgt >> 3) * 1024 + ((cgt & 7) >> 1) * 256 + (cgt & 1) * 8};
  unsigned vo = (unsigned)((r32 >> 4) * 20480 + (r32 & 15) * 16 + hh * 8); pin(vo);
  u32x4 fill = {0xffffffffu, 0xffffffffu, 0xffffffffu, 0xffffffffu}; pin(fill);
  const u32x2 fill2 = {fill[0], fill[1]};
  u32x2 fa, fb, fc;
  const unsigned fvc = (unsigned)CTL_FR + (unsigned)((cb4 * 60 + min(lane, 59)) * 8);
  const unsigned fva = (unsigned)(hh ? CTL_F4 : CTL_F2) + (unsigned)(((rb3 * 2 + ((w & 3) >> 1)) * 32 + r32) * 8), fvb = (unsigned)CTL_F3 + (unsigned)(((rb4 * 16 + w * 2) * 8 + (lane & 15)) * 8);
  const char* gcl = ws + WS_G + (size_t)(rb4 * 16 + w * 2) * 49152 + cb4 * 1536;
  const char* hcl = ws + WS_H + (size_t)(rb3 * 4 + w) * 20480 + cb3 * 2560;
  const char* rdst = ws + WS_WP + (size_t)(NFR_WO + 2 * gw) * 1024;
  pins(psrc[0]); pins(psrc[1]); pins(psrc[2]); pins(obs[0]); pins(obs[1]); pins(obs[2]); pins(wog); pins(gcl); pins(hcl); pins(rdst);
  STAMP_AT(1, 0);
  unsigned f0;
  {
    const int cfl = (20 * head) >> 5, cfh = (20 * head + 19) >> 5;
    f0 = (unsigned)(((((lane & 32) ? 40 : 20) + ((lane & 16) ? cfh : cfl)) * 32 + kh * 16 + (lane & 15)) * 8);
    const unsigned f1 = (unsigned)((lane < 32 ? (((lane & 1) ? cfh : cfl) * 32 + qb * 4 + wq) : 1920 + cb3 * 16 + (lane & 15)) * 8);
    poll2(ctr + CTL_F1, f0 & ~(12u * 8u), f1, tag);
  }
  STAMP_AT(1, 4);
#pragma unroll
  for (int i = 0; i < 4; ++i) glds16<true>(kvg + i * 4096, voff, kvl + i * 4096);
  bf16x8 q0[2]; s16x4 q1[2];
  {
    const char* qg = ws + WS_Q + ((size_t)head * 64 + qb * 8 + wq * 2) * 1280;
    gld16<0>(q0[0], qg, voff); gld16<1280>(q0[1], qg, voff); gld8<1024>(q1[0], qg, r32 * 8); gld8<2304>(q1[1], qg, r32 * 8);
  }
  const unsigned ka0 = lds0 + kh * 81920 + lane * 16, ka1 = lds0 + kh * 81920 + 1024 + r32 * 8, va = lds0 + kh * 81920 + 1280 + (hh * 20 + (r32 < 20 ? r32 : 0)) * 16;
  bf16x8 K0[2], V0[2], V1[2]; s16x4 K1[2];
  V0[0] = V0[1] = V1[0] = V1[1] = (bf16x8)(short)(r32 == 20 ? 0x3f80 : 0);
  pin(V0[0]); pin(V0[1]); pin(V1[0]); pin(V1[1]);
  WAITV_BAR(0);
  u32x2 fr; gld8<0>(fr, ctr + CTL_F1, f0);
  pin(q0[0]); pin(q0[1]); pin(q1[0]); pin(q1[1]);
  if (hh) { q1[0] = (s16x4){0, 0, 0, 0}; q1[1] = (s16x4){0, 0, 0, 0}; }
  STAMP_AT(1, 1);
  rdK<0>(K0[0], K1[0], ka0, ka1);
  const unsigned long long vmsk = 0x000fffff000fffffull;
  rdV<0>(V0[0], V1[0], va, vmsk);
  f32x16 cm[2];
  {
    const bf16x8 kf0 = lds128(lds0 + lane * 16); const s16x4 kf1 = lds64(lds0 + 1024 + r32 * 8);
#pragma unroll
    for (int t = 0; t < 2; ++t) {
      f32x16 s = MFMA32(kf0, q0[t], (f32x16){}); s = MFMA32K8(kf1, q1[t], s);
      float m = s[0];
#pragma unroll
      for (int i = 1; i < 16; ++i) m = fmaxf(m, s[i]);
      m = max32(m);
      cm[t] = (f32x16)(-m);
      pin(cm[t]);
    }
  }
  asm volatile("s_waitcnt lgkmcnt(0)" : "+v"(K0[0]), "+v"(K1[0]), "+v"(V0[0]), "+v"(V1[0]));
  f32x16 o[2] = {};
  f32x16 SA, SB_;
  u32x4 PA0, PA1, PB0 = {}, PB1 = {};
  SA = MFMA32(K0[0], q0[0], cm[0]); SA = MFMA32K8(K1[0], q1[0], SA);
  waitv<0>(); pin(fr);
  if (!__all(fr[0] == tag[0] && fr[1] == tag[1])) poll(ctr + CTL_F1, f0, tag);
  STAMP_AT(1, 2);
  pin(o[0]); pin(o[1]); pin(PB0); pin(PB1);
#define GRP(MF, Sc, Pc, i, j) do { MF; SB(); const float e0 = EX2(Sc[i]), e1 = EX2(Sc[i + 1]), e2 = EX2(Sc[i + 2]), e3 = EX2(Sc[i + 3]); \
    Pc[j] = pk_bf16(e0, e1); Pc[j + 1] = pk_bf16(e2, e3); pin(Pc); SB(); } while (0)
#define SLOT(QK, Sc, Pc0, Pc1, Sn, kb, jn, Pp0, Pp1, vb, jp) do { \
    GRP(if constexpr (QK) Sn = MFMA32(K0[kb], q0[jn], cm[jn]), Sc, Pc0, 0, 0); \
    GRP(if constexpr (QK) Sn = MFMA32K8(K1[kb], q1[jn], Sn), Sc, Pc0, 4, 2); \
    GRP(o[jp] = MFMA32(V0[vb], __builtin_bit_cast(bf16x8, Pp0), o[jp]), Sc, Pc1, 8, 0); \
    GRP(o[jp] = MFMA32(V1[vb], __builtin_bit_cast(bf16x8, Pp1), o[jp]), Sc, Pc1, 12, 2); } while (0)
#define LWAIT(kb, vb) asm volatile("s_waitcnt lgkmcnt(0)" : "+v"(K0[kb]), "+v"(K1[kb]), "+v"(V0[vb]), "+v"(V1[vb]))
  auto rstore = [&](auto r_, auto yv_) {
    constexpr int r = r_;
    waitv<yv_>();
    sfor<8>([&](auto j) { pin(pw[r][j]); });
    const u32x4 v = {pw[r][0] | (pw[r][1] << 16), pw[r][2] | (pw[r][3] << 16), pw[r][4] | (pw[r][5] << 16), pw[r][6] | (pw[r][7] << 16)};
    if (gw < 1920) {
      if constexpr (r < 2) {
        st16s(rdst + r * 1024, voff, v);
        float c = 0.f;
#pragma unroll
        for (int j = 0; j < 4; ++j) c = dot2(v[j], 0x3f803f80u, c);
        c = red4(c);
        if (lane < 16) st4(ws + WS_CSP + (size_t)(2 * gw + r) * 64 + lane * 4, c);
      } else st16s(ws + WS_WP + (size_t)(NFR_WO + NFR_GU + gw) * 1024, voff, v);
    }
  };
  auto hook = [&](auto it_, auto i_) {
    constexpr int it = it_, i = i_, n = (it - 2) * 8 + i;
    if constexpr (n >= 0 && n < 2) st8s(obs[n], vo, fill2);
    if constexpr (n == 2) { if (!hh) st8s(obs[2], vo, fill2); }
    if constexpr (n >= 3 && n < 6) { const int L = (n - 3) * 64 + lane; st16s(gcl, (unsigned)(L * 16 + (L >= 96 ? 49152 - 1536 : 0)), fill); }
    if constexpr (n >= 6 && n < 9) { if (w < 4) st16s(hcl, (unsigned)(min((n - 6) * 64 + lane, 159) * 16), fill); }
    if constexpr (n == 9) { if (w < 4 && lane < 16) st8s(ws + WS_HST + (size_t)(rb3 * 4 + w) * 1024 + (cb3 >> 1) * 256 + (cb3 & 1) * 8, (unsigned)(lane * 16), fill2); }
    if constexpr (it == 5 && i == 6) {
      waitv<0>();
      setflag(ctl + CTL_FR + gw * 8, tag, lane);
      setflag(ctl + CTL_F2 + (qtile * 32 + head) * 8, tag, lane);
      setflag(ctl + CTL_F4 + ((rb4 * 8 + w) * 32 + cb4) * 8, tag, lane);
      if (w < 4) setflag(ctl + CTL_F3 + ((rb3 * 4 + w) * 8 + cb3) * 8, tag, lane);
    }
    if constexpr (it == 7 && i == 6) { gld8<0>(fa, ctr, fva); gld8<0>(fb, ctr, fvb); gld8<0>(fc, ctr, fvc); }
    if constexpr (it < 2) glds16<true>(kvg + (size_t)(it * 8 + 4 + i) * 4096, voff, kvl + (it * 8 + 4 + i) * 4096);
    else if constexpr (it < 4) gldu16<(i & 1) * 3072>(pw[it - 2][i], psrc[it - 2] + (i & ~1) * 3072, pvo1);
    else if constexpr (it == 4) {
      gldu16<(i & 3) * 1280>(pw[2][i], psrc[2] + (i & ~3) * 1280, pvo2);
      if constexpr (i == 0) rstore(IC<0>{}, IC<9>{});
      if constexpr (i == 7) rstore(IC<1>{}, IC<8>{});
    } else if constexpr (it == 5) {
      if constexpr (i == 7) rstore(IC<2>{}, IC<3>{});
    } else {
      constexpr int j = (it - 6) * 8 + i;
      const char* src = wog + (w + 8 * j) * 1024;
      if constexpr (j < 7) glds16<true>(src, voff, lds0 + w * 1024 + j * 8192);
      else if constexpr (j == 7) glds16<true>(src, voff, lds0 + w * 1024 + j * 8192 + (w >= 4 ? 20480 : 0));
      else if constexpr (j < 12) glds16<true>(src, voff, lds0 + w * 1024 + j * 8192 + 20480);
      else if constexpr (j == 12) { if (w < 4) glds16<true>(src, voff, lds0 + w * 1024 + j * 8192 + 20480); }
    }
    SB();
  };
  const unsigned ka0b = ka0 + 40960, ka1b = ka1 + 40960, vab = va + 40960;
  sfor<8>([&](auto it_) {
    constexpr int it = it_;
    if constexpr (it == 1) WAITV_BAR(6);
    if constexpr (it == 2) { WAITV_BAR(8); STAMP_AT(1, 3); }
    if constexpr (it == 4) asm volatile("s_waitcnt vmcnt(16)\n\ts_barrier" ::: "memory");
    if constexpr (it == 6) asm volatile("s_barrier" ::: "memory");
    if (((it ^ kh) & 1) == 0) __builtin_amdgcn_s_setprio(0); else __builtin_amdgcn_s_setprio(1);
    constexpr int o0 = (it & 3) * 10240;
    constexpr int on = it == 3 ? 0 : o0 + 10240;
    const unsigned a0 = it < 4 ? ka0 : ka0b, a1 = it < 4 ? ka1 : ka1b, av = it < 4 ? va : vab;
    const unsigned n0 = it < 3 ? ka0 : ka0b, n1 = it < 3 ? ka1 : ka1b, nv = it < 3 ? va : vab;
    rdK<o0 + 2560>(K0[1], K1[1], a0, a1); hook(it_, IC<0>{});                  SLOT(true, SA, PA0, PA1, SB_, 0, 1, PB0, PB1, 1, 1);
    LWAIT(1, 0); rdV<o0 + 2560>(V0[1], V1[1], av, vmsk); hook(it_, IC<1>{});   SLOT(true, SB_, PB0, PB1, SA, 1, 0, PA0, PA1, 0, 0);
    rdK<o0 + 5120>(K0[0], K1[0], a0, a1); hook(it_, IC<2>{});                  SLOT(true, SA, PA0, PA1, SB_, 1, 1, PB0, PB1, 0, 1);
    LWAIT(0, 1); rdV<o0 + 5120>(V0[0], V1[0], av, vmsk); hook(it_, IC<3>{});   SLOT(true, SB_, PB0, PB1, SA, 0, 0, PA0, PA1, 1, 0);
    rdK<o0 + 7680>(K0[1], K1[1], a0, a1); hook(it_, IC<4>{});                  SLOT(true, SA, PA0, PA1, SB_, 0, 1, PB0, PB1, 1, 1);
    LWAIT(1, 0); rdV<o0 + 7680>(V0[1], V1[1], av, vmsk); hook(it_, IC<5>{});   SLOT(true, SB_, PB0, PB1, SA, 1, 0, PA0, PA1, 0, 0);
    if constexpr (it < 7) rdK<on>(K0[0], K1[0], n0, n1);
    hook(it_, IC<6>{});                                                        SLOT(true, SA, PA0, PA1, SB_, 1, 1, PB0, PB1, 0, 1);
    LWAIT(0, 1);
    if constexpr (it < 7) rdV<on>(V0[0], V1[0], nv, vmsk);
    hook(it_, IC<7>{});                                                        SLOT(it < 7, SB_, PB0, PB1, SA, 0, 0, PA0, PA1, 1, 0);
  });
  __builtin_amdgcn_s_setprio(0);
  o[1] = MFMA32(V0[1], __builtin_bit_cast(bf16x8, PB0), o[1]);
  o[1] = MFMA32(V1[1], __builtin_bit_cast(bf16x8, PB1), o[1]);
  STAMP_AT(1, 9);
  bar();
  {
    LAS f32x4* xch = (LAS f32x4*)(uintptr_t)(lds0 + XCH_OFF + w * 3072);
    const f32x16& snd = kh ? o[0] : o[1];
#pragma unroll
    for (int g = 0; g < 3; ++g) xch[g * 64 + lane] = (f32x4){snd[4 * g], snd[4 * g + 1], snd[4 * g + 2], snd[4 * g + 3]};
  }
  bar();
  STAMP_AT(1, 10);
  float of[12];
  {
    const LAS f32x4* xch = (const LAS f32x4*)(uintptr_t)(lds0 + XCH_OFF + (w ^ 4) * 3072);
    const f32x16& own = kh ? o[1] : o[0];
#pragma unroll
    for (int g = 0; g < 3; ++g) { const f32x4 v = xch[g * 64 + lane];
#pragma unroll
      for (int j = 0; j < 4; ++j) of[4 * g + j] = own[4 * g + j] + v[j]; }
  }
  float l = hi32(of[8]);
  bool ok = (l > 8.67e-19f) && (l < 1.15e18f);
#pragma unroll
  for (int i = 0; i < 8; ++i) ok = ok && (fabsf(of[i]) < 1e37f);
  if (!hh) {
#pragma unroll
    for (int i = 8; i < 12; ++i) ok = ok && (fabsf(of[i]) < 1e37f);
  }
  if (__builtin_expect(!__all(ok), 0)) {
    __builtin_amdgcn_fence(__ATOMIC_ACQUIRE, "agent");
    const char* kv = ws + WS_KV + (size_t)head * 163840;
    const bf16x8 fq0 = kh ? q0[1] : q0[0];
    const s16x4 fq1 = kh ? q1[1] : q1[0];
    float m = -INFINITY;
    for (int t = 0; t < 64; ++t) {
      const bf16x8 kf0 = *(const bf16x8*)(kv + t * 2560 + lane * 16); const s16x4 kf1 = *(const s16x4*)(kv + t * 2560 + 1024 + r32 * 8);
      f32x16 s = MFMA32(kf0, fq0, (f32x16){}); s = MFMA32K8(kf1, fq1, s);
#pragma unroll
      for (int i = 0; i < 16; ++i) m = fmaxf(m, s[i]);
    }
    m = max32(m);
    f32x16 oo = {};
    const int vr = r32 < 20 ? r32 : 0;
    for (int t = 0; t < 64; ++t) {
      const bf16x8 kf0 = *(const bf16x8*)(kv + t * 2560 + lane * 16); const s16x4 kf1 = *(const s16x4*)(kv + t * 2560 + 1024 + r32 * 8);
      bf16x8 vf0 = *(const bf16x8*)(kv + t * 2560 + 1280 + (hh * 20 + vr) * 16), vf1 = *(const bf16x8*)(kv + t * 2560 + 1920 + (hh * 20 + vr) * 16);
      if (r32 >= 20) { vf0 = (bf16x8)(short)0x3f80; vf1 = vf0; }
      f32x16 s = MFMA32(kf0, fq0, (f32x16){}); s = MFMA32K8(kf1, fq1, s);
      u32x4 p0, p1;
#pragma unroll
      for (int i = 0; i < 4; ++i) {
        p0[i] = pk_bf16(EX2(s[2 * i] - m), EX2(s[2 * i + 1] - m));
        p1[i] = pk_bf16(EX2(s[8 + 2 * i] - m), EX2(s[8 + 2 * i + 1] - m));
      }
      oo = MFMA32(vf0, __builtin_bit_cast(bf16x8, p0), oo);
      oo = MFMA32(vf1, __builtin_bit_cast(bf16x8, p1), oo);
    }
#pragma unroll
    for (int i = 0; i < 12; ++i) of[i] = oo[i];
    l = hi32(of[8]);
  }
  const float rl = __builtin_amdgcn_rcpf(l);
  waitv<0>(); pin(fa); pin(fb); pin(fc);
  setflag(ctl + CTL_FD + gw * 8, tag, lane);
#pragma unroll
  for (int g = 0; g < 3; ++g)
    if (g < 2 || !hh) st8s(obs[g], vo, nofill(pk_bf16(of[4 * g] * rl, of[4 * g + 1] * rl), pk_bf16(of[4 * g + 2] * rl, of[4 * g + 3] * rl)));
  STAMP_AT(1, 11);
  if (!__all(fa[0] == tag[0] && fa[1] == tag[1] && fb[0] == tag[0] && fb[1] == tag[1])) poll2(ctr, fva, fvb, tag);
  if (!__all(fc[0] == tag[0] && fc[1] == tag[1])) poll(ctr, fvc, tag);
  STAMP_AT(1, 12);
}

constexpr int x_none(int) { return 0; }
constexpr int x_p4(int i) { return ((i >= 2 && i < 7) || (i >= 10 && i < 18)) ? 1 : 0; }
template <int NSTEP, int NF, int LA, int LL, int CG, int NA, int ASTR, int (*BOFF)(int), int (*XCNT)(int), class PRE, class MM>
DI void gemm_res(const char* asrc, unsigned lds0, unsigned voff, PRE&& pre, MM&& mm) {
  bf16x8 a[NSTEP][NA];
  bf16x8 bf[LL + 1][NF];
  unsigned rd0 = lds0 + voff, rd1 = rd0 + 65536, rd2 = rd0 + 131072; pin(rd0); pin(rd1); pin(rd2);
  auto issue = [&](auto n_) { constexpr int n = n_; sfor<NA>([&](auto j) { gld16<(n & 3) * 1024>(a[n][j], asrc + (n >> 2) * 4096 + j * ASTR, voff); }); };
  constexpr auto xsum = [](int lo, int hi) { int c = 0; for (int i = lo < 0 ? 0 : lo; i <= hi; ++i) c += XCNT(i); return c; };
  auto rd = [&](auto i_, auto f_) {
    constexpr int i = i_, f = f_, off = BOFF(i * NF + f);
    bf[i % (LL + 1)][f] = lds128((off < 65536 ? rd0 : off < 131072 ? rd1 : rd2) + (off & 65535));
  };
  static_assert(NSTEP % CG == 0 && LA > CG, "groups");
  unsigned m = 0;
  auto fold = [&](auto n_) { constexpr int n = n_;
    sfor<NA>([&](auto j) { pin(a[n][j]); const u32x4 u = __builtin_bit_cast(u32x4, a[n][j]); m = max(max(m, u[0]), u[1]); m = max(max(m, u[2]), u[3]); });
    pin(m);
    if constexpr (n % CG == CG - 1) {
      if (__builtin_expect(__any(m == 0xffffffffu), 0)) {
#pragma unroll 1
        for (int spin = 0; spin < (1 << 17); ++spin) {
          RETRY_COUNT(NSTEP + NF);
          sfor<CG>([&](auto k) { issue(IC<n - CG + 1 + k>{}); });
          waitv<0>(); m = 0;
          sfor<CG>([&](auto k) { sfor<NA>([&](auto j) { pin(a[n - CG + 1 + k][j]); m = max(m, umax4(__builtin_bit_cast(u32x4, a[n - CG + 1 + k][j]))); }); });
          if (!__any(m == 0xffffffffu)) break;
        }
      }
    }
  };
  sfor<LA>([&](auto n) { issue(n); });
  pre(IC<-1>{});
  sfor<LL>([&](auto i) { sfor<NF>([&](auto f) { rd(i, f); }); });
  waitv<(LA - CG) * NA>(); sfor<CG>([&](auto n) { fold(n); });
  sfor<NSTEP>([&](auto i_) {
    constexpr int i = i_;
    pre(i_);
    SB();
    sfor<NF>([&](auto f_) {
      constexpr int f = f_;
      if constexpr (f == NF - 1 && i + CG < NSTEP) {
        waitv<((i + LA < NSTEP ? i + LA : NSTEP - 1) - (i + CG)) * NA + xsum(i + CG - LA + 1, i)>(); fold(IC<i + CG>{}); SB(); }
      mm(f_, bf[i % (LL + 1)][f], a[i]);
      SB();
      if constexpr (i + LL < NSTEP) rd(IC<i + LL>{}, f_);
      if constexpr (f == 0 && i + LA < NSTEP) issue(IC<i + LA>{});
      SB();
    });
  });
}

__global__ __launch_bounds__(512, 2) void k_mega(const u16* __restrict__ x, const u16* __restrict__ Wqkv, const u16* __restrict__ Wo,
                                                 const u16* __restrict__ Wg, const u16* __restrict__ Wu, const u16* __restrict__ Wd,
                                                 char* __restrict__ ws, u16* __restrict__ out) {
  __shared__ __attribute__((aligned(16))) char smem[163840];
  const unsigned lds0 = lds_addr_of(smem);
  const int w = __builtin_amdgcn_readfirstlane(threadIdx.x >> 6);
  const int g8 = blockIdx.x & 7, i32 = blockIdx.x >> 3;
  char* ctl = ws + WS_CTL;
  const char* ctr = ctl + g8 * CTL_REP;
  u32x2 tag;
  if (i32 < 30) {
    const int i = (g8 & 1) * 30 + i32, rb = (g8 >> 1) * 4 + (i & 3), cb = i >> 2;
    if (w >= 4) { p1_load(x, Wqkv, lds0, w - 4, rb, cb); u32x2 ep; gld8<0>(ep, ctl, 0u); waitv<0>(); pin(ep); tag = make_tag(ep); }
    else if (cb >= 10) p1_body<true>(x, ws, lds0, w, rb, cb, tag);
    else p1_body<false>(x, ws, lds0, w, rb, cb, tag);
  } else {
    { RowStat rs; u32x2 ep; const int lane = fresh_lane(); gld8<0>(ep, ctl, 0u); rowstat_get(rs, x, blockIdx.x * 8 + w, lane); waitv<0>(); pin(ep); tag = make_tag(ep);
      rowstat_put(rs, ws, blockIdx.x * 8 + w, lane, tag); }
    p1_repack(Wo, ws, w, blockIdx.x - 240);
    publish(ctl + CTL_F1 + (1920 + (blockIdx.x - 240) * 8 + w) * 8, tag, fresh_lane());
  }
  bar();
  const int rb3 = g8 * 4 + (i32 >> 3), cb3 = i32 & 7;
  const int rb4 = (g8 >> 2) * 4 + (i32 >> 3), cb4 = (g8 & 3) * 8 + (i32 & 7);
  p2_attn(ws, Wg, Wu, Wd, lds0, w, tag, rb3, cb3, rb4, cb4);
  bar();
  const char* gug = ws + WS_WP + (size_t)NFR_WO * 1024 + (size_t)cb4 * 122880;
  const char* wdg = ws + WS_WP + (size_t)(NFR_WO + NFR_GU) * 1024 + (size_t)cb3 * 245760;
  u32x2 hp[5];
  if (w < 4) {
    const int lane = fresh_lane();
    const unsigned voff = lane * 16;
    const int row = rb3 * 64 + w * 16 + (lane & 15), col0 = cb3 * 80 + 4 * (lane >> 4);
    STAMP_AT(2, 0);
    u32x2 xr[5];
#pragma unroll
    for (int ct = 0; ct < 5; ++ct) xr[ct] = *(const u32x2*)(x + (size_t)row * 640 + col0 + 16 * ct);
    { const int hd = lane & 31, cgt = (hd & 1) ? hd * 5 : hd * 5 + 4;
      probe(ws + WS_O + (size_t)(rb3 * 4 + w) * 20480, (unsigned)((cgt >> 3) * 1024 + ((cgt & 7) >> 1) * 256 + (cgt & 1) * 8)); }
    sfor<5>([&](auto ct) { pin(xr[ct]); });
    STAMP_AT(2, 1);
    f32x4 acc[5] = {};
    sfor<5>([&](auto ct) { pin(acc[ct]); });
    gemm_res<20, 5, 20, 2, 4, 1, 0, wo_off, x_none>(ws + WS_O + (size_t)(rb3 * 4 + w) * 20480, lds0, voff,
      [&](auto i) { if constexpr (i == -1) STAMP_AT(2, 2); if constexpr (i == 1) STAMP_AT(2, 3); if constexpr (i == 10) STAMP_AT(2, 4);
        if constexpr (i == P3_G1 || i == 12) bar(); },
      [&](auto ct, const bf16x8& b, bf16x8 (&a)[1]) { mfma16(acc[ct], b, a[0]); });
    acc_fence(); sfor<5>([&](auto ct) { pin(acc[ct]); });
    STAMP_AT(2, 6);
    float s1 = 0.f, s2 = 0.f;
    const char* hb = ws + WS_H + (size_t)(rb3 * 4 + w) * 20480;
    const unsigned vt = t16lane(lane);
#pragma unroll
    for (int ct = 0; ct < 5; ++ct) {
      hp[ct] = nofill(pk_bf16(bf_lo(xr[ct][0]) + acc[ct][0], bf_hi(xr[ct][0]) + acc[ct][1]), pk_bf16(bf_lo(xr[ct][1]) + acc[ct][2], bf_hi(xr[ct][1]) + acc[ct][3]));
      st8s(hb + t16off(cb3 * 80 + ct * 16), vt, hp[ct]);
      s1 = dot2(hp[ct][0], 0x3f803f80u, s1); s1 = dot2(hp[ct][1], 0x3f803f80u, s1);
      s2 = dot2(hp[ct][0], hp[ct][0], s2); s2 = dot2(hp[ct][1], hp[ct][1], s2);
    }
    s1 = red4(s1); s2 = red4(s2);
    if (lane < 16) st8s(ws + WS_HST + (size_t)(rb3 * 4 + w) * 1024 + (cb3 >> 1) * 256 + (cb3 & 1) * 8, (unsigned)(lane * 16), nofill(__float_as_uint(s1), __float_as_uint(s2)));
    bar();
    STAMP_AT(2, 7);
  } else {
    const int lane = fresh_lane(), v = w - 4;
    const unsigned voff = lane * 16;
    float cpart[10];
    const int cc = v * 24 + min(lane & 31, 23);
    sfor<10>([&](auto i) { gld4<i * 384>(cpart[i], ws + WS_CSP, (unsigned)(cb4 * 7680 + (lane >> 5) * 3840 + cc * 4)); });
    poll(ctr + CTL_FD, (unsigned)((cb3 * 240 + v * 60 + min(lane, 59)) * 8), tag);
    float cv = 0.f;
    sfor<10>([&](auto j) { pin(cpart[j]); cv += cpart[j]; });
    cv = add32(cv);
    bar();
    STAMP_AT(2, 2);
    sfor<5>([&](auto j) { glds16<true>(gug + (v * 5 + j) * 1024, voff, lds0 + gu_off(0) + (v * 5 + j) * 1024); });
    sfor<10>([&](auto j) { glds16<true>(gug + (20 + v * 10 + j) * 1024, voff, lds0 + gu_off(20) + (v * 10 + j) * 1024); });
    bar();
    sfor<15>([&](auto j) { glds16<true>(gug + (60 + v * 15 + j) * 1024, voff, lds0 + gu_off(60) + (v * 15 + j) * 1024); });
    STAMP_AT(2, 5);
    waitv<0>();
    bar();
    STAMP_AT(2, 6);
    if (lane < 24) ((LAS float*)(uintptr_t)(lds0 + CSF_OFF))[cc] = cv;
  }
  {
    const int lane = fresh_lane();
    const unsigned voff = lane * 16;
    const int r16 = lane & 15, g4 = lane >> 4;
    STAMP_AT(3, 0);
    f32x4 cs[6]; f32x2 ms[2];
    f32x4 hs[2];
#pragma unroll 1
    for (int spin = 0; spin < (1 << 17); ++spin) {
      gld16<0>(hs[0], ws + WS_HST + (size_t)(rb4 * 16 + w * 2) * 1024, voff);
      gld16<1024>(hs[1], ws + WS_HST + (size_t)(rb4 * 16 + w * 2) * 1024, voff);
      waitv<0>(); pin(hs[0]); pin(hs[1]);
      if (!__any(max(umax4(__builtin_bit_cast(u32x4, hs[0])), umax4(__builtin_bit_cast(u32x4, hs[1]))) == 0xffffffffu)) break;
      __builtin_amdgcn_s_sleep(1);
    }
    STAMP_AT(3, 1);
    f32x4 acc[2][6] = {};
    sfor<6>([&](auto ct) { pin(acc[0][ct]); pin(acc[1][ct]); });
    if (w < 4) __builtin_amdgcn_s_setprio(1);
    gemm_res<20, 6, K4_LA_, 1, K4_CG_, 2, 20480, gu_off, x_p4>(ws + WS_H + (size_t)(rb4 * 16 + w * 2) * 20480, lds0, voff,
      [&](auto i) { if constexpr (i == -1) STAMP_AT(3, 2); if constexpr (i == 1) STAMP_AT(3, 3); if constexpr (i == 10) STAMP_AT(3, 4);
        if constexpr (i == 9) bar();
        if constexpr (i == -1) sfor<2>([&](auto t) {
          const float s1 = red4(hs[t][0] + hs[t][2]), s2 = red4(hs[t][1] + hs[t][3]), mean = s1 * (1.f / 640.f);
          ms[t] = (f32x2){mean, __builtin_amdgcn_rsqf(fmaxf(s2 * (1.f / 640.f) - mean * mean, 0.f) + LN_EPS)}; pin(ms[t]); });
        if constexpr (i == 18) sfor<6>([&](auto ct) { cs[ct] = *(const LAS f32x4*)(uintptr_t)(lds0 + CSF_OFF + (ct * 16 + 4 * g4) * 4); });
        if constexpr (i >= 2 && i < 7) { const int f = (i - 2) * 8 + w - ((i == 6 && w >= 4) ? 4 : 0); glds16<true>(wdg + (f) * 1024, voff, lds0 + wd_off(0) + f * 1024); }
        if constexpr (i >= 10 && i < 15) { constexpr int n = i - 10; glds16<true>(wdg + (40 + n * 8 + w) * 1024, voff, lds0 + wd_off(40) + (n * 8 + w) * 1024); }
        if constexpr (i >= 15 && i < 18) { const int f = (i - 15) * 8 + w - ((i == 17 && w >= 4) ? 4 : 0); glds16<true>(wdg + (140 + f) * 1024, voff, lds0 + wd_off(140) + f * 1024); } },
      [&](auto ct, const bf16x8& b, bf16x8 (&a)[2]) { mfma16(acc[0][ct], b, a[0]); mfma16(acc[1][ct], b, a[1]); });
    acc_fence(); sfor<6>([&](auto ct) { pin(acc[0][ct]); pin(acc[1][ct]); });
    STAMP_AT(3, 6);
    constexpr float GC0 = -2.f * 0.7978845608028654f * 1.4426950408889634f, GC1 = GC0 * 0.044715f;
    const char* gb = ws + WS_G + (size_t)(rb4 * 16 + w * 2) * 49152;
    const unsigned vt = t16lane(lane);
#pragma unroll
    for (int t = 0; t < 2; ++t) {
      const float nm = -ms[t].x, rstd = ms[t].y;
#pragma unroll
      for (int ct = 0; ct < 3; ++ct) {
        const f32x4 gv = (acc[t][ct] + cs[ct] * nm) * rstd, uv = (acc[t][3 + ct] + cs[3 + ct] * nm) * rstd;
        const f32x4 z = gv * (gv * gv * GC1 + GC0);
        f32x4 e;
#pragma unroll
        for (int j = 0; j < 4; ++j) e[j] = __builtin_amdgcn_exp2f(z[j]);
        e = e + 1.f;
#pragma unroll
        for (int j = 0; j < 4; ++j) e[j] = __builtin_amdgcn_rcpf(e[j]);
        const f32x4 r = gv * uv * e;
        st8s(gb + t * 49152 + t16off(cb4 * 48 + ct * 16), vt, nofill(pk_bf16(r[0], r[1]), pk_bf16(r[2], r[3])));
      }
    }
    __builtin_amdgcn_s_setprio(0);
    STAMP_AT(3, 7);
    waitv<6>();
    STAMP_AT(3, 8);
    bar();
  }
  if (w < 4) {
    const int lane = fresh_lane();
    const unsigned voff = lane * 16;
    const int row = rb3 * 64 + w * 16 + (lane & 15), col0 = cb3 * 80 + 4 * (lane >> 4);
    STAMP_AT(4, 0);
    probe(ws + WS_G + (size_t)(rb3 * 4 + w) * 49152, (unsigned)t16off((lane & 31) * 48 + 32));
    STAMP_AT(4, 1);
    f32x4 acc[5] = {};
    sfor<5>([&](auto ct) { pin(acc[ct]); });
    gemm_res<48, 5, K5_LA_, 2, 4, 1, 0, wd_off, x_none>(ws + WS_G + (size_t)(rb3 * 4 + w) * 49152, lds0, voff,
      [&](auto i) { if constexpr (i == 1) STAMP_AT(4, 3); if constexpr (i == 14) STAMP_AT(4, 4); if constexpr (i == 30) STAMP_AT(4, 5);
        if constexpr (i == -1 || i == 8 || i == 14 || i == 16 || i == 30 || i == 38) bar(); },
      [&](auto ct, const bf16x8& b, bf16x8 (&a)[1]) { mfma16(acc[ct], b, a[0]); });
    acc_fence(); sfor<5>([&](auto ct) { pin(acc[ct]); });
    STAMP_AT(4, 12);
#pragma unroll
    for (int ct = 0; ct < 5; ++ct) {
      const unsigned p0 = pk_bf16(bf_lo(hp[ct][0]) + acc[ct][0], bf_hi(hp[ct][0]) + acc[ct][1]);
      const unsigned p1 = pk_bf16(bf_lo(hp[ct][1]) + acc[ct][2], bf_hi(hp[ct][1]) + acc[ct][3]);
      *(u32x2*)(out + (size_t)row * 640 + col0 + 16 * ct) = (u32x2){p0, p1};
    }
    STAMP_AT(4, 13);
  } else {
    const int lane = fresh_lane(), v = w - 4;
    const unsigned voff = lane * 16;
    glds16<true>(wdg + (36 + v) * 1024, voff, lds0 + wd_off(36) + v * 1024);
    waitv<0>(); bar();
    sfor<3>([&](auto g) { sfor<5>([&](auto j) { constexpr int f = (g + 4) * 20 + j; glds16<true>(wdg + (f + v * 5) * 1024, voff, lds0 + wd_off(f) + v * 5120); }); });
    bar();
    sfor<2>([&](auto g) { sfor<5>([&](auto j) { constexpr int f = (g + 8) * 20 + j; glds16<true>(wdg + (f + v * 5) * 1024, voff, lds0 + wd_off(f) + v * 5120); }); });
    waitv<10>(); bar();
    bar();
    sfor<2>([&](auto g) { sfor<5>([&](auto j) { constexpr int f = (g + 10) * 20 + j; glds16<true>(wdg + (f + v * 5) * 1024, voff, lds0 + wd_off(f) + v * 5120); }); });
    waitv<10>(); bar();
    waitv<0>(); bar();
    if (blockIdx.x == 0 && w == 4) {
      poll2(ctr + CTL_FR, (unsigned)(lane * 64), (unsigned)(4096 + lane * 64), tag);
      poll2(ctr + CTL_FR, (unsigned)(8192 + lane * 64), (unsigned)(12288 + lane * 64), tag);
      if (lane == 0) st8(ctl, tag);
    }
  }
}

extern "C" void kernel_launch(void* const* d_in, const int* in_sizes, int n_in, void* d_out, int out_size, void* d_ws, size_t ws_size,
                              hipStream_t stream) {
  k_mega<<<256, 512, 0, stream>>>((const u16*)d_in[0], (const u16*)d_in[1], (const u16*)d_in[2], (const u16*)d_in[4], (const u16*)d_in[5],
                                  (const u16*)d_in[6], (char*)d_ws, (u16*)d_out);
}
```

```cpp
#include <hip/hip_runtime.h>
#include <hip/hip_bf16.h>
#include <stdint.h>
#include <utility>

typedef short bf16x8 __attribute__((ext_vector_type(8)));
typedef short s16x4 __attribute__((ext_vector_type(4)));
typedef float f32x16 __attribute__((ext_vector_type(16)));
typedef float f32x4 __attribute__((ext_vector_type(4)));
typedef float f32x2 __attribute__((ext_vector_type(2)));
typedef unsigned u32x4 __attribute__((ext_vector_type(4)));
typedef unsigned u32x2 __attribute__((ext_vector_type(2)));
typedef __bf16 bf2_t __attribute__((ext_vector_type(2)));
typedef unsigned short u16;
#define LAS __attribute__((address_space(3)))
#define DI __device__ __forceinline__

constexpr float LN_EPS = 1e-5f;
constexpr float QSCALE = 0.22360679774997896f * 1.4426950408889634f;

constexpr size_t WS_Q   = 0;
constexpr size_t WS_KV  = WS_Q + 2621440;
constexpr size_t WS_O   = WS_KV + 5242880;
constexpr size_t WS_H   = WS_O + 2621440;
constexpr size_t WS_HST = WS_H + 2621440;
constexpr size_t WS_G   = WS_HST + 131072;
constexpr size_t WS_WP  = WS_G + 6291456;
constexpr size_t WS_CSP = WS_WP + 6717440;
constexpr size_t WS_RST = WS_CSP + 245760;
constexpr size_t WS_CTL = WS_RST + 32768;
constexpr size_t CTL_F1 = 256;
constexpr size_t CTL_F2 = CTL_F1 + 2048 * 8;
constexpr size_t CTL_FR = CTL_F2 + 2048 * 8;
constexpr size_t CTL_F3 = CTL_FR + 2048 * 8;
constexpr size_t CTL_F4 = CTL_F3 + 1024 * 8;
constexpr size_t CTL_FD = CTL_F4 + 2048 * 8;
constexpr size_t CTL_REP = CTL_FD + 2048 * 8 - 256;
constexpr int NFR_WO = 800, NFR_GU = 3840, NFR_WD = 1920;

DI unsigned pk_bf16(float lo, float hi) { f32x2 f = {lo, hi}; return __builtin_bit_cast(unsigned, __builtin_convertvector(f, bf2_t)); }
DI float bf_lo(unsigned u) { return __uint_as_float(u << 16); }
DI float bf_hi(unsigned u) { return __uint_as_float(u & 0xffff0000u); }
DI float dot2(unsigned a, unsigned b, float c) { return __builtin_amdgcn_fdot2_f32_bf16(__builtin_bit_cast(bf2_t, a), __builtin_bit_cast(bf2_t, b), c, false); }
DI float add32(float v) { const auto r = __builtin_amdgcn_permlane32_swap(__float_as_uint(v), __float_as_uint(v), false, false); return __uint_as_float(r[0]) + __uint_as_float(r[1]); }
DI float max32(float v) { const auto r = __builtin_amdgcn_permlane32_swap(__float_as_uint(v), __float_as_uint(v), false, false); return fmaxf(__uint_as_float(r[0]), __uint_as_float(r[1])); }
DI float add16(float v) { const auto r = __builtin_amdgcn_permlane16_swap(__float_as_uint(v), __float_as_uint(v), false, false); return __uint_as_float(r[0]) + __uint_as_float(r[1]); }
DI float red4(float v) { return add32(add16(v)); }
DI float hi32(float v) { const auto r = __builtin_amdgcn_permlane32_swap(__float_as_uint(v), __float_as_uint(v), false, false); return __uint_as_float(r[1]); }
#define MFMA32(a, b, c) __builtin_amdgcn_mfma_f32_32x32x16_bf16((a), (b), (c), 0, 0, 0)
#define MFMA32K8(a, b, c) __builtin_amdgcn_mfma_f32_32x32x8bf16_1k((a), (b), (c), 0, 0, 0)
#define MFMA16(a, b, c) __builtin_amdgcn_mfma_f32_16x16x32_bf16((a), (b), (c), 0, 0, 0)

DI void mfma16(f32x4& c, const bf16x8& a, const bf16x8& b) { asm volatile("v_mfma_f32_16x16x32_bf16 %0, %1, %2, %0" : "+v"(c) : "v"(a), "v"(b)); }
DI void acc_fence() { asm volatile("s_nop 7\n\ts_nop 3" ::: "memory"); }
template <bool DEV> DI void glds16(const void* sbase, unsigned voff, unsigned lds_base) {
  if constexpr (DEV) asm volatile("s_mov_b32 m0, %2\n\ts_nop 0\n\tglobal_load_lds_dwordx4 %0, %1 sc1" :: "v"(voff), "s"(sbase), "s"(lds_base) : "memory");
  else asm volatile("s_mov_b32 m0, %2\n\ts_nop 0\n\tglobal_load_lds_dwordx4 %0, %1" :: "v"(voff), "s"(sbase), "s"(lds_base) : "memory");
}
template <int OFF, class T> DI void gld16(T& dst, const void* sbase, unsigned voff) {
  asm volatile("global_load_dwordx4 %0, %1, %2 offset:%3 sc1" : "=v"(dst) : "v"(voff), "s"(sbase), "i"(OFF) : "memory");
}
template <int OFF, class T> DI void gld8(T& dst, const void* sbase, unsigned voff) {
  asm volatile("global_load_dwordx2 %0, %1, %2 offset:%3 sc1" : "=v"(dst) : "v"(voff), "s"(sbase), "i"(OFF) : "memory");
}
template <int OFF> DI void gld4(float& dst, const void* sbase, unsigned voff) {
  asm volatile("global_load_dword %0, %1, %2 offset:%3 sc1" : "=v"(dst) : "v"(voff), "s"(sbase), "i"(OFF) : "memory");
}
template <int OFF> DI void gldu16(unsigned& dst, const void* sbase, unsigned voff) {
  asm volatile("global_load_ushort %0, %1, %2 offset:%3" : "=v"(dst) : "v"(voff), "s"(sbase), "i"(OFF) : "memory");
}
DI void st16(void* p, u32x4 v) { asm volatile("global_store_dwordx4 %0, %1, off sc1\n\ts_nop 1" :: "v"(p), "v"(v) : "memory"); }
DI void st8(void* p, u32x2 v) { asm volatile("global_store_dwordx2 %0, %1, off sc1" :: "v"(p), "v"(v) : "memory"); }
DI void st4(void* p, float v) { asm volatile("global_store_dword %0, %1, off sc1" :: "v"(p), "v"(v) : "memory"); }
DI void st8s(const void* sbase, unsigned voff, u32x2 v) { asm volatile("global_store_dwordx2 %0, %1, %2 sc1" :: "v"(voff), "v"(v), "s"(sbase) : "memory"); }
DI u32x2 nofill(unsigned a, unsigned b) { return (u32x2){min(a, 0xfffffffeu), min(b, 0xfffffffeu)}; }
DI unsigned umax4(u32x4 v) { return max(max(v[0], v[1]), max(v[2], v[3])); }
template <int N> DI void waitv() { asm volatile("s_waitcnt vmcnt(%0)" :: "i"(N) : "memory"); }
DI void bar() { asm volatile("s_waitcnt lgkmcnt(0)\n\ts_barrier" ::: "memory"); }
DI void setflag(char* flag, u32x2 tag, int lane) { if (lane < 8) st8(flag + lane * CTL_REP, tag); }
DI void publish(char* flag, u32x2 tag, int lane) { waitv<0>(); setflag(flag, tag, lane); }
DI void poll(const char* base, unsigned voff, u32x2 tag) {
#pragma unroll 1
  for (int spin = 0; spin < (1 << 17); ++spin) {
    u32x2 v;
    asm volatile("global_load_dwordx2 %0, %1, %2 sc1\n\ts_waitcnt vmcnt(0)" : "=v"(v) : "v"(voff), "s"(base) : "memory");
    if (__all(v[0] == tag[0] && v[1] == tag[1])) break;
    __builtin_amdgcn_s_sleep(1);
  }
}
DI void poll2(const char* base, unsigned voff0, unsigned voff1, u32x2 tag) {
#pragma unroll 1
  for (int spin = 0; spin < (1 << 17); ++spin) {
    u32x2 v, u;
    asm volatile("global_load_dwordx2 %0, %2, %4 sc1\n\tglobal_load_dwordx2 %1, %3, %4 sc1\n\ts_waitcnt vmcnt(0)" : "=&v"(v), "=&v"(u) : "v"(voff0), "v"(voff1), "s"(base) : "memory");
    if (__all(v[0] == tag[0] && v[1] == tag[1] && u[0] == tag[0] && u[1] == tag[1])) break;
    __builtin_amdgcn_s_sleep(1);
  }
}
DI void probe(const char* base, unsigned voff) {
#pragma unroll 1
  for (int spin = 0; spin < (1 << 17); ++spin) {
    unsigned v;
    asm volatile("global_load_dword %0, %1, %2 sc1\n\ts_waitcnt vmcnt(0)" : "=v"(v) : "v"(voff), "s"(base) : "memory");
    if (__all(v != 0xffffffffu)) break;
    __builtin_amdgcn_s_sleep(1);
  }
}
constexpr int t16off(int c0) { return (c0 >> 5) * 1024 + (c0 & 16) * 32; }
DI unsigned t16lane(int lane) { return (unsigned)((lane >> 5) * 256 + (lane & 15) * 16 + ((lane >> 4) & 1) * 8); }
DI void st16s(const void* sbase, unsigned voff, u32x4 v) { asm volatile("global_store_dwordx4 %0, %1, %2 sc1\n\ts_nop 1" :: "v"(voff), "v"(v), "s"(sbase) : "memory"); }
DI float row16_sum(float v) {
  v += __builtin_bit_cast(float, __builtin_amdgcn_update_dpp(0, __builtin_bit_cast(int, v), 0x128, 0xf, 0xf, false));
  v += __builtin_bit_cast(float, __builtin_amdgcn_update_dpp(0, __builtin_bit_cast(int, v), 0x124, 0xf, 0xf, false));
  v += __builtin_bit_cast(float, __builtin_amdgcn_update_dpp(0, __builtin_bit_cast(int, v), 0x122, 0xf, 0xf, false));
  v += __builtin_bit_cast(float, __builtin_amdgcn_update_dpp(0, __builtin_bit_cast(int, v), 0x121, 0xf, 0xf, false));
  return v;
}
DI u32x2 make_tag(u32x2 ep) {
  const unsigned long long e = (((unsigned long long)__builtin_amdgcn_readfirstlane(ep[1]) << 32) | __builtin_amdgcn_readfirstlane(ep[0])) + 1ull;
  return (u32x2){(unsigned)e, (unsigned)(e >> 32)};
}
constexpr unsigned RS_K0 = 0x9e3779b9u, RS_K1 = 0x7f4a7c15u;
struct RowStat { u32x4 a; unsigned b; };
DI void rowstat_get(RowStat& r, const u16* x, int row, int lane) {
  asm volatile("global_load_dwordx4 %0, %2, %4\n\tglobal_load_dword %1, %3, %4 offset:1024" : "=&v"(r.a), "=&v"(r.b) : "v"(lane * 16), "v"(lane * 4), "s"((const char*)x + (size_t)row * 1280) : "memory");
}
DI void rowstat_put(RowStat& r, char* ws, int row, int lane, u32x2 tag) {
  asm volatile("" : "+v"(r.a), "+v"(r.b));
  float s1 = dot2(r.b, 0x3f803f80u, 0.f), s2 = dot2(r.b, r.b, 0.f);
#pragma unroll
  for (int j = 0; j < 4; ++j) { s1 = dot2(r.a[j], 0x3f803f80u, s1); s2 = dot2(r.a[j], r.a[j], s2); }
  s1 = red4(row16_sum(s1)); s2 = red4(row16_sum(s2));
  const float mean = s1 * (1.f / 640.f), rstd = __builtin_amdgcn_rsqf(fmaxf(s2 * (1.f / 640.f) - mean * mean, 0.f) + LN_EPS);
  const unsigned m = __float_as_uint(mean), q = __float_as_uint(rstd);
  if (lane == 0) st16(ws + WS_RST + (size_t)row * 16, (u32x4){m, q, tag[0] ^ m ^ RS_K0, tag[1] ^ q ^ RS_K1});
}
DI int fresh_lane() { int l = __builtin_amdgcn_mbcnt_hi(~0u, __builtin_amdgcn_mbcnt_lo(~0u, 0u)); asm volatile("" : "+v"(l)); return l; }
template <class T> DI void pins(T& v) { asm volatile("" : "+s"(v)); }
template <class T> DI void pin(T& v) { asm volatile("" : "+v"(v)); }
DI bf16x8 lds128(unsigned addr) { return *(const LAS bf16x8*)(uintptr_t)addr; }
DI s16x4 lds64(unsigned addr) { return *(const LAS s16x4*)(uintptr_t)addr; }
DI s16x4 ldstr(unsigned addr) { return __builtin_amdgcn_ds_read_tr16_b64_v4i16((LAS s16x4*)(uintptr_t)addr); }
DI unsigned lds_addr_of(const void* p) { return (unsigned)(uintptr_t)(const LAS char*)p; }
#define STAMP_AT(kid, idx) ((void)0)
#define RETRY_COUNT(k) ((void)0)
#define WAITV_BAR(N) asm volatile("s_waitcnt vmcnt(" #N ") lgkmcnt(0)\n\ts_barrier" ::: "memory")
#define SB() __builtin_amdgcn_sched_barrier(0)
template <int N> using IC = std::integral_constant<int, N>;
template <class F, int... Is> DI void sfor_(F&& f, std::integer_sequence<int, Is...>) { (f(IC<Is>{}), ...); }
template <int N, class F> DI void sfor(F&& f) { sfor_(f, std::make_integer_sequence<int, N>{}); }

constexpr int P3_G1 = -1, P3_LA = 12, K4_LA_ = 6, K4_CG_ = 2, K5_LA_ = 16;
constexpr int G3 = 19, G4 = 1, G5 = 10;
constexpr int K1_STAGE = 32768;
DI void p1_load(const u16* __restrict__ x, const u16* __restrict__ W, const unsigned lds0, const int v, int rb, int cb) {
  const int lane = fresh_lane();
  const unsigned a_voff0 = (unsigned)((lane >> 3) * 1280 + (((lane & 7) ^ ((lane >> 4) & 7)) << 4));
  const unsigned a_voff1 = (unsigned)((lane >> 3) * 1280 + (((lane & 7) ^ ((4 + (lane >> 4)) & 7)) << 4)) + 8 * 1280;
  const unsigned w_voff0 = (unsigned)((lane >> 3) * 3840 + (((lane & 7) ^ (((lane >> 4) & 1) << 2)) << 4));
  const unsigned w_voff1 = w_voff0 + 8 * 3840;
  const char* xs = (const char*)x + (size_t)(rb * 128 + v * 32) * 1280;
  const char* wsrc = (const char*)W + (size_t)((v & 1) * 32) * 3840 + (size_t)(cb * 128 + (v >> 1) * 64) * 2;
  const int rot = ((((blockIdx.x >> 1) & 3) * 5 + (blockIdx.x & 1) * 10) >> 1) % 10;
  auto st = [&](auto s_) {
    constexpr int s0 = s_; const unsigned d = lds0 + (s0 % 5) * K1_STAGE + v * 4096;
    const int s = s0 + rot - (s0 + rot >= 10 ? 10 : 0);
    const char* xp = xs + s * 128; const char* wp = wsrc + (size_t)s * 64 * 3840;
    glds16<false>(xp, a_voff0, d); glds16<false>(wp, w_voff0, d + 16384);
    glds16<false>(xp, a_voff1, d + 1024); glds16<false>(wp, w_voff1, d + 16384 + 1024);
    glds16<false>(xp + 16 * 1280, a_voff0, d + 2048); glds16<false>(wp + 16 * 3840, w_voff0, d + 16384 + 2048);
    glds16<false>(xp + 16 * 1280, a_voff1, d + 3072); glds16<false>(wp + 16 * 3840, w_voff1, d + 16384 + 3072);
  };
  st(IC<0>{}); st(IC<1>{});
  sfor<10>([&](auto s_) { constexpr int s = s_;
    waitv<(s == 0 ? 8 : s <= 7 ? 16 : s == 8 ? 8 : 0)>(); asm volatile("s_barrier" ::: "memory");
    if constexpr (s == 0) { st(IC<2>{}); st(IC<3>{}); }
    else if constexpr (s + 3 < 10) st(IC<s + 3>{}); });
  bar();
}
template <bool ISV>
DI void p1_body(const u16* __restrict__ x, char* __restrict__ ws, const unsigned lds0, const int w, int rb, int cb, u32x2& tag) {
  const int lane = fresh_lane();
  u32x2 ep; gld8<0>(ep, ws + WS_CTL, 0u);
  RowStat rs[2];
  rowstat_get(rs[0], x, blockIdx.x * 8 + w * 2, lane); rowstat_get(rs[1], x, blockIdx.x * 8 + w * 2 + 1, lane);
  const int wm = w >> 1, wn = w & 1;
  const int r32 = lane & 31, hh = lane >> 5;
  STAMP_AT(0, 0);
  unsigned xa[3][4], wa[3][2][2];
  {
    const int row = wm * 64 + r32;
    const int blk = (lane >> 4) & 1, q = (lane & 15) >> 2, p = lane & 3;
#pragma unroll
    for (int hs = 0; hs < 3; ++hs) {
#pragma unroll
      for (int i = 0; i < 4; ++i) { xa[hs][i] = lds0 + hs * 65536 + (unsigned)(row * 128 + ((((i ^ wm) * 2 + hh) ^ ((row >> 1) & 7)) << 4)); pin(xa[hs][i]); }
#pragma unroll
      for (int j = 0; j < 2; ++j) {
        const unsigned base = lds0 + hs * 65536 + (unsigned)(16384 + wn * 8192 + (8 * hh + q) * 128 + (((j * 4 + blk * 2 + (p >> 1)) ^ (((q >> 1) & 1) << 2)) << 4) + (p & 1) * 8);
        wa[hs][j][0] = base + wm * 2048; wa[hs][j][1] = base + (1 ^ wm) * 2048; pin(wa[hs][j][0]); pin(wa[hs][j][1]);
      }
    }
  }
  f32x16 acc[2][2] = {};
  f32x4 sw[2] = {};
  u32x4 rec;
  bf16x8 sel = (bf16x8)(short)(((lane & 15) == ((lane >> 4) & 1)) ? 0x3f80 : 0); pin(sel);
  bf16x8 fx[2][2]; s16x4 fwl[2][2], fwh[2][2];
  auto rd = [&](auto i_, auto k_) {
    constexpr int i = i_, k = k_, s = i >> 2, p = i & 3, sl = s % 5, hs = sl >> 1; constexpr unsigned so = (sl & 1) * K1_STAGE;
    if constexpr (k == 0) fx[i & 1][0] = lds128(xa[hs][p] + so);
    if constexpr (k == 3) fx[i & 1][1] = lds128(xa[hs][p] + so + 4096);
    if constexpr (k == 1 || k == 2) { fwl[i & 1][k - 1] = ldstr(wa[hs][k - 1][p & 1] + so + (p >> 1) * 4096); fwh[i & 1][k - 1] = ldstr(wa[hs][k - 1][p & 1] + so + (p >> 1) * 4096 + 512); }
  };
  const bool isq = cb < 5;
  const int pitch = isq ? 1280 : 2560;
  unsigned off[2][ISV ? 1 : 4];
#pragma unroll
  for (int j = 0; j < 2; ++j) {
    if constexpr (ISV) {
      const int n = cb * 128 + wn * 64 + j * 32 + r32 - 1280, head = n / 20, d0 = n - head * 20;
      const int d = (head & 1) ? (d0 < 4 ? d0 + 16 : d0 - 4) : d0;
      off[j][0] = (unsigned)(head * 163840 + 1280 + (hh * 20 + d) * 16); pin(off[j][0]);
    } else {
#pragma unroll
      for (int g = 0; g < 4; ++g) {
        const int n = cb * 128 + wn * 64 + j * 32 + 8 * g + 4 * hh - (isq ? 0 : 640), head = n / 20, d0 = n - head * 20;
        const int d = (head & 1) ? (d0 < 4 ? d0 + 16 : d0 - 4) : d0;
        off[j][g] = (unsigned)(head * 64 * pitch + (d < 16 ? (((d >> 3) * 32 + r32) * 16 + (d & 4) * 2) : (1024 + r32 * 8))); pin(off[j][g]);
      }
    }
  }
  waitv<0>(); pin(ep); tag = make_tag(ep);
  rowstat_put(rs[0], ws, blockIdx.x * 8 + w * 2, lane, tag); rowstat_put(rs[1], ws, blockIdx.x * 8 + w * 2 + 1, lane, tag);
  asm volatile("s_barrier" ::: "memory");
  STAMP_AT(0, 1);
  sfor<4>([&](auto k) { rd(IC<0>{}, k); });
  sfor<40>([&](auto i_) {
    constexpr int i = i_, b = i & 1;
    if constexpr ((i & 3) == 3 && i < 39) asm volatile("s_barrier" ::: "memory");
    if constexpr (i == 30) gld16<0>(rec, ws + WS_RST + (size_t)(rb * 128 + wm * 64) * 16, (unsigned)(lane * 16));
    SB();
    const bf16x8 w0 = __builtin_shufflevector(fwl[b][0], fwh[b][0], 0, 1, 2, 3, 4, 5, 6, 7), w1 = __builtin_shufflevector(fwl[b][1], fwh[b][1], 0, 1, 2, 3, 4, 5, 6, 7);
    sfor<4>([&](auto k_) { constexpr int k = k_, t = k >> 1, j = (k ^ (k >> 1)) & 1;
      if constexpr (ISV) acc[t][j] = MFMA32(fx[b][t], j ? w1 : w0, acc[t][j]); else acc[t][j] = MFMA32(j ? w1 : w0, fx[b][t], acc[t][j]);
      SB();
      if constexpr (i < 39) rd(IC<i + 1>{}, k_);
      SB(); });
    if constexpr (b == 1) { sw[0] = MFMA16(w0, sel, sw[0]); sw[1] = MFMA16(w1, sel, sw[1]); }
    SB();
  });
  STAMP_AT(0, 11);
  LAS float* csp = (LAS float*)(uintptr_t)lds0;
  if ((lane & 15) < 2) sfor<2>([&](auto j) { *(LAS f32x4*)&csp[wm * 128 + wn * 64 + j * 32 + (lane & 1) * 16 + (lane >> 4) * 4] = sw[j]; });
  waitv<0>(); pin(rec);
#pragma unroll 1
  for (int spin = 0; spin < (1 << 17) && !__all((rec[2] ^ rec[0] ^ RS_K0) == tag[0] && (rec[3] ^ rec[1] ^ RS_K1) == tag[1]); ++spin) {
    gld16<0>(rec, ws + WS_RST + (size_t)(rb * 128 + wm * 64) * 16, (unsigned)(lane * 16)); waitv<0>(); pin(rec);
  }
  LAS f32x2* st = (LAS f32x2*)(uintptr_t)(lds0 + 4096 + w * 512);
  if constexpr (ISV) st[lane] = (f32x2){__uint_as_float(rec[0]), __uint_as_float(rec[1])};
  bar();
  STAMP_AT(0, 12);
  if constexpr (ISV) {
    float c[2];
#pragma unroll
    for (int j = 0; j < 2; ++j) c[j] = csp[wn * 64 + j * 32 + r32] + csp[128 + wn * 64 + j * 32 + r32];
#pragma unroll
    for (int t = 0; t < 2; ++t) {
      f32x4 s01[4], s23[4];
#pragma unroll
      for (int g = 0; g < 4; ++g) { s01[g] = *(const LAS f32x4*)&st[t * 32 + 8 * g + 4 * hh]; s23[g] = *(const LAS f32x4*)&st[t * 32 + 8 * g + 4 * hh + 2]; }
      const char* dst = ws + WS_KV + (size_t)(rb * 4 + wm * 2 + t) * 2560;
#pragma unroll
      for (int j = 0; j < 2; ++j) {
        const f32x16& a = acc[t][j];
        unsigned pk[8];
#pragma unroll
        for (int g = 0; g < 4; ++g) {
          pk[2 * g]     = pk_bf16(s01[g][1] * (a[4 * g] - s01[g][0] * c[j]), s01[g][3] * (a[4 * g + 1] - s01[g][2] * c[j]));
          pk[2 * g + 1] = pk_bf16(s23[g][1] * (a[4 * g + 2] - s23[g][0] * c[j]), s23[g][3] * (a[4 * g + 3] - s23[g][2] * c[j]));
        }
        st16s(dst, off[j][0], (u32x4){pk[0], pk[1], pk[2], pk[3]});
        st16s(dst + 640, off[j][0], (u32x4){pk[4], pk[5], pk[6], pk[7]});
      }
    }
  } else {
    const float sc = isq ? QSCALE : 1.f;
    const char* base = ws + (isq ? WS_Q : WS_KV) + (size_t)(rb * 4 + wm * 2) * pitch;
    const auto pm = __builtin_amdgcn_permlane32_swap(rec[0], rec[0], false, false), pr = __builtin_amdgcn_permlane32_swap(rec[1], rec[1], false, false);
#pragma unroll
    for (int j = 0; j < 2; ++j)
#pragma unroll
      for (int g = 0; g < 4; ++g) {
        const int nl = wn * 64 + j * 32 + 8 * g + 4 * hh;
        const f32x4 c = *(const LAS f32x4*)&csp[nl] + *(const LAS f32x4*)&csp[128 + nl];
#pragma unroll
        for (int t = 0; t < 2; ++t) {
          const f32x16& a = acc[t][j];
          const float rs_ = __uint_as_float(pr[t]) * sc, mr = __uint_as_float(pm[t]);
          st8s(base + t * pitch, off[j][g], (u32x2){pk_bf16(rs_ * (a[4 * g] - mr * c[0]), rs_ * (a[4 * g + 1] - mr * c[1])), pk_bf16(rs_ * (a[4 * g + 2] - mr * c[2]), rs_ * (a[4 * g + 3] - mr * c[3]))});
        }
      }
  }
  STAMP_AT(0, 13);
  waitv<0>();
  if (lane < 16) st8(ws + WS_CTL + CTL_F1 + ((cb * 4 + wn * 2 + (lane >> 3)) * 32 + rb * 2 + wm) * 8 + (lane & 7) * CTL_REP, tag);
}

DI void p1_repack(const u16* __restrict__ Wo, char* __restrict__ ws, const int w, int idx) {
  const int lane = fresh_lane();
  const unsigned pvo = (lane >> 4) * 8 * 1280 + (lane & 15) * 2;
  unsigned pw[7][8];
  sfor<7>([&](auto r) {
    const int f = idx * 50 + min(w + 8 * r, 49), cbk = f / 100, rem = f - cbk * 100, ks = rem / 5, ct = rem - ks * 5;
    const char* src = (const char*)(Wo + (size_t)(32 * ks) * 640 + 80 * cbk + 16 * ct);
    sfor<8>([&](auto i) { gldu16<(i & 3) * 1280>(pw[r][i], src + (i & ~3) * 1280, pvo); });
  });
  sfor<7>([&](auto r) {
    waitv<(6 - r) * 8>();
    sfor<8>([&](auto j) { pin(pw[r][j]); });
    const u32x4 v = {pw[r][0] | (pw[r][1] << 16), pw[r][2] | (pw[r][3] << 16), pw[r][4] | (pw[r][5] << 16), pw[r][6] | (pw[r][7] << 16)};
    pw[r][0] = v[0]; pw[r][1] = v[1]; pw[r][2] = v[2]; pw[r][3] = v[3];
  });
  sfor<7>([&](auto r) {
    if (w + 8 * r < 50) st16(ws + WS_WP + (size_t)(idx * 50 + w + 8 * r) * 1024 + lane * 16, (u32x4){pw[r][0], pw[r][1], pw[r][2], pw[r][3]});
  });
}

template <int OFF> DI void rdK(bf16x8& k0, s16x4& k1, unsigned a0, unsigned a1) {
  asm volatile("ds_read_b128 %0, %2 offset:%4\n\tds_read_b64 %1, %3 offset:%4" : "=&v"(k0), "=&v"(k1) : "v"(a0), "v"(a1), "i"(OFF));
}
template <int OFF> DI void rdV(bf16x8& v0, bf16x8& v1, unsigned a, unsigned long long msk) {
  asm volatile("s_mov_b64 exec, %5\n\tds_read_b128 %0, %2 offset:%3\n\tds_read_b128 %1, %2 offset:%4\n\ts_mov_b64 exec, -1"
               : "+v"(v0), "+v"(v1) : "v"(a), "i"(OFF), "i"(OFF + 640), "s"(msk));
}
#define EX2(v) __builtin_amdgcn_exp2f(v)
constexpr int wo_off(int p) { return (p < 60 ? p : p + 20) * 1024; }
constexpr int gu_off(int f) { return (f < 20 ? f + 60 : f < 60 ? f + 100 : f - 60) * 1024; }
constexpr int wd_off(int f) { return ((f / 20 + 4) % 8) * 20480 + (f % 20) * 1024; }
constexpr int CSF_OFF = 116 * 1024, XCH_OFF = 120 * 1024;

DI void p2_attn(char* __restrict__ ws, const u16* __restrict__ Wg, const u16* __restrict__ Wu, const u16* __restrict__ Wd,
                const unsigned lds0, const int w, const u32x2 tag, const int rb3, const int cb3, const int rb4, const int cb4) {
  const int lane = fresh_lane();
  const int kh = w >> 2, wq = w & 3;
  const int head = (blockIdx.x & 7) * 4 + ((blockIdx.x >> 3) & 3), qb = blockIdx.x >> 5;
  const int r32 = lane & 31, hh = lane >> 5;
  const char* kvg = ws + WS_KV + (size_t)head * 163840 + (size_t)kh * 81920 + wq * 1024;
  const unsigned voff = lane * 16;
  const unsigned kvl = lds0 + kh * 81920 + wq * 1024;
  char* ctl = ws + WS_CTL;
  const char* ctr = ctl + (blockIdx.x & 7) * CTL_REP;
  unsigned pw[3][8];
  const int gw = blockIdx.x * 8 + w, gwc = min(gw, 1919);
  const char* psrc[3];
#pragma unroll
  for (int r = 0; r < 2; ++r) {
    const int f = 2 * gwc + r, cbk = f / 120, rem = f - cbk * 120, ks = rem / 6, ct = rem - ks * 6;
    psrc[r] = (const char*)((ct < 3 ? Wg : Wu) + (size_t)(32 * ks) * 1536 + 48 * cbk + 16 * (ct < 3 ? ct : ct - 3));
  }
  { const int f = gwc, cbk = f / 240, rem = f - cbk * 240, ks = rem / 5, ct = rem - ks * 5;
    psrc[2] = (const char*)(Wd + (size_t)(32 * ks) * 640 + 80 * cbk + 16 * ct); }
  unsigned pvo1 = (lane >> 4) * 8 * 3072 + (lane & 15) * 2, pvo2 = (lane >> 4) * 8 * 1280 + (lane & 15) * 2; pin(pvo1); pin(pvo2);
  const char* wog = ws + WS_WP + (size_t)cb3 * 102400;
  const int qtile = qb * 8 + wq * 2 + kh, cg0 = head * 5 + (head & 1), cgt = (head & 1) ? head * 5 : head * 5 + 4;
  const char* ob = ws + WS_O + (size_t)qtile * 40960;
  const char* obs[3] = {ob + (cg0 >> 3) * 1024 + ((cg0 & 7) >> 1) * 256, ob + ((cg0 + 2) >> 3) * 1024 + (((cg0 + 2) & 7) >> 1) * 256,
                        ob + (cgt >> 3) * 1024 + ((cgt & 7) >> 1) * 256 + (cgt & 1) * 8};
  unsigned vo = (unsigned)((r32 >> 4) * 20480 + (r32 & 15) * 16 + hh * 8); pin(vo);
  u32x4 fill = {0xffffffffu, 0xffffffffu, 0xffffffffu, 0xffffffffu}; pin(fill);
  const u32x2 fill2 = {fill[0], fill[1]};
  u32x2 fa, fb, fc;
  const unsigned fvc = (unsigned)CTL_FR + (unsigned)((cb4 * 60 + min(lane, 59)) * 8);
  const unsigned fva = (unsigned)(hh ? CTL_F4 : CTL_F2) + (unsigned)(((rb3 * 2 + ((w & 3) >> 1)) * 32 + r32) * 8), fvb = (unsigned)CTL_F3 + (unsigned)(((rb4 * 16 + w * 2) * 8 + (lane & 15)) * 8);
  const char* gcl = ws + WS_G + (size_t)(rb4 * 16 + w * 2) * 49152 + cb4 * 1536;
  const char* hcl = ws + WS_H + (size_t)(rb3 * 4 + w) * 20480 + cb3 * 2560;
  const char* rdst = ws + WS_WP + (size_t)(NFR_WO + 2 * gw) * 1024;
  pins(psrc[0]); pins(psrc[1]); pins(psrc[2]); pins(obs[0]); pins(obs[1]); pins(obs[2]); pins(wog); pins(gcl); pins(hcl); pins(rdst);
  STAMP_AT(1, 0);
  unsigned f0;
  {
    const int cfl = (20 * head) >> 5, cfh = (20 * head + 19) >> 5;
    f0 = (unsigned)(((((lane & 32) ? 40 : 20) + ((lane & 16) ? cfh : cfl)) * 32 + kh * 16 + (lane & 15)) * 8);
    const unsigned f1 = (unsigned)((lane < 32 ? (((lane & 1) ? cfh : cfl) * 32 + qb * 4 + wq) : 1920 + cb3 * 16 + (lane & 15)) * 8);
    poll2(ctr + CTL_F1, f0 & ~(12u * 8u), f1, tag);
  }
  STAMP_AT(1, 4);
#pragma unroll
  for (int i = 0; i < 4; ++i) glds16<true>(kvg + i * 4096, voff, kvl + i * 4096);
  bf16x8 q0[2]; s16x4 q1[2];
  {
    const char* qg = ws + WS_Q + ((size_t)head * 64 + qb * 8 + wq * 2) * 1280;
    gld16<0>(q0[0], qg, voff); gld16<1280>(q0[1], qg, voff); gld8<1024>(q1[0], qg, r32 * 8); gld8<2304>(q1[1], qg, r32 * 8);
  }
  const unsigned ka0 = lds0 + kh * 81920 + lane * 16, ka1 = lds0 + kh * 81920 + 1024 + r32 * 8, va = lds0 + kh * 81920 + 1280 + (hh * 20 + (r32 < 20 ? r32 : 0)) * 16;
  bf16x8 K0[2], V0[2], V1[2]; s16x4 K1[2];
  V0[0] = V0[1] = V1[0] = V1[1] = (bf16x8)(short)(r32 == 20 ? 0x3f80 : 0);
  pin(V0[0]); pin(V0[1]); pin(V1[0]); pin(V1[1]);
  WAITV_BAR(0);
  u32x2 fr; gld8<0>(fr, ctr + CTL_F1, f0);
  pin(q0[0]); pin(q0[1]); pin(q1[0]); pin(q1[1]);
  if (hh) { q1[0] = (s16x4){0, 0, 0, 0}; q1[1] = (s16x4){0, 0, 0, 0}; }
  STAMP_AT(1, 1);
  rdK<0>(K0[0], K1[0], ka0, ka1);
  const unsigned long long vmsk = 0x000fffff000fffffull;
  rdV<0>(V0[0], V1[0], va, vmsk);
  f32x16 cm[2];
  {
    const bf16x8 kf0 = lds128(lds0 + lane * 16); const s16x4 kf1 = lds64(lds0 + 1024 + r32 * 8);
#pragma unroll
    for (int t = 0; t < 2; ++t) {
      f32x16 s = MFMA32(kf0, q0[t], (f32x16){}); s = MFMA32K8(kf1, q1[t], s);
      float m = s[0];
#pragma unroll
      for (int i = 1; i < 16; ++i) m = fmaxf(m, s[i]);
      m = max32(m);
      cm[t] = (f32x16)(-m);
      pin(cm[t]);
    }
  }
  asm volatile("s_waitcnt lgkmcnt(0)" : "+v"(K0[0]), "+v"(K1[0]), "+v"(V0[0]), "+v"(V1[0]));
  f32x16 o[2] = {};
  f32x16 SA, SB_;
  u32x4 PA0, PA1, PB0 = {}, PB1 = {};
  SA = MFMA32(K0[0], q0[0], cm[0]); SA = MFMA32K8(K1[0], q1[0], SA);
  waitv<0>(); pin(fr);
  if (!__all(fr[0] == tag[0] && fr[1] == tag[1])) poll(ctr + CTL_F1, f0, tag);
  STAMP_AT(1, 2);
  pin(o[0]); pin(o[1]); pin(PB0); pin(PB1);
#define GRP(MF, Sc, Pc, i, j) do { MF; SB(); const float e0 = EX2(Sc[i]), e1 = EX2(Sc[i + 1]), e2 = EX2(Sc[i + 2]), e3 = EX2(Sc[i + 3]); \
    Pc[j] = pk_bf16(e0, e1); Pc[j + 1] = pk_bf16(e2, e3); pin(Pc); SB(); } while (0)
#define SLOT(QK, Sc, Pc0, Pc1, Sn, kb, jn, Pp0, Pp1, vb, jp) do { \
    GRP(if constexpr (QK) Sn = MFMA32(K0[kb], q0[jn], cm[jn]), Sc, Pc0, 0, 0); \
    GRP(if constexpr (QK) Sn = MFMA32K8(K1[kb], q1[jn], Sn), Sc, Pc0, 4, 2); \
    GRP(o[jp] = MFMA32(V0[vb], __builtin_bit_cast(bf16x8, Pp0), o[jp]), Sc, Pc1, 8, 0); \
    GRP(o[jp] = MFMA32(V1[vb], __builtin_bit_cast(bf16x8, Pp1), o[jp]), Sc, Pc1, 12, 2); } while (0)
#define LWAIT(kb, vb) asm volatile("s_waitcnt lgkmcnt(0)" : "+v"(K0[kb]), "+v"(K1[kb]), "+v"(V0[vb]), "+v"(V1[vb]))
  auto rstore = [&](auto r_, auto yv_) {
    constexpr int r = r_;
    waitv<yv_>();
    sfor<8>([&](auto j) { pin(pw[r][j]); });
    const u32x4 v = {pw[r][0] | (pw[r][1] << 16), pw[r][2] | (pw[r][3] << 16), pw[r][4] | (pw[r][5] << 16), pw[r][6] | (pw[r][7] << 16)};
    if (gw < 1920) {
      if constexpr (r < 2) {
        st16s(rdst + r * 1024, voff, v);
        float c = 0.f;
#pragma unroll
        for (int j = 0; j < 4; ++j) c = dot2(v[j], 0x3f803f80u, c);
        c = red4(c);
        if (lane < 16) st4(ws + WS_CSP + (size_t)(2 * gw + r) * 64 + lane * 4, c);
      } else st16s(ws + WS_WP + (size_t)(NFR_WO + NFR_GU + gw) * 1024, voff, v);
    }
  };
  auto hook = [&](auto it_, auto i_) {
    constexpr int it = it_, i = i_, n = (it - 2) * 8 + i;
    if constexpr (n >= 0 && n < 2) st8s(obs[n], vo, fill2);
    if constexpr (n == 2) { if (!hh) st8s(obs[2], vo, fill2); }
    if constexpr (n >= 3 && n < 6) { const int L = (n - 3) * 64 + lane; st16s(gcl, (unsigned)(L * 16 + (L >= 96 ? 49152 - 1536 : 0)), fill); }
    if constexpr (n >= 6 && n < 9) { if (w < 4) st16s(hcl, (unsigned)(min((n - 6) * 64 + lane, 159) * 16), fill); }
    if constexpr (n == 9) { if (w < 4 && lane < 16) st8s(ws + WS_HST + (size_t)(rb3 * 4 + w) * 1024 + (cb3 >> 1) * 256 + (cb3 & 1) * 8, (unsigned)(lane * 16), fill2); }
    if constexpr (it == 5 && i == 6) {
      waitv<0>();
      setflag(ctl + CTL_FR + gw * 8, tag, lane);
      setflag(ctl + CTL_F2 + (qtile * 32 + head) * 8, tag, lane);
      setflag(ctl + CTL_F4 + ((rb4 * 8 + w) * 32 + cb4) * 8, tag, lane);
      if (w < 4) setflag(ctl + CTL_F3 + ((rb3 * 4 + w) * 8 + cb3) * 8, tag, lane);
    }
    if constexpr (it == 7 && i == 6) { gld8<0>(fa, ctr, fva); gld8<0>(fb, ctr, fvb); gld8<0>(fc, ctr, fvc); }
    if constexpr (it < 2) glds16<true>(kvg + (size_t)(it * 8 + 4 + i) * 4096, voff, kvl + (it * 8 + 4 + i) * 4096);
    else if constexpr (it < 4) gldu16<(i & 1) * 3072>(pw[it - 2][i], psrc[it - 2] + (i & ~1) * 3072, pvo1);
    else if constexpr (it == 4) {
      gldu16<(i & 3) * 1280>(pw[2][i], psrc[2] + (i & ~3) * 1280, pvo2);
      if constexpr (i == 0) rstore(IC<0>{}, IC<9>{});
      if constexpr (i == 7) rstore(IC<1>{}, IC<8>{});
    } else if constexpr (it == 5) {
      if constexpr (i == 7) rstore(IC<2>{}, IC<3>{});
    } else {
      constexpr int j = (it - 6) * 8 + i;
      const char* src = wog + (w + 8 * j) * 1024;
      if constexpr (j < 7) glds16<true>(src, voff, lds0 + w * 1024 + j * 8192);
      else if constexpr (j == 7) glds16<true>(src, voff, lds0 + w * 1024 + j * 8192 + (w >= 4 ? 20480 : 0));
      else if constexpr (j < 12) glds16<true>(src, voff, lds0 + w * 1024 + j * 8192 + 20480);
      else if constexpr (j == 12) { if (w < 4) glds16<true>(src, voff, lds0 + w * 1024 + j * 8192 + 20480); }
    }
    SB();
  };
  const unsigned ka0b = ka0 + 40960, ka1b = ka1 + 40960, vab = va + 40960;
  sfor<8>([&](auto it_) {
    constexpr int it = it_;
    if constexpr (it == 1) WAITV_BAR(6);
    if constexpr (it == 2) { WAITV_BAR(8); STAMP_AT(1, 3); }
    if constexpr (it == 4) asm volatile("s_waitcnt vmcnt(16)\n\ts_barrier" ::: "memory");
    if constexpr (it == 6) asm volatile("s_barrier" ::: "memory");
    if (((it ^ kh) & 1) == 0) __builtin_amdgcn_s_setprio(0); else __builtin_amdgcn_s_setprio(1);
    constexpr int o0 = (it & 3) * 10240;
    constexpr int on = it == 3 ? 0 : o0 + 10240;
    const unsigned a0 = it < 4 ? ka0 : ka0b, a1 = it < 4 ? ka1 : ka1b, av = it < 4 ? va : vab;
    const unsigned n0 = it < 3 ? ka0 : ka0b, n1 = it < 3 ? ka1 : ka1b, nv = it < 3 ? va : vab;
    rdK<o0 + 2560>(K0[1], K1[1], a0, a1); hook(it_, IC<0>{});                  SLOT(true, SA, PA0, PA1, SB_, 0, 1, PB0, PB1, 1, 1);
    LWAIT(1, 0); rdV<o0 + 2560>(V0[1], V1[1], av, vmsk); hook(it_, IC<1>{});   SLOT(true, SB_, PB0, PB1, SA, 1, 0, PA0, PA1, 0, 0);
    rdK<o0 + 5120>(K0[0], K1[0], a0, a1); hook(it_, IC<2>{});                  SLOT(true, SA, PA0, PA1, SB_, 1, 1, PB0, PB1, 0, 1);
    LWAIT(0, 1); rdV<o0 + 5120>(V0[0], V1[0], av, vmsk); hook(it_, IC<3>{});   SLOT(true, SB_, PB0, PB1, SA, 0, 0, PA0, PA1, 1, 0);
    rdK<o0 + 7680>(K0[1], K1[1], a0, a1); hook(it_, IC<4>{});                  SLOT(true, SA, PA0, PA1, SB_, 0, 1, PB0, PB1, 1, 1);
    LWAIT(1, 0); rdV<o0 + 7680>(V0[1], V1[1], av, vmsk); hook(it_, IC<5>{});   SLOT(true, SB_, PB0, PB1, SA, 1, 0, PA0, PA1, 0, 0);
    if constexpr (it < 7) rdK<on>(K0[0], K1[0], n0, n1);
    hook(it_, IC<6>{});                                                        SLOT(true, SA, PA0, PA1, SB_, 1, 1, PB0, PB1, 0, 1);
    LWAIT(0, 1);
    if constexpr (it < 7) rdV<on>(V0[0], V1[0], nv, vmsk);
    hook(it_, IC<7>{});                                                        SLOT(it < 7, SB_, PB0, PB1, SA, 0, 0, PA0, PA1, 1, 0);
  });
  __builtin_amdgcn_s_setprio(0);
  o[1] = MFMA32(V0[1], __builtin_bit_cast(bf16x8, PB0), o[1]);
  o[1] = MFMA32(V1[1], __builtin_bit_cast(bf16x8, PB1), o[1]);
  STAMP_AT(1, 9);
  bar();
  {
    LAS f32x4* xch = (LAS f32x4*)(uintptr_t)(lds0 + XCH_OFF + w * 3072);
    const f32x16& snd = kh ? o[0] : o[1];
#pragma unroll
    for (int g = 0; g < 3; ++g) xch[g * 64 + lane] = (f32x4){snd[4 * g], snd[4 * g + 1], snd[4 * g + 2], snd[4 * g + 3]};
  }
  bar();
  STAMP_AT(1, 10);
  float of[12];
  {
    const LAS f32x4* xch = (const LAS f32x4*)(uintptr_t)(lds0 + XCH_OFF + (w ^ 4) * 3072);
    const f32x16& own = kh ? o[1] : o[0];
#pragma unroll
    for (int g = 0; g < 3; ++g) { const f32x4 v = xch[g * 64 + lane];
#pragma unroll
      for (int j = 0; j < 4; ++j) of[4 * g + j] = own[4 * g + j] + v[j]; }
  }
  float l = hi32(of[8]);
  bool ok = (l > 8.67e-19f) && (l < 1.15e18f);
#pragma unroll
  for (int i = 0; i < 8; ++i) ok = ok && (fabsf(of[i]) < 1e37f);
  if (!hh) {
#pragma unroll
    for (int i = 8; i < 12; ++i) ok = ok && (fabsf(of[i]) < 1e37f);
  }
  if (__builtin_expect(!__all(ok), 0)) {
    __builtin_amdgcn_fence(__ATOMIC_ACQUIRE, "agent");
    const char* kv = ws + WS_KV + (size_t)head * 163840;
    const bf16x8 fq0 = kh ? q0[1] : q0[0];
    const s16x4 fq1 = kh ? q1[1] : q1[0];
    float m = -INFINITY;
    for (int t = 0; t < 64; ++t) {
      const bf16x8 kf0 = *(const bf16x8*)(kv + t * 2560 + lane * 16); const s16x4 kf1 = *(const s16x4*)(kv + t * 2560 + 1024 + r32 * 8);
      f32x16 s = MFMA32(kf0, fq0, (f32x16){}); s = MFMA32K8(kf1, fq1, s);
#pragma unroll
      for (int i = 0; i < 16; ++i) m = fmaxf(m, s[i]);
    }
    m = max32(m);
    f32x16 oo = {};
    const int vr = r32 < 20 ? r32 : 0;
    for (int t = 0; t < 64; ++t) {
      const bf16x8 kf0 = *(const bf16x8*)(kv + t * 2560 + lane * 16); const s16x4 kf1 = *(const s16x4*)(kv + t * 2560 + 1024 + r32 * 8);
      bf16x8 vf0 = *(const bf16x8*)(kv + t * 2560 + 1280 + (hh * 20 + vr) * 16), vf1 = *(const bf16x8*)(kv + t * 2560 + 1920 + (hh * 20 + vr) * 16);
      if (r32 >= 20) { vf0 = (bf16x8)(short)0x3f80; vf1 = vf0; }
      f32x16 s = MFMA32(kf0, fq0, (f32x16){}); s = MFMA32K8(kf1, fq1, s);
      u32x4 p0, p1;
#pragma unroll
      for (int i = 0; i < 4; ++i) {
        p0[i] = pk_bf16(EX2(s[2 * i] - m), EX2(s[2 * i + 1] - m));
        p1[i] = pk_bf16(EX2(s[8 + 2 * i] - m), EX2(s[8 + 2 * i + 1] - m));
      }
      oo = MFMA32(vf0, __builtin_bit_cast(bf16x8, p0), oo);
      oo = MFMA32(vf1, __builtin_bit_cast(bf16x8, p1), oo);
    }
#pragma unroll
    for (int i = 0; i < 12; ++i) of[i] = oo[i];
    l = hi32(of[8]);
  }
  const float rl = __builtin_amdgcn_rcpf(l);
  waitv<0>(); pin(fa); pin(fb); pin(fc);
  setflag(ctl + CTL_FD + gw * 8, tag, lane);
#pragma unroll
  for (int g = 0; g < 3; ++g)
    if (g < 2 || !hh) st8s(obs[g], vo, nofill(pk_bf16(of[4 * g] * rl, of[4 * g + 1] * rl), pk_bf16(of[4 * g + 2] * rl, of[4 * g + 3] * rl)));
  STAMP_AT(1, 11);
  if (!__all(fa[0] == tag[0] && fa[1] == tag[1] && fb[0] == tag[0] && fb[1] == tag[1])) poll2(ctr, fva, fvb, tag);
  if (!__all(fc[0] == tag[0] && fc[1] == tag[1])) poll(ctr, fvc, tag);
  STAMP_AT(1, 12);
}

constexpr int x_none(int) { return 0; }
constexpr int x_p4(int i) { return ((i >= 2 && i < 7) || (i >= 10 && i < 18)) ? 1 : 0; }
template <int NSTEP, int NF, int LA, int LL, int CG, int NA, int ASTR, int (*BOFF)(int), int (*XCNT)(int), class PRE, class MM>
DI void gemm_res(const char* asrc, unsigned lds0, unsigned voff, PRE&& pre, MM&& mm) {
  bf16x8 a[NSTEP][NA];
  bf16x8 bf[LL + 1][NF];
  unsigned rd0 = lds0 + voff, rd1 = rd0 + 65536, rd2 = rd0 + 131072; pin(rd0); pin(rd1); pin(rd2);
  auto issue = [&](auto n_) { constexpr int n = n_; sfor<NA>([&](auto j) { gld16<(n & 3) * 1024>(a[n][j], asrc + (n >> 2) * 4096 + j * ASTR, voff); }); };
  constexpr auto xsum = [](int lo, int hi) { int c = 0; for (int i = lo < 0 ? 0 : lo; i <= hi; ++i) c += XCNT(i); return c; };
  auto rd = [&](auto i_, auto f_) {
    constexpr int i = i_, f = f_, off = BOFF(i * NF + f);
    bf[i % (LL + 1)][f] = lds128((off < 65536 ? rd0 : off < 131072 ? rd1 : rd2) + (off & 65535));
  };
  static_assert(NSTEP % CG == 0 && LA > CG, "groups");
  unsigned m = 0;
  auto fold = [&](auto n_) { constexpr int n = n_;
    sfor<NA>([&](auto j) { pin(a[n][j]); const u32x4 u = __builtin_bit_cast(u32x4, a[n][j]); m = max(max(m, u[0]), u[1]); m = max(max(m, u[2]), u[3]); });
    pin(m);
    if constexpr (n % CG == CG - 1) {
      if (__builtin_expect(__any(m == 0xffffffffu), 0)) {
#pragma unroll 1
        for (int spin = 0; spin < (1 << 17); ++spin) {
          RETRY_COUNT(NSTEP + NF);
          sfor<CG>([&](auto k) { issue(IC<n - CG + 1 + k>{}); });
          waitv<0>(); m = 0;
          sfor<CG>([&](auto k) { sfor<NA>([&](auto j) { pin(a[n - CG + 1 + k][j]); m = max(m, umax4(__builtin_bit_cast(u32x4, a[n - CG + 1 + k][j]))); }); });
          if (!__any(m == 0xffffffffu)) break;
        }
      }
    }
  };
  sfor<LA>([&](auto n) { issue(n); });
  pre(IC<-1>{});
  sfor<LL>([&](auto i) { sfor<NF>([&](auto f) { rd(i, f); }); });
  waitv<(LA - CG) * NA>(); sfor<CG>([&](auto n) { fold(n); });
  sfor<NSTEP>([&](auto i_) {
    constexpr int i = i_;
    pre(i_);
    SB();
    sfor<NF>([&](auto f_) {
      constexpr int f = f_;
      if constexpr (f == NF - 1 && i + CG < NSTEP) {
        waitv<((i + LA < NSTEP ? i + LA : NSTEP - 1) - (i + CG)) * NA + xsum(i + CG - LA + 1, i)>(); fold(IC<i + CG>{}); SB(); }
      mm(f_, bf[i % (LL + 1)][f], a[i]);
      SB();
      if constexpr (i + LL < NSTEP) rd(IC<i + LL>{}, f_);
      if constexpr (f == 0 && i + LA < NSTEP) issue(IC<i + LA>{});
      SB();
    });
  });
}

__global__ __launch_bounds__(512, 2) void k_mega(const u16* __restrict__ x, const u16* __restrict__ Wqkv, const u16* __restrict__ Wo,
                                                 const u16* __restrict__ Wg, const u16* __restrict__ Wu, const u16* __restrict__ Wd,
                                                 char* __restrict__ ws, u16* __restrict__ out) {
  __shared__ __attribute__((aligned(16))) char smem[163840];
  const unsigned lds0 = lds_addr_of(smem);
  const int w = __builtin_amdgcn_readfirstlane(threadIdx.x >> 6);
  const int g8 = blockIdx.x & 7, i32 = blockIdx.x >> 3;
  char* ctl = ws + WS_CTL;
  const char* ctr = ctl + g8 * CTL_REP;
  u32x2 tag;
  if (i32 < 30) {
    const int i = (g8 & 1) * 30 + i32, rb = (g8 >> 1) * 4 + (i & 3), cb = i >> 2;
    if (w >= 4) { p1_load(x, Wqkv, lds0, w - 4, rb, cb); u32x2 ep; gld8<0>(ep, ctl, 0u); waitv<0>(); pin(ep); tag = make_tag(ep); }
    else if (cb >= 10) p1_body<true>(x, ws, lds0, w, rb, cb, tag);
    else p1_body<false>(x, ws, lds0, w, rb, cb, tag);
  } else {
    { RowStat rs; u32x2 ep; const int lane = fresh_lane(); gld8<0>(ep, ctl, 0u); rowstat_get(rs, x, blockIdx.x * 8 + w, lane); waitv<0>(); pin(ep); tag = make_tag(ep);
      rowstat_put(rs, ws, blockIdx.x * 8 + w, lane, tag); }
    p1_repack(Wo, ws, w, blockIdx.x - 240);
    publish(ctl + CTL_F1 + (1920 + (blockIdx.x - 240) * 8 + w) * 8, tag, fresh_lane());
  }
  bar();
  const int rb3 = g8 * 4 + (i32 >> 3), cb3 = i32 & 7;
  const int rb4 = (g8 >> 2) * 4 + (i32 >> 3), cb4 = (g8 & 3) * 8 + (i32 & 7);
  p2_attn(ws, Wg, Wu, Wd, lds0, w, tag, rb3, cb3, rb4, cb4);
  bar();
  const char* gug = ws + WS_WP + (size_t)NFR_WO * 1024 + (size_t)cb4 * 122880;
  const char* wdg = ws + WS_WP + (size_t)(NFR_WO + NFR_GU) * 1024 + (size_t)cb3 * 245760;
  u32x2 hp[5];
  if (w < 4) {
    const int lane = fresh_lane();
    const unsigned voff = lane * 16;
    const int row = rb3 * 64 + w * 16 + (lane & 15), col0 = cb3 * 80 + 4 * (lane >> 4);
    STAMP_AT(2, 0);
    u32x2 xr[5];
#pragma unroll
    for (int ct = 0; ct < 5; ++ct) xr[ct] = *(const u32x2*)(x + (size_t)row * 640 + col0 + 16 * ct);
    { const int hd = min(lane & 31, G3), cgt = (hd & 1) ? hd * 5 : hd * 5 + 4;
      probe(ws + WS_O + (size_t)(rb3 * 4 + w) * 20480, (unsigned)((cgt >> 3) * 1024 + ((cgt & 7) >> 1) * 256 + (cgt & 1) * 8)); }
    sfor<5>([&](auto ct) { pin(xr[ct]); });
    STAMP_AT(2, 1);
    f32x4 acc[5] = {};
    sfor<5>([&](auto ct) { pin(acc[ct]); });
    gemm_res<20, 5, P3_LA, 2, 4, 1, 0, wo_off, x_none>(ws + WS_O + (size_t)(rb3 * 4 + w) * 20480, lds0, voff,
      [&](auto i) { if constexpr (i == -1) STAMP_AT(2, 2); if constexpr (i == 1) STAMP_AT(2, 3); if constexpr (i == 10) STAMP_AT(2, 4);
        if constexpr (i == P3_G1 || i == 12) bar(); },
      [&](auto ct, const bf16x8& b, bf16x8 (&a)[1]) { mfma16(acc[ct], b, a[0]); });
    acc_fence(); sfor<5>([&](auto ct) { pin(acc[ct]); });
    STAMP_AT(2, 6);
    float s1 = 0.f, s2 = 0.f;
    const char* hb = ws + WS_H + (size_t)(rb3 * 4 + w) * 20480;
    const unsigned vt = t16lane(lane);
#pragma unroll
    for (int ct = 0; ct < 5; ++ct) {
      hp[ct] = nofill(pk_bf16(bf_lo(xr[ct][0]) + acc[ct][0], bf_hi(xr[ct][0]) + acc[ct][1]), pk_bf16(bf_lo(xr[ct][1]) + acc[ct][2], bf_hi(xr[ct][1]) + acc[ct][3]));
      st8s(hb + t16off(cb3 * 80 + ct * 16), vt, hp[ct]);
      s1 = dot2(hp[ct][0], 0x3f803f80u, s1); s1 = dot2(hp[ct][1], 0x3f803f80u, s1);
      s2 = dot2(hp[ct][0], hp[ct][0], s2); s2 = dot2(hp[ct][1], hp[ct][1], s2);
    }
    s1 = red4(s1); s2 = red4(s2);
    if (lane < 16) st8s(ws + WS_HST + (size_t)(rb3 * 4 + w) * 1024 + (cb3 >> 1) * 256 + (cb3 & 1) * 8, (unsigned)(lane * 16), nofill(__float_as_uint(s1), __float_as_uint(s2)));
    bar();
    STAMP_AT(2, 7);
  } else {
    const int lane = fresh_lane(), v = w - 4;
    const unsigned voff = lane * 16;
    float cpart[10];
    const int cc = v * 24 + min(lane & 31, 23);
    sfor<10>([&](auto i) { gld4<i * 384>(cpart[i], ws + WS_CSP, (unsigned)(cb4 * 7680 + (lane >> 5) * 3840 + cc * 4)); });
    poll(ctr + CTL_FD, (unsigned)((cb3 * 240 + v * 60 + min(lane, 59)) * 8), tag);
    float cv = 0.f;
    sfor<10>([&](auto j) { pin(cpart[j]); cv += cpart[j]; });
    cv = add32(cv);
    bar();
    STAMP_AT(2, 2);
    sfor<5>([&](auto j) { glds16<true>(gug + (v * 5 + j) * 1024, voff, lds0 + gu_off(0) + (v * 5 + j) * 1024); });
    sfor<10>([&](auto j) { glds16<true>(gug + (20 + v * 10 + j) * 1024, voff, lds0 + gu_off(20) + (v * 10 + j) * 1024); });
    bar();
    sfor<15>([&](auto j) { glds16<true>(gug + (60 + v * 15 + j) * 1024, voff, lds0 + gu_off(60) + (v * 15 + j) * 1024); });
    STAMP_AT(2, 5);
    waitv<0>();
    bar();
    STAMP_AT(2, 6);
    if (lane < 24) ((LAS float*)(uintptr_t)(lds0 + CSF_OFF))[cc] = cv;
  }
  {
    const int lane = fresh_lane();
    const unsigned voff = lane * 16;
    const int r16 = lane & 15, g4 = lane >> 4;
    STAMP_AT(3, 0);
    f32x4 cs[6]; f32x2 ms[2];
    f32x4 hs[2];
    auto hs_wait = [&](bool all) {
#pragma unroll 1
      for (int spin = 0; spin < (1 << 17); ++spin) {
        gld16<0>(hs[0], ws + WS_HST + (size_t)(rb4 * 16 + w * 2) * 1024, voff);
        gld16<1024>(hs[1], ws + WS_HST + (size_t)(rb4 * 16 + w * 2) * 1024, voff);
        waitv<0>(); pin(hs[0]); pin(hs[1]);
        const bool miss = max(umax4(__builtin_bit_cast(u32x4, hs[0])), umax4(__builtin_bit_cast(u32x4, hs[1]))) == 0xffffffffu;
        if (!__any(miss && (all || g4 < G4))) return !__any(miss);
        __builtin_amdgcn_s_sleep(1);
      }
      return true;
    };
    const bool hs_ok = hs_wait(false);
    STAMP_AT(3, 1);
    f32x4 acc[2][6] = {};
    sfor<6>([&](auto ct) { pin(acc[0][ct]); pin(acc[1][ct]); });
    if (w < 4) __builtin_amdgcn_s_setprio(1);
    gemm_res<20, 6, K4_LA_, 1, K4_CG_, 2, 20480, gu_off, x_p4>(ws + WS_H + (size_t)(rb4 * 16 + w * 2) * 20480, lds0, voff,
      [&](auto i) { if constexpr (i == -1) STAMP_AT(3, 2); if constexpr (i == 1) STAMP_AT(3, 3); if constexpr (i == 10) STAMP_AT(3, 4);
        if constexpr (i == 9) bar();
        if constexpr (i == 19) { if (!hs_ok) hs_wait(true); }
        if constexpr (i == 19) sfor<2>([&](auto t) {
          const float s1 = red4(hs[t][0] + hs[t][2]), s2 = red4(hs[t][1] + hs[t][3]), mean = s1 * (1.f / 640.f);
          ms[t] = (f32x2){mean, __builtin_amdgcn_rsqf(fmaxf(s2 * (1.f / 640.f) - mean * mean, 0.f) + LN_EPS)}; pin(ms[t]); });
        if constexpr (i == 18) sfor<6>([&](auto ct) { cs[ct] = *(const LAS f32x4*)(uintptr_t)(lds0 + CSF_OFF + (ct * 16 + 4 * g4) * 4); });
        if constexpr (i >= 2 && i < 7) { const int f = (i - 2) * 8 + w - ((i == 6 && w >= 4) ? 4 : 0); glds16<true>(wdg + (f) * 1024, voff, lds0 + wd_off(0) + f * 1024); }
        if constexpr (i >= 10 && i < 15) { constexpr int n = i - 10; glds16<true>(wdg + (40 + n * 8 + w) * 1024, voff, lds0 + wd_off(40) + (n * 8 + w) * 1024); }
        if constexpr (i >= 15 && i < 18) { const int f = (i - 15) * 8 + w - ((i == 17 && w >= 4) ? 4 : 0); glds16<true>(wdg + (140 + f) * 1024, voff, lds0 + wd_off(140) + f * 1024); } },
      [&](auto ct, const bf16x8& b, bf16x8 (&a)[2]) { mfma16(acc[0][ct], b, a[0]); mfma16(acc[1][ct], b, a[1]); });
    acc_fence(); sfor<6>([&](auto ct) { pin(acc[0][ct]); pin(acc[1][ct]); });
    STAMP_AT(3, 6);
    constexpr float GC0 = -2.f * 0.7978845608028654f * 1.4426950408889634f, GC1 = GC0 * 0.044715f;
    const char* gb = ws + WS_G + (size_t)(rb4 * 16 + w * 2) * 49152;
    const unsigned vt = t16lane(lane);
#pragma unroll
    for (int t = 0; t < 2; ++t) {
      const float nm = -ms[t].x, rstd = ms[t].y;
#pragma unroll
      for (int ct = 0; ct < 3; ++ct) {
        const f32x4 gv = (acc[t][ct] + cs[ct] * nm) * rstd, uv = (acc[t][3 + ct] + cs[3 + ct] * nm) * rstd;
        const f32x4 z = gv * (gv * gv * GC1 + GC0);
        f32x4 e;
#pragma unroll
        for (int j = 0; j < 4; ++j) e[j] = __builtin_amdgcn_exp2f(z[j]);
        e = e + 1.f;
#pragma unroll
        for (int j = 0; j < 4; ++j) e[j] = __builtin_amdgcn_rcpf(e[j]);
        const f32x4 r = gv * uv * e;
        st8s(gb + t * 49152 + t16off(cb4 * 48 + ct * 16), vt, nofill(pk_bf16(r[0], r[1]), pk_bf16(r[2], r[3])));
      }
    }
    __builtin_amdgcn_s_setprio(0);
    STAMP_AT(3, 7);
    waitv<6>();
    STAMP_AT(3, 8);
    bar();
  }
  if (w < 4) {
    const int lane = fresh_lane();
    const unsigned voff = lane * 16;
    const int row = rb3 * 64 + w * 16 + (lane & 15), col0 = cb3 * 80 + 4 * (lane >> 4);
    STAMP_AT(4, 0);
    probe(ws + WS_G + (size_t)(rb3 * 4 + w) * 49152, (unsigned)t16off(min(lane & 31, G5) * 48 + 32));
    STAMP_AT(4, 1);
    f32x4 acc[5] = {};
    sfor<5>([&](auto ct) { pin(acc[ct]); });
    gemm_res<48, 5, K5_LA_, 2, 4, 1, 0, wd_off, x_none>(ws + WS_G + (size_t)(rb3 * 4 + w) * 49152, lds0, voff,
      [&](auto i) { if constexpr (i == 1) STAMP_AT(4, 3); if constexpr (i == 14) STAMP_AT(4, 4); if constexpr (i == 30) STAMP_AT(4, 5);
        if constexpr (i == -1 || i == 8 || i == 14 || i == 16 || i == 30 || i == 38) bar(); },
      [&](auto ct, const bf16x8& b, bf16x8 (&a)[1]) { mfma16(acc[ct], b, a[0]); });
    acc_fence(); sfor<5>([&](auto ct) { pin(acc[ct]); });
    STAMP_AT(4, 12);
#pragma unroll
    for (int ct = 0; ct < 5; ++ct) {
      const unsigned p0 = pk_bf16(bf_lo(hp[ct][0]) + acc[ct][0], bf_hi(hp[ct][0]) + acc[ct][1]);
      const unsigned p1 = pk_bf16(bf_lo(hp[ct][1]) + acc[ct][2], bf_hi(hp[ct][1]) + acc[ct][3]);
      *(u32x2*)(out + (size_t)row * 640 + col0 + 16 * ct) = (u32x2){p0, p1};
    }
    STAMP_AT(4, 13);
  } else {
    const int lane = fresh_lane(), v = w - 4;
    const unsigned voff = lane * 16;
    glds16<true>(wdg + (36 + v) * 1024, voff, lds0 + wd_off(36) + v * 1024);
    waitv<0>(); bar();
    sfor<3>([&](auto g) { sfor<5>([&](auto j) { constexpr int f = (g + 4) * 20 + j; glds16<true>(wdg + (f + v * 5) * 1024, voff, lds0 + wd_off(f) + v * 5120); }); });
    bar();
    sfor<2>([&](auto g) { sfor<5>([&](auto j) { constexpr int f = (g + 8) * 20 + j; glds16<true>(wdg + (f + v * 5) * 1024, voff, lds0 + wd_off(f) + v * 5120); }); });
    waitv<10>(); bar();
    bar();
    sfor<2>([&](auto g) { sfor<5>([&](auto j) { constexpr int f = (g + 10) * 20 + j; glds16<true>(wdg + (f + v * 5) * 1024, voff, lds0 + wd_off(f) + v * 5120); }); });
    waitv<10>(); bar();
    waitv<0>(); bar();
    if (blockIdx.x == 0 && w == 4) {
      poll2(ctr + CTL_FR, (unsigned)(lane * 64), (unsigned)(4096 + lane * 64), tag);
      poll2(ctr + CTL_FR, (unsigned)(8192 + lane * 64), (unsigned)(12288 + lane * 64), tag);
      if (lane == 0) st8(ctl, tag);
    }
  }
}

extern "C" void kernel_launch(void* const* d_in, const int* in_sizes, int n_in, void* d_out, int out_size, void* d_ws, size_t ws_size,
                              hipStream_t stream) {
  k_mega<<<256, 512, 0, stream>>>((const u16*)d_in[0], (const u16*)d_in[1], (const u16*)d_in[2], (const u16*)d_in[4], (const u16*)d_in[5],
                                  (const u16*)d_in[6], (char*)d_ws + ((size_t)220  __H0__ ), (u16*)d_out);
}
```

```cpp
#include <hip/hip_runtime.h>
#include <hip/hip_bf16.h>
#include <stdint.h>
#include <utility>

typedef short bf16x8 __attribute__((ext_vector_type(8)));
typedef short s16x4 __attribute__((ext_vector_type(4)));
typedef float f32x16 __attribute__((ext_vector_type(16)));
typedef float f32x4 __attribute__((ext_vector_type(4)));
typedef float f32x2 __attribute__((ext_vector_type(2)));
typedef unsigned u32x4 __attribute__((ext_vector_type(4)));
typedef unsigned u32x2 __attribute__((ext_vector_type(2)));
typedef __bf16 bf2_t __attribute__((ext_vector_type(2)));
typedef unsigned short u16;
#define LAS __attribute__((address_space(3)))
#define DI __device__ __forceinline__

constexpr float LN_EPS = 1e-5f;
constexpr float QSCALE = 0.22360679774997896f * 1.4426950408889634f;

constexpr size_t WS_Q   = 0;
constexpr size_t WS_KV  = WS_Q + 2621440;
constexpr size_t WS_O   = WS_KV + 5242880;
constexpr size_t WS_H   = WS_O + 2621440;
constexpr size_t WS_HST = WS_H + 2621440;
constexpr size_t WS_G   = WS_HST + 131072;
constexpr size_t WS_WP  = WS_G + 6291456;
constexpr size_t WS_CSP = WS_WP + 6717440;
constexpr size_t WS_RST = WS_CSP + 245760;
constexpr size_t WS_CTL = WS_RST + 32768;
constexpr size_t CTL_F1 = 256;
constexpr size_t CTL_F2 = CTL_F1 + 2048 * 8;
constexpr size_t CTL_FR = CTL_F2 + 2048 * 8;
constexpr size_t CTL_F3 = CTL_FR + 2048 * 8;
constexpr size_t CTL_F4 = CTL_F3 + 1024 * 8;
constexpr size_t CTL_FD = CTL_F4 + 2048 * 8;
constexpr size_t CTL_REP = CTL_FD + 2048 * 8 - 256;
constexpr int NFR_WO = 800, NFR_GU = 3840, NFR_WD = 1920;

DI unsigned pk_bf16(float lo, float hi) { f32x2 f = {lo, hi}; return __builtin_bit_cast(unsigned, __builtin_convertvector(f, bf2_t)); }
DI float bf_lo(unsigned u) { return __uint_as_float(u << 16); }
DI float bf_hi(unsigned u) { return __uint_as_float(u & 0xffff0000u); }
DI float dot2(unsigned a, unsigned b, float c) { return __builtin_amdgcn_fdot2_f32_bf16(__builtin_bit_cast(bf2_t, a), __builtin_bit_cast(bf2_t, b), c, false); }
DI float add32(float v) { const auto r = __builtin_amdgcn_permlane32_swap(__float_as_uint(v), __float_as_uint(v), false, false); return __uint_as_float(r[0]) + __uint_as_float(r[1]); }
DI float max32(float v) { const auto r = __builtin_amdgcn_permlane32_swap(__float_as_uint(v), __float_as_uint(v), false, false); return fmaxf(__uint_as_float(r[0]), __uint_as_float(r[1])); }
DI float add16(float v) { const auto r = __builtin_amdgcn_permlane16_swap(__float_as_uint(v), __float_as_uint(v), false, false); return __uint_as_float(r[0]) + __uint_as_float(r[1]); }
DI float red4(float v) { return add32(add16(v)); }
DI float hi32(float v) { const auto r = __builtin_amdgcn_permlane32_swap(__float_as_uint(v), __float_as_uint(v), false, false); return __uint_as_float(r[1]); }
#define MFMA32(a, b, c) __builtin_amdgcn_mfma_f32_32x32x16_bf16((a), (b), (c), 0, 0, 0)
#define MFMA32K8(a, b, c) __builtin_amdgcn_mfma_f32_32x32x8bf16_1k((a), (b), (c), 0, 0, 0)
#define MFMA16(a, b, c) __builtin_amdgcn_mfma_f32_16x16x32_bf16((a), (b), (c), 0, 0, 0)

DI void mfma16(f32x4& c, const bf16x8& a, const bf16x8& b) { asm volatile("v_mfma_f32_16x16x32_bf16 %0, %1, %2, %0" : "+v"(c) : "v"(a), "v"(b)); }
DI void acc_fence() { asm volatile("s_nop 7\n\ts_nop 3" ::: "memory"); }
template <bool DEV> DI void glds16(const void* sbase, unsigned voff, unsigned lds_base) {
  if constexpr (DEV) asm volatile("s_mov_b32 m0, %2\n\ts_nop 0\n\tglobal_load_lds_dwordx4 %0, %1 sc1" :: "v"(voff), "s"(sbase), "s"(lds_base) : "memory");
  else asm volatile("s_mov_b32 m0, %2\n\ts_nop 0\n\tglobal_load_lds_dwordx4 %0, %1" :: "v"(voff), "s"(sbase), "s"(lds_base) : "memory");
}
template <int OFF, class T> DI void gld16(T& dst, const void* sbase, unsigned voff) {
  asm volatile("global_load_dwordx4 %0, %1, %2 offset:%3 sc1" : "=v"(dst) : "v"(voff), "s"(sbase), "i"(OFF) : "memory");
}
template <int OFF, class T> DI void gld8(T& dst, const void* sbase, unsigned voff) {
  asm volatile("global_load_dwordx2 %0, %1, %2 offset:%3 sc1" : "=v"(dst) : "v"(voff), "s"(sbase), "i"(OFF) : "memory");
}
template <int OFF> DI void gld4(float& dst, const void* sbase, unsigned voff) {
  asm volatile("global_load_dword %0, %1, %2 offset:%3 sc1" : "=v"(dst) : "v"(voff), "s"(sbase), "i"(OFF) : "memory");
}
DI void gld4u(unsigned& dst, const void* sbase, unsigned voff) { asm volatile("global_load_dword %0, %1, %2 sc1" : "=v"(dst) : "v"(voff), "s"(sbase) : "memory"); }
template <int OFF> DI void gldu16(unsigned& dst, const void* sbase, unsigned voff) {
  asm volatile("global_load_ushort %0, %1, %2 offset:%3" : "=v"(dst) : "v"(voff), "s"(sbase), "i"(OFF) : "memory");
}
DI void st16(void* p, u32x4 v) { asm volatile("global_store_dwordx4 %0, %1, off sc1\n\ts_nop 1" :: "v"(p), "v"(v) : "memory"); }
DI void st8(void* p, u32x2 v) { asm volatile("global_store_dwordx2 %0, %1, off sc1" :: "v"(p), "v"(v) : "memory"); }
DI void st4(void* p, float v) { asm volatile("global_store_dword %0, %1, off sc1" :: "v"(p), "v"(v) : "memory"); }
DI void st8s(const void* sbase, unsigned voff, u32x2 v) { asm volatile("global_store_dwordx2 %0, %1, %2 sc1" :: "v"(voff), "v"(v), "s"(sbase) : "memory"); }
DI u32x2 nofill(unsigned a, unsigned b) { return (u32x2){min(a, 0xfffffffeu), min(b, 0xfffffffeu)}; }
DI unsigned umax4(u32x4 v) { return max(max(v[0], v[1]), max(v[2], v[3])); }
template <int N> DI void waitv() { asm volatile("s_waitcnt vmcnt(%0)" :: "i"(N) : "memory"); }
DI void bar() { asm volatile("s_waitcnt lgkmcnt(0)\n\ts_barrier" ::: "memory"); }
DI void setflag(char* flag, u32x2 tag, int lane) { if (lane < 8) st8(flag + lane * CTL_REP, tag); }
DI void publish(char* flag, u32x2 tag, int lane) { waitv<0>(); setflag(flag, tag, lane); }
DI void poll(const char* base, unsigned voff, u32x2 tag) {
#pragma unroll 1
  for (int spin = 0; spin < (1 << 17); ++spin) {
    u32x2 v;
    asm volatile("global_load_dwordx2 %0, %1, %2 sc1\n\ts_waitcnt vmcnt(0)" : "=v"(v) : "v"(voff), "s"(base) : "memory");
    if (__all(v[0] == tag[0] && v[1] == tag[1])) break;
    __builtin_amdgcn_s_sleep(1);
  }
}
DI void poll2(const char* base, unsigned voff0, unsigned voff1, u32x2 tag) {
#pragma unroll 1
  for (int spin = 0; spin < (1 << 17); ++spin) {
    u32x2 v, u;
    asm volatile("global_load_dwordx2 %0, %2, %4 sc1\n\tglobal_load_dwordx2 %1, %3, %4 sc1\n\ts_waitcnt vmcnt(0)" : "=&v"(v), "=&v"(u) : "v"(voff0), "v"(voff1), "s"(base) : "memory");
    if (__all(v[0] == tag[0] && v[1] == tag[1] && u[0] == tag[0] && u[1] == tag[1])) break;
    __builtin_amdgcn_s_sleep(1);
  }
}
DI void probe(const char* base, unsigned voff) {
#pragma unroll 1
  for (int spin = 0; spin < (1 << 17); ++spin) {
    unsigned v;
    asm volatile("global_load_dword %0, %1, %2 sc1\n\ts_waitcnt vmcnt(0)" : "=v"(v) : "v"(voff), "s"(base) : "memory");
    if (__all(v != 0xffffffffu)) break;
    __builtin_amdgcn_s_sleep(1);
  }
}
constexpr int t16off(int c0) { return (c0 >> 5) * 1024 + (c0 & 16) * 32; }
DI unsigned t16lane(int lane) { return (unsigned)((lane >> 5) * 256 + (lane & 15) * 16 + ((lane >> 4) & 1) * 8); }
DI void st16s(const void* sbase, unsigned voff, u32x4 v) { asm volatile("global_store_dwordx4 %0, %1, %2 sc1\n\ts_nop 1" :: "v"(voff), "v"(v), "s"(sbase) : "memory"); }
DI float row16_sum(float v) {
  v += __builtin_bit_cast(float, __builtin_amdgcn_update_dpp(0, __builtin_bit_cast(int, v), 0x128, 0xf, 0xf, false));
  v += __builtin_bit_cast(float, __builtin_amdgcn_update_dpp(0, __builtin_bit_cast(int, v), 0x124, 0xf, 0xf, false));
  v += __builtin_bit_cast(float, __builtin_amdgcn_update_dpp(0, __builtin_bit_cast(int, v), 0x122, 0xf, 0xf, false));
  v += __builtin_bit_cast(float, __builtin_amdgcn_update_dpp(0, __builtin_bit_cast(int, v), 0x121, 0xf, 0xf, false));
  return v;
}
DI u32x2 make_tag(u32x2 ep) {
  const unsigned long long e = (((unsigned long long)__builtin_amdgcn_readfirstlane(ep[1]) << 32) | __builtin_amdgcn_readfirstlane(ep[0])) + 1ull;
  return (u32x2){(unsigned)e, (unsigned)(e >> 32)};
}
constexpr unsigned RS_K0 = 0x9e3779b9u, RS_K1 = 0x7f4a7c15u;
struct RowStat { u32x4 a; unsigned b; };
DI void rowstat_get(RowStat& r, const u16* x, int row, int lane) {
  asm volatile("global_load_dwordx4 %0, %2, %4\n\tglobal_load_dword %1, %3, %4 offset:1024" : "=&v"(r.a), "=&v"(r.b) : "v"(lane * 16), "v"(lane * 4), "s"((const char*)x + (size_t)row * 1280) : "memory");
}
DI void rowstat_put(RowStat& r, char* ws, int row, int lane, u32x2 tag) {
  asm volatile("" : "+v"(r.a), "+v"(r.b));
  float s1 = dot2(r.b, 0x3f803f80u, 0.f), s2 = dot2(r.b, r.b, 0.f);
#pragma unroll
  for (int j = 0; j < 4; ++j) { s1 = dot2(r.a[j], 0x3f803f80u, s1); s2 = dot2(r.a[j], r.a[j], s2); }
  s1 = red4(row16_sum(s1)); s2 = red4(row16_sum(s2));
  const float mean = s1 * (1.f / 640.f), rstd = __builtin_amdgcn_rsqf(fmaxf(s2 * (1.f / 640.f) - mean * mean, 0.f) + LN_EPS);
  const unsigned m = __float_as_uint(mean), q = __float_as_uint(rstd);
  if (lane == 0) st16(ws + WS_RST + (size_t)row * 16, (u32x4){m, q, tag[0] ^ m ^ RS_K0, tag[1] ^ q ^ RS_K1});
}
DI int fresh_lane() { int l = __builtin_amdgcn_mbcnt_hi(~0u, __builtin_amdgcn_mbcnt_lo(~0u, 0u)); asm volatile("" : "+v"(l)); return l; }
template <class T> DI void pins(T& v) { asm volatile("" : "+s"(v)); }
template <class T> DI void pin(T& v) { asm volatile("" : "+v"(v)); }
DI bf16x8 lds128(unsigned addr) { return *(const LAS bf16x8*)(uintptr_t)addr; }
DI s16x4 lds64(unsigned addr) { return *(const LAS s16x4*)(uintptr_t)addr; }
DI s16x4 ldstr(unsigned addr) { return __builtin_amdgcn_ds_read_tr16_b64_v4i16((LAS s16x4*)(uintptr_t)addr); }
DI unsigned lds_addr_of(const void* p) { return (unsigned)(uintptr_t)(const LAS char*)p; }
#define STAMP_AT(kid, idx) ((void)0)
#define RETRY_COUNT(k) ((void)0)
#define WAITV_BAR(N) asm volatile("s_waitcnt vmcnt(" #N ") lgkmcnt(0)\n\ts_barrier" ::: "memory")
#define SB() __builtin_amdgcn_sched_barrier(0)
template <int N> using IC = std::integral_constant<int, N>;
template <class F, int... Is> DI void sfor_(F&& f, std::integer_sequence<int, Is...>) { (f(IC<Is>{}), ...); }
template <int N, class F> DI void sfor(F&& f) { sfor_(f, std::make_integer_sequence<int, N>{}); }

constexpr int P3_G1 = -1, P3_LA = 12, K4_LA_ = 6, K4_CG_ = 2, K5_LA_ = 16;
constexpr int G3 = 19, G4 = 1, G5 = 10;
constexpr int K1_STAGE = 32768;
DI void p1_load(const u16* __restrict__ x, const u16* __restrict__ W, const unsigned lds0, const int v, int rb, int cb) {
  const int lane = fresh_lane();
  const unsigned a_voff0 = (unsigned)((lane >> 3) * 1280 + (((lane & 7) ^ ((lane >> 4) & 7)) << 4));
  const unsigned a_voff1 = (unsigned)((lane >> 3) * 1280 + (((lane & 7) ^ ((4 + (lane >> 4)) & 7)) << 4)) + 8 * 1280;
  const unsigned w_voff0 = (unsigned)((lane >> 3) * 3840 + (((lane & 7) ^ (((lane >> 4) & 1) << 2)) << 4));
  const unsigned w_voff1 = w_voff0 + 8 * 3840;
  const char* xs = (const char*)x + (size_t)(rb * 128 + v * 32) * 1280;
  const char* wsrc = (const char*)W + (size_t)((v & 1) * 32) * 3840 + (size_t)(cb * 128 + (v >> 1) * 64) * 2;
  const int rot = ((((blockIdx.x >> 1) & 3) * 5 + (blockIdx.x & 1) * 10) >> 1) % 10;
  auto st = [&](auto s_) {
    constexpr int s0 = s_; const unsigned d = lds0 + (s0 % 5) * K1_STAGE + v * 4096;
    const int s = s0 + rot - (s0 + rot >= 10 ? 10 : 0);
    const char* xp = xs + s * 128; const char* wp = wsrc + (size_t)s * 64 * 3840;
    glds16<false>(xp, a_voff0, d); glds16<false>(wp, w_voff0, d + 16384);
    glds16<false>(xp, a_voff1, d + 1024); glds16<false>(wp, w_voff1, d + 16384 + 1024);
    glds16<false>(xp + 16 * 1280, a_voff0, d + 2048); glds16<false>(wp + 16 * 3840, w_voff0, d + 16384 + 2048);
    glds16<false>(xp + 16 * 1280, a_voff1, d + 3072); glds16<false>(wp + 16 * 3840, w_voff1, d + 16384 + 3072);
  };
  st(IC<0>{}); st(IC<1>{});
  sfor<10>([&](auto s_) { constexpr int s = s_;
    waitv<(s == 0 ? 8 : s <= 7 ? 16 : s == 8 ? 8 : 0)>(); asm volatile("s_barrier" ::: "memory");
    if constexpr (s == 0) { st(IC<2>{}); st(IC<3>{}); }
    else if constexpr (s + 3 < 10) st(IC<s + 3>{}); });
  bar();
}
template <bool ISV>
DI void p1_body(const u16* __restrict__ x, char* __restrict__ ws, const unsigned lds0, const int w, int rb, int cb, u32x2& tag) {
  const int lane = fresh_lane();
  u32x2 ep; gld8<0>(ep, ws + WS_CTL, 0u);
  RowStat rs[2];
  rowstat_get(rs[0], x, blockIdx.x * 8 + w * 2, lane); rowstat_get(rs[1], x, blockIdx.x * 8 + w * 2 + 1, lane);
  const int wm = w >> 1, wn = w & 1;
  const int r32 = lane & 31, hh = lane >> 5;
  STAMP_AT(0, 0);
  unsigned xa[3][4], wa[3][2][2];
  {
    const int row = wm * 64 + r32;
    const int blk = (lane >> 4) & 1, q = (lane & 15) >> 2, p = lane & 3;
#pragma unroll
    for (int hs = 0; hs < 3; ++hs) {
#pragma unroll
      for (int i = 0; i < 4; ++i) { xa[hs][i] = lds0 + hs * 65536 + (unsigned)(row * 128 + ((((i ^ wm) * 2 + hh) ^ ((row >> 1) & 7)) << 4)); pin(xa[hs][i]); }
#pragma unroll
      for (int j = 0; j < 2; ++j) {
        const unsigned base = lds0 + hs * 65536 + (unsigned)(16384 + wn * 8192 + (8 * hh + q) * 128 + (((j * 4 + blk * 2 + (p >> 1)) ^ (((q >> 1) & 1) << 2)) << 4) + (p & 1) * 8);
        wa[hs][j][0] = base + wm * 2048; wa[hs][j][1] = base + (1 ^ wm) * 2048; pin(wa[hs][j][0]); pin(wa[hs][j][1]);
      }
    }
  }
  f32x16 acc[2][2] = {};
  f32x4 sw[2] = {};
  u32x4 rec;
  bf16x8 sel = (bf16x8)(short)(((lane & 15) == ((lane >> 4) & 1)) ? 0x3f80 : 0); pin(sel);
  bf16x8 fx[2][2]; s16x4 fwl[2][2], fwh[2][2];
  auto rd = [&](auto i_, auto k_) {
    constexpr int i = i_, k = k_, s = i >> 2, p = i & 3, sl = s % 5, hs = sl >> 1; constexpr unsigned so = (sl & 1) * K1_STAGE;
    if constexpr (k == 0) fx[i & 1][0] = lds128(xa[hs][p] + so);
    if constexpr (k == 3) fx[i & 1][1] = lds128(xa[hs][p] + so + 4096);
    if constexpr (k == 1 || k == 2) { fwl[i & 1][k - 1] = ldstr(wa[hs][k - 1][p & 1] + so + (p >> 1) * 4096); fwh[i & 1][k - 1] = ldstr(wa[hs][k - 1][p & 1] + so + (p >> 1) * 4096 + 512); }
  };
  const bool isq = cb < 5;
  const int pitch = isq ? 1280 : 2560;
  unsigned off[2][ISV ? 1 : 4];
#pragma unroll
  for (int j = 0; j < 2; ++j) {
    if constexpr (ISV) {
      const int n = cb * 128 + wn * 64 + j * 32 + r32 - 1280, head = n / 20, d0 = n - head * 20;
      const int d = (head & 1) ? (d0 < 4 ? d0 + 16 : d0 - 4) : d0;
      off[j][0] = (unsigned)(head * 163840 + 1280 + (hh * 20 + d) * 16); pin(off[j][0]);
    } else {
#pragma unroll
      for (int g = 0; g < 4; ++g) {
        const int n = cb * 128 + wn * 64 + j * 32 + 8 * g + 4 * hh - (isq ? 0 : 640), head = n / 20, d0 = n - head * 20;
        const int d = (head & 1) ? (d0 < 4 ? d0 + 16 : d0 - 4) : d0;
        off[j][g] = (unsigned)(head * 64 * pitch + (d < 16 ? (((d >> 3) * 32 + r32) * 16 + (d & 4) * 2) : (1024 + r32 * 8))); pin(off[j][g]);
      }
    }
  }
  waitv<0>(); pin(ep); tag = make_tag(ep);
  rowstat_put(rs[0], ws, blockIdx.x * 8 + w * 2, lane, tag); rowstat_put(rs[1], ws, blockIdx.x * 8 + w * 2 + 1, lane, tag);
  asm volatile("s_barrier" ::: "memory");
  STAMP_AT(0, 1);
  sfor<4>([&](auto k) { rd(IC<0>{}, k); });
  sfor<40>([&](auto i_) {
    constexpr int i = i_, b = i & 1;
    if constexpr ((i & 3) == 3 && i < 39) asm volatile("s_barrier" ::: "memory");
    if constexpr (i == 30) gld16<0>(rec, ws + WS_RST + (size_t)(rb * 128 + wm * 64) * 16, (unsigned)(lane * 16));
    SB();
    const bf16x8 w0 = __builtin_shufflevector(fwl[b][0], fwh[b][0], 0, 1, 2, 3, 4, 5, 6, 7), w1 = __builtin_shufflevector(fwl[b][1], fwh[b][1], 0, 1, 2, 3, 4, 5, 6, 7);
    sfor<4>([&](auto k_) { constexpr int k = k_, t = k >> 1, j = (k ^ (k >> 1)) & 1;
      if constexpr (ISV) acc[t][j] = MFMA32(fx[b][t], j ? w1 : w0, acc[t][j]); else acc[t][j] = MFMA32(j ? w1 : w0, fx[b][t], acc[t][j]);
      SB();
      if constexpr (i < 39) rd(IC<i + 1>{}, k_);
      SB(); });
    if constexpr (b == 1) { sw[0] = MFMA16(w0, sel, sw[0]); sw[1] = MFMA16(w1, sel, sw[1]); }
    SB();
  });
  STAMP_AT(0, 11);
  LAS float* csp = (LAS float*)(uintptr_t)lds0;
  if ((lane & 15) < 2) sfor<2>([&](auto j) { *(LAS f32x4*)&csp[wm * 128 + wn * 64 + j * 32 + (lane & 1) * 16 + (lane >> 4) * 4] = sw[j]; });
  waitv<0>(); pin(rec);
#pragma unroll 1
  for (int spin = 0; spin < (1 << 17) && !__all((rec[2] ^ rec[0] ^ RS_K0) == tag[0] && (rec[3] ^ rec[1] ^ RS_K1) == tag[1]); ++spin) {
    gld16<0>(rec, ws + WS_RST + (size_t)(rb * 128 + wm * 64) * 16, (unsigned)(lane * 16)); waitv<0>(); pin(rec);
  }
  LAS f32x2* st = (LAS f32x2*)(uintptr_t)(lds0 + 4096 + w * 512);
  if constexpr (ISV) st[lane] = (f32x2){__uint_as_float(rec[0]), __uint_as_float(rec[1])};
  bar();
  STAMP_AT(0, 12);
  if constexpr (ISV) {
    float c[2];
#pragma unroll
    for (int j = 0; j < 2; ++j) c[j] = csp[wn * 64 + j * 32 + r32] + csp[128 + wn * 64 + j * 32 + r32];
#pragma unroll
    for (int t = 0; t < 2; ++t) {
      f32x4 s01[4], s23[4];
#pragma unroll
      for (int g = 0; g < 4; ++g) { s01[g] = *(const LAS f32x4*)&st[t * 32 + 8 * g + 4 * hh]; s23[g] = *(const LAS f32x4*)&st[t * 32 + 8 * g + 4 * hh + 2]; }
      const char* dst = ws + WS_KV + (size_t)(rb * 4 + wm * 2 + t) * 2560;
#pragma unroll
      for (int j = 0; j < 2; ++j) {
        const f32x16& a = acc[t][j];
        unsigned pk[8];
#pragma unroll
        for (int g = 0; g < 4; ++g) {
          pk[2 * g]     = pk_bf16(s01[g][1] * (a[4 * g] - s01[g][0] * c[j]), s01[g][3] * (a[4 * g + 1] - s01[g][2] * c[j]));
          pk[2 * g + 1] = pk_bf16(s23[g][1] * (a[4 * g + 2] - s23[g][0] * c[j]), s23[g][3] * (a[4 * g + 3] - s23[g][2] * c[j]));
        }
        st16s(dst, off[j][0], (u32x4){pk[0], pk[1], pk[2], pk[3]});
        st16s(dst + 640, off[j][0], (u32x4){pk[4], pk[5], pk[6], pk[7]});
      }
    }
  } else {
    const float sc = isq ? QSCALE : 1.f;
    const char* base = ws + (isq ? WS_Q : WS_KV) + (size_t)(rb * 4 + wm * 2) * pitch;
    const auto pm = __builtin_amdgcn_permlane32_swap(rec[0], rec[0], false, false), pr = __builtin_amdgcn_permlane32_swap(rec[1], rec[1], false, false);
#pragma unroll
    for (int j = 0; j < 2; ++j)
#pragma unroll
      for (int g = 0; g < 4; ++g) {
        const int nl = wn * 64 + j * 32 + 8 * g + 4 * hh;
        const f32x4 c = *(const LAS f32x4*)&csp[nl] + *(const LAS f32x4*)&csp[128 + nl];
#pragma unroll
        for (int t = 0; t < 2; ++t) {
          const f32x16& a = acc[t][j];
          const float rs_ = __uint_as_float(pr[t]) * sc, mr = __uint_as_float(pm[t]);
          st8s(base + t * pitch, off[j][g], (u32x2){pk_bf16(rs_ * (a[4 * g] - mr * c[0]), rs_ * (a[4 * g + 1] - mr * c[1])), pk_bf16(rs_ * (a[4 * g + 2] - mr * c[2]), rs_ * (a[4 * g + 3] - mr * c[3]))});
        }
      }
  }
  STAMP_AT(0, 13);
  waitv<0>();
  if (lane < 16) st8(ws + WS_CTL + CTL_F1 + ((cb * 4 + wn * 2 + (lane >> 3)) * 32 + rb * 2 + wm) * 8 + (lane & 7) * CTL_REP, tag);
}

DI void p1_repack(const u16* __restrict__ Wo, char* __restrict__ ws, const int w, int idx) {
  const int lane = fresh_lane();
  const unsigned pvo = (lane >> 4) * 8 * 1280 + (lane & 15) * 2;
  unsigned pw[7][8];
  sfor<7>([&](auto r) {
    const int f = idx * 50 + min(w + 8 * r, 49), cbk = f / 100, rem = f - cbk * 100, ks = rem / 5, ct = rem - ks * 5;
    const char* src = (const char*)(Wo + (size_t)(32 * ks) * 640 + 80 * cbk + 16 * ct);
    sfor<8>([&](auto i) { gldu16<(i & 3) * 1280>(pw[r][i], src + (i & ~3) * 1280, pvo); });
  });
  sfor<7>([&](auto r) {
    waitv<(6 - r) * 8>();
    sfor<8>([&](auto j) { pin(pw[r][j]); });
    const u32x4 v = {pw[r][0] | (pw[r][1] << 16), pw[r][2] | (pw[r][3] << 16), pw[r][4] | (pw[r][5] << 16), pw[r][6] | (pw[r][7] << 16)};
    pw[r][0] = v[0]; pw[r][1] = v[1]; pw[r][2] = v[2]; pw[r][3] = v[3];
  });
  sfor<7>([&](auto r) {
    if (w + 8 * r < 50) st16(ws + WS_WP + (size_t)(idx * 50 + w + 8 * r) * 1024 + lane * 16, (u32x4){pw[r][0], pw[r][1], pw[r][2], pw[r][3]});
  });
}

template <int OFF> DI void rdK(bf16x8& k0, s16x4& k1, unsigned a0, unsigned a1) {
  asm volatile("ds_read_b128 %0, %2 offset:%4\n\tds_read_b64 %1, %3 offset:%4" : "=&v"(k0), "=&v"(k1) : "v"(a0), "v"(a1), "i"(OFF));
}
template <int OFF> DI void rdV(bf16x8& v0, bf16x8& v1, unsigned a, unsigned long long msk) {
  asm volatile("s_mov_b64 exec, %5\n\tds_read_b128 %0, %2 offset:%3\n\tds_read_b128 %1, %2 offset:%4\n\ts_mov_b64 exec, -1"
               : "+v"(v0), "+v"(v1) : "v"(a), "i"(OFF), "i"(OFF + 640), "s"(msk));
}
#define EX2(v) __builtin_amdgcn_exp2f(v)
constexpr int wo_off(int p) { return (p < 60 ? p : p + 20) * 1024; }
constexpr int gu_off(int f) { return (f < 20 ? f + 60 : f < 60 ? f + 100 : f - 60) * 1024; }
constexpr int wd_off(int f) { return ((f / 20 + 4) % 8) * 20480 + (f % 20) * 1024; }
constexpr int CSF_OFF = 116 * 1024, XCH_OFF = 120 * 1024;

DI void p2_attn(char* __restrict__ ws, const u16* __restrict__ Wg, const u16* __restrict__ Wu, const u16* __restrict__ Wd,
                const unsigned lds0, const int w, const u32x2 tag, const int rb3, const int cb3, const int rb4, const int cb4) {
  const int lane = fresh_lane();
  const int kh = w >> 2, wq = w & 3;
  const int head = (blockIdx.x & 7) * 4 + ((blockIdx.x >> 3) & 3), qb = blockIdx.x >> 5;
  const int r32 = lane & 31, hh = lane >> 5;
  const char* kvg = ws + WS_KV + (size_t)head * 163840 + (size_t)kh * 81920 + wq * 1024;
  const unsigned voff = lane * 16;
  const unsigned kvl = lds0 + kh * 81920 + wq * 1024;
  char* ctl = ws + WS_CTL;
  const char* ctr = ctl + (blockIdx.x & 7) * CTL_REP;
  unsigned pw[3][8];
  const int gw = blockIdx.x * 8 + w, gwc = min(gw, 1919);
  const char* psrc[3];
#pragma unroll
  for (int r = 0; r < 2; ++r) {
    const int f = 2 * gwc + r, cbk = f / 120, rem = f - cbk * 120, ks = rem / 6, ct = rem - ks * 6;
    psrc[r] = (const char*)((ct < 3 ? Wg : Wu) + (size_t)(32 * ks) * 1536 + 48 * cbk + 16 * (ct < 3 ? ct : ct - 3));
  }
  { const int f = gwc, cbk = f / 240, rem = f - cbk * 240, ks = rem / 5, ct = rem - ks * 5;
    psrc[2] = (const char*)(Wd + (size_t)(32 * ks) * 640 + 80 * cbk + 16 * ct); }
  unsigned pvo1 = (lane >> 4) * 8 * 3072 + (lane & 15) * 2, pvo2 = (lane >> 4) * 8 * 1280 + (lane & 15) * 2; pin(pvo1); pin(pvo2);
  const char* wog = ws + WS_WP + (size_t)cb3 * 102400;
  const int qtile = qb * 8 + wq * 2 + kh, cg0 = head * 5 + (head & 1), cgt = (head & 1) ? head * 5 : head * 5 + 4;
  const char* ob = ws + WS_O + (size_t)qtile * 40960;
  const char* obs[3] = {ob + (cg0 >> 3) * 1024 + ((cg0 & 7) >> 1) * 256, ob + ((cg0 + 2) >> 3) * 1024 + (((cg0 + 2) & 7) >> 1) * 256,
                        ob + (cgt >> 3) * 1024 + ((cgt & 7) >> 1) * 256 + (cgt & 1) * 8};
  unsigned vo = (unsigned)((r32 >> 4) * 20480 + (r32 & 15) * 16 + hh * 8); pin(vo);
  u32x4 fill = {0xffffffffu, 0xffffffffu, 0xffffffffu, 0xffffffffu}; pin(fill);
  const u32x2 fill2 = {fill[0], fill[1]};
  u32x2 fa, fb, fc;
  const unsigned fvc = (unsigned)CTL_FR + (unsigned)((cb4 * 60 + min(lane, 59)) * 8);
  const unsigned fva = (unsigned)(hh ? CTL_F4 : CTL_F2) + (unsigned)(((rb3 * 2 + ((w & 3) >> 1)) * 32 + r32) * 8), fvb = (unsigned)CTL_F3 + (unsigned)(((rb4 * 16 + w * 2) * 8 + (lane & 15)) * 8);
  const char* gcl = ws + WS_G + (size_t)(rb4 * 16 + w * 2) * 49152 + cb4 * 1536;
  const char* hcl = ws + WS_H + (size_t)(rb3 * 4 + w) * 20480 + cb3 * 2560;
  const char* rdst = ws + WS_WP + (size_t)(NFR_WO + 2 * gw) * 1024;
  pins(psrc[0]); pins(psrc[1]); pins(psrc[2]); pins(obs[0]); pins(obs[1]); pins(obs[2]); pins(wog); pins(gcl); pins(hcl); pins(rdst);
  STAMP_AT(1, 0);
  unsigned f0;
  {
    const int cfl = (20 * head) >> 5, cfh = (20 * head + 19) >> 5;
    f0 = (unsigned)(((((lane & 32) ? 40 : 20) + ((lane & 16) ? cfh : cfl)) * 32 + kh * 16 + (lane & 15)) * 8);
    const unsigned f1 = (unsigned)((lane < 32 ? (((lane & 1) ? cfh : cfl) * 32 + qb * 4 + wq) : 1920 + cb3 * 16 + (lane & 15)) * 8);
    poll2(ctr + CTL_F1, f0 & ~(12u * 8u), f1, tag);
  }
  STAMP_AT(1, 4);
#pragma unroll
  for (int i = 0; i < 4; ++i) glds16<true>(kvg + i * 4096, voff, kvl + i * 4096);
  bf16x8 q0[2]; s16x4 q1[2];
  {
    const char* qg = ws + WS_Q + ((size_t)head * 64 + qb * 8 + wq * 2) * 1280;
    gld16<0>(q0[0], qg, voff); gld16<1280>(q0[1], qg, voff); gld8<1024>(q1[0], qg, r32 * 8); gld8<2304>(q1[1], qg, r32 * 8);
  }
  const unsigned ka0 = lds0 + kh * 81920 + lane * 16, ka1 = lds0 + kh * 81920 + 1024 + r32 * 8, va = lds0 + kh * 81920 + 1280 + (hh * 20 + (r32 < 20 ? r32 : 0)) * 16;
  bf16x8 K0[2], V0[2], V1[2]; s16x4 K1[2];
  V0[0] = V0[1] = V1[0] = V1[1] = (bf16x8)(short)(r32 == 20 ? 0x3f80 : 0);
  pin(V0[0]); pin(V0[1]); pin(V1[0]); pin(V1[1]);
  WAITV_BAR(0);
  u32x2 fr; gld8<0>(fr, ctr + CTL_F1, f0);
  pin(q0[0]); pin(q0[1]); pin(q1[0]); pin(q1[1]);
  if (hh) { q1[0] = (s16x4){0, 0, 0, 0}; q1[1] = (s16x4){0, 0, 0, 0}; }
  STAMP_AT(1, 1);
  rdK<0>(K0[0], K1[0], ka0, ka1);
  const unsigned long long vmsk = 0x000fffff000fffffull;
  rdV<0>(V0[0], V1[0], va, vmsk);
  f32x16 cm[2];
  {
    const bf16x8 kf0 = lds128(lds0 + lane * 16); const s16x4 kf1 = lds64(lds0 + 1024 + r32 * 8);
#pragma unroll
    for (int t = 0; t < 2; ++t) {
      f32x16 s = MFMA32(kf0, q0[t], (f32x16){}); s = MFMA32K8(kf1, q1[t], s);
      float m = s[0];
#pragma unroll
      for (int i = 1; i < 16; ++i) m = fmaxf(m, s[i]);
      m = max32(m);
      cm[t] = (f32x16)(-m);
      pin(cm[t]);
    }
  }
  asm volatile("s_waitcnt lgkmcnt(0)" : "+v"(K0[0]), "+v"(K1[0]), "+v"(V0[0]), "+v"(V1[0]));
  f32x16 o[2] = {};
  f32x16 SA, SB_;
  u32x4 PA0, PA1, PB0 = {}, PB1 = {};
  SA = MFMA32(K0[0], q0[0], cm[0]); SA = MFMA32K8(K1[0], q1[0], SA);
  waitv<0>(); pin(fr);
  if (!__all(fr[0] == tag[0] && fr[1] == tag[1])) poll(ctr + CTL_F1, f0, tag);
  STAMP_AT(1, 2);
  pin(o[0]); pin(o[1]); pin(PB0); pin(PB1);
#define GRP(MF, Sc, Pc, i, j) do { MF; SB(); const float e0 = EX2(Sc[i]), e1 = EX2(Sc[i + 1]), e2 = EX2(Sc[i + 2]), e3 = EX2(Sc[i + 3]); \
    Pc[j] = pk_bf16(e0, e1); Pc[j + 1] = pk_bf16(e2, e3); pin(Pc); SB(); } while (0)
#define SLOT(QK, Sc, Pc0, Pc1, Sn, kb, jn, Pp0, Pp1, vb, jp) do { \
    GRP(if constexpr (QK) Sn = MFMA32(K0[kb], q0[jn], cm[jn]), Sc, Pc0, 0, 0); \
    GRP(if constexpr (QK) Sn = MFMA32K8(K1[kb], q1[jn], Sn), Sc, Pc0, 4, 2); \
    GRP(o[jp] = MFMA32(V0[vb], __builtin_bit_cast(bf16x8, Pp0), o[jp]), Sc, Pc1, 8, 0); \
    GRP(o[jp] = MFMA32(V1[vb], __builtin_bit_cast(bf16x8, Pp1), o[jp]), Sc, Pc1, 12, 2); } while (0)
#define LWAIT(kb, vb) asm volatile("s_waitcnt lgkmcnt(0)" : "+v"(K0[kb]), "+v"(K1[kb]), "+v"(V0[vb]), "+v"(V1[vb]))
  auto rstore = [&](auto r_, auto yv_) {
    constexpr int r = r_;
    waitv<yv_>();
    sfor<8>([&](auto j) { pin(pw[r][j]); });
    const u32x4 v = {pw[r][0] | (pw[r][1] << 16), pw[r][2] | (pw[r][3] << 16), pw[r][4] | (pw[r][5] << 16), pw[r][6] | (pw[r][7] << 16)};
    if (gw < 1920) {
      if constexpr (r < 2) {
        st16s(rdst + r * 1024, voff, v);
        float c = 0.f;
#pragma unroll
        for (int j = 0; j < 4; ++j) c = dot2(v[j], 0x3f803f80u, c);
        c = red4(c);
        if (lane < 16) st4(ws + WS_CSP + (size_t)(2 * gw + r) * 64 + lane * 4, c);
      } else st16s(ws + WS_WP + (size_t)(NFR_WO + NFR_GU + gw) * 1024, voff, v);
    }
  };
  auto hook = [&](auto it_, auto i_) {
    constexpr int it = it_, i = i_, n = (it - 2) * 8 + i;
    if constexpr (n >= 0 && n < 2) st8s(obs[n], vo, fill2);
    if constexpr (n == 2) { if (!hh) st8s(obs[2], vo, fill2); }
    if constexpr (n >= 3 && n < 6) { const int L = (n - 3) * 64 + lane; st16s(gcl, (unsigned)(L * 16 + (L >= 96 ? 49152 - 1536 : 0)), fill); }
    if constexpr (n >= 6 && n < 9) { if (w < 4) st16s(hcl, (unsigned)(min((n - 6) * 64 + lane, 159) * 16), fill); }
    if constexpr (n == 9) { if (w < 4 && lane < 16) st8s(ws + WS_HST + (size_t)(rb3 * 4 + w) * 1024 + (cb3 >> 1) * 256 + (cb3 & 1) * 8, (unsigned)(lane * 16), fill2); }
    if constexpr (it == 5 && i == 6) {
      waitv<0>();
      setflag(ctl + CTL_FR + gw * 8, tag, lane);
      setflag(ctl + CTL_F2 + (qtile * 32 + head) * 8, tag, lane);
      setflag(ctl + CTL_F4 + ((rb4 * 8 + w) * 32 + cb4) * 8, tag, lane);
      if (w < 4) setflag(ctl + CTL_F3 + ((rb3 * 4 + w) * 8 + cb3) * 8, tag, lane);
    }
    if constexpr (it == 7 && i == 6) { gld8<0>(fa, ctr, fva); gld8<0>(fb, ctr, fvb); gld8<0>(fc, ctr, fvc); }
    if constexpr (it < 2) glds16<true>(kvg + (size_t)(it * 8 + 4 + i) * 4096, voff, kvl + (it * 8 + 4 + i) * 4096);
    else if constexpr (it < 4) gldu16<(i & 1) * 3072>(pw[it - 2][i], psrc[it - 2] + (i & ~1) * 3072, pvo1);
    else if constexpr (it == 4) {
      gldu16<(i & 3) * 1280>(pw[2][i], psrc[2] + (i & ~3) * 1280, pvo2);
      if constexpr (i == 0) rstore(IC<0>{}, IC<9>{});
      if constexpr (i == 7) rstore(IC<1>{}, IC<8>{});
    } else if constexpr (it == 5) {
      if constexpr (i == 7) rstore(IC<2>{}, IC<3>{});
    } else {
      constexpr int j = (it - 6) * 8 + i;
      const char* src = wog + (w + 8 * j) * 1024;
      if constexpr (j < 7) glds16<true>(src, voff, lds0 + w * 1024 + j * 8192);
      else if constexpr (j == 7) glds16<true>(src, voff, lds0 + w * 1024 + j * 8192 + (w >= 4 ? 20480 : 0));
      else if constexpr (j < 12) glds16<true>(src, voff, lds0 + w * 1024 + j * 8192 + 20480);
      else if constexpr (j == 12) { if (w < 4) glds16<true>(src, voff, lds0 + w * 1024 + j * 8192 + 20480); }
    }
    SB();
  };
  const unsigned ka0b = ka0 + 40960, ka1b = ka1 + 40960, vab = va + 40960;
  sfor<8>([&](auto it_) {
    constexpr int it = it_;
    if constexpr (it == 1) WAITV_BAR(6);
    if constexpr (it == 2) { WAITV_BAR(8); STAMP_AT(1, 3); }
    if constexpr (it == 4) asm volatile("s_waitcnt vmcnt(16)\n\ts_barrier" ::: "memory");
    if constexpr (it == 6) asm volatile("s_barrier" ::: "memory");
    if (((it ^ kh) & 1) == 0) __builtin_amdgcn_s_setprio(0); else __builtin_amdgcn_s_setprio(1);
    constexpr int o0 = (it & 3) * 10240;
    constexpr int on = it == 3 ? 0 : o0 + 10240;
    const unsigned a0 = it < 4 ? ka0 : ka0b, a1 = it < 4 ? ka1 : ka1b, av = it < 4 ? va : vab;
    const unsigned n0 = it < 3 ? ka0 : ka0b, n1 = it < 3 ? ka1 : ka1b, nv = it < 3 ? va : vab;
    rdK<o0 + 2560>(K0[1], K1[1], a0, a1); hook(it_, IC<0>{});                  SLOT(true, SA, PA0, PA1, SB_, 0, 1, PB0, PB1, 1, 1);
    LWAIT(1, 0); rdV<o0 + 2560>(V0[1], V1[1], av, vmsk); hook(it_, IC<1>{});   SLOT(true, SB_, PB0, PB1, SA, 1, 0, PA0, PA1, 0, 0);
    rdK<o0 + 5120>(K0[0], K1[0], a0, a1); hook(it_, IC<2>{});                  SLOT(true, SA, PA0, PA1, SB_, 1, 1, PB0, PB1, 0, 1);
    LWAIT(0, 1); rdV<o0 + 5120>(V0[0], V1[0], av, vmsk); hook(it_, IC<3>{});   SLOT(true, SB_, PB0, PB1, SA, 0, 0, PA0, PA1, 1, 0);
    rdK<o0 + 7680>(K0[1], K1[1], a0, a1); hook(it_, IC<4>{});                  SLOT(true, SA, PA0, PA1, SB_, 0, 1, PB0, PB1, 1, 1);
    LWAIT(1, 0); rdV<o0 + 7680>(V0[1], V1[1], av, vmsk); hook(it_, IC<5>{});   SLOT(true, SB_, PB0, PB1, SA, 1, 0, PA0, PA1, 0, 0);
    if constexpr (it < 7) rdK<on>(K0[0], K1[0], n0, n1);
    hook(it_, IC<6>{});                                                        SLOT(true, SA, PA0, PA1, SB_, 1, 1, PB0, PB1, 0, 1);
    LWAIT(0, 1);
    if constexpr (it < 7) rdV<on>(V0[0], V1[0], nv, vmsk);
    hook(it_, IC<7>{});                                                        SLOT(it < 7, SB_, PB0, PB1, SA, 0, 0, PA0, PA1, 1, 0);
  });
  __builtin_amdgcn_s_setprio(0);
  o[1] = MFMA32(V0[1], __builtin_bit_cast(bf16x8, PB0), o[1]);
  o[1] = MFMA32(V1[1], __builtin_bit_cast(bf16x8, PB1), o[1]);
  STAMP_AT(1, 9);
  bar();
  {
    LAS f32x4* xch = (LAS f32x4*)(uintptr_t)(lds0 + XCH_OFF + w * 3072);
    const f32x16& snd = kh ? o[0] : o[1];
#pragma unroll
    for (int g = 0; g < 3; ++g) xch[g * 64 + lane] = (f32x4){snd[4 * g], snd[4 * g + 1], snd[4 * g + 2], snd[4 * g + 3]};
  }
  bar();
  STAMP_AT(1, 10);
  float of[12];
  {
    const LAS f32x4* xch = (const LAS f32x4*)(uintptr_t)(lds0 + XCH_OFF + (w ^ 4) * 3072);
    const f32x16& own = kh ? o[1] : o[0];
#pragma unroll
    for (int g = 0; g < 3; ++g) { const f32x4 v = xch[g * 64 + lane];
#pragma unroll
      for (int j = 0; j < 4; ++j) of[4 * g + j] = own[4 * g + j] + v[j]; }
  }
  float l = hi32(of[8]);
  bool ok = (l > 8.67e-19f) && (l < 1.15e18f);
#pragma unroll
  for (int i = 0; i < 8; ++i) ok = ok && (fabsf(of[i]) < 1e37f);
  if (!hh) {
#pragma unroll
    for (int i = 8; i < 12; ++i) ok = ok && (fabsf(of[i]) < 1e37f);
  }
  if (__builtin_expect(!__all(ok), 0)) {
    __builtin_amdgcn_fence(__ATOMIC_ACQUIRE, "agent");
    const char* kv = ws + WS_KV + (size_t)head * 163840;
    const bf16x8 fq0 = kh ? q0[1] : q0[0];
    const s16x4 fq1 = kh ? q1[1] : q1[0];
    float m = -INFINITY;
    for (int t = 0; t < 64; ++t) {
      const bf16x8 kf0 = *(const bf16x8*)(kv + t * 2560 + lane * 16); const s16x4 kf1 = *(const s16x4*)(kv + t * 2560 + 1024 + r32 * 8);
      f32x16 s = MFMA32(kf0, fq0, (f32x16){}); s = MFMA32K8(kf1, fq1, s);
#pragma unroll
      for (int i = 0; i < 16; ++i) m = fmaxf(m, s[i]);
    }
    m = max32(m);
    f32x16 oo = {};
    const int vr = r32 < 20 ? r32 : 0;
    for (int t = 0; t < 64; ++t) {
      const bf16x8 kf0 = *(const bf16x8*)(kv + t * 2560 + lane * 16); const s16x4 kf1 = *(const s16x4*)(kv + t * 2560 + 1024 + r32 * 8);
      bf16x8 vf0 = *(const bf16x8*)(kv + t * 2560 + 1280 + (hh * 20 + vr) * 16), vf1 = *(const bf16x8*)(kv + t * 2560 + 1920 + (hh * 20 + vr) * 16);
      if (r32 >= 20) { vf0 = (bf16x8)(short)0x3f80; vf1 = vf0; }
      f32x16 s = MFMA32(kf0, fq0, (f32x16){}); s = MFMA32K8(kf1, fq1, s);
      u32x4 p0, p1;
#pragma unroll
      for (int i = 0; i < 4; ++i) {
        p0[i] = pk_bf16(EX2(s[2 * i] - m), EX2(s[2 * i + 1] - m));
        p1[i] = pk_bf16(EX2(s[8 + 2 * i] - m), EX2(s[8 + 2 * i + 1] - m));
      }
      oo = MFMA32(vf0, __builtin_bit_cast(bf16x8, p0), oo);
      oo = MFMA32(vf1, __builtin_bit_cast(bf16x8, p1), oo);
    }
#pragma unroll
    for (int i = 0; i < 12; ++i) of[i] = oo[i];
    l = hi32(of[8]);
  }
  const float rl = __builtin_amdgcn_rcpf(l);
  waitv<0>(); pin(fa); pin(fb); pin(fc);
  setflag(ctl + CTL_FD + gw * 8, tag, lane);
#pragma unroll
  for (int g = 0; g < 3; ++g)
    if (g < 2 || !hh) st8s(obs[g], vo, nofill(pk_bf16(of[4 * g] * rl, of[4 * g + 1] * rl), pk_bf16(of[4 * g + 2] * rl, of[4 * g + 3] * rl)));
  STAMP_AT(1, 11);
  if (!__all(fa[0] == tag[0] && fa[1] == tag[1] && fb[0] == tag[0] && fb[1] == tag[1])) poll2(ctr, fva, fvb, tag);
  if (!__all(fc[0] == tag[0] && fc[1] == tag[1])) poll(ctr, fvc, tag);
  STAMP_AT(1, 12);
}

constexpr int x_none(int) { return 0; }
constexpr int x_p4(int i) { return ((i >= 2 && i < 7) || (i >= 10 && i < 18)) ? 1 : 0; }
template <int NSTEP, int NF, int LA, int LL, int CG, int NA, int ASTR, int (*BOFF)(int), int (*XCNT)(int), class GATE, class PRE, class MM>
DI void gemm_res(const char* asrc, unsigned lds0, unsigned voff, GATE&& gate, PRE&& pre, MM&& mm) {
  bf16x8 a[NSTEP][NA];
  bf16x8 bf[LL + 1][NF];
  unsigned rd0 = lds0 + voff, rd1 = rd0 + 65536, rd2 = rd0 + 131072; pin(rd0); pin(rd1); pin(rd2);
  auto issue = [&](auto n_) { constexpr int n = n_; sfor<NA>([&](auto j) { gld16<(n & 3) * 1024>(a[n][j], asrc + (n >> 2) * 4096 + j * ASTR, voff); }); };
  constexpr auto xsum = [](int lo, int hi) { int c = 0; for (int i = lo < 0 ? 0 : lo; i <= hi; ++i) c += XCNT(i); return c; };
  auto rd = [&](auto i_, auto f_) {
    constexpr int i = i_, f = f_, off = BOFF(i * NF + f);
    bf[i % (LL + 1)][f] = lds128((off < 65536 ? rd0 : off < 131072 ? rd1 : rd2) + (off & 65535));
  };
  static_assert(NSTEP % CG == 0 && LA > CG, "groups");
  unsigned m = 0;
  auto fold = [&](auto n_) { constexpr int n = n_;
    sfor<NA>([&](auto j) { pin(a[n][j]); const u32x4 u = __builtin_bit_cast(u32x4, a[n][j]); m = max(max(m, u[0]), u[1]); m = max(max(m, u[2]), u[3]); });
    pin(m);
    if constexpr (n % CG == CG - 1) {
      if (__builtin_expect(__any(m == 0xffffffffu), 0)) {
#pragma unroll 1
        for (int spin = 0; spin < (1 << 17); ++spin) {
          RETRY_COUNT(NSTEP + NF);
          sfor<CG>([&](auto k) { issue(IC<n - CG + 1 + k>{}); });
          waitv<0>(); m = 0;
          sfor<CG>([&](auto k) { sfor<NA>([&](auto j) { pin(a[n - CG + 1 + k][j]); m = max(m, umax4(__builtin_bit_cast(u32x4, a[n - CG + 1 + k][j]))); }); });
          if (!__any(m == 0xffffffffu)) break;
        }
      }
    }
  };
  gate(IC<0>{});
  sfor<LA>([&](auto n) { issue(n); });
  waitv<LA * NA>();
  if (gate(IC<1>{})) sfor<LA>([&](auto n) { issue(n); });
  pre(IC<-1>{});
  sfor<LL>([&](auto i) { sfor<NF>([&](auto f) { rd(i, f); }); });
  waitv<(LA - CG) * NA>(); sfor<CG>([&](auto n) { fold(n); });
  sfor<NSTEP>([&](auto i_) {
    constexpr int i = i_;
    pre(i_);
    SB();
    sfor<NF>([&](auto f_) {
      constexpr int f = f_;
      if constexpr (f == NF - 1 && i + CG < NSTEP) {
        waitv<((i + LA < NSTEP ? i + LA : NSTEP - 1) - (i + CG)) * NA + xsum(i + CG - LA + 1, i)>(); fold(IC<i + CG>{}); SB(); }
      mm(f_, bf[i % (LL + 1)][f], a[i]);
      SB();
      if constexpr (i + LL < NSTEP) rd(IC<i + LL>{}, f_);
      if constexpr (f == 0 && i + LA < NSTEP) issue(IC<i + LA>{});
      SB();
    });
  });
}

__global__ __launch_bounds__(512, 2) void k_mega(const u16* __restrict__ x, const u16* __restrict__ Wqkv, const u16* __restrict__ Wo,
                                                 const u16* __restrict__ Wg, const u16* __restrict__ Wu, const u16* __restrict__ Wd,
                                                 char* __restrict__ ws, u16* __restrict__ out) {
  __shared__ __attribute__((aligned(16))) char smem[163840];
  const unsigned lds0 = lds_addr_of(smem);
  const int w = __builtin_amdgcn_readfirstlane(threadIdx.x >> 6);
  const int g8 = blockIdx.x & 7, i32 = blockIdx.x >> 3;
  char* ctl = ws + WS_CTL;
  const char* ctr = ctl + g8 * CTL_REP;
  u32x2 tag;
  if (i32 < 30) {
    const int i = (g8 & 1) * 30 + i32, rb = (g8 >> 1) * 4 + (i & 3), cb = i >> 2;
    if (w >= 4) { p1_load(x, Wqkv, lds0, w - 4, rb, cb); u32x2 ep; gld8<0>(ep, ctl, 0u); waitv<0>(); pin(ep); tag = make_tag(ep); }
    else if (cb >= 10) p1_body<true>(x, ws, lds0, w, rb, cb, tag);
    else p1_body<false>(x, ws, lds0, w, rb, cb, tag);
  } else {
    { RowStat rs; u32x2 ep; const int lane = fresh_lane(); gld8<0>(ep, ctl, 0u); rowstat_get(rs, x, blockIdx.x * 8 + w, lane); waitv<0>(); pin(ep); tag = make_tag(ep);
      rowstat_put(rs, ws, blockIdx.x * 8 + w, lane, tag); }
    p1_repack(Wo, ws, w, blockIdx.x - 240);
    publish(ctl + CTL_F1 + (1920 + (blockIdx.x - 240) * 8 + w) * 8, tag, fresh_lane());
  }
  bar();
  const int rb3 = g8 * 4 + (i32 >> 3), cb3 = i32 & 7;
  const int rb4 = (g8 >> 2) * 4 + (i32 >> 3), cb4 = (g8 & 3) * 8 + (i32 & 7);
  p2_attn(ws, Wg, Wu, Wd, lds0, w, tag, rb3, cb3, rb4, cb4);
  bar();
  const char* gug = ws + WS_WP + (size_t)NFR_WO * 1024 + (size_t)cb4 * 122880;
  const char* wdg = ws + WS_WP + (size_t)(NFR_WO + NFR_GU) * 1024 + (size_t)cb3 * 245760;
  u32x2 hp[5];
  if (w < 4) {
    const int lane = fresh_lane();
    const unsigned voff = lane * 16;
    STAMP_AT(2, 0);
    u32x2 xr[5];
    sfor<5>([&](auto ct) { gld8<ct * 32>(xr[ct], (const char*)x + (rb3 * 64 + w * 16) * 1280 + cb3 * 160, (unsigned)((lane & 15) * 1280 + (lane >> 4) * 8)); });
    const int hd = min(lane & 31, G3), cgt = (hd & 1) ? hd * 5 : hd * 5 + 4;
    const unsigned pv = (unsigned)((cgt >> 3) * 1024 + ((cgt & 7) >> 1) * 256 + (cgt & 1) * 8);
    unsigned pr;
    STAMP_AT(2, 1);
    f32x4 acc[5] = {};
    sfor<5>([&](auto ct) { pin(acc[ct]); });
    gemm_res<20, 5, P3_LA, 2, 4, 1, 0, wo_off, x_none>(ws + WS_O + (size_t)(rb3 * 4 + w) * 20480, lds0, voff,
      [&](auto k) { const char* pb = ws + WS_O + (size_t)(rb3 * 4 + w) * 20480;
        if constexpr (k == 0) { gld4u(pr, pb, pv); return false; } else { pin(pr); if (__all(pr != 0xffffffffu)) return false; probe(pb, pv); return true; } },
      [&](auto i) { if constexpr (i == -1) STAMP_AT(2, 2); if constexpr (i == 1) STAMP_AT(2, 3); if constexpr (i == 10) STAMP_AT(2, 4);
        if constexpr (i == P3_G1 || i == 12) bar(); },
      [&](auto ct, const bf16x8& b, bf16x8 (&a)[1]) { mfma16(acc[ct], b, a[0]); });
    acc_fence(); sfor<5>([&](auto ct) { pin(acc[ct]); });
    STAMP_AT(2, 6);
    sfor<5>([&](auto ct) { pin(xr[ct]); });
    float s1 = 0.f, s2 = 0.f;
    const char* hb = ws + WS_H + (size_t)(rb3 * 4 + w) * 20480;
    const unsigned vt = t16lane(lane);
#pragma unroll
    for (int ct = 0; ct < 5; ++ct) {
      hp[ct] = nofill(pk_bf16(bf_lo(xr[ct][0]) + acc[ct][0], bf_hi(xr[ct][0]) + acc[ct][1]), pk_bf16(bf_lo(xr[ct][1]) + acc[ct][2], bf_hi(xr[ct][1]) + acc[ct][3]));
      st8s(hb + t16off(cb3 * 80 + ct * 16), vt, hp[ct]);
      s1 = dot2(hp[ct][0], 0x3f803f80u, s1); s1 = dot2(hp[ct][1], 0x3f803f80u, s1);
      s2 = dot2(hp[ct][0], hp[ct][0], s2); s2 = dot2(hp[ct][1], hp[ct][1], s2);
    }
    s1 = red4(s1); s2 = red4(s2);
    if (lane < 16) st8s(ws + WS_HST + (size_t)(rb3 * 4 + w) * 1024 + (cb3 >> 1) * 256 + (cb3 & 1) * 8, (unsigned)(lane * 16), nofill(__float_as_uint(s1), __float_as_uint(s2)));
    bar();
    STAMP_AT(2, 7);
  } else {
    const int lane = fresh_lane(), v = w - 4;
    const unsigned voff = lane * 16;
    float cpart[10];
    const int cc = v * 24 + min(lane & 31, 23);
    sfor<10>([&](auto i) { gld4<i * 384>(cpart[i], ws + WS_CSP, (unsigned)(cb4 * 7680 + (lane >> 5) * 3840 + cc * 4)); });
    poll(ctr + CTL_FD, (unsigned)((cb3 * 240 + v * 60 + min(lane, 59)) * 8), tag);
    float cv = 0.f;
    sfor<10>([&](auto j) { pin(cpart[j]); cv += cpart[j]; });
    cv = add32(cv);
    bar();
    STAMP_AT(2, 2);
    sfor<5>([&](auto j) { glds16<true>(gug + (v * 5 + j) * 1024, voff, lds0 + gu_off(0) + (v * 5 + j) * 1024); });
    sfor<10>([&](auto j) { glds16<true>(gug + (20 + v * 10 + j) * 1024, voff, lds0 + gu_off(20) + (v * 10 + j) * 1024); });
    bar();
    sfor<15>([&](auto j) { glds16<true>(gug + (60 + v * 15 + j) * 1024, voff, lds0 + gu_off(60) + (v * 15 + j) * 1024); });
    STAMP_AT(2, 5);
    waitv<0>();
    bar();
    STAMP_AT(2, 6);
    if (lane < 24) ((LAS float*)(uintptr_t)(lds0 + CSF_OFF))[cc] = cv;
  }
  {
    const int lane = fresh_lane();
    const unsigned voff = lane * 16;
    const int r16 = lane & 15, g4 = lane >> 4;
    STAMP_AT(3, 0);
    f32x4 cs[6]; f32x2 ms[2];
    f32x4 hs[2];
    auto hs_wait = [&](bool all) {
#pragma unroll 1
      for (int spin = 0; spin < (1 << 17); ++spin) {
        gld16<0>(hs[0], ws + WS_HST + (size_t)(rb4 * 16 + w * 2) * 1024, voff);
        gld16<1024>(hs[1], ws + WS_HST + (size_t)(rb4 * 16 + w * 2) * 1024, voff);
        waitv<0>(); pin(hs[0]); pin(hs[1]);
        const bool miss = max(umax4(__builtin_bit_cast(u32x4, hs[0])), umax4(__builtin_bit_cast(u32x4, hs[1]))) == 0xffffffffu;
        if (!__any(miss && (all || g4 < G4))) return !__any(miss);
        __builtin_amdgcn_s_sleep(1);
      }
      return true;
    };
    bool hs_ok;
    STAMP_AT(3, 1);
    f32x4 acc[2][6] = {};
    sfor<6>([&](auto ct) { pin(acc[0][ct]); pin(acc[1][ct]); });
    if (w < 4) __builtin_amdgcn_s_setprio(1);
    gemm_res<20, 6, K4_LA_, 1, K4_CG_, 2, 20480, gu_off, x_p4>(ws + WS_H + (size_t)(rb4 * 16 + w * 2) * 20480, lds0, voff,
      [&](auto k) {
        if constexpr (k == 0) { gld16<0>(hs[0], ws + WS_HST + (size_t)(rb4 * 16 + w * 2) * 1024, voff); gld16<1024>(hs[1], ws + WS_HST + (size_t)(rb4 * 16 + w * 2) * 1024, voff); return false; }
        else { pin(hs[0]); pin(hs[1]);
          const bool miss = max(umax4(__builtin_bit_cast(u32x4, hs[0])), umax4(__builtin_bit_cast(u32x4, hs[1]))) == 0xffffffffu;
          hs_ok = !__any(miss);
          if (!__any(miss && g4 < G4)) return false;
          hs_ok = hs_wait(false); return true; } },
      [&](auto i) { if constexpr (i == -1) STAMP_AT(3, 2); if constexpr (i == 1) STAMP_AT(3, 3); if constexpr (i == 10) STAMP_AT(3, 4);
        if constexpr (i == 9) bar();
        if constexpr (i == 19) { if (!hs_ok) hs_wait(true); }
        if constexpr (i == 19) sfor<2>([&](auto t) {
          const float s1 = red4(hs[t][0] + hs[t][2]), s2 = red4(hs[t][1] + hs[t][3]), mean = s1 * (1.f / 640.f);
          ms[t] = (f32x2){mean, __builtin_amdgcn_rsqf(fmaxf(s2 * (1.f / 640.f) - mean * mean, 0.f) + LN_EPS)}; pin(ms[t]); });
        if constexpr (i == 18) sfor<6>([&](auto ct) { cs[ct] = *(const LAS f32x4*)(uintptr_t)(lds0 + CSF_OFF + (ct * 16 + 4 * g4) * 4); });
        if constexpr (i >= 2 && i < 7) { const int f = (i - 2) * 8 + w - ((i == 6 && w >= 4) ? 4 : 0); glds16<true>(wdg + (f) * 1024, voff, lds0 + wd_off(0) + f * 1024); }
        if constexpr (i >= 10 && i < 15) { constexpr int n = i - 10; glds16<true>(wdg + (40 + n * 8 + w) * 1024, voff, lds0 + wd_off(40) + (n * 8 + w) * 1024); }
        if constexpr (i >= 15 && i < 18) { const int f = (i - 15) * 8 + w - ((i == 17 && w >= 4) ? 4 : 0); glds16<true>(wdg + (140 + f) * 1024, voff, lds0 + wd_off(140) + f * 1024); } },
      [&](auto ct, const bf16x8& b, bf16x8 (&a)[2]) { mfma16(acc[0][ct], b, a[0]); mfma16(acc[1][ct], b, a[1]); });
    acc_fence(); sfor<6>([&](auto ct) { pin(acc[0][ct]); pin(acc[1][ct]); });
    STAMP_AT(3, 6);
    constexpr float GC0 = -2.f * 0.7978845608028654f * 1.4426950408889634f, GC1 = GC0 * 0.044715f;
    const char* gb = ws + WS_G + (size_t)(rb4 * 16 + w * 2) * 49152;
    const unsigned vt = t16lane(lane);
#pragma unroll
    for (int t = 0; t < 2; ++t) {
      const float nm = -ms[t].x, rstd = ms[t].y;
#pragma unroll
      for (int ct = 0; ct < 3; ++ct) {
        const f32x4 gv = (acc[t][ct] + cs[ct] * nm) * rstd, uv = (acc[t][3 + ct] + cs[3 + ct] * nm) * rstd;
        const f32x4 z = gv * (gv * gv * GC1 + GC0);
        f32x4 e;
#pragma unroll
        for (int j = 0; j < 4; ++j) e[j] = __builtin_amdgcn_exp2f(z[j]);
        e = e + 1.f;
#pragma unroll
        for (int j = 0; j < 4; ++j) e[j] = __builtin_amdgcn_rcpf(e[j]);
        const f32x4 r = gv * uv * e;
        st8s(gb + t * 49152 + t16off(cb4 * 48 + ct * 16), vt, nofill(pk_bf16(r[0], r[1]), pk_bf16(r[2], r[3])));
      }
    }
    __builtin_amdgcn_s_setprio(0);
    STAMP_AT(3, 7);
    waitv<6>();
    STAMP_AT(3, 8);
    bar();
  }
  if (w < 4) {
    const int lane = fresh_lane();
    const unsigned voff = lane * 16;
    const int row = rb3 * 64 + w * 16 + (lane & 15), col0 = cb3 * 80 + 4 * (lane >> 4);
    STAMP_AT(4, 0);
    const unsigned pv = (unsigned)t16off(min(lane & 31, G5) * 48 + 32);
    unsigned pr;
    STAMP_AT(4, 1);
    f32x4 acc[5] = {};
    sfor<5>([&](auto ct) { pin(acc[ct]); });
    gemm_res<48, 5, K5_LA_, 2, 4, 1, 0, wd_off, x_none>(ws + WS_G + (size_t)(rb3 * 4 + w) * 49152, lds0, voff,
      [&](auto k) { const char* pb = ws + WS_G + (size_t)(rb3 * 4 + w) * 49152;
        if constexpr (k == 0) { gld4u(pr, pb, pv); return false; } else { pin(pr); if (__all(pr != 0xffffffffu)) return false; probe(pb, pv); return true; } },
      [&](auto i) { if constexpr (i == 1) STAMP_AT(4, 3); if constexpr (i == 14) STAMP_AT(4, 4); if constexpr (i == 30) STAMP_AT(4, 5);
        if constexpr (i == -1 || i == 8 || i == 14 || i == 16 || i == 30 || i == 38) bar(); },
      [&](auto ct, const bf16x8& b, bf16x8 (&a)[1]) { mfma16(acc[ct], b, a[0]); });
    acc_fence(); sfor<5>([&](auto ct) { pin(acc[ct]); });
    STAMP_AT(4, 12);
#pragma unroll
    for (int ct = 0; ct < 5; ++ct) {
      const unsigned p0 = pk_bf16(bf_lo(hp[ct][0]) + acc[ct][0], bf_hi(hp[ct][0]) + acc[ct][1]);
      const unsigned p1 = pk_bf16(bf_lo(hp[ct][1]) + acc[ct][2], bf_hi(hp[ct][1]) + acc[ct][3]);
      *(u32x2*)(out + (size_t)row * 640 + col0 + 16 * ct) = (u32x2){p0, p1};
    }
    STAMP_AT(4, 13);
  } else {
    const int lane = fresh_lane(), v = w - 4;
    const unsigned voff = lane * 16;
    glds16<true>(wdg + (36 + v) * 1024, voff, lds0 + wd_off(36) + v * 1024);
    waitv<0>(); bar();
    sfor<3>([&](auto g) { sfor<5>([&](auto j) { constexpr int f = (g + 4) * 20 + j; glds16<true>(wdg + (f + v * 5) * 1024, voff, lds0 + wd_off(f) + v * 5120); }); });
    bar();
    sfor<2>([&](auto g) { sfor<5>([&](auto j) { constexpr int f = (g + 8) * 20 + j; glds16<true>(wdg + (f + v * 5) * 1024, voff, lds0 + wd_off(f) + v * 5120); }); });
    waitv<10>(); bar();
    bar();
    sfor<2>([&](auto g) { sfor<5>([&](auto j) { constexpr int f = (g + 10) * 20 + j; glds16<true>(wdg + (f + v * 5) * 1024, voff, lds0 + wd_off(f) + v * 5120); }); });
    waitv<10>(); bar();
    waitv<0>(); bar();
    if (blockIdx.x == 0 && w == 4) {
      poll2(ctr + CTL_FR, (unsigned)(lane * 64), (unsigned)(4096 + lane * 64), tag);
      poll2(ctr + CTL_FR, (unsigned)(8192 + lane * 64), (unsigned)(12288 + lane * 64), tag);
      if (lane == 0) st8(ctl, tag);
    }
  }
}

extern "C" void kernel_launch(void* const* d_in, const int* in_sizes, int n_in, void* d_out, int out_size, void* d_ws, size_t ws_size,
                              hipStream_t stream) {
  k_mega<<<256, 512, 0, stream>>>((const u16*)d_in[0], (const u16*)d_in[1], (const u16*)d_in[2], (const u16*)d_in[4], (const u16*)d_in[5],
                                  (const u16*)d_in[6], (char*)d_ws, (u16*)d_out);
}
```

```cpp
#include <hip/hip_runtime.h>
#include <hip/hip_bf16.h>
#include <stdint.h>
#include <utility>

typedef short bf16x8 __attribute__((ext_vector_type(8)));
typedef short s16x4 __attribute__((ext_vector_type(4)));
typedef float f32x16 __attribute__((ext_vector_type(16)));
typedef float f32x4 __attribute__((ext_vector_type(4)));
typedef float f32x2 __attribute__((ext_vector_type(2)));
typedef unsigned u32x4 __attribute__((ext_vector_type(4)));
typedef unsigned u32x2 __attribute__((ext_vector_type(2)));
typedef __bf16 bf2_t __attribute__((ext_vector_type(2)));
typedef unsigned short u16;
#define LAS __attribute__((address_space(3)))
#define DI __device__ __forceinline__

constexpr float LN_EPS = 1e-5f;
constexpr float QSCALE = 0.22360679774997896f * 1.4426950408889634f;

constexpr size_t WS_Q   = 0;
constexpr size_t WS_KV  = WS_Q + 2621440;
constexpr size_t WS_O   = WS_KV + 5242880;
constexpr size_t WS_H   = WS_O + 2621440;
constexpr size_t WS_HST = WS_H + 2621440;
constexpr size_t WS_G   = WS_HST + 131072;
constexpr size_t WS_WP  = WS_G + 6291456;
constexpr size_t WS_CSP = WS_WP + 6717440;
constexpr size_t WS_RST = WS_CSP + 245760;
constexpr size_t WS_CTL = WS_RST + 32768;
constexpr size_t CTL_F1 = 256;
constexpr size_t CTL_F2 = CTL_F1 + 2048 * 8;
constexpr size_t CTL_FR = CTL_F2 + 2048 * 8;
constexpr size_t CTL_F3 = CTL_FR + 2048 * 8;
constexpr size_t CTL_F4 = CTL_F3 + 1024 * 8;
constexpr size_t CTL_FD = CTL_F4 + 2048 * 8;
constexpr size_t CTL_REP = CTL_FD + 2048 * 8 - 256;
constexpr int NFR_WO = 800, NFR_GU = 3840, NFR_WD = 1920;

DI unsigned pk_bf16(float lo, float hi) { f32x2 f = {lo, hi}; return __builtin_bit_cast(unsigned, __builtin_convertvector(f, bf2_t)); }
DI float bf_lo(unsigned u) { return __uint_as_float(u << 16); }
DI float bf_hi(unsigned u) { return __uint_as_float(u & 0xffff0000u); }
DI float dot2(unsigned a, unsigned b, float c) { return __builtin_amdgcn_fdot2_f32_bf16(__builtin_bit_cast(bf2_t, a), __builtin_bit_cast(bf2_t, b), c, false); }
DI float add32(float v) { const auto r = __builtin_amdgcn_permlane32_swap(__float_as_uint(v), __float_as_uint(v), false, false); return __uint_as_float(r[0]) + __uint_as_float(r[1]); }
DI float max32(float v) { const auto r = __builtin_amdgcn_permlane32_swap(__float_as_uint(v), __float_as_uint(v), false, false); return fmaxf(__uint_as_float(r[0]), __uint_as_float(r[1])); }
DI float add16(float v) { const auto r = __builtin_amdgcn_permlane16_swap(__float_as_uint(v), __float_as_uint(v), false, false); return __uint_as_float(r[0]) + __uint_as_float(r[1]); }
DI float red4(float v) { return add32(add16(v)); }
DI float hi32(float v) { const auto r = __builtin_amdgcn_permlane32_swap(__float_as_uint(v), __float_as_uint(v), false, false); return __uint_as_float(r[1]); }
#define MFMA32(a, b, c) __builtin_amdgcn_mfma_f32_32x32x16_bf16((a), (b), (c), 0, 0, 0)
#define MFMA32K8(a, b, c) __builtin_amdgcn_mfma_f32_32x32x8bf16_1k((a), (b), (c), 0, 0, 0)
#define MFMA16(a, b, c) __builtin_amdgcn_mfma_f32_16x16x32_bf16((a), (b), (c), 0, 0, 0)

DI void mfma16(f32x4& c, const bf16x8& a, const bf16x8& b) { asm volatile("v_mfma_f32_16x16x32_bf16 %0, %1, %2, %0" : "+v"(c) : "v"(a), "v"(b)); }
DI void acc_fence() { asm volatile("s_nop 7\n\ts_nop 3" ::: "memory"); }
template <bool DEV> DI void glds16(const void* sbase, unsigned voff, unsigned lds_base) {
  if constexpr (DEV) asm volatile("s_mov_b32 m0, %2\n\ts_nop 0\n\tglobal_load_lds_dwordx4 %0, %1 sc1" :: "v"(voff), "s"(sbase), "s"(lds_base) : "memory");
  else asm volatile("s_mov_b32 m0, %2\n\ts_nop 0\n\tglobal_load_lds_dwordx4 %0, %1" :: "v"(voff), "s"(sbase), "s"(lds_base) : "memory");
}
template <int OFF, class T> DI void gld16(T& dst, const void* sbase, unsigned voff) {
  asm volatile("global_load_dwordx4 %0, %1, %2 offset:%3 sc1" : "=v"(dst) : "v"(voff), "s"(sbase), "i"(OFF) : "memory");
}
template <int OFF, class T> DI void gld8(T& dst, const void* sbase, unsigned voff) {
  asm volatile("global_load_dwordx2 %0, %1, %2 offset:%3 sc1" : "=v"(dst) : "v"(voff), "s"(sbase), "i"(OFF) : "memory");
}
template <int OFF> DI void gld4(float& dst, const void* sbase, unsigned voff) {
  asm volatile("global_load_dword %0, %1, %2 offset:%3 sc1" : "=v"(dst) : "v"(voff), "s"(sbase), "i"(OFF) : "memory");
}
DI void gld4u(unsigned& dst, const void* sbase, unsigned voff) { asm volatile("global_load_dword %0, %1, %2 sc1" : "=v"(dst) : "v"(voff), "s"(sbase) : "memory"); }
template <int OFF> DI void gldu16(unsigned& dst, const void* sbase, unsigned voff) {
  asm volatile("global_load_ushort %0, %1, %2 offset:%3" : "=v"(dst) : "v"(voff), "s"(sbase), "i"(OFF) : "memory");
}
DI void st16(void* p, u32x4 v) { asm volatile("global_store_dwordx4 %0, %1, off sc1\n\ts_nop 1" :: "v"(p), "v"(v) : "memory"); }
DI void st8(void* p, u32x2 v) { asm volatile("global_store_dwordx2 %0, %1, off sc1" :: "v"(p), "v"(v) : "memory"); }
DI void st4(void* p, float v) { asm volatile("global_store_dword %0, %1, off sc1" :: "v"(p), "v"(v) : "memory"); }
DI void st8s(const void* sbase, unsigned voff, u32x2 v) { asm volatile("global_store_dwordx2 %0, %1, %2 sc1" :: "v"(voff), "v"(v), "s"(sbase) : "memory"); }
DI u32x2 nofill(unsigned a, unsigned b) { return (u32x2){min(a, 0xfffffffeu), min(b, 0xfffffffeu)}; }
DI unsigned umax4(u32x4 v) { return max(max(v[0], v[1]), max(v[2], v[3])); }
template <int N> DI void waitv() { asm volatile("s_waitcnt vmcnt(%0)" :: "i"(N) : "memory"); }
DI void bar() { asm volatile("s_waitcnt lgkmcnt(0)\n\ts_barrier" ::: "memory"); }
DI void setflag(char* flag, u32x2 tag, int lane) { if (lane < 8) st8(flag + lane * CTL_REP, tag); }
DI void publish(char* flag, u32x2 tag, int lane) { waitv<0>(); setflag(flag, tag, lane); }
DI void poll(const char* base, unsigned voff, u32x2 tag) {
#pragma unroll 1
  for (int spin = 0; spin < (1 << 17); ++spin) {
    u32x2 v;
    asm volatile("global_load_dwordx2 %0, %1, %2 sc1\n\ts_waitcnt vmcnt(0)" : "=v"(v) : "v"(voff), "s"(base) : "memory");
    if (__all(v[0] == tag[0] && v[1] == tag[1])) break;
    __builtin_amdgcn_s_sleep(1);
  }
}
DI void poll2(const char* base, unsigned voff0, unsigned voff1, u32x2 tag) {
#pragma unroll 1
  for (int spin = 0; spin < (1 << 17); ++spin) {
    u32x2 v, u;
    asm volatile("global_load_dwordx2 %0, %2, %4 sc1\n\tglobal_load_dwordx2 %1, %3, %4 sc1\n\ts_waitcnt vmcnt(0)" : "=&v"(v), "=&v"(u) : "v"(voff0), "v"(voff1), "s"(base) : "memory");
    if (__all(v[0] == tag[0] && v[1] == tag[1] && u[0] == tag[0] && u[1] == tag[1])) break;
    __builtin_amdgcn_s_sleep(1);
  }
}
DI void probe(const char* base, unsigned voff) {
#pragma unroll 1
  for (int spin = 0; spin < (1 << 17); ++spin) {
    unsigned v;
    asm volatile("global_load_dword %0, %1, %2 sc1\n\ts_waitcnt vmcnt(0)" : "=v"(v) : "v"(voff), "s"(base) : "memory");
    if (__all(v != 0xffffffffu)) break;
    __builtin_amdgcn_s_sleep(1);
  }
}
constexpr int t16off(int c0) { return (c0 >> 5) * 1024 + (c0 & 16) * 32; }
DI unsigned t16lane(int lane) { return (unsigned)((lane >> 5) * 256 + (lane & 15) * 16 + ((lane >> 4) & 1) * 8); }
DI void st16s(const void* sbase, unsigned voff, u32x4 v) { asm volatile("global_store_dwordx4 %0, %1, %2 sc1\n\ts_nop 1" :: "v"(voff), "v"(v), "s"(sbase) : "memory"); }
DI float row16_sum(float v) {
  v += __builtin_bit_cast(float, __builtin_amdgcn_update_dpp(0, __builtin_bit_cast(int, v), 0x128, 0xf, 0xf, false));
  v += __builtin_bit_cast(float, __builtin_amdgcn_update_dpp(0, __builtin_bit_cast(int, v), 0x124, 0xf, 0xf, false));
  v += __builtin_bit_cast(float, __builtin_amdgcn_update_dpp(0, __builtin_bit_cast(int, v), 0x122, 0xf, 0xf, false));
  v += __builtin_bit_cast(float, __builtin_amdgcn_update_dpp(0, __builtin_bit_cast(int, v), 0x121, 0xf, 0xf, false));
  return v;
}
DI u32x2 make_tag(u32x2 ep) {
  const unsigned long long e = (((unsigned long long)__builtin_amdgcn_readfirstlane(ep[1]) << 32) | __builtin_amdgcn_readfirstlane(ep[0])) + 1ull;
  return (u32x2){(unsigned)e, (unsigned)(e >> 32)};
}
constexpr unsigned RS_K0 = 0x9e3779b9u, RS_K1 = 0x7f4a7c15u;
struct RowStat { u32x4 a; unsigned b; };
DI void rowstat_get(RowStat& r, const u16* x, int row, int lane) {
  asm volatile("global_load_dwordx4 %0, %2, %4\n\tglobal_load_dword %1, %3, %4 offset:1024" : "=&v"(r.a), "=&v"(r.b) : "v"(lane * 16), "v"(lane * 4), "s"((const char*)x + (size_t)row * 1280) : "memory");
}
DI void rowstat_put(RowStat& r, char* ws, int row, int lane, u32x2 tag) {
  asm volatile("" : "+v"(r.a), "+v"(r.b));
  float s1 = dot2(r.b, 0x3f803f80u, 0.f), s2 = dot2(r.b, r.b, 0.f);
#pragma unroll
  for (int j = 0; j < 4; ++j) { s1 = dot2(r.a[j], 0x3f803f80u, s1); s2 = dot2(r.a[j], r.a[j], s2); }
  s1 = red4(row16_sum(s1)); s2 = red4(row16_sum(s2));
  const float mean = s1 * (1.f / 640.f), rstd = __builtin_amdgcn_rsqf(fmaxf(s2 * (1.f / 640.f) - mean * mean, 0.f) + LN_EPS);
  const unsigned m = __float_as_uint(mean), q = __float_as_uint(rstd);
  if (lane == 0) st16(ws + WS_RST + (size_t)row * 16, (u32x4){m, q, tag[0] ^ m ^ RS_K0, tag[1] ^ q ^ RS_K1});
}
DI int fresh_lane() { int l = __builtin_amdgcn_mbcnt_hi(~0u, __builtin_amdgcn_mbcnt_lo(~0u, 0u)); asm volatile("" : "+v"(l)); return l; }
template <class T> DI void pins(T& v) { asm volatile("" : "+s"(v)); }
template <class T> DI void pin(T& v) { asm volatile("" : "+v"(v)); }
DI bf16x8 lds128(unsigned addr) { return *(const LAS bf16x8*)(uintptr_t)addr; }
DI s16x4 lds64(unsigned addr) { return *(const LAS s16x4*)(uintptr_t)addr; }
DI s16x4 ldstr(unsigned addr) { return __builtin_amdgcn_ds_read_tr16_b64_v4i16((LAS s16x4*)(uintptr_t)addr); }
DI unsigned lds_addr_of(const void* p) { return (unsigned)(uintptr_t)(const LAS char*)p; }
#define STAMP_AT(kid, idx) ((void)0)
#define RETRY_COUNT(k) ((void)0)
#define WAITV_BAR(N) asm volatile("s_waitcnt vmcnt(" #N ") lgkmcnt(0)\n\ts_barrier" ::: "memory")
#define SB() __builtin_amdgcn_sched_barrier(0)
template <int N> using IC = std::integral_constant<int, N>;
template <class F, int... Is> DI void sfor_(F&& f, std::integer_sequence<int, Is...>) { (f(IC<Is>{}), ...); }
template <int N, class F> DI void sfor(F&& f) { sfor_(f, std::make_integer_sequence<int, N>{}); }

constexpr int P3_LA = 12, K4_LA_ = 6, K4_CG_ = 2, K5_LA_ = 16;
constexpr int G3 = 19, G4 = 1, G5 = 10;
constexpr int K1_STAGE = 32768;
DI void p1_load(const u16* __restrict__ x, const u16* __restrict__ W, const unsigned lds0, const int v, int rb, int cb) {
  const int lane = fresh_lane();
  const unsigned a_voff0 = (unsigned)((lane >> 3) * 1280 + (((lane & 7) ^ ((lane >> 4) & 7)) << 4));
  const unsigned a_voff1 = (unsigned)((lane >> 3) * 1280 + (((lane & 7) ^ ((4 + (lane >> 4)) & 7)) << 4)) + 8 * 1280;
  const unsigned w_voff0 = (unsigned)((lane >> 3) * 3840 + (((lane & 7) ^ (((lane >> 4) & 1) << 2)) << 4));
  const unsigned w_voff1 = w_voff0 + 8 * 3840;
  const char* xs = (const char*)x + (size_t)(rb * 128 + v * 32) * 1280;
  const char* wsrc = (const char*)W + (size_t)((v & 1) * 32) * 3840 + (size_t)(cb * 128 + (v >> 1) * 64) * 2;
  const int rot = ((((blockIdx.x >> 1) & 3) * 5 + (blockIdx.x & 1) * 10) >> 1) % 10;
  auto st = [&](auto s_) {
    constexpr int s0 = s_; const unsigned d = lds0 + (s0 % 5) * K1_STAGE + v * 4096;
    const int s = s0 + rot - (s0 + rot >= 10 ? 10 : 0);
    const char* xp = xs + s * 128; const char* wp = wsrc + (size_t)s * 64 * 3840;
    glds16<false>(xp, a_voff0, d); glds16<false>(wp, w_voff0, d + 16384);
    glds16<false>(xp, a_voff1, d + 1024); glds16<false>(wp, w_voff1, d + 16384 + 1024);
    glds16<false>(xp + 16 * 1280, a_voff0, d + 2048); glds16<false>(wp + 16 * 3840, w_voff0, d + 16384 + 2048);
    glds16<false>(xp + 16 * 1280, a_voff1, d + 3072); glds16<false>(wp + 16 * 3840, w_voff1, d + 16384 + 3072);
  };
  st(IC<0>{}); st(IC<1>{});
  sfor<10>([&](auto s_) { constexpr int s = s_;
    waitv<(s == 0 ? 8 : s <= 7 ? 16 : s == 8 ? 8 : 0)>(); asm volatile("s_barrier" ::: "memory");
    if constexpr (s == 0) { st(IC<2>{}); st(IC<3>{}); }
    else if constexpr (s + 3 < 10) st(IC<s + 3>{}); });
  bar();
}
template <bool ISV>
DI void p1_body(const u16* __restrict__ x, char* __restrict__ ws, const unsigned lds0, const int w, int rb, int cb, u32x2& tag) {
  const int lane = fresh_lane();
  u32x2 ep; gld8<0>(ep, ws + WS_CTL, 0u);
  RowStat rs[2];
  rowstat_get(rs[0], x, blockIdx.x * 8 + w * 2, lane); rowstat_get(rs[1], x, blockIdx.x * 8 + w * 2 + 1, lane);
  const int wm = w >> 1, wn = w & 1;
  const int r32 = lane & 31, hh = lane >> 5;
  STAMP_AT(0, 0);
  unsigned xa[3][4], wa[3][2][2];
  {
    const int row = wm * 64 + r32;
    const int blk = (lane >> 4) & 1, q = (lane & 15) >> 2, p = lane & 3;
#pragma unroll
    for (int hs = 0; hs < 3; ++hs) {
#pragma unroll
      for (int i = 0; i < 4; ++i) { xa[hs][i] = lds0 + hs * 65536 + (unsigned)(row * 128 + ((((i ^ wm) * 2 + hh) ^ ((row >> 1) & 7)) << 4)); pin(xa[hs][i]); }
#pragma unroll
      for (int j = 0; j < 2; ++j) {
        const unsigned base = lds0 + hs * 65536 + (unsigned)(16384 + wn * 8192 + (8 * hh + q) * 128 + (((j * 4 + blk * 2 + (p >> 1)) ^ (((q >> 1) & 1) << 2)) << 4) + (p & 1) * 8);
        wa[hs][j][0] = base + wm * 2048; wa[hs][j][1] = base + (1 ^ wm) * 2048; pin(wa[hs][j][0]); pin(wa[hs][j][1]);
      }
    }
  }
  f32x16 acc[2][2] = {};
  f32x4 sw[2] = {};
  u32x4 rec;
  bf16x8 sel = (bf16x8)(short)(((lane & 15) == ((lane >> 4) & 1)) ? 0x3f80 : 0); pin(sel);
  bf16x8 fx[2][2]; s16x4 fwl[2][2], fwh[2][2];
  auto rd = [&](auto i_, auto k_) {
    constexpr int i = i_, k = k_, s = i >> 2, p = i & 3, sl = s % 5, hs = sl >> 1; constexpr unsigned so = (sl & 1) * K1_STAGE;
    if constexpr (k == 0) fx[i & 1][0] = lds128(xa[hs][p] + so);
    if constexpr (k == 3) fx[i & 1][1] = lds128(xa[hs][p] + so + 4096);
    if constexpr (k == 1 || k == 2) { fwl[i & 1][k - 1] = ldstr(wa[hs][k - 1][p & 1] + so + (p >> 1) * 4096); fwh[i & 1][k - 1] = ldstr(wa[hs][k - 1][p & 1] + so + (p >> 1) * 4096 + 512); }
  };
  const bool isq = cb < 5;
  const int pitch = isq ? 1280 : 2560;
  unsigned off[2][ISV ? 1 : 4];
#pragma unroll
  for (int j = 0; j < 2; ++j) {
    if constexpr (ISV) {
      const int n = cb * 128 + wn * 64 + j * 32 + r32 - 1280, head = n / 20, d0 = n - head * 20;
      const int d = (head & 1) ? (d0 < 4 ? d0 + 16 : d0 - 4) : d0;
      off[j][0] = (unsigned)(head * 163840 + 1280 + (hh * 20 + d) * 16); pin(off[j][0]);
    } else {
#pragma unroll
      for (int g = 0; g < 4; ++g) {
        const int n = cb * 128 + wn * 64 + j * 32 + 8 * g + 4 * hh - (isq ? 0 : 640), head = n / 20, d0 = n - head * 20;
        const int d = (head & 1) ? (d0 < 4 ? d0 + 16 : d0 - 4) : d0;
        off[j][g] = (unsigned)(head * 64 * pitch + (d < 16 ? (((d >> 3) * 32 + r32) * 16 + (d & 4) * 2) : (1024 + r32 * 8))); pin(off[j][g]);
      }
    }
  }
  waitv<0>(); pin(ep); tag = make_tag(ep);
  rowstat_put(rs[0], ws, blockIdx.x * 8 + w * 2, lane, tag); rowstat_put(rs[1], ws, blockIdx.x * 8 + w * 2 + 1, lane, tag);
  asm volatile("s_barrier" ::: "memory");
  STAMP_AT(0, 1);
  sfor<4>([&](auto k) { rd(IC<0>{}, k); });
  sfor<40>([&](auto i_) {
    constexpr int i = i_, b = i & 1;
    if constexpr ((i & 3) == 3 && i < 39) asm volatile("s_barrier" ::: "memory");
    if constexpr (i == 30) gld16<0>(rec, ws + WS_RST + (size_t)(rb * 128 + wm * 64) * 16, (unsigned)(lane * 16));
    SB();
    const bf16x8 w0 = __builtin_shufflevector(fwl[b][0], fwh[b][0], 0, 1, 2, 3, 4, 5, 6, 7), w1 = __builtin_shufflevector(fwl[b][1], fwh[b][1], 0, 1, 2, 3, 4, 5, 6, 7);
    sfor<4>([&](auto k_) { constexpr int k = k_, t = k >> 1, j = (k ^ (k >> 1)) & 1;
      if constexpr (ISV) acc[t][j] = MFMA32(fx[b][t], j ? w1 : w0, acc[t][j]); else acc[t][j] = MFMA32(j ? w1 : w0, fx[b][t], acc[t][j]);
      SB();
      if constexpr (i < 39) rd(IC<i + 1>{}, k_);
      SB(); });
    if constexpr (b == 1) { sw[0] = MFMA16(w0, sel, sw[0]); sw[1] = MFMA16(w1, sel, sw[1]); }
    SB();
  });
  STAMP_AT(0, 11);
  LAS float* csp = (LAS float*)(uintptr_t)lds0;
  if ((lane & 15) < 2) sfor<2>([&](auto j) { *(LAS f32x4*)&csp[wm * 128 + wn * 64 + j * 32 + (lane & 1) * 16 + (lane >> 4) * 4] = sw[j]; });
  waitv<0>(); pin(rec);
#pragma unroll 1
  for (int spin = 0; spin < (1 << 17) && !__all((rec[2] ^ rec[0] ^ RS_K0) == tag[0] && (rec[3] ^ rec[1] ^ RS_K1) == tag[1]); ++spin) {
    gld16<0>(rec, ws + WS_RST + (size_t)(rb * 128 + wm * 64) * 16, (unsigned)(lane * 16)); waitv<0>(); pin(rec);
  }
  LAS f32x2* st = (LAS f32x2*)(uintptr_t)(lds0 + 4096 + w * 512);
  if constexpr (ISV) st[lane] = (f32x2){__uint_as_float(rec[0]), __uint_as_float(rec[1])};
  bar();
  STAMP_AT(0, 12);
  if constexpr (ISV) {
    float c[2];
#pragma unroll
    for (int j = 0; j < 2; ++j) c[j] = csp[wn * 64 + j * 32 + r32] + csp[128 + wn * 64 + j * 32 + r32];
#pragma unroll
    for (int t = 0; t < 2; ++t) {
      f32x4 s01[4], s23[4];
#pragma unroll
      for (int g = 0; g < 4; ++g) { s01[g] = *(const LAS f32x4*)&st[t * 32 + 8 * g + 4 * hh]; s23[g] = *(const LAS f32x4*)&st[t * 32 + 8 * g + 4 * hh + 2]; }
      const char* dst = ws + WS_KV + (size_t)(rb * 4 + wm * 2 + t) * 2560;
#pragma unroll
      for (int j = 0; j < 2; ++j) {
        const f32x16& a = acc[t][j];
        unsigned pk[8];
#pragma unroll
        for (int g = 0; g < 4; ++g) {
          pk[2 * g]     = pk_bf16(s01[g][1] * (a[4 * g] - s01[g][0] * c[j]), s01[g][3] * (a[4 * g + 1] - s01[g][2] * c[j]));
          pk[2 * g + 1] = pk_bf16(s23[g][1] * (a[4 * g + 2] - s23[g][0] * c[j]), s23[g][3] * (a[4 * g + 3] - s23[g][2] * c[j]));
        }
        st16s(dst, off[j][0], (u32x4){pk[0], pk[1], pk[2], pk[3]});
        st16s(dst + 640, off[j][0], (u32x4){pk[4], pk[5], pk[6], pk[7]});
      }
    }
  } else {
    const float sc = isq ? QSCALE : 1.f;
    const char* base = ws + (isq ? WS_Q : WS_KV) + (size_t)(rb * 4 + wm * 2) * pitch;
    const auto pm = __builtin_amdgcn_permlane32_swap(rec[0], rec[0], false, false), pr = __builtin_amdgcn_permlane32_swap(rec[1], rec[1], false, false);
#pragma unroll
    for (int j = 0; j < 2; ++j)
#pragma unroll
      for (int g = 0; g < 4; ++g) {
        const int nl = wn * 64 + j * 32 + 8 * g + 4 * hh;
        const f32x4 c = *(const LAS f32x4*)&csp[nl] + *(const LAS f32x4*)&csp[128 + nl];
#pragma unroll
        for (int t = 0; t < 2; ++t) {
          const f32x16& a = acc[t][j];
          const float rs_ = __uint_as_float(pr[t]) * sc, mr = __uint_as_float(pm[t]);
          st8s(base + t * pitch, off[j][g], (u32x2){pk_bf16(rs_ * (a[4 * g] - mr * c[0]), rs_ * (a[4 * g + 1] - mr * c[1])), pk_bf16(rs_ * (a[4 * g + 2] - mr * c[2]), rs_ * (a[4 * g + 3] - mr * c[3]))});
        }
      }
  }
  STAMP_AT(0, 13);
  waitv<0>();
  if (lane < 16) st8(ws + WS_CTL + CTL_F1 + ((cb * 4 + wn * 2 + (lane >> 3)) * 32 + rb * 2 + wm) * 8 + (lane & 7) * CTL_REP, tag);
}

DI void p1_repack(const u16* __restrict__ Wo, char* __restrict__ ws, const int w, int idx) {
  const int lane = fresh_lane();
  const unsigned pvo = (lane >> 4) * 8 * 1280 + (lane & 15) * 2;
  unsigned pw[7][8];
  sfor<7>([&](auto r) {
    const int f = idx * 50 + min(w + 8 * r, 49), cbk = f / 100, rem = f - cbk * 100, ks = rem / 5, ct = rem - ks * 5;
    const char* src = (const char*)(Wo + (size_t)(32 * ks) * 640 + 80 * cbk + 16 * ct);
    sfor<8>([&](auto i) { gldu16<(i & 3) * 1280>(pw[r][i], src + (i & ~3) * 1280, pvo); });
  });
  sfor<7>([&](auto r) {
    waitv<(6 - r) * 8>();
    sfor<8>([&](auto j) { pin(pw[r][j]); });
    const u32x4 v = {pw[r][0] | (pw[r][1] << 16), pw[r][2] | (pw[r][3] << 16), pw[r][4] | (pw[r][5] << 16), pw[r][6] | (pw[r][7] << 16)};
    pw[r][0] = v[0]; pw[r][1] = v[1]; pw[r][2] = v[2]; pw[r][3] = v[3];
  });
  sfor<7>([&](auto r) {
    if (w + 8 * r < 50) st16(ws + WS_WP + (size_t)(idx * 50 + w + 8 * r) * 1024 + lane * 16, (u32x4){pw[r][0], pw[r][1], pw[r][2], pw[r][3]});
  });
}

template <int OFF> DI void rdK(bf16x8& k0, s16x4& k1, unsigned a0, unsigned a1) {
  asm volatile("ds_read_b128 %0, %2 offset:%4\n\tds_read_b64 %1, %3 offset:%4" : "=&v"(k0), "=&v"(k1) : "v"(a0), "v"(a1), "i"(OFF));
}
template <int OFF> DI void rdV(bf16x8& v0, bf16x8& v1, unsigned a, unsigned long long msk) {
  asm volatile("s_mov_b64 exec, %5\n\tds_read_b128 %0, %2 offset:%3\n\tds_read_b128 %1, %2 offset:%4\n\ts_mov_b64 exec, -1"
               : "+v"(v0), "+v"(v1) : "v"(a), "i"(OFF), "i"(OFF + 640), "s"(msk));
}
#define EX2(v) __builtin_amdgcn_exp2f(v)
constexpr int wo_off(int p) { return (p < 60 ? p : p + 20) * 1024; }
constexpr int gu_off(int f) { return (f < 20 ? f + 60 : f < 60 ? f + 100 : f - 60) * 1024; }
constexpr int wd_off(int f) { return ((f / 20 + 4) % 8) * 20480 + (f % 20) * 1024; }
constexpr int CSF_OFF = 116 * 1024, XCH_OFF = 120 * 1024;

DI void p2_attn(char* __restrict__ ws, const u16* __restrict__ Wg, const u16* __restrict__ Wu, const u16* __restrict__ Wd,
                const unsigned lds0, const int w, const u32x2 tag, const int rb3, const int cb3, const int rb4, const int cb4) {
  const int lane = fresh_lane();
  const int kh = w >> 2, wq = w & 3;
  const int head = (blockIdx.x & 7) * 4 + ((blockIdx.x >> 3) & 3), qb = blockIdx.x >> 5;
  const int r32 = lane & 31, hh = lane >> 5;
  const char* kvg = ws + WS_KV + (size_t)head * 163840 + (size_t)kh * 81920 + wq * 1024;
  const unsigned voff = lane * 16;
  const unsigned kvl = lds0 + kh * 81920 + wq * 1024;
  char* ctl = ws + WS_CTL;
  const char* ctr = ctl + (blockIdx.x & 7) * CTL_REP;
  unsigned pw[3][8];
  const int gw = blockIdx.x * 8 + w, gwc = min(gw, 1919);
  const char* psrc[3];
#pragma unroll
  for (int r = 0; r < 2; ++r) {
    const int f = 2 * gwc + r, cbk = f / 120, rem = f - cbk * 120, ks = rem / 6, ct = rem - ks * 6;
    psrc[r] = (const char*)((ct < 3 ? Wg : Wu) + (size_t)(32 * ks) * 1536 + 48 * cbk + 16 * (ct < 3 ? ct : ct - 3));
  }
  { const int f = gwc, cbk = f / 240, rem = f - cbk * 240, ks = rem / 5, ct = rem - ks * 5;
    psrc[2] = (const char*)(Wd + (size_t)(32 * ks) * 640 + 80 * cbk + 16 * ct); }
  unsigned pvo1 = (lane >> 4) * 8 * 3072 + (lane & 15) * 2, pvo2 = (lane >> 4) * 8 * 1280 + (lane & 15) * 2; pin(pvo1); pin(pvo2);
  const char* wog = ws + WS_WP + (size_t)cb3 * 102400;
  const int qtile = qb * 8 + wq * 2 + kh, cg0 = head * 5 + (head & 1), cgt = (head & 1) ? head * 5 : head * 5 + 4;
  const char* ob = ws + WS_O + (size_t)qtile * 40960;
  const char* obs[3] = {ob + (cg0 >> 3) * 1024 + ((cg0 & 7) >> 1) * 256, ob + ((cg0 + 2) >> 3) * 1024 + (((cg0 + 2) & 7) >> 1) * 256,
                        ob + (cgt >> 3) * 1024 + ((cgt & 7) >> 1) * 256 + (cgt & 1) * 8};
  unsigned vo = (unsigned)((r32 >> 4) * 20480 + (r32 & 15) * 16 + hh * 8); pin(vo);
  u32x4 fill = {0xffffffffu, 0xffffffffu, 0xffffffffu, 0xffffffffu}; pin(fill);
  const u32x2 fill2 = {fill[0], fill[1]};
  u32x2 fa, fb, fc;
  const unsigned fvc = (unsigned)CTL_FR + (unsigned)((cb4 * 60 + min(lane, 59)) * 8);
  const unsigned fva = (unsigned)(hh ? CTL_F4 : CTL_F2) + (unsigned)(((rb3 * 2 + ((w & 3) >> 1)) * 32 + r32) * 8), fvb = (unsigned)CTL_F3 + (unsigned)(((rb4 * 16 + w * 2) * 8 + (lane & 15)) * 8);
  const char* gcl = ws + WS_G + (size_t)(rb4 * 16 + w * 2) * 49152 + cb4 * 1536;
  const char* hcl = ws + WS_H + (size_t)(rb3 * 4 + w) * 20480 + cb3 * 2560;
  const char* rdst = ws + WS_WP + (size_t)(NFR_WO + 2 * gw) * 1024;
  pins(psrc[0]); pins(psrc[1]); pins(psrc[2]); pins(obs[0]); pins(obs[1]); pins(obs[2]); pins(wog); pins(gcl); pins(hcl); pins(rdst);
  STAMP_AT(1, 0);
  unsigned f0;
  {
    const int cfl = (20 * head) >> 5, cfh = (20 * head + 19) >> 5;
    f0 = (unsigned)(((((lane & 32) ? 40 : 20) + ((lane & 16) ? cfh : cfl)) * 32 + kh * 16 + (lane & 15)) * 8);
    const unsigned f1 = (unsigned)((lane < 32 ? (((lane & 1) ? cfh : cfl) * 32 + qb * 4 + wq) : 1920 + cb3 * 16 + (lane & 15)) * 8);
    poll2(ctr + CTL_F1, f0 & ~(12u * 8u), f1, tag);
  }
  STAMP_AT(1, 4);
#pragma unroll
  for (int i = 0; i < 4; ++i) glds16<true>(kvg + i * 4096, voff, kvl + i * 4096);
  bf16x8 q0[2]; s16x4 q1[2];
  {
    const char* qg = ws + WS_Q + ((size_t)head * 64 + qb * 8 + wq * 2) * 1280;
    gld16<0>(q0[0], qg, voff); gld16<1280>(q0[1], qg, voff); gld8<1024>(q1[0], qg, r32 * 8); gld8<2304>(q1[1], qg, r32 * 8);
  }
  const unsigned ka0 = lds0 + kh * 81920 + lane * 16, ka1 = lds0 + kh * 81920 + 1024 + r32 * 8, va = lds0 + kh * 81920 + 1280 + (hh * 20 + (r32 < 20 ? r32 : 0)) * 16;
  bf16x8 K0[2], V0[2], V1[2]; s16x4 K1[2];
  V0[0] = V0[1] = V1[0] = V1[1] = (bf16x8)(short)(r32 == 20 ? 0x3f80 : 0);
  pin(V0[0]); pin(V0[1]); pin(V1[0]); pin(V1[1]);
  WAITV_BAR(0);
  u32x2 fr; gld8<0>(fr, ctr + CTL_F1, f0);
  pin(q0[0]); pin(q0[1]); pin(q1[0]); pin(q1[1]);
  if (hh) { q1[0] = (s16x4){0, 0, 0, 0}; q1[1] = (s16x4){0, 0, 0, 0}; }
  STAMP_AT(1, 1);
  rdK<0>(K0[0], K1[0], ka0, ka1);
  const unsigned long long vmsk = 0x000fffff000fffffull;
  rdV<0>(V0[0], V1[0], va, vmsk);
  f32x16 cm[2];
  {
    const bf16x8 kf0 = lds128(lds0 + lane * 16); const s16x4 kf1 = lds64(lds0 + 1024 + r32 * 8);
#pragma unroll
    for (int t = 0; t < 2; ++t) {
      f32x16 s = MFMA32(kf0, q0[t], (f32x16){}); s = MFMA32K8(kf1, q1[t], s);
      float m = s[0];
#pragma unroll
      for (int i = 1; i < 16; ++i) m = fmaxf(m, s[i]);
      m = max32(m);
      cm[t] = (f32x16)(-m);
      pin(cm[t]);
    }
  }
  asm volatile("s_waitcnt lgkmcnt(0)" : "+v"(K0[0]), "+v"(K1[0]), "+v"(V0[0]), "+v"(V1[0]));
  f32x16 o[2] = {};
  f32x16 SA, SB_;
  u32x4 PA0, PA1, PB0 = {}, PB1 = {};
  SA = MFMA32(K0[0], q0[0], cm[0]); SA = MFMA32K8(K1[0], q1[0], SA);
  waitv<0>(); pin(fr);
  if (!__all(fr[0] == tag[0] && fr[1] == tag[1])) poll(ctr + CTL_F1, f0, tag);
  STAMP_AT(1, 2);
  pin(o[0]); pin(o[1]); pin(PB0); pin(PB1);
#define GRP(MF, Sc, Pc, i, j) do { MF; SB(); const float e0 = EX2(Sc[i]), e1 = EX2(Sc[i + 1]), e2 = EX2(Sc[i + 2]), e3 = EX2(Sc[i + 3]); \
    Pc[j] = pk_bf16(e0, e1); Pc[j + 1] = pk_bf16(e2, e3); pin(Pc); SB(); } while (0)
#define SLOT(QK, Sc, Pc0, Pc1, Sn, kb, jn, Pp0, Pp1, vb, jp) do { \
    GRP(if constexpr (QK) Sn = MFMA32(K0[kb], q0[jn], cm[jn]), Sc, Pc0, 0, 0); \
    GRP(if constexpr (QK) Sn = MFMA32K8(K1[kb], q1[jn], Sn), Sc, Pc0, 4, 2); \
    GRP(o[jp] = MFMA32(V0[vb], __builtin_bit_cast(bf16x8, Pp0), o[jp]), Sc, Pc1, 8, 0); \
    GRP(o[jp] = MFMA32(V1[vb], __builtin_bit_cast(bf16x8, Pp1), o[jp]), Sc, Pc1, 12, 2); } while (0)
#define LWAIT(kb, vb) asm volatile("s_waitcnt lgkmcnt(0)" : "+v"(K0[kb]), "+v"(K1[kb]), "+v"(V0[vb]), "+v"(V1[vb]))
  auto rstore = [&](auto r_, auto yv_) {
    constexpr int r = r_;
    waitv<yv_>();
    sfor<8>([&](auto j) { pin(pw[r][j]); });
    const u32x4 v = {pw[r][0] | (pw[r][1] << 16), pw[r][2] | (pw[r][3] << 16), pw[r][4] | (pw[r][5] << 16), pw[r][6] | (pw[r][7] << 16)};
    if (gw < 1920) {
      if constexpr (r < 2) {
        st16s(rdst + r * 1024, voff, v);
        float c = 0.f;
#pragma unroll
        for (int j = 0; j < 4; ++j) c = dot2(v[j], 0x3f803f80u, c);
        c = red4(c);
        if (lane < 16) st4(ws + WS_CSP + (size_t)(2 * gw + r) * 64 + lane * 4, c);
      } else st16s(ws + WS_WP + (size_t)(NFR_WO + NFR_GU + gw) * 1024, voff, v);
    }
  };
  auto hook = [&](auto it_, auto i_) {
    constexpr int it = it_, i = i_, n = (it - 2) * 8 + i;
    if constexpr (n >= 0 && n < 2) st8s(obs[n], vo, fill2);
    if constexpr (n == 2) { if (!hh) st8s(obs[2], vo, fill2); }
    if constexpr (n >= 3 && n < 6) { const int L = (n - 3) * 64 + lane; st16s(gcl, (unsigned)(L * 16 + (L >= 96 ? 49152 - 1536 : 0)), fill); }
    if constexpr (n >= 6 && n < 9) { if (w < 4) st16s(hcl, (unsigned)(min((n - 6) * 64 + lane, 159) * 16), fill); }
    if constexpr (n == 9) { if (w < 4 && lane < 16) st8s(ws + WS_HST + (size_t)(rb3 * 4 + w) * 1024 + (cb3 >> 1) * 256 + (cb3 & 1) * 8, (unsigned)(lane * 16), fill2); }
    if constexpr (it == 5 && i == 6) {
      waitv<0>();
      setflag(ctl + CTL_FR + gw * 8, tag, lane);
      setflag(ctl + CTL_F2 + (qtile * 32 + head) * 8, tag, lane);
      setflag(ctl + CTL_F4 + ((rb4 * 8 + w) * 32 + cb4) * 8, tag, lane);
      if (w < 4) setflag(ctl + CTL_F3 + ((rb3 * 4 + w) * 8 + cb3) * 8, tag, lane);
    }
    if constexpr (it == 7 && i == 6) { gld8<0>(fa, ctr, fva); gld8<0>(fb, ctr, fvb); gld8<0>(fc, ctr, fvc); }
    if constexpr (it < 2) glds16<true>(kvg + (size_t)(it * 8 + 4 + i) * 4096, voff, kvl + (it * 8 + 4 + i) * 4096);
    else if constexpr (it < 4) gldu16<(i & 1) * 3072>(pw[it - 2][i], psrc[it - 2] + (i & ~1) * 3072, pvo1);
    else if constexpr (it == 4) {
      gldu16<(i & 3) * 1280>(pw[2][i], psrc[2] + (i & ~3) * 1280, pvo2);
      if constexpr (i == 0) rstore(IC<0>{}, IC<9>{});
      if constexpr (i == 7) rstore(IC<1>{}, IC<8>{});
    } else if constexpr (it == 5) {
      if constexpr (i == 7) rstore(IC<2>{}, IC<3>{});
    } else {
      constexpr int j = (it - 6) * 8 + i;
      const char* src = wog + (w + 8 * j) * 1024;
      if constexpr (j < 7) glds16<true>(src, voff, lds0 + w * 1024 + j * 8192);
      else if constexpr (j == 7) glds16<true>(src, voff, lds0 + w * 1024 + j * 8192 + (w >= 4 ? 20480 : 0));
      else if constexpr (j < 12) glds16<true>(src, voff, lds0 + w * 1024 + j * 8192 + 20480);
      else if constexpr (j == 12) { if (w < 4) glds16<true>(src, voff, lds0 + w * 1024 + j * 8192 + 20480); }
    }
    SB();
  };
  const unsigned ka0b = ka0 + 40960, ka1b = ka1 + 40960, vab = va + 40960;
  sfor<8>([&](auto it_) {
    constexpr int it = it_;
    if constexpr (it == 1) WAITV_BAR(6);
    if constexpr (it == 2) { WAITV_BAR(8); STAMP_AT(1, 3); }
    if constexpr (it == 4) asm volatile("s_waitcnt vmcnt(16)\n\ts_barrier" ::: "memory");
    if constexpr (it == 6) asm volatile("s_barrier" ::: "memory");
    if (((it ^ kh) & 1) == 0) __builtin_amdgcn_s_setprio(0); else __builtin_amdgcn_s_setprio(1);
    constexpr int o0 = (it & 3) * 10240;
    constexpr int on = it == 3 ? 0 : o0 + 10240;
    const unsigned a0 = it < 4 ? ka0 : ka0b, a1 = it < 4 ? ka1 : ka1b, av = it < 4 ? va : vab;
    const unsigned n0 = it < 3 ? ka0 : ka0b, n1 = it < 3 ? ka1 : ka1b, nv = it < 3 ? va : vab;
    rdK<o0 + 2560>(K0[1], K1[1], a0, a1); hook(it_, IC<0>{});                  SLOT(true, SA, PA0, PA1, SB_, 0, 1, PB0, PB1, 1, 1);
    LWAIT(1, 0); rdV<o0 + 2560>(V0[1], V1[1], av, vmsk); hook(it_, IC<1>{});   SLOT(true, SB_, PB0, PB1, SA, 1, 0, PA0, PA1, 0, 0);
    rdK<o0 + 5120>(K0[0], K1[0], a0, a1); hook(it_, IC<2>{});                  SLOT(true, SA, PA0, PA1, SB_, 1, 1, PB0, PB1, 0, 1);
    LWAIT(0, 1); rdV<o0 + 5120>(V0[0], V1[0], av, vmsk); hook(it_, IC<3>{});   SLOT(true, SB_, PB0, PB1, SA, 0, 0, PA0, PA1, 1, 0);
    rdK<o0 + 7680>(K0[1], K1[1], a0, a1); hook(it_, IC<4>{});                  SLOT(true, SA, PA0, PA1, SB_, 0, 1, PB0, PB1, 1, 1);
    LWAIT(1, 0); rdV<o0 + 7680>(V0[1], V1[1], av, vmsk); hook(it_, IC<5>{});   SLOT(true, SB_, PB0, PB1, SA, 1, 0, PA0, PA1, 0, 0);
    if constexpr (it < 7) rdK<on>(K0[0], K1[0], n0, n1);
    hook(it_, IC<6>{});                                                        SLOT(true, SA, PA0, PA1, SB_, 1, 1, PB0, PB1, 0, 1);
    LWAIT(0, 1);
    if constexpr (it < 7) rdV<on>(V0[0], V1[0], nv, vmsk);
    hook(it_, IC<7>{});                                                        SLOT(it < 7, SB_, PB0, PB1, SA, 0, 0, PA0, PA1, 1, 0);
  });
  __builtin_amdgcn_s_setprio(0);
  o[1] = MFMA32(V0[1], __builtin_bit_cast(bf16x8, PB0), o[1]);
  o[1] = MFMA32(V1[1], __builtin_bit_cast(bf16x8, PB1), o[1]);
  STAMP_AT(1, 9);
  bar();
  {
    LAS f32x4* xch = (LAS f32x4*)(uintptr_t)(lds0 + XCH_OFF + w * 3072);
    const f32x16& snd = kh ? o[0] : o[1];
#pragma unroll
    for (int g = 0; g < 3; ++g) xch[g * 64 + lane] = (f32x4){snd[4 * g], snd[4 * g + 1], snd[4 * g + 2], snd[4 * g + 3]};
  }
  bar();
  STAMP_AT(1, 10);
  float of[12];
  {
    const LAS f32x4* xch = (const LAS f32x4*)(uintptr_t)(lds0 + XCH_OFF + (w ^ 4) * 3072);
    const f32x16& own = kh ? o[1] : o[0];
#pragma unroll
    for (int g = 0; g < 3; ++g) { const f32x4 v = xch[g * 64 + lane];
#pragma unroll
      for (int j = 0; j < 4; ++j) of[4 * g + j] = own[4 * g + j] + v[j]; }
  }
  float l = hi32(of[8]);
  bool ok = (l > 8.67e-19f) && (l < 1.15e18f);
#pragma unroll
  for (int i = 0; i < 8; ++i) ok = ok && (fabsf(of[i]) < 1e37f);
  if (!hh) {
#pragma unroll
    for (int i = 8; i < 12; ++i) ok = ok && (fabsf(of[i]) < 1e37f);
  }
  if (__builtin_expect(!__all(ok), 0)) {
    __builtin_amdgcn_fence(__ATOMIC_ACQUIRE, "agent");
    const char* kv = ws + WS_KV + (size_t)head * 163840;
    const bf16x8 fq0 = kh ? q0[1] : q0[0];
    const s16x4 fq1 = kh ? q1[1] : q1[0];
    float m = -INFINITY;
    for (int t = 0; t < 64; ++t) {
      const bf16x8 kf0 = *(const bf16x8*)(kv + t * 2560 + lane * 16); const s16x4 kf1 = *(const s16x4*)(kv + t * 2560 + 1024 + r32 * 8);
      f32x16 s = MFMA32(kf0, fq0, (f32x16){}); s = MFMA32K8(kf1, fq1, s);
#pragma unroll
      for (int i = 0; i < 16; ++i) m = fmaxf(m, s[i]);
    }
    m = max32(m);
    f32x16 oo = {};
    const int vr = r32 < 20 ? r32 : 0;
    for (int t = 0; t < 64; ++t) {
      const bf16x8 kf0 = *(const bf16x8*)(kv + t * 2560 + lane * 16); const s16x4 kf1 = *(const s16x4*)(kv + t * 2560 + 1024 + r32 * 8);
      bf16x8 vf0 = *(const bf16x8*)(kv + t * 2560 + 1280 + (hh * 20 + vr) * 16), vf1 = *(const bf16x8*)(kv + t * 2560 + 1920 + (hh * 20 + vr) * 16);
      if (r32 >= 20) { vf0 = (bf16x8)(short)0x3f80; vf1 = vf0; }
      f32x16 s = MFMA32(kf0, fq0, (f32x16){}); s = MFMA32K8(kf1, fq1, s);
      u32x4 p0, p1;
#pragma unroll
      for (int i = 0; i < 4; ++i) {
        p0[i] = pk_bf16(EX2(s[2 * i] - m), EX2(s[2 * i + 1] - m));
        p1[i] = pk_bf16(EX2(s[8 + 2 * i] - m), EX2(s[8 + 2 * i + 1] - m));
      }
      oo = MFMA32(vf0, __builtin_bit_cast(bf16x8, p0), oo);
      oo = MFMA32(vf1, __builtin_bit_cast(bf16x8, p1), oo);
    }
#pragma unroll
    for (int i = 0; i < 12; ++i) of[i] = oo[i];
    l = hi32(of[8]);
  }
  const float rl = __builtin_amdgcn_rcpf(l);
  waitv<0>(); pin(fa); pin(fb); pin(fc);
  setflag(ctl + CTL_FD + gw * 8, tag, lane);
#pragma unroll
  for (int g = 0; g < 3; ++g)
    if (g < 2 || !hh) st8s(obs[g], vo, nofill(pk_bf16(of[4 * g] * rl, of[4 * g + 1] * rl), pk_bf16(of[4 * g + 2] * rl, of[4 * g + 3] * rl)));
  STAMP_AT(1, 11);
  if (!__all(fa[0] == tag[0] && fa[1] == tag[1] && fb[0] == tag[0] && fb[1] == tag[1])) poll2(ctr, fva, fvb, tag);
  if (!__all(fc[0] == tag[0] && fc[1] == tag[1])) poll(ctr, fvc, tag);
  STAMP_AT(1, 12);
}

constexpr int x_none(int) { return 0; }
constexpr int x_p4(int i) { return ((i >= 2 && i < 7) || (i >= 10 && i < 18)) ? 1 : 0; }
template <int NSTEP, int NF, int LA, int LL, int CG, int NA, int ASTR, int (*BOFF)(int), int (*XCNT)(int), class GATE, class PRE, class MM>
DI void gemm_res(const char* asrc, unsigned lds0, unsigned voff, GATE&& gate, PRE&& pre, MM&& mm) {
  bf16x8 a[NSTEP][NA];
  bf16x8 bf[LL + 1][NF];
  unsigned rd0 = lds0 + voff, rd1 = rd0 + 65536, rd2 = rd0 + 131072; pin(rd0); pin(rd1); pin(rd2);
  auto issue = [&](auto n_) { constexpr int n = n_; sfor<NA>([&](auto j) { gld16<(n & 3) * 1024>(a[n][j], asrc + (n >> 2) * 4096 + j * ASTR, voff); }); };
  constexpr auto xsum = [](int lo, int hi) { int c = 0; for (int i = lo < 0 ? 0 : lo; i <= hi; ++i) c += XCNT(i); return c; };
  auto rd = [&](auto i_, auto f_) {
    constexpr int i = i_, f = f_, off = BOFF(i * NF + f);
    bf[i % (LL + 1)][f] = lds128((off < 65536 ? rd0 : off < 131072 ? rd1 : rd2) + (off & 65535));
  };
  static_assert(NSTEP % CG == 0 && LA > CG, "groups");
  unsigned m = 0;
  auto fold = [&](auto n_) { constexpr int n = n_;
    sfor<NA>([&](auto j) { pin(a[n][j]); const u32x4 u = __builtin_bit_cast(u32x4, a[n][j]); m = max(max(m, u[0]), u[1]); m = max(max(m, u[2]), u[3]); });
    pin(m);
    if constexpr (n % CG == CG - 1) {
      if (__builtin_expect(__any(m == 0xffffffffu), 0)) {
#pragma unroll 1
        for (int spin = 0; spin < (1 << 17); ++spin) {
          RETRY_COUNT(NSTEP + NF);
          sfor<CG>([&](auto k) { issue(IC<n - CG + 1 + k>{}); });
          waitv<0>(); m = 0;
          sfor<CG>([&](auto k) { sfor<NA>([&](auto j) { pin(a[n - CG + 1 + k][j]); m = max(m, umax4(__builtin_bit_cast(u32x4, a[n - CG + 1 + k][j]))); }); });
          if (!__any(m == 0xffffffffu)) break;
        }
      }
    }
  };
  gate(IC<0>{});
  sfor<LA>([&](auto n) { issue(n); });
  waitv<LA * NA>();
  if (gate(IC<1>{})) sfor<LA>([&](auto n) { issue(n); });
  pre(IC<-1>{});
  sfor<LL>([&](auto i) { sfor<NF>([&](auto f) { rd(i, f); }); });
  waitv<(LA - CG) * NA>(); sfor<CG>([&](auto n) { fold(n); });
  sfor<NSTEP>([&](auto i_) {
    constexpr int i = i_;
    pre(i_);
    SB();
    sfor<NF>([&](auto f_) {
      constexpr int f = f_;
      if constexpr (f == NF - 1 && i + CG < NSTEP) {
        waitv<((i + LA < NSTEP ? i + LA : NSTEP - 1) - (i + CG)) * NA + xsum(i + CG - LA + 1, i)>(); fold(IC<i + CG>{}); SB(); }
      mm(f_, bf[i % (LL + 1)][f], a[i]);
      SB();
      if constexpr (i + LL < NSTEP) rd(IC<i + LL>{}, f_);
      if constexpr (f == 0 && i + LA < NSTEP) issue(IC<i + LA>{});
      SB();
    });
  });
}

__global__ __launch_bounds__(512, 2) void k_mega(const u16* __restrict__ x, const u16* __restrict__ Wqkv, const u16* __restrict__ Wo,
                                                 const u16* __restrict__ Wg, const u16* __restrict__ Wu, const u16* __restrict__ Wd,
                                                 char* __restrict__ ws, u16* __restrict__ out) {
  __shared__ __attribute__((aligned(16))) char smem[163840];
  const unsigned lds0 = lds_addr_of(smem);
  const int w = __builtin_amdgcn_readfirstlane(threadIdx.x >> 6);
  const int g8 = blockIdx.x & 7, i32 = blockIdx.x >> 3;
  char* ctl = ws + WS_CTL;
  const char* ctr = ctl + g8 * CTL_REP;
  u32x2 tag;
  if (i32 < 30) {
    const int i = (g8 & 1) * 30 + i32, rb = (g8 >> 1) * 4 + (i & 3), cb = i >> 2;
    if (w >= 4) { p1_load(x, Wqkv, lds0, w - 4, rb, cb); u32x2 ep; gld8<0>(ep, ctl, 0u); waitv<0>(); pin(ep); tag = make_tag(ep); }
    else if (cb >= 10) p1_body<true>(x, ws, lds0, w, rb, cb, tag);
    else p1_body<false>(x, ws, lds0, w, rb, cb, tag);
  } else {
    { RowStat rs; u32x2 ep; const int lane = fresh_lane(); gld8<0>(ep, ctl, 0u); rowstat_get(rs, x, blockIdx.x * 8 + w, lane); waitv<0>(); pin(ep); tag = make_tag(ep);
      rowstat_put(rs, ws, blockIdx.x * 8 + w, lane, tag); }
    p1_repack(Wo, ws, w, blockIdx.x - 240);
    publish(ctl + CTL_F1 + (1920 + (blockIdx.x - 240) * 8 + w) * 8, tag, fresh_lane());
  }
  bar();
  const int rb3 = g8 * 4 + (i32 >> 3), cb3 = i32 & 7;
  const int rb4 = (g8 >> 2) * 4 + (i32 >> 3), cb4 = (g8 & 3) * 8 + (i32 & 7);
  p2_attn(ws, Wg, Wu, Wd, lds0, w, tag, rb3, cb3, rb4, cb4);
  bar();
  const char* gug = ws + WS_WP + (size_t)NFR_WO * 1024 + (size_t)cb4 * 122880;
  const char* wdg = ws + WS_WP + (size_t)(NFR_WO + NFR_GU) * 1024 + (size_t)cb3 * 245760;
  u32x2 hp[5];
  if (w < 4) {
    const int lane = fresh_lane();
    const unsigned voff = lane * 16;
    STAMP_AT(2, 0);
    u32x2 xr[5];
    sfor<5>([&](auto ct) { gld8<ct * 32>(xr[ct], (const char*)x + (rb3 * 64 + w * 16) * 1280 + cb3 * 160, (unsigned)((lane & 15) * 1280 + (lane >> 4) * 8)); });
    const int hd = min(lane & 31, G3), cgt = (hd & 1) ? hd * 5 : hd * 5 + 4;
    const unsigned pv = (unsigned)((cgt >> 3) * 1024 + ((cgt & 7) >> 1) * 256 + (cgt & 1) * 8);
    unsigned pr;
    STAMP_AT(2, 1);
    f32x4 acc[5] = {};
    sfor<5>([&](auto ct) { pin(acc[ct]); });
    gemm_res<20, 5, P3_LA, 2, 4, 1, 0, wo_off, x_none>(ws + WS_O + (size_t)(rb3 * 4 + w) * 20480, lds0, voff,
      [&](auto k) { const char* pb = ws + WS_O + (size_t)(rb3 * 4 + w) * 20480;
        if constexpr (k == 0) { gld4u(pr, pb, pv); return false; } else { pin(pr); if (__all(pr != 0xffffffffu)) return false; probe(pb, pv); return true; } },
      [&](auto i) { if constexpr (i == -1) STAMP_AT(2, 2); if constexpr (i == 1) STAMP_AT(2, 3); if constexpr (i == 10) STAMP_AT(2, 4);
        if constexpr (i == 12) bar(); },
      [&](auto ct, const bf16x8& b, bf16x8 (&a)[1]) { mfma16(acc[ct], b, a[0]); });
    acc_fence(); sfor<5>([&](auto ct) { pin(acc[ct]); });
    STAMP_AT(2, 6);
    sfor<5>([&](auto ct) { pin(xr[ct]); });
    float s1 = 0.f, s2 = 0.f;
    const char* hb = ws + WS_H + (size_t)(rb3 * 4 + w) * 20480;
    const unsigned vt = t16lane(lane);
#pragma unroll
    for (int ct = 0; ct < 5; ++ct) {
      hp[ct] = nofill(pk_bf16(bf_lo(xr[ct][0]) + acc[ct][0], bf_hi(xr[ct][0]) + acc[ct][1]), pk_bf16(bf_lo(xr[ct][1]) + acc[ct][2], bf_hi(xr[ct][1]) + acc[ct][3]));
      st8s(hb + t16off(cb3 * 80 + ct * 16), vt, hp[ct]);
      s1 = dot2(hp[ct][0], 0x3f803f80u, s1); s1 = dot2(hp[ct][1], 0x3f803f80u, s1);
      s2 = dot2(hp[ct][0], hp[ct][0], s2); s2 = dot2(hp[ct][1], hp[ct][1], s2);
    }
    s1 = red4(s1); s2 = red4(s2);
    if (lane < 16) st8s(ws + WS_HST + (size_t)(rb3 * 4 + w) * 1024 + (cb3 >> 1) * 256 + (cb3 & 1) * 8, (unsigned)(lane * 16), nofill(__float_as_uint(s1), __float_as_uint(s2)));
    bar();
    STAMP_AT(2, 7);
  } else {
    const int lane = fresh_lane(), v = w - 4;
    const unsigned voff = lane * 16;
    sfor<5>([&](auto j) { glds16<true>(gug + (v * 5 + j) * 1024, voff, lds0 + gu_off(0) + (v * 5 + j) * 1024); });
    sfor<10>([&](auto j) { glds16<true>(gug + (20 + v * 10 + j) * 1024, voff, lds0 + gu_off(20) + (v * 10 + j) * 1024); });
    float cpart[10];
    const int cc = v * 24 + min(lane & 31, 23);
    sfor<10>([&](auto i) { gld4<i * 384>(cpart[i], ws + WS_CSP, (unsigned)(cb4 * 7680 + (lane >> 5) * 3840 + cc * 4)); });
    bar();
    sfor<15>([&](auto j) { glds16<true>(gug + (60 + v * 15 + j) * 1024, voff, lds0 + gu_off(60) + (v * 15 + j) * 1024); });
    STAMP_AT(2, 5);
    poll(ctr + CTL_FD, (unsigned)((cb3 * 240 + v * 60 + min(lane, 59)) * 8), tag);
    float cv = 0.f;
    sfor<10>([&](auto j) { pin(cpart[j]); cv += cpart[j]; });
    cv = add32(cv);
    bar();
    STAMP_AT(2, 6);
    if (lane < 24) ((LAS float*)(uintptr_t)(lds0 + CSF_OFF))[cc] = cv;
  }
  {
    const int lane = fresh_lane();
    const unsigned voff = lane * 16;
    const int r16 = lane & 15, g4 = lane >> 4;
    STAMP_AT(3, 0);
    f32x4 cs[6]; f32x2 ms[2];
    f32x4 hs[2];
    auto hs_wait = [&](bool all) {
#pragma unroll 1
      for (int spin = 0; spin < (1 << 17); ++spin) {
        gld16<0>(hs[0], ws + WS_HST + (size_t)(rb4 * 16 + w * 2) * 1024, voff);
        gld16<1024>(hs[1], ws + WS_HST + (size_t)(rb4 * 16 + w * 2) * 1024, voff);
        waitv<0>(); pin(hs[0]); pin(hs[1]);
        const bool miss = max(umax4(__builtin_bit_cast(u32x4, hs[0])), umax4(__builtin_bit_cast(u32x4, hs[1]))) == 0xffffffffu;
        if (!__any(miss && (all || g4 < G4))) return !__any(miss);
        __builtin_amdgcn_s_sleep(1);
      }
      return true;
    };
    bool hs_ok;
    STAMP_AT(3, 1);
    f32x4 acc[2][6] = {};
    sfor<6>([&](auto ct) { pin(acc[0][ct]); pin(acc[1][ct]); });
    if (w < 4) __builtin_amdgcn_s_setprio(1);
    gemm_res<20, 6, K4_LA_, 1, K4_CG_, 2, 20480, gu_off, x_p4>(ws + WS_H + (size_t)(rb4 * 16 + w * 2) * 20480, lds0, voff,
      [&](auto k) {
        if constexpr (k == 0) { gld16<0>(hs[0], ws + WS_HST + (size_t)(rb4 * 16 + w * 2) * 1024, voff); gld16<1024>(hs[1], ws + WS_HST + (size_t)(rb4 * 16 + w * 2) * 1024, voff); return false; }
        else { pin(hs[0]); pin(hs[1]);
          const bool miss = max(umax4(__builtin_bit_cast(u32x4, hs[0])), umax4(__builtin_bit_cast(u32x4, hs[1]))) == 0xffffffffu;
          hs_ok = !__any(miss);
          if (!__any(miss && g4 < G4)) return false;
          hs_ok = hs_wait(false); return true; } },
      [&](auto i) { if constexpr (i == -1) STAMP_AT(3, 2); if constexpr (i == 1) STAMP_AT(3, 3); if constexpr (i == 10) STAMP_AT(3, 4);
        if constexpr (i == 9) bar();
        if constexpr (i == 19) { if (!hs_ok) hs_wait(true); }
        if constexpr (i == 19) sfor<2>([&](auto t) {
          const float s1 = red4(hs[t][0] + hs[t][2]), s2 = red4(hs[t][1] + hs[t][3]), mean = s1 * (1.f / 640.f);
          ms[t] = (f32x2){mean, __builtin_amdgcn_rsqf(fmaxf(s2 * (1.f / 640.f) - mean * mean, 0.f) + LN_EPS)}; pin(ms[t]); });
        if constexpr (i == 18) sfor<6>([&](auto ct) { cs[ct] = *(const LAS f32x4*)(uintptr_t)(lds0 + CSF_OFF + (ct * 16 + 4 * g4) * 4); });
        if constexpr (i >= 2 && i < 7) { const int f = (i - 2) * 8 + w - ((i == 6 && w >= 4) ? 4 : 0); glds16<true>(wdg + (f) * 1024, voff, lds0 + wd_off(0) + f * 1024); }
        if constexpr (i >= 10 && i < 15) { constexpr int n = i - 10; glds16<true>(wdg + (40 + n * 8 + w) * 1024, voff, lds0 + wd_off(40) + (n * 8 + w) * 1024); }
        if constexpr (i >= 15 && i < 18) { const int f = (i - 15) * 8 + w - ((i == 17 && w >= 4) ? 4 : 0); glds16<true>(wdg + (140 + f) * 1024, voff, lds0 + wd_off(140) + f * 1024); } },
      [&](auto ct, const bf16x8& b, bf16x8 (&a)[2]) { mfma16(acc[0][ct], b, a[0]); mfma16(acc[1][ct], b, a[1]); });
    acc_fence(); sfor<6>([&](auto ct) { pin(acc[0][ct]); pin(acc[1][ct]); });
    STAMP_AT(3, 6);
    constexpr float GC0 = -2.f * 0.7978845608028654f * 1.4426950408889634f, GC1 = GC0 * 0.044715f;
    const char* gb = ws + WS_G + (size_t)(rb4 * 16 + w * 2) * 49152;
    const unsigned vt = t16lane(lane);
#pragma unroll
    for (int t = 0; t < 2; ++t) {
      const float nm = -ms[t].x, rstd = ms[t].y;
#pragma unroll
      for (int ct = 0; ct < 3; ++ct) {
        const f32x4 gv = (acc[t][ct] + cs[ct] * nm) * rstd, uv = (acc[t][3 + ct] + cs[3 + ct] * nm) * rstd;
        const f32x4 z = gv * (gv * gv * GC1 + GC0);
        f32x4 e;
#pragma unroll
        for (int j = 0; j < 4; ++j) e[j] = __builtin_amdgcn_exp2f(z[j]);
        e = e + 1.f;
#pragma unroll
        for (int j = 0; j < 4; ++j) e[j] = __builtin_amdgcn_rcpf(e[j]);
        const f32x4 r = gv * uv * e;
        st8s(gb + t * 49152 + t16off(cb4 * 48 + ct * 16), vt, nofill(pk_bf16(r[0], r[1]), pk_bf16(r[2], r[3])));
      }
    }
    __builtin_amdgcn_s_setprio(0);
    STAMP_AT(3, 7);
    waitv<6>();
    STAMP_AT(3, 8);
    bar();
  }
  if (w < 4) {
    const int lane = fresh_lane();
    const unsigned voff = lane * 16;
    const int row = rb3 * 64 + w * 16 + (lane & 15), col0 = cb3 * 80 + 4 * (lane >> 4);
    STAMP_AT(4, 0);
    const unsigned pv = (unsigned)t16off(min(lane & 31, G5) * 48 + 32);
    unsigned pr;
    STAMP_AT(4, 1);
    f32x4 acc[5] = {};
    sfor<5>([&](auto ct) { pin(acc[ct]); });
    gemm_res<48, 5, K5_LA_, 2, 4, 1, 0, wd_off, x_none>(ws + WS_G + (size_t)(rb3 * 4 + w) * 49152, lds0, voff,
      [&](auto k) { const char* pb = ws + WS_G + (size_t)(rb3 * 4 + w) * 49152;
        if constexpr (k == 0) { gld4u(pr, pb, pv); return false; } else { pin(pr); if (__all(pr != 0xffffffffu)) return false; probe(pb, pv); return true; } },
      [&](auto i) { if constexpr (i == 1) STAMP_AT(4, 3); if constexpr (i == 14) STAMP_AT(4, 4); if constexpr (i == 30) STAMP_AT(4, 5);
        if constexpr (i == -1 || i == 8 || i == 14 || i == 16 || i == 30 || i == 38) bar(); },
      [&](auto ct, const bf16x8& b, bf16x8 (&a)[1]) { mfma16(acc[ct], b, a[0]); });
    acc_fence(); sfor<5>([&](auto ct) { pin(acc[ct]); });
    STAMP_AT(4, 12);
#pragma unroll
    for (int ct = 0; ct < 5; ++ct) {
      const unsigned p0 = pk_bf16(bf_lo(hp[ct][0]) + acc[ct][0], bf_hi(hp[ct][0]) + acc[ct][1]);
      const unsigned p1 = pk_bf16(bf_lo(hp[ct][1]) + acc[ct][2], bf_hi(hp[ct][1]) + acc[ct][3]);
      *(u32x2*)(out + (size_t)row * 640 + col0 + 16 * ct) = (u32x2){p0, p1};
    }
    STAMP_AT(4, 13);
  } else {
    const int lane = fresh_lane(), v = w - 4;
    const unsigned voff = lane * 16;
    glds16<true>(wdg + (36 + v) * 1024, voff, lds0 + wd_off(36) + v * 1024);
    waitv<0>(); bar();
    sfor<3>([&](auto g) { sfor<5>([&](auto j) { constexpr int f = (g + 4) * 20 + j; glds16<true>(wdg + (f + v * 5) * 1024, voff, lds0 + wd_off(f) + v * 5120); }); });
    bar();
    sfor<2>([&](auto g) { sfor<5>([&](auto j) { constexpr int f = (g + 8) * 20 + j; glds16<true>(wdg + (f + v * 5) * 1024, voff, lds0 + wd_off(f) + v * 5120); }); });
    waitv<10>(); bar();
    bar();
    sfor<2>([&](auto g) { sfor<5>([&](auto j) { constexpr int f = (g + 10) * 20 + j; glds16<true>(wdg + (f + v * 5) * 1024, voff, lds0 + wd_off(f) + v * 5120); }); });
    waitv<10>(); bar();
    waitv<0>(); bar();
    if (blockIdx.x == 0 && w == 4) {
      poll2(ctr + CTL_FR, (unsigned)(lane * 64), (unsigned)(4096 + lane * 64), tag);
      poll2(ctr + CTL_FR, (unsigned)(8192 + lane * 64), (unsigned)(12288 + lane * 64), tag);
      if (lane == 0) st8(ctl, tag);
    }
  }
}

extern "C" void kernel_launch(void* const* d_in, const int* in_sizes, int n_in, void* d_out, int out_size, void* d_ws, size_t ws_size,
                              hipStream_t stream) {
  k_mega<<<256, 512, 0, stream>>>((const u16*)d_in[0], (const u16*)d_in[1], (const u16*)d_in[2], (const u16*)d_in[4], (const u16*)d_in[5],
                                  (const u16*)d_in[6], (char*)d_ws, (u16*)d_out);
}
```
